```python
import torch

def _softmax(x, axis=-1):
    return torch.softmax(x, dim=axis)
def _gelu(x):
    return torch.nn.functional.gelu(x, approximate="tanh")
def _silu(x):
    return torch.nn.functional.silu(x)
def _sigmoid(x):
    return torch.sigmoid(x)
def _layernorm(x, axis=-1, eps=1e-5):
    m = x.mean(dim=axis, keepdim=True)
    v = x.var(dim=axis, keepdim=True, unbiased=False)
    return (x - m) / torch.sqrt(v + eps)
def _scatter_add(tgt, idx, src, axis=0):
    return tgt.clone().index_add_(axis, idx, src)
def _segment_sum(src, seg, n):
    out = torch.zeros((n,) + src.shape[1:], dtype=src.dtype)
    return out.index_add_(0, seg, src)
def _pad(x, pw):
    flat = tuple(v for pair in reversed(pw) for v in pair)
    return torch.nn.functional.pad(x, flat)

kernel_name = 'interleave_layers__sliding_block_cached__glu_gated_swiglu__decode'

def setup_inputs():

    _w = (lambda a: (lambda t: t.to(torch.bfloat16) if t.is_floating_point() else t)(torch.as_tensor(a)))
    def _A_setup():
        B, Sq, D = 1, 1, 640
        Hq, Hkv, Sk, Dh = 32, 32, 32768, 192
        assert Hq % Hkv == 0, (Hq, Hkv)
        return {
            "x": _w((lambda *s: torch.randn(*s, dtype=torch.bfloat16) * 0.05)(B, Sq, D)),
            "k_cache": _w((lambda *s: torch.randn(*s, dtype=torch.bfloat16) * 0.8)(B, Hkv, Sk, Dh)),
            "v_cache": _w((lambda *s: torch.randn(*s, dtype=torch.bfloat16) * 1.6)(B, Hkv, Sk, Dh)),
            "wq": _w((lambda *s: torch.randn(*s, dtype=torch.bfloat16) * 0.8)(D, Hq * Dh) * (D ** -0.5)),
            "wo": _w((lambda *s: torch.randn(*s, dtype=torch.bfloat16) * 1.6)(Hq * Dh, D) * ((Hq * Dh) ** -0.5)),
        }
    _A_in = _A_setup()

    def _B_setup():
        B, S, D, F = 1, 1, 640, 1536
        return {
            "x": _w((lambda *s: torch.randn(*s, dtype=torch.bfloat16) * 0.05)(B, S, D)),
            "w1": _w((lambda *s: torch.randn(*s, dtype=torch.bfloat16) * 0.4)(D, F) * (2.0 / D) ** 0.5),
            "w3": _w((lambda *s: torch.randn(*s, dtype=torch.bfloat16) * 0.4)(D, F) * (2.0 / D) ** 0.5),
            "w2": _w((lambda *s: torch.randn(*s, dtype=torch.bfloat16) * 0.4)(F, D) * (2.0 / F) ** 0.5),
        }
    _B_in = _B_setup()

    return {
        "A_x": _A_in["x"],
        "A_k_cache": _A_in["k_cache"],
        "A_v_cache": _A_in["v_cache"],
        "A_wq": _A_in["wq"],
        "A_wo": _A_in["wo"],
        "B_x": _B_in["x"],
        "B_w1": _B_in["w1"],
        "B_w3": _B_in["w3"],
        "B_w2": _B_in["w2"],
    }

def reference(A_x, A_k_cache, A_v_cache, A_wq, A_wo, B_x, B_w1, B_w3, B_w2):
    def _A_ref(x, k_cache, v_cache, wq, wo):
        B, Sq, D = x.shape
        Hq, Dh = 32, 192
        Hkv, Sk = k_cache.shape[1], k_cache.shape[2]
        W = min(4096, max(256, Sk // 4))
        rep = Hq // Hkv
        q = (lambda x, *d: x.permute(*d))((x @ wq).reshape(B, Sq, Hq, Dh), 0, 2, 1, 3)
        k = (lambda x, n, axis: torch.repeat_interleave(x, n, dim=axis))(k_cache[:, :, Sk - W:, :], rep, axis=1)
        v = (lambda x, n, axis: torch.repeat_interleave(x, n, dim=axis))(v_cache[:, :, Sk - W:, :], rep, axis=1)
        scores = torch.einsum("bhqd,bhkd->bhqk", q, k) * (Dh ** -0.5)
        p = _softmax(scores, axis=-1)
        o = torch.einsum("bhqk,bhkd->bhqd", p, v)
        return (lambda x, *d: x.permute(*d))(o, 0, 2, 1, 3).reshape(B, Sq, Hq * Dh) @ wo

    def _B_ref(x, w1, w3, w2):
        gate = torch.einsum("bsd,df->bsf", x, w1)
        up = torch.einsum("bsd,df->bsf", x, w3)
        h = _silu(gate) * up
        out = torch.einsum("bsf,fd->bsd", h, w2)
        return out

    x = A_x
    x = x + _A_ref(_layernorm(x, axis=-1), A_k_cache, A_v_cache, A_wq, A_wo)
    x = x + _B_ref(_layernorm(x, axis=-1), B_w1, B_w3, B_w2)
    x = x + _A_ref(_layernorm(x, axis=-1), A_k_cache, A_v_cache, A_wq, A_wo)
    x = x + _B_ref(_layernorm(x, axis=-1), B_w1, B_w3, B_w2)
    return x
```

```cpp
#define PROBE 2
#include <hip/hip_runtime.h>
#include <hip/hip_bf16.h>
#include <stdint.h>

namespace {

typedef unsigned int u32;
typedef unsigned short u16;
typedef unsigned long long u64;
using u32x2 = __attribute__((ext_vector_type(2))) unsigned;
using u32x4 = __attribute__((ext_vector_type(4))) unsigned;
using f32x2 = __attribute__((ext_vector_type(2))) float;
using f32x4 = __attribute__((ext_vector_type(4))) float;
using s16x4 = __attribute__((ext_vector_type(4))) short;
using bf16x2 = __attribute__((ext_vector_type(2))) __bf16;
using bf16x8 = __attribute__((ext_vector_type(8))) __bf16;
#define LAS __attribute__((address_space(3)))

constexpr int D = 640, H = 32, DH = 192, WIN = 4096, SK = 32768, F = 1536;
constexpr int NWG = 256, NT = 512;
constexpr u32 SPIN_CAP = 1u << 16;
constexpr float SCL = 0.07216878364870322f * 1.4426950408889634f;

#define T_O 43
#define T_YF 122
#define T_H 120
#define T_AN 35
#define PTHR 60
#define T_LAZY 60

#define A_E 122
#define A_Q (A_E + 84)
#define A_O (A_Q + 146)
#define A_Y (A_O + 96)
#define A_H (A_Y + 94)
#define A_F (A_H + 58)

constexpr u32 SIDE = 8192, AREA = 16 * 16384;
constexpr u32 A_GO = 0;
constexpr u32 A_GQ = 4 * 16384;
constexpr u32 A_GH = A_GQ + 7168;
constexpr u32 A_GY = 8 * 16384;
constexpr u32 GO_SZ = 32768, GY_PAIR = 65536, GY_INST = 32768;
constexpr u32 WS_CLR = 4 * AREA;
constexpr u32 WS_PB = WS_CLR;
constexpr u32 WS_GOF = WS_PB + (128 << 10), WS_HB = WS_GOF + 4096;
constexpr u32 WS_GQ1 = WS_HB + 256 * 128;
constexpr u32 WS_END = WS_GQ1 + 256 * 2048;
static_assert(WS_END + 4096 <= (2u << 20) && WS_CLR == 256 * 4096, "layout");
__device__ __forceinline__ u32 go_slot(int w) { return (u32)(w >> 2) * 16384u + (u32)(w & 3) * 2048u; }
__device__ __forceinline__ u32 gy_row(int i) { return go_slot(i); }
__device__ __forceinline__ u32 gy_chunk(int u) {
  const int i = (u * 205) >> 11;
  return gy_row(i) + (u32)(u - 10 * i) * 16u;
}
__device__ __forceinline__ u32 st_line(int g) { return A_GH + 768u + (u32)(g >> 1) * 16384u + (u32)(g & 1) * 128u; }
__device__ __forceinline__ u32 gy_rep(int r) { return (u32)(r & 3) * AREA + (u32)(r >> 2) * SIDE; }

constexpr int L_ZERO = 0;
constexpr int L_CNT = 16;
constexpr int L_RI = 28;
constexpr int L_ONE = 32;
constexpr int L_TAG = 48;
constexpr int L_ST = 64;
constexpr int L_VOTE = L_ST;
constexpr int L_ANC = 192;
constexpr int L_Q = 256;
constexpr int L_CS = 640;
constexpr int L_HB = L_CS + 768;
constexpr int L_ACT = L_HB + D * 2;
constexpr int L_RED = L_ACT + 2 * 384;
constexpr int L_P = L_RED + 32 * 144;
constexpr int P_W = 1280, P_U = 592, P_PAR = 288;
constexpr int OM_S = 400, OM_P = 200;
constexpr int L_OM = L_P + 8 * P_W;
constexpr int L_LN = L_OM;
constexpr int L_LAND = (L_OM + 8 * OM_S * 4 + 255) / 256 * 256;
constexpr int L_WQ = L_LAND + 8 * 2048;
constexpr int L_WB = L_WQ + 6 * 5120;
constexpr int L_W2 = L_WB + 768 * 64;
constexpr int L_PF = L_W2 + 192 * 64;
constexpr int L_TAB = L_PF + 256;
constexpr int L_TOTAL = L_TAB + 8 * 2048;
static_assert(L_LAND % 256 == 0 && L_P % 16 == 0 && L_WQ % 16 == 0 && L_HB % 16 == 0 && L_RED % 16 == 0, "alignment");

struct Params {
  const u16* x0;
  const u16* kc;
  const u16* vc;
  const u16* wq;
  const u16* wo;
  const u16* w1;
  const u16* w3;
  const u16* w2;
  u16* out;
  u32* ws;
};

__device__ __forceinline__ u32 cvt2(float lo, float hi) {
  return __builtin_bit_cast(u32, __builtin_convertvector(f32x2{lo, hi}, bf16x2));
}
__device__ __forceinline__ float bflo(u32 w) { return __uint_as_float(w << 16); }
__device__ __forceinline__ float bfhi(u32 w) { return __uint_as_float(w & 0xffff0000u); }
__device__ __forceinline__ float bf16r(float x) { return bflo(cvt2(x, x)); }

template <int CTRL>
__device__ __forceinline__ float dpp(float x) {
  return __builtin_bit_cast(float, __builtin_amdgcn_mov_dpp(__builtin_bit_cast(int, x), CTRL, 0xf, 0xf, true));
}
constexpr int XOR1 = 0xB1, XOR2 = 0x4E, XOR7 = 0x141, ROR8 = 0x128;
__device__ __forceinline__ float fmx(float a, float b) {
  float r;
  asm("v_max_f32 %0, %1, %2" : "=v"(r) : "v"(a), "v"(b));
  return r;
}
__device__ __forceinline__ float xrow16_max(float x) {
  auto s = __builtin_amdgcn_permlane16_swap(__float_as_uint(x), __float_as_uint(x), false, false);
  x = fmx(__uint_as_float(s[0]), __uint_as_float(s[1]));
  auto t = __builtin_amdgcn_permlane32_swap(__float_as_uint(x), __float_as_uint(x), false, false);
  return fmx(__uint_as_float(t[0]), __uint_as_float(t[1]));
}
__device__ __forceinline__ float xrow16_sum(float x) {
  auto s = __builtin_amdgcn_permlane16_swap(__float_as_uint(x), __float_as_uint(x), false, false);
  x = __uint_as_float(s[0]) + __uint_as_float(s[1]);
  auto t = __builtin_amdgcn_permlane32_swap(__float_as_uint(x), __float_as_uint(x), false, false);
  return __uint_as_float(t[0]) + __uint_as_float(t[1]);
}
__device__ __forceinline__ float sum8(float v) {
  v += dpp<XOR1>(v);
  v += dpp<XOR2>(v);
  v += dpp<XOR7>(v);
  return v;
}
__device__ __forceinline__ float max8(float v) {
  asm("s_nop 1\n\tv_max_f32_dpp %0, %0, %0 quad_perm:[1,0,3,2] row_mask:0xf bank_mask:0xf\n\t"
      "s_nop 1\n\tv_max_f32_dpp %0, %0, %0 quad_perm:[2,3,0,1] row_mask:0xf bank_mask:0xf\n\t"
      "s_nop 1\n\tv_max_f32_dpp %0, %0, %0 row_half_mirror row_mask:0xf bank_mask:0xf"
      : "+v"(v));
  return v;
}
__device__ __forceinline__ float max_ror8(float v) {
  asm("s_nop 1\n\tv_max_f32_dpp %0, %0, %0 row_ror:8 row_mask:0xf bank_mask:0xf" : "+v"(v));
  return v;
}
__device__ __forceinline__ f32x4 sum4x4(f32x4 v) {
  float a = v[0], b = v[1], c = v[2], d = v[3];
#define S4(CTL)                                               \
  "v_add_f32_dpp %0, %0, %0 " CTL " row_mask:0xf bank_mask:0xf\n\t" \
  "v_add_f32_dpp %1, %1, %1 " CTL " row_mask:0xf bank_mask:0xf\n\t" \
  "v_add_f32_dpp %2, %2, %2 " CTL " row_mask:0xf bank_mask:0xf\n\t" \
  "v_add_f32_dpp %3, %3, %3 " CTL " row_mask:0xf bank_mask:0xf\n\t"
  asm("s_nop 1\n\t" S4("quad_perm:[1,0,3,2]") S4("quad_perm:[2,3,0,1]") : "+v"(a), "+v"(b), "+v"(c), "+v"(d));
#undef S4
  return f32x4{a, b, c, d};
}
__device__ __forceinline__ float wave_sum(float v) {
  v = sum8(v);
  v += dpp<ROR8>(v);
  return xrow16_sum(v);
}
__device__ __forceinline__ float tsum8(f32x4 a) {
  auto s0 = __builtin_amdgcn_permlane32_swap(__float_as_uint(a[0]), __float_as_uint(a[1]), false, false);
  auto s1 = __builtin_amdgcn_permlane32_swap(__float_as_uint(a[2]), __float_as_uint(a[3]), false, false);
  const float r0 = __uint_as_float(s0[0]) + __uint_as_float(s0[1]);
  const float r1 = __uint_as_float(s1[0]) + __uint_as_float(s1[1]);
  auto t = __builtin_amdgcn_permlane16_swap(__float_as_uint(r0), __float_as_uint(r1), false, false);
  float r = __uint_as_float(t[0]) + __uint_as_float(t[1]);
  r += dpp<ROR8>(r);
  return r;
}

__device__ __forceinline__ u32 tagv(float v) { return __float_as_uint(v) | 1u; }
__device__ __forceinline__ void store_quad(__amdgpu_buffer_rsrc_t rs, u32 off, f32x4 v) {
  const u32x4 s = {tagv(v[0]), tagv(v[1]), tagv(v[2]), tagv(v[3])};
  __builtin_amdgcn_raw_buffer_store_b128(s, rs, off, 0, 16);
}
template <int N>
__device__ __forceinline__ void gpoll4(u32x4 rs, const u32 (&off)[N], f32x4 (&v)[N]) {
  bool need = true;
  u32 budget = SPIN_CAP;
#pragma unroll
  for (int k = 0; k < N; ++k) asm volatile("" : "=v"(v[k]));
#pragma unroll 1
  for (;;) {
    if (need) {
#pragma unroll
      for (int k = 0; k < N; ++k)
        asm volatile("buffer_load_dwordx4 %0, %1, %2, 0 offen sc1" : "+v"(v[k]) : "v"(off[k]), "s"(rs) : "memory");
    }
    asm volatile("s_waitcnt vmcnt(0)" ::: "memory");
    u32 a = 1u;
#pragma unroll
    for (int k = 0; k < N; ++k) {
      asm volatile("" : "+v"(v[k]));
      a &= __float_as_uint(v[k][0]) & __float_as_uint(v[k][1]) & __float_as_uint(v[k][2]) & __float_as_uint(v[k][3]);
    }
    need = a == 0u;
    if (__builtin_amdgcn_ballot_w64(need) == 0ull || budget == 0u) break;
    --budget;
    __builtin_amdgcn_s_sleep(1);
  }
}
__device__ __forceinline__ u32 now() { return (u32)__builtin_amdgcn_s_memtime(); }
__device__ __forceinline__ void wait_until(u32 t) {
#pragma unroll 1
  while ((int)(now() - t) < -160) __builtin_amdgcn_s_sleep(1);
#pragma unroll 1
  while ((int)(now() - t) < 0) {
  }
}
__device__ __forceinline__ u32 rtnow() { return (u32)__builtin_amdgcn_s_memrealtime(); }
__device__ __forceinline__ void wait_rt(u32 t) {
  u32 spins = 0;
#pragma unroll 1
  while ((int)(rtnow() - t) < -8 && ++spins < 400u) __builtin_amdgcn_s_sleep(1);
#pragma unroll 1
  while ((int)(rtnow() - t) < 0 && ++spins < 440u) {
  }
}
template <int BITS>
__device__ __forceinline__ u32 youngest(u32 t);
__device__ __forceinline__ void lds_wait(volatile LAS u32* p, u32 want) {
  u32 spins = 0;
#pragma unroll 1
  while (__builtin_amdgcn_readfirstlane(*p) != want) {
    if (++spins > (SPIN_CAP << 6)) break;
    __builtin_amdgcn_s_sleep(1);
  }
  asm volatile("" ::: "memory");
}
__device__ __forceinline__ u32 lds_wait_nz(volatile LAS u32* p) {
  u32 spins = 0, v;
#pragma unroll 1
  while ((v = __builtin_amdgcn_readfirstlane(*p)) == 0u) {
    if (++spins > (SPIN_CAP << 6)) break;
    __builtin_amdgcn_s_sleep(1);
  }
  asm volatile("" ::: "memory");
  return v;
}
template <int BITS>
__device__ __forceinline__ u32 youngest(u32 t) {
  const u32 nw = rtnow();
  const float age = (float)((nw - t) & ((1u << BITS) - 1u));
  return nw - (u32)__builtin_amdgcn_readfirstlane((int)-xrow16_max(max_ror8(max8(-age))));
}
__device__ __forceinline__ void stamp_poll(unsigned char* smem, u32x4 rq, u32 so) {
  u32 sv, budget = SPIN_CAP;
#pragma unroll 1
  for (;;) {
    asm volatile("buffer_load_dword %0, %1, %2, 0 offen sc1\n\ts_waitcnt vmcnt(0)" : "=&v"(sv) : "v"(so), "s"(rq) : "memory");
    if (__builtin_amdgcn_ballot_w64((sv & 1u) == 0u) == 0ull || budget == 0u) break;
    --budget;
    __builtin_amdgcn_s_sleep(1);
  }
  *(volatile LAS u32*)(smem + L_ANC) = youngest<24>(sv >> 1) | 1u;
}
__device__ __forceinline__ void lds_add(unsigned char* smem, int off, u32 v) {
  __hip_atomic_fetch_add((LAS u32*)(smem + off), v, __ATOMIC_RELAXED, __HIP_MEMORY_SCOPE_WORKGROUP);
}

__device__ __forceinline__ int fresh(int t) {
  asm volatile("" : "+v"(t));
  return t;
}
__device__ __forceinline__ f32x4 zero4() {
  f32x2 a, b;
  asm volatile("v_mov_b64 %0, 0\n\tv_mov_b64 %1, 0" : "=v"(a), "=v"(b));
  return f32x4{a[0], a[1], b[0], b[1]};
}
__device__ __forceinline__ void bar() { asm volatile("s_waitcnt lgkmcnt(0)\n\ts_barrier" ::: "memory"); }

__device__ __forceinline__ u32x2 trread(unsigned char* smem, int off) {
  return __builtin_bit_cast(u32x2, __builtin_amdgcn_ds_read_tr16_b64_v4i16((LAS s16x4*)(smem + off)));
}
__device__ __forceinline__ u32x4 wfrag(unsigned char* smem, int lb, int off) {
  const u32x2 a = trread(smem, lb + off), b = trread(smem, lb + off + 256);
  return u32x4{a[0], a[1], b[0], b[1]};
}
__device__ __forceinline__ int wlane(int lane, int cb) {
  const int i = lane & 15, G = lane >> 4;
  return (8 * G + (i >> 2)) * 64 + (i & 3) * 8 + ((cb ^ (G & 1)) * 32);
}
__device__ __forceinline__ u32x4 afrag(unsigned char* smem, int a) { return *reinterpret_cast<const u32x4*>(smem + a); }
__device__ __forceinline__ f32x4 mfma32(u32x4 w, u32x4 a, f32x4 c) {
  return __builtin_amdgcn_mfma_f32_16x16x32_bf16(__builtin_bit_cast(bf16x8, w), __builtin_bit_cast(bf16x8, a), c, 0, 0, 0);
}

__device__ __forceinline__ void dma_piece(const u16* src, int ld, unsigned char* smem, int ldsoff, int p, int lane) {
  const int row = 16 * p + (lane >> 2), part = lane & 3;
  const int cb = (part >> 1) ^ (lane >> 5);
  const u32 voff = (u32)(row * ld + cb * 16 + (part & 1) * 8) * 2u;
  const u32 dst = (u32)(uintptr_t)(LAS unsigned char*)smem + (u32)(ldsoff + p * 1024);
  u32 keep;
  asm volatile("s_mov_b32 %0, m0\n\ts_mov_b32 m0, %2\n\ts_nop 2\n\tglobal_load_lds_dwordx4 %1, %3 nt\n\ts_mov_b32 m0, %0"
               : "=&s"(keep)
               : "v"(voff), "s"(dst), "s"(src)
               : "memory");
}

template <int CTRL>
__device__ __forceinline__ u32 dppu(u32 x) {
  return (u32)__builtin_amdgcn_mov_dpp((int)x, CTRL, 0xf, 0xf, true);
}
__device__ __forceinline__ float sum4(f32x4 d) { return (d[0] + d[1]) + (d[2] + d[3]); }
__device__ __forceinline__ u32 and4(u32x4 v) { return v[0] & v[1] & v[2] & v[3]; }
__device__ __forceinline__ void store16(__amdgpu_buffer_rsrc_t rs, u32 off, u32x4 v) {
  __builtin_amdgcn_raw_buffer_store_b128(v, rs, off, 0, 16);
}
#define POLL16(dst, voff, imm) \
  asm volatile("buffer_load_dwordx4 %0, %1, %2, 0 offen offset:%3 sc1" : "+v"(dst) : "v"(voff), "s"(rq), "n"(imm) : "memory")

struct LnS {
  float mb, ri;
};
__device__ __forceinline__ LnS ln_stats(float s1, float s2) {
#define S2(CTL)                                                     \
  "v_add_f32_dpp %0, %0, %0 " CTL " row_mask:0xf bank_mask:0xf\n\t" \
  "v_add_f32_dpp %1, %1, %1 " CTL " row_mask:0xf bank_mask:0xf\n\ts_nop 0\n\t"
  asm("s_nop 1\n\t" S2("quad_perm:[1,0,3,2]") S2("quad_perm:[2,3,0,1]") S2("row_half_mirror") : "+v"(s1), "+v"(s2));
#undef S2
  const float dm = s1 * (1.0f / D);
  const u32 mv = cvt2(dm, __builtin_fmaxf(fmaf(-dm, dm, s2 * (1.0f / D)), 0.f));
  LnS r;
  r.mb = bflo(mv);
  r.ri = __builtin_amdgcn_rcpf(bf16r(__builtin_amdgcn_sqrtf(bf16r(bfhi(mv) + 1e-5f))));
  return r;
}

struct G80 {
  u32x4 w[6];
  int ra, rb, rc;
};
__device__ __forceinline__ void g80_load(G80& a, unsigned char* smem, int lane, int wb, int xs, int land) {
  const int m = lane & 15, G = lane >> 4;
  const int l0 = wb + wlane(lane, 0), l1 = wb + wlane(lane, 1), bk = 4096 - (G >> 1) * 1024;
  a.w[0] = wfrag(smem, l0, 0);
  a.w[1] = wfrag(smem, l1, 0);
  a.w[2] = wfrag(smem, l0, 2048);
  a.w[3] = wfrag(smem, l1, 2048);
  a.w[4] = wfrag(smem, l0 + bk, 0);
  a.w[5] = wfrag(smem, l1 + bk, 0);
  const bool vec = m == 8 || (m < 8 && land >= 0);
  const int st = vec ? 64 : 0;
  const int sa = vec ? (m == 8 ? xs : land + 256 * m) + 16 * G : (m == 15 ? L_ONE : L_ZERO);
  const int sw = (vec && m < 8) ? (m & 3) * 64 : 0;
  a.ra = fresh(sa ^ sw);
  a.rb = fresh((sa + st) ^ sw);
  a.rc = fresh(G < 2 ? (sa + 2 * st) ^ sw : L_TAG);
}
template <bool WAIT = true>
__device__ __forceinline__ void dma_poll(u32x4 rq, u32 voff, u32 land, u64 ea, u64 eb) {
  u32 keep;
  asm volatile("s_mov_b32 %0, m0\n\ts_mov_b32 m0, %3\n\ts_mov_b64 exec, %4\n\t"
               "buffer_load_dwordx4 %1, %2, 0 offen sc1 lds\n\t"
               "s_mov_b64 exec, %5\n\t"
               "buffer_load_dwordx4 %1, %2, 0 offen offset:1024 sc1 lds\n\t"
               "s_mov_b64 exec, -1\n\ts_mov_b32 m0, %0"
               : "=&s"(keep)
               : "v"(voff), "s"(rq), "s"(land), "s"(ea), "s"(eb)
               : "memory");
  if (WAIT) asm volatile("s_waitcnt vmcnt(0)" ::: "memory");
}
template <bool X4>
__device__ __forceinline__ void ctl_load(u32x4 rq, u32 voff, u32 lds) {
  u32 keep;
  if (X4)
    asm volatile("s_mov_b32 %0, m0\n\ts_mov_b32 m0, %3\n\ts_nop 2\n\tbuffer_load_dwordx4 %1, %2, 0 offen sc1 lds\n\ts_mov_b32 m0, %0"
                 : "=&s"(keep) : "v"(voff), "s"(rq), "s"(lds) : "memory");
  else
    asm volatile("s_mov_b32 %0, m0\n\ts_mov_b32 m0, %3\n\ts_nop 2\n\tbuffer_load_dword %1, %2, 0 offen sc1 lds\n\ts_mov_b32 m0, %0"
                 : "=&s"(keep) : "v"(voff), "s"(rq), "s"(lds) : "memory");
}
template <int UN>
__device__ __forceinline__ void retry_masks(u64 need, int lane, u64& ea, u64& eb) {
  u32 m = (u32)need | (u32)(need >> 32);
  m |= m >> 16;
  m = (m | (m >> 8)) & 0xffu;
  const int ln = fresh(lane);
  ea = __builtin_amdgcn_ballot_w64(((m >> (ln / UN)) & 1u) != 0u);
  eb = __builtin_amdgcn_ballot_w64(((m >> ((ln + 64) / UN)) & 1u) != 0u);
}
#define MF "v_mfma_f32_16x16x32_bf16 "
#define F0 "v[52:55]"
#define F1 "v[56:59]"
#define F2 "v[60:63]"
#define FCLOB "v52", "v53", "v54", "v55", "v56", "v57", "v58", "v59", "v60", "v61", "v62", "v63"
#define AND3(a, b, c) "v_bitop3_b32 %[t], " a ", " b ", " c " bitop3:0x80\n\t"
template <bool POLL>
__device__ __forceinline__ void g80_run(G80& a, unsigned char* smem, u32x4 rq, int lane, u32 voff, u32 land, float& r0,
                                        float& r1, float& z) {
  f32x4 d0, d1;
  if (POLL) {
    f32x4 d2;
    constexpr u64 USED = 0xff30ff0330ff03ffull;
    u64 ea = USED, eb = USED;
    u32 budget = SPIN_CAP;
#pragma unroll 1
    for (;;) {
      dma_poll(rq, voff, land, ea, eb);
      u64 need;
      u32 t;
      asm volatile("ds_read_b128 " F0 ", %[ra]\n\tds_read_b128 " F1 ", %[rb]\n\tds_read_b128 " F2 ", %[rc]\n\t"
                   "s_waitcnt lgkmcnt(2)\n\t"
                   MF "%[d2], " F0 ", " F0 ", 0\n\t" AND3("v52", "v53", "v54")
                   MF "%[d0], " F0 ", %[w0], 0\n\t" MF "%[d1], " F0 ", %[w1], 0\n\t"
                   "s_waitcnt lgkmcnt(1)\n\t"
                   MF "%[d2], " F1 ", " F1 ", %[d2]\n\t" AND3("%[t]", "v55", "v56")
                   MF "%[d0], " F1 ", %[w2], %[d0]\n\t" AND3("%[t]", "v57", "v58")
                   MF "%[d1], " F1 ", %[w3], %[d1]\n\t"
                   "s_waitcnt lgkmcnt(0)\n\t"
                   MF "%[d2], " F2 ", " F2 ", %[d2]\n\t" AND3("%[t]", "v59", "v60") AND3("%[t]", "v61", "v62")
                   MF "%[d0], " F2 ", %[w4], %[d0]\n\t" AND3("%[t]", "1", "v63")
                   "v_cmp_eq_u32 %[need], 0, %[t]\n\t"
                   MF "%[d1], " F2 ", %[w5], %[d1]"
                   : [d0] "=&v"(d0), [d1] "=&v"(d1), [d2] "=&v"(d2), [t] "=&v"(t), [need] "=&s"(need)
                   : [ra] "v"(a.ra), [rb] "v"(a.rb), [rc] "v"(a.rc), [w0] "v"(a.w[0]), [w1] "v"(a.w[1]), [w2] "v"(a.w[2]),
                     [w3] "v"(a.w[3]), [w4] "v"(a.w[4]), [w5] "v"(a.w[5])
                   : "memory", FCLOB);
      need &= 0x00ff00ff00ff00ffull;
      if (need == 0ull || budget == 0u) break;
      --budget;
      retry_masks<16>(need, lane, ea, eb);
      ea &= USED;
      eb &= USED;
    }
    asm volatile("s_nop 1" : "+v"(d2));
    z = sum4(d2);
    asm volatile("s_nop 1" : "+v"(z), "+v"(d0), "+v"(d1));
    z = (lane & 15) == 15 ? 0.f : z;
    z = sum8(z);
    z += dpp<ROR8>(z);
  } else {
    const u32x4 f0 = afrag(smem, a.ra), f1 = afrag(smem, a.rb), f2 = afrag(smem, a.rc);
    d0 = d1 = f32x4{0.f, 0.f, 0.f, 0.f};
    d0 = mfma32(f0, a.w[0], d0);
    d1 = mfma32(f0, a.w[1], d1);
    d0 = mfma32(f1, a.w[2], d0);
    d1 = mfma32(f1, a.w[3], d1);
    d0 = mfma32(f2, a.w[4], d0);
    d1 = mfma32(f2, a.w[5], d1);
  }
  r0 = sum4(d0);
  r1 = sum4(d1);
}
__device__ __forceinline__ f32x4 quad4(float r) { return f32x4{r, dpp<0x55>(r), dpp<0xAA>(r), dpp<0xFF>(r)}; }

__device__ __forceinline__ int q_addr(int d) {
  const int w = d >> 1;
  return L_Q + ((w & 1) * 48 + (w >> 1)) * 4 + (d & 1) * 2;
}

__device__ __forceinline__ void q1_finish(unsigned char* smem, u32x4 rq, int lane, int wave, u32 voff, u32 landa) {
  const int e = wave * 32 + (lane & 7) * 4 + ((lane >> 4) & 1) * 2 + (lane >> 5);
  u32 budget = SPIN_CAP;
  u32x4 v, w;
#pragma unroll 1
  for (;;) {
    asm volatile("" ::: "memory");
    v = afrag(smem, L_LAND + wave * 2048 + lane * 16);
    w = afrag(smem, L_LAND + wave * 2048 + 1024 + lane * 16);
    if (__builtin_expect(__builtin_amdgcn_ballot_w64((and4(v) & and4(w) & 1u) == 0u) == 0ull || budget == 0u, 1)) break;
    --budget;
    dma_poll(rq, voff, landa, ~0ull, ~0ull);
  }
  const float t = tsum8(__builtin_bit_cast(f32x4, v));
  *reinterpret_cast<u16*>(smem + q_addr(e)) = (u16)cvt2(t, t);
  *reinterpret_cast<float*>(smem + L_CS + e * 4) = tsum8(__builtin_bit_cast(f32x4, w)) * (-1.0f / D);
}
template <bool LDS>
__device__ __forceinline__ void x_update(unsigned char* smem, u32x4 rq, int lane, u32 src, int i) {
  const int l = lane < 40 ? lane : 0;
  const u32 off = src + 4 * l;
  u32 v[8];
  if (LDS) {
#pragma unroll
    for (int k = 0; k < 8; ++k) v[k] = *reinterpret_cast<const u32*>(smem + (off ^ ((k & 3) * 64)) + 256 * k);
  } else {
    u32 budget = SPIN_CAP;
#pragma unroll 1
    for (;;) {
#pragma unroll
      for (int k = 0; k < 8; ++k)
        asm volatile("buffer_load_dword %0, %1, %2, 0 offen offset:%3 sc1" : "=v"(v[k]) : "v"(off), "s"(rq), "n"(256 * k) : "memory");
      asm volatile("s_waitcnt vmcnt(0)"
                   : "+v"(v[0]), "+v"(v[1]), "+v"(v[2]), "+v"(v[3]), "+v"(v[4]), "+v"(v[5]), "+v"(v[6]), "+v"(v[7])::"memory");
      const u32 a = v[0] & v[1] & v[2] & v[3] & v[4] & v[5] & v[6] & v[7] & 1u;
      if (__builtin_amdgcn_ballot_w64(a == 0u) == 0ull || budget == 0u) break;
      --budget;
      __builtin_amdgcn_s_sleep(2);
    }
  }
  float lo = 0.f, hi = 0.f;
#pragma unroll
  for (int k = 0; k < 8; ++k) {
    lo += bflo(v[k]);
    hi += bfhi(v[k]);
  }
  u32* px = reinterpret_cast<u32*>(smem + L_HB + 160 * i + 4 * l);
  const u32 xo = *px, y = cvt2(lo, hi);
  if (lane < 40) *px = cvt2(bflo(xo) + bflo(y), bfhi(xo) + bfhi(y));
}
template <int N>
__device__ __forceinline__ void col_poll(u32x4 rq, const u32 (&off)[N], float (&y)[N]) {
  u32x2 v[N];
  u32 budget = SPIN_CAP;
#pragma unroll 1
  for (;;) {
    u32 a = 1u;
#pragma unroll
    for (int k = 0; k < N; ++k)
      asm volatile("buffer_load_dwordx2 %0, %1, %2, 0 offen sc1" : "=v"(v[k]) : "v"(off[k]), "s"(rq) : "memory");
    asm volatile("s_waitcnt vmcnt(0)" ::: "memory");
#pragma unroll
    for (int k = 0; k < N; ++k) {
      asm volatile("" : "+v"(v[k]));
      a &= v[k][0] & v[k][1];
    }
    if (__builtin_amdgcn_ballot_w64(a == 0u) == 0ull || budget == 0u) break;
    --budget;
  }
#pragma unroll
  for (int k = 0; k < N; ++k) y[k] = bf16r(tsum8(f32x4{bflo(v[k][0]), bfhi(v[k][0]), bflo(v[k][1]), bfhi(v[k][1])}));
}

#define WMAX 6
#define CKQ 23
#define BEAT0 8
#define WBIG 10
#define LANDED asm volatile("s_waitcnt vmcnt(%0)" ::"n"(WMAX - 1) : "memory")
#define TSTEP 246
#define QI 6
#define NINO 3
#define NINF 2
static_assert(5 - NINF >= 6 - NINO, "counts");
#define TPF 220
#define TPV 220
#define TFD 0
#define XA 15
#define XB 19
#define TS_O (TSTEP * (20 * (48 + NINO) + 12 * (48 + NINF)) / (32 * (48 + NINO)))
#define TS_F (TSTEP * (20 * (48 + NINO) + 12 * (48 + NINF)) / (32 * (48 + NINF)))
#define TSQ 256
#define ROT (wave * 3 + j + g * 5)
static_assert(QI >= WMAX && CKQ - WBIG > BEAT0 + 3 && BEAT0 > QI && XB - WBIG <= WBIG + 2 && WBIG + 2 <= XA, "window");
#define PACE_ASM                                                                                                     \
  "1:\n\ts_memrealtime s[98:99]\n\ts_waitcnt lgkmcnt(0)\n\ts_lshl_b32 s98, s98, 3\n\ts_sub_i32 s98, s98, %[tn]\n\t"  \
  "s_cmp_lt_i32 s98, 0\n\ts_cbranch_scc0 2f\n\ts_sleep 1\n\ts_branch 1b\n2:\n\ts_add_i32 %[tn], %[tn], %[ts]\n\t"
__device__ __forceinline__ void pace(u32& tnext, u32 tstep) {
  asm volatile(PACE_ASM : [tn] "+s"(tnext) : [ts] "s"(tstep) : "scc", "s98", "s99");
}
constexpr int kreg(int i) { return i % 3 < 2 ? 64 + 8 * (i / 3) + 4 * (i % 3) : 128 + 4 * (i / 3); }
constexpr int vreg(int i) { return 160 + 24 * (i / 6) + 8 * (i % 3) + 4 * ((i / 3) & 1); }
#define LOADW(reg, voff, sbase, imm)                                                                                \
  asm volatile(PACE_ASM "global_load_dwordx4 v[%[r0]:%[r1]], %[p], %[sb] offset:%[im] nt\n\ts_waitcnt vmcnt(%[w])"  \
               : [tn] "+s"(tnext)                                                                                   \
               : [r0] "n"(reg), [r1] "n"((reg) + 3), [p] "v"(voff), [sb] "s"(sbase), [im] "n"(imm), [ts] "s"(tstep), \
                 [w] "n"(WBIG - 1)                                                                                  \
               : "scc", "s98", "s99")

#define HS_WAIT(COND)                                                                                \
  _Pragma("unroll 1") for (;;) {                                                                     \
    gd = CTL[512 + lane0] - CTL[576 + lane0];                                                        \
    const u64 ch = __builtin_amdgcn_ballot_w64(gd != 0u) & 0x1ffull;                                 \
    if (ch > 0xffull || (COND) || tmo == 0u) break;                                                  \
    if ((tmo & 3u) == 0u && lane0 == 0)                                                              \
      __builtin_amdgcn_raw_buffer_store_b32(hb, rs, WS_HB + h * 128 + c * 4, 0, 16);                 \
    hb += 4u;                                                                                        \
    --tmo;                                                                                           \
    __builtin_amdgcn_s_sleep(3);                                                                     \
    ctl_load<false>(rq, hso, (u32)(smb + L_CTL + 2048));                                             \
    asm volatile("s_waitcnt vmcnt(0)" ::: "memory");                                                 \
  }

struct QKAddr {
  int qad[6], idxq, wa;
  f32x4 D0, D1;
};
struct PVAddr {
  int ra, idx, oa, sa, la, ma;
  u32 dst;
  f32x4 O0, O1, O2;
  u32x4 A0, A1, A2, A3;
};
__device__ __forceinline__ void att_table(unsigned char* smem, int lane, int wave, u32 go) {
  const int n = lane & 15, G = lane >> 4;
  const int seg = n & 1, pr = (n >> 1) & 1, tq = n >> 2, xy = G >> 1;
  const int qa = L_Q + (pr * 48 + seg * 8 + (G & 1) * 4) * 4;
  u32 t[16];
#pragma unroll
  for (int k = 0; k < 4; ++k) t[k] = tq == k ? qa + xy * 64 : L_ZERO;
#pragma unroll
  for (int k = 0; k < 2; ++k) t[4 + k] = tq == 2 * k + xy ? qa + 128 : L_ZERO;
  t[6] = 0x4444 + pr * 0xAAAA;
  t[7] = L_P + wave * P_W + ((n >> 1) & 1) * P_PAR + ((((G & 1) * 2 + (n >> 3)) * 4 + (n & 1) * 2 + (G >> 1)) * 16) + ((n >> 2) & 1) * 8;
  t[8] = L_P + wave * P_W + ((n >> 2) & 1) * P_U + (n >> 3) * P_PAR + G * 64;
  t[9] = (n & 3) * 0x5555;
  t[10] = L_OM + (wave * OM_S + (n >> 3) * OM_P + (n & 7) * 4 + (G & 1) * 32) * 4;
  t[11] = L_OM + (wave * OM_S + DH + G) * 4;
  t[12] = L_OM + ((lane & 3) * OM_S + (wave & 3) * 64 + (lane >> 3) * 4 + (lane & 4) * 8) * 4;
  t[13] = L_OM + ((lane & 3) * OM_S + DH + 4) * 4;
  const int d0 = wave * 64 + (lane >> 3) * 8;
  t[14] = (go + (wave < 3 ? (d0 >= 96 ? 2048 - 192 : 0) + d0 * 2 : lane * 2048 + 192)) & 0xffffu;
  t[15] = q_addr(wave * 32 + (lane & 7) * 4 + ((lane >> 4) & 1) * 2 + (lane >> 5));
#pragma unroll
  for (int k = 0; k < 2; ++k)
    *reinterpret_cast<u32x4*>(smem + L_TAB + wave * 2048 + k * 1024 + lane * 16) =
        u32x4{t[8 * k] | (t[8 * k + 4] << 16), t[8 * k + 1] | (t[8 * k + 5] << 16), t[8 * k + 2] | (t[8 * k + 6] << 16),
              t[8 * k + 3] | (t[8 * k + 7] << 16)};
}
__device__ __forceinline__ void att_setup(QKAddr& r, PVAddr& v, unsigned char* smem, int lane, int wave, int& qw) {
  const u32x4 a = afrag(smem, L_TAB + wave * 2048 + lane * 16), b = afrag(smem, L_TAB + wave * 2048 + 1024 + lane * 16);
  r.D0 = zero4();
  r.D1 = zero4();
  qw = fresh(b[3] >> 16);
#pragma unroll
  for (int k = 0; k < 4; ++k) r.qad[k] = fresh(a[k] & 0xffffu);
  r.qad[4] = fresh(a[0] >> 16);
  r.qad[5] = fresh(a[1] >> 16);
  r.idxq = fresh(a[2] >> 16);
  r.wa = fresh(a[3] >> 16);
  v.ra = fresh(b[0] & 0xffffu);
  v.idx = fresh(b[1] & 0xffffu);
  v.oa = fresh(b[2] & 0xffffu);
  v.la = fresh(b[3] & 0xffffu);
  v.sa = fresh(b[0] >> 16);
  v.ma = fresh(b[1] >> 16);
  v.dst = fresh(b[2] >> 16);
  asm volatile("" : "+v"(r.D0), "+v"(r.D1));
}
#define SM "v_smfmac_f32_16x16x64_bf16 "
#define P_WRITE_READ(QA)                                                                                              \
  asm volatile("ds_write2_b32 %[wa], %[p], %[p] offset1:%[o1]\n\t"                                                    \
               "ds_read_b128 %[a0], %[ra]\n\tds_read_b128 %[a1], %[ra] offset:16\n\t"                                 \
               "ds_read_b128 %[a2], %[ra] offset:32\n\tds_read_b128 %[a3], %[ra] offset:48"                           \
               : [a0] "=&v"(pv.A0), [a1] "=&v"(pv.A1), [a2] "=&v"(pv.A2), [a3] "=&v"(pv.A3), "+v"(sk)                 \
               : [wa] "v"(QA.wa + smb), [p] "v"(cvt2(pend, 0.f)), [ra] "v"(pv.ra + smb), [o1] "n"(P_U / 4 + 1)        \
               : "memory");
#define QK_SOFTMAX(QA)                                                                                                \
  {                                                                                                                   \
    u32x4 A0, A1, A2, A3, A4, A5;                                                                                     \
    float scl;                                                                                                        \
    asm volatile("ds_read_b128 %[a0], %[q0]\n\tds_read_b128 %[a1], %[q1]\n\tds_read_b128 %[a2], %[q2]\n\t"            \
                 "ds_read_b128 %[a3], %[q3]\n\tds_read_b128 %[a4], %[q4]\n\tds_read_b128 %[a5], %[q5]"                \
                 : [a0] "=&v"(A0), [a1] "=&v"(A1), [a2] "=&v"(A2), [a3] "=&v"(A3), [a4] "=&v"(A4), [a5] "=&v"(A5)     \
                 : [q0] "v"(QA.qad[0] + smb), [q1] "v"(QA.qad[1] + smb), [q2] "v"(QA.qad[2] + smb),                   \
                   [q3] "v"(QA.qad[3] + smb), [q4] "v"(QA.qad[4] + smb), [q5] "v"(QA.qad[5] + smb)                    \
                 : "memory");                                                                                         \
    asm volatile("ds_read_b32 %[s], %[z] offset:%[ri]\n\t"                                                            \
                 "s_waitcnt lgkmcnt(6)\n\t" SM "%[d0], %[a0], v[64:71], %[ix]\n\t" SM "%[d1], %[a0], v[96:103], %[ix]\n\t"   \
                 "s_waitcnt lgkmcnt(5)\n\t" SM "%[d0], %[a1], v[72:79], %[ix]\n\t" SM "%[d1], %[a1], v[104:111], %[ix]\n\t"   \
                 "s_waitcnt lgkmcnt(4)\n\t" SM "%[d0], %[a2], v[80:87], %[ix]\n\t" SM "%[d1], %[a2], v[112:119], %[ix]\n\t"   \
                 "s_waitcnt lgkmcnt(3)\n\t" SM "%[d0], %[a3], v[88:95], %[ix]\n\t" SM "%[d1], %[a3], v[120:127], %[ix]\n\t"   \
                 "s_waitcnt lgkmcnt(2)\n\t" SM "%[d0], %[a4], v[128:135], %[ix]\n\t" SM "%[d1], %[a4], v[144:151], %[ix]\n\t"   \
                 "s_waitcnt lgkmcnt(0)\n\t" SM "%[d0], %[a5], v[136:143], %[ix]\n\t" SM "%[d1], %[a5], v[152:159], %[ix]\n\t"   \
                                      \
                 "ds_read_b128 %[a0], %[z]\n\tds_read_b128 %[a1], %[z]\n\tds_read_b128 %[a2], %[z]\n\ts_nop 4"        \
                 : [d0] "+v"(QA.D0), [d1] "+v"(QA.D1), [s] "=&v"(scl), [a0] "+v"(A0), [a1] "+v"(A1), [a2] "+v"(A2),   \
                   [a3] "+v"(A3), [a4] "+v"(A4), [a5] "+v"(A5)                                                        \
                 : [ix] "v"(QA.idxq), [z] "v"(smb), [ri] "n"(L_RI), "{v[64:95]}"(kv0), "{v[96:127]}"(kv1),           \
                   "{v[128:159]}"(kv2)                                                                                \
                 : "memory");                                                                                         \
    pv.O0 = __builtin_bit_cast(f32x4, A0);                                                                            \
    pv.O1 = __builtin_bit_cast(f32x4, A1);                                                                            \
    pv.O2 = __builtin_bit_cast(f32x4, A2);                                                                            \
    const f32x2 e0 = f32x2{QA.D0[0], QA.D0[1]} + f32x2{QA.D0[2], QA.D0[3]};                                           \
    const f32x2 e1 = f32x2{QA.D1[0], QA.D1[1]} + f32x2{QA.D1[2], QA.D1[3]};                                           \
    const float s0 = e0[0] + dpp<XOR1>(e0[1]), s1 = e1[1] + dpp<XOR1>(e1[0]);                                         \
    const float sc = ((lane0 & 1) ? s1 : s0) * scl;                                                                   \
    pend = __builtin_amdgcn_exp2f(sc);                                                                                \
    pmax = 0.f;                                                                                                       \
    float sk = sc;                                                                                                    \
    P_WRITE_READ(QA)                                                                                                  \
    const bool unsafe =                                                                                               \
        (__builtin_amdgcn_ballot_w64(!(sk <= 24.f)) != 0ull) | (__builtin_amdgcn_ballot_w64(sk >= -24.f) == 0ull);    \
    if (__builtin_expect(unsafe, 0)) {                                                                                \
      pmax = xrow16_max(max_ror8(max8(sk)));                                                                          \
      pend = __builtin_amdgcn_exp2f(sk - pmax);                                                                       \
      asm volatile("s_waitcnt lgkmcnt(0)" : "+v"(pv.A0), "+v"(pv.A1), "+v"(pv.A2), "+v"(pv.A3));                      \
      P_WRITE_READ(QA)                                                                                                \
      asm volatile("s_waitcnt lgkmcnt(0)" : "+v"(pv.A0), "+v"(pv.A1), "+v"(pv.A2), "+v"(pv.A3));                      \
    }                                                                                                                 \
  }

using u32x16 = __attribute__((ext_vector_type(16))) unsigned;
using u32x32 = __attribute__((ext_vector_type(32))) unsigned;
template <int OFF>
__device__ __forceinline__ u16* karg() {
  u64 p;
  asm volatile("s_load_dwordx2 %0, %1, %2\n\ts_waitcnt lgkmcnt(0)" : "=s"(p) : "s"(__builtin_amdgcn_kernarg_segment_ptr()), "n"(OFF));
  return (u16*)p;
}
template <class T>
__device__ __forceinline__ T* mkptr(u32 lo, u32 hi) {
  return (T*)(((u64)hi << 32) | lo);
}

__global__ __launch_bounds__(NT, 2) void mega_kernel(Params) {
  __shared__ __attribute__((aligned(16))) unsigned char smem[L_TOTAL + 16];
  u32x16 ka;
  u32x4 kb4;
  {
    u64 pc;
    u32 keep;
    asm volatile("s_load_dwordx16 %[ka], %[kp], 0x0\n\ts_load_dwordx4 %[kb], %[kp], 0x40\n\t"
                 "s_getpc_b64 %[pc]\n.Lmk_pc:\n\t"
                 "s_mov_b32 %[keep], m0\n\ts_mov_b32 m0, %[dst]\n\t"
                 "v_lshlrev_b32 v1, 7, %[tid]\n\tv_min_u32 v1, .Lmk_code_end-.Lmk_pc-4, v1\n\t"
                 "v_cmp_gt_u32 vcc, 0x140, %[tid]\n\ts_mov_b64 exec, vcc\n\t"
                 "global_load_lds_dword v1, %[pc]\n\ts_mov_b64 exec, -1\n\ts_mov_b32 m0, %[keep]\n\t"
                 "v_lshrrev_b32 v1, 2, %[tid]\n\tv_cmp_eq_u32 vcc, 3, v1\n\tv_mov_b32 v3, 0x3f803f80\n\ts_nop 0\n\t"
                 "v_cndmask_b32 v2, 0, 1, vcc\n\tv_cmp_eq_u32 vcc, 2, v1\n\tv_lshlrev_b32 v1, 2, %[tid]\n\ts_nop 0\n\t"
                 "v_cndmask_b32 v2, v2, v3, vcc\n\tv_cmp_eq_u32 vcc, 7, %[tid]\n\tv_mov_b32 v3, %[scl]\n\t"
                 "v_min_u32 v1, 80, v1\n\tv_cndmask_b32 v2, v2, v3, vcc\n\t"
                 "ds_write_b32 v1, v2 offset:%[sm]\n\t"
                 "s_waitcnt lgkmcnt(0)\n\ts_barrier"
                 : [ka] "=&s"(ka), [kb] "=&s"(kb4), [pc] "=&s"(pc), [keep] "=&s"(keep)
                 : [kp] "s"(__builtin_amdgcn_kernarg_segment_ptr()), [tid] "v"(threadIdx.x), [dst] "n"(L_PF),
                   [scl] "n"(__builtin_bit_cast(u32, SCL)), [sm] "n"(0)
                 : "memory", "vcc", "v1", "v2", "v3");
  }
  const u32 tstart = (u32)__builtin_amdgcn_s_memrealtime();
  if (threadIdx.x == 128) *(volatile LAS u32*)(smem + L_TOTAL) = tstart;
  const int tid = threadIdx.x, lane0 = tid & 63;
  const int wave = __builtin_amdgcn_readfirstlane(tid >> 6);
  const int b = blockIdx.x;
  const int g = b & 7, j = b >> 3, hsub = j & 3, c = j >> 2, h = g * 4 + hsub;
  const bool isO = j < 20;
  const bool leader = b == 0 && wave == 6;
  const u32 fslot = (u32)((j & 1) * 16384 + ((j - 20) >> 1) * 128);
  u32 tmo = SPIN_CAP;
  volatile LAS u32* cnt = (volatile LAS u32*)(smem + L_CNT);
  Params P;
  P.x0 = mkptr<const u16>(ka[0], ka[1]);
  P.kc = mkptr<const u16>(ka[2], ka[3]);
  P.vc = mkptr<const u16>(ka[4], ka[5]);
  P.wq = mkptr<const u16>(ka[6], ka[7]);
  P.wo = mkptr<const u16>(ka[8], ka[9]);
  P.w1 = mkptr<const u16>(ka[10], ka[11]);
  P.w3 = mkptr<const u16>(ka[12], ka[13]);
  P.ws = mkptr<u32>(kb4[2], kb4[3]);
  u32* const wsb = (u32*)(((uintptr_t)P.ws + 0x1fffffu) & ~(uintptr_t)0x1fffffu);
  const __amdgpu_buffer_rsrc_t rs = __builtin_amdgcn_make_buffer_rsrc(wsb, 0, (int)WS_END + 4096, 0x00020000);
  const u32x4 rq = {(u32)(uintptr_t)wsb, (u32)((uintptr_t)wsb >> 32) & 0xffffu, WS_END + 4096, 0x00020000u};

  u32 anc = 0u;
  u32 tref = 0u, ar = 0u;
  u32x32 kv0, kv1, kv2, kv3, kv4, kv5;
  asm volatile(""
               : "={v[64:95]}"(kv0), "={v[96:127]}"(kv1), "={v[128:159]}"(kv2), "={v[160:191]}"(kv3), "={v[192:223]}"(kv4),
                 "={v[224:255]}"(kv5));
  const int smb = (int)(uintptr_t)(LAS unsigned char*)smem;
  const u32 gop = A_GO + go_slot(hsub * 2) + c * 256;
  const u32 tstep = isO ? TS_O : TS_F;
  u32 tnext = tstart * 8u + (u32)wave * (TSTEP / 8) + 5 * TSQ;
  const size_t rowbase = (size_t)h * SK + (SK - WIN) + c * 512 + wave * 64;
  const u16* vb = P.vc + rowbase * DH;
  const int rot = ROT;
  const u16* ws0 = isO ? P.wo + (size_t)(768 * g) * D + 32 * j : ((j & 1) ? P.w3 : P.w1) + 192 * g + 32 * ((j - 20) >> 1);
  {
    constexpr int L_CTL = L_LAND + 6 * 2048;
    volatile LAS u32* const CTL = (volatile LAS u32*)(smem + L_CTL);
    u32 gd, hb = 0u;
    const u32 lho = WS_HB + (lane0 >> 1) * 128 + (lane0 & 1) * 16;
    const u32 hso = lane0 < 8 ? WS_HB + (u32)h * 128 + lane0 * 4 : WS_GOF + g * 128;
    if (wave < 6) {
      if (lane0 < 16) store16(rs, WS_GQ1 + (u32)(h * 8 + c) * 2048 + wave * 128 + (lane0 >> 3) * 1024 + (lane0 & 7) * 16, u32x4{0u, 0u, 0u, 0u});
      const u16* s0 = P.wq + (size_t)(80 * c) * (H * DH) + h * DH + wave * 32;
      u32 tq = tnext - 5 * TSQ;
#pragma unroll 1
      for (int p = 0; p < 5; ++p) {
        pace(tq, TSQ);
        dma_piece(s0, H * DH, smem, L_WQ + wave * 5120, p, lane0);
      }
    } else {
      if (leader) {
        ctl_load<true>(rq, lho, (u32)(smb + L_CTL));
        ctl_load<false>(rq, WS_GOF + (lane0 & 7) * 128, (u32)(smb + L_CTL + 2560));
      }
      u32 rtt;
      {
        u32 pa = 0u, pb = 0u;
        u64 t1, t2;
        const u32 pbo = WS_PB + (u32)(wave & 1) * (SIDE + 128u) + (u32)((b >> 3) & 7) * 16384u + (u32)((b & 7) * 4 + (b >> 6)) * 256u;
        asm volatile(".p2align 6\n\ts_mov_b64 exec, 1\n\t"
                     "buffer_atomic_or %0, off, %4, %5 sc0 sc1\n\tbuffer_atomic_or %1, off, %4, %6 sc0 sc1\n\t"
                     "s_mov_b64 exec, -1\n\t"
                     "s_waitcnt vmcnt(1)\n\ts_memtime %2\n\ts_waitcnt vmcnt(0)\n\ts_memtime %3\n\ts_waitcnt lgkmcnt(0)"
                     : "+v"(pa), "+v"(pb), "=&s"(t1), "=&s"(t2)
                     : "s"(rq), "s"(pbo), "s"(pbo ^ SIDE)
                     : "memory");
        rtt = __builtin_amdgcn_readfirstlane((u32)t2 - (u32)t1) + 1u;
      }
      if (wave == 6) {
        *(volatile LAS u32*)(smem + L_VOTE) = rtt;
        u32 keep;
        asm volatile("s_mov_b32 %0, m0\n\ts_mov_b32 m0, %4\n\ts_nop 2\n\tglobal_load_lds_dwordx4 %1, %3\n\t"
                     "global_load_lds_dword %2, %3 offset:1024\n\ts_mov_b32 m0, %0"
                     : "=&s"(keep)
                     : "v"(lane0 * 16), "v"(lane0 * 4), "s"(P.x0), "s"((u32)(smb + L_HB))
                     : "memory");
      } else {
        ctl_load<false>(rq, hso, (u32)(smb + L_CTL + 2304));
#pragma unroll
        for (int k = 0; k < 4; ++k) store16(rs, (u32)b * 4096 + k * 1024 + lane0 * 16, u32x4{0u, 0u, 0u, 0u});
        const int df = (int)(lds_wait_nz((volatile LAS u32*)(smem + L_VOTE)) - rtt);
        hb = df > PTHR ? 1u << (g >> 2) : (df < -PTHR ? 2u >> (g >> 2) : 0u);
      }
    }
    if (tid == 0) *(volatile LAS u32*)(smem + L_ANC) = 0u;
    *reinterpret_cast<u32x4*>(smem + L_P + wave * P_W + lane0 * 16) = u32x4{0u, 0u, 0u, 0u};
    *reinterpret_cast<u32x4*>(smem + (lane0 < 16 ? L_P + wave * P_W + 1024 + lane0 * 16 : L_OM + (wave * OM_S + OM_P + DH) * 4)) =
        u32x4{0u, 0u, 0u, 0u};
    const u16* kb = P.kc + rowbase * DH;
    const u32 ko = ((lane0 & 15) >> 1) * 384 + 16 * ((lane0 & 1) * 4 + (lane0 >> 4)), vo = (lane0 >> 3) * 384 + 16 * (lane0 & 7);
#pragma unroll
    for (int i = 0; i < 24; ++i) {
      LOADW(kreg(i), ko, kb + ((i / 3 + rot) & 7) * 8 * DH, 128 * (i % 3));
      if (i == 3) att_table(smem, fresh(lane0), wave, gop);
      if (i == 1 && wave == 6) {
        asm volatile("s_waitcnt vmcnt(2)" ::: "memory");
        const u32x4 lv = afrag(smem, L_HB + lane0 * 16);
        const u32 xb = *reinterpret_cast<const u32*>(smem + L_HB + 1024 + lane0 * 4);
        float v[10];
#pragma unroll
        for (int k = 0; k < 4; ++k) {
          v[2 * k] = bflo(lv[k]);
          v[2 * k + 1] = bfhi(lv[k]);
        }
        v[8] = bflo(xb);
        v[9] = bfhi(xb);
        float sm = 0.f;
#pragma unroll
        for (int k = 0; k < 10; ++k) sm += v[k];
        const float mean = wave_sum(sm) * (1.0f / D);
        float q = 0.f;
#pragma unroll
        for (int k = 0; k < 10; ++k) q = fmaf(v[k] - mean, v[k] - mean, q);
        const float var = wave_sum(q) * (1.0f / D);
        const float mb = bf16r(mean);
        const float ri = __builtin_amdgcn_rcpf(bf16r(__builtin_amdgcn_sqrtf(bf16r(bf16r(var) + 1e-5f))));
        u32 hp[5];
#pragma unroll
        for (int k = 0; k < 5; ++k) {
          const u32 cc = cvt2(v[2 * k] - mb, v[2 * k + 1] - mb);
          hp[k] = cvt2(bflo(cc) * ri, bfhi(cc) * ri);
        }
        *reinterpret_cast<u32x4*>(smem + L_LN + lane0 * 16) = u32x4{hp[0], hp[1], hp[2], hp[3]};
        *reinterpret_cast<u32*>(smem + L_LN + 1024 + lane0 * 4) = hp[4];
        asm volatile("s_waitcnt lgkmcnt(0)" ::: "memory");
        *(volatile LAS u32*)(smem + L_VOTE + 4) = 1u;
      }
      if (i == QI && wave < 6) {
        LANDED;
        if (lane0 == 0) lds_add(smem, L_CNT + 4, 1u);
        lds_wait((volatile LAS u32*)(smem + L_VOTE + 4), 1u);
        G80 a;
        g80_load(a, smem, lane0, L_WQ + wave * 5120, L_LN + 160 * c, -1);
        float r0, r1, z;
        g80_run<false>(a, smem, rq, lane0, 0u, 0u, r0, r1, z);
        auto sw = __builtin_amdgcn_permlane32_swap(__float_as_uint(r0), __float_as_uint(r1), false, false);
        const f32x4 qd = quad4(__uint_as_float(sw[1]));
        if ((lane0 & 3) == 0)
          store_quad(rs, WS_GQ1 + (u32)(h * 8 + c) * 2048 + ((lane0 >> 4) & 1) * 1024 + wave * 128 + (lane0 >> 5) * 64 + (lane0 & 15) * 4, qd);
      }
      if (wave == 7) {
        if (i == BEAT0) {
          asm volatile("s_waitcnt vmcnt(%0)" ::"n"(BEAT0 + 1) : "memory");
          lds_wait(cnt + 1, 6u);
          hb += (__builtin_amdgcn_readfirstlane(CTL[576 + c]) & ~3u) + 4u;
        }
        if (i == BEAT0 || i == BEAT0 + 3) {
          if (lane0 == 0) __builtin_amdgcn_raw_buffer_store_b32(hb, rs, WS_HB + h * 128 + c * 4, 0, 16);
          hb += 4u;
        }
        if (i == CKQ - WBIG) ctl_load<false>(rq, hso, (u32)(smb + L_CTL + 2048));
        if (i == CKQ) {
          HS_WAIT((u32)ch == 0xffu)
          if (lane0 == 0) cnt[1] = 7u;
        }
      }
      if (i == 25 - WBIG && leader) {
        ctl_load<true>(rq, lho, (u32)(smb + L_CTL + 1024));
      }
    }
#define VLOADS(A, B)                                                                           \
  _Pragma("unroll") for (int i = A; i < B; ++i) {                                              \
    LOADW(vreg(i), vo, vb + ((i / 3 + rot) & 7) * 8 * DH, 128 * (i % 3));                      \
    if (i % 2 == 1 && i / 2 < (isO ? NINO : NINF)) {                                           \
      pace(tnext, tstep);                                                                      \
      dma_piece(ws0, isO ? D : F, smem, L_WB, wave * (isO ? 6 : 5) + i / 2, lane0);            \
    }                                                                                          \
  }
    VLOADS(0, 2)
    if (leader) {
      const u32x4 xa = afrag(smem, L_CTL + lane0 * 16);
      u32x4 lv;
#pragma unroll 1
      for (;;) {
        asm volatile("" ::: "memory");
        lv = afrag(smem, L_CTL + 1024 + lane0 * 16);
        const bool same = lv[0] == xa[0] || lv[1] == xa[1] || lv[2] == xa[2] || lv[3] == xa[3];
        if (__builtin_amdgcn_ballot_w64(same) == 0ull || tmo == 0u) break;
        --tmo;
        __builtin_amdgcn_s_sleep(4);
        ctl_load<true>(rq, lho, (u32)(smb + L_CTL + 1024));
        asm volatile("s_waitcnt vmcnt(0)" ::: "memory");
      }
      u32 nv = 256u;
#pragma unroll
      for (int e = 0; e < 4; ++e)
        nv += (u32)__builtin_popcountll(__builtin_amdgcn_ballot_w64((lv[e] & 2u) != 0u)) -
              (u32)__builtin_popcountll(__builtin_amdgcn_ballot_w64((lv[e] & 1u) != 0u));
      if (lane0 < 8) __builtin_amdgcn_raw_buffer_store_b32(CTL[640 + lane0] + (nv > 256u ? 2u : 1u), rs, WS_GOF + lane0 * 128, 0, 16);
    }
    const u32 qo = WS_GQ1 + (u32)(h * 8 + (lane0 >> 3)) * 2048 + wave * 128 + (lane0 & 7) * 16;
    if (wave < 6) {
      lds_wait(cnt + 1, 7u);
      dma_poll<false>(rq, qo, (u32)(smb + L_LAND + wave * 2048), ~0ull, ~0ull);
    }
    VLOADS(2, XB - WBIG)
    if (wave == 7) ctl_load<false>(rq, hso, (u32)(smb + L_CTL + 2048));
    VLOADS(XB - WBIG, WBIG + 2)
    if (wave < 6) {
      q1_finish(smem, rq, fresh(lane0), wave, qo, (u32)(smb + L_LAND + wave * 2048));
      asm volatile("s_waitcnt lgkmcnt(0)" ::: "memory");
      if (lane0 == 0) lds_add(smem, L_CNT, 1u);
    }
    VLOADS(WBIG + 2, XA)
    if (wave >= 4) {
      VLOADS(XA, XB)
      if (wave == 7) {
        HS_WAIT(false)
        if (lane0 == 8) *(volatile LAS u32*)(smem + L_VOTE) = gd >> 1;
      }
    }
  }

#pragma unroll 1
  for (int half = 0;; ++half) {
    {
      float pend, pmax;
      PVAddr pv;
      QKAddr qa;
      const int lane = fresh(lane0);
      const u32 off[1] = {ar + A_GQ + hsub * 16384 + (wave < 6 ? (lane >> 3) * 896 + wave * 128 + (lane & 7) * 16 : (lane & 7) * 896 + 768)};
      f32x4 qv[1];
      asm volatile("" : "=v"(qv[0]));
      if (half == 1) anc = lds_wait_nz((volatile LAS u32*)(smem + L_ANC));
      if (half == 1 && wave < 7) {
        wait_rt(anc + A_Q);
        POLL16(qv[0], off[0], 0);
      }
      int qw;
      att_setup(qa, pv, smem, lane, wave, qw);
      if (half == 0) {
        lds_wait(cnt, 6u);
      } else {
        if (wave < 7) {
          asm volatile("s_waitcnt vmcnt(0)" : "+v"(qv[0])::"memory");
          if (__builtin_expect(__builtin_amdgcn_ballot_w64((and4(__builtin_bit_cast(u32x4, qv[0])) & 1u) == 0u) != 0ull, 0))
            gpoll4<1>(rq, off, qv);
          if (wave < 6) {
            const float t = tsum8(qv[0]);
            *reinterpret_cast<u16*>(smem + qw) = (u16)cvt2(t, t);
          } else {
            const LnS ls = ln_stats(qv[0][3], qv[0][0] + qv[0][1] + qv[0][2]);
            *reinterpret_cast<float*>(smem + L_RI) = ls.ri * SCL;
          }
        }
        if (wave < 4) __builtin_amdgcn_s_setprio(2);
        bar();
      }
      QK_SOFTMAX(qa)
      if (half == 0) {
        const int lv_ = fresh(lane0);
        const u32 vo = (lv_ >> 3) * 384 + 16 * (lv_ & 7);
        if (wave < 4) {
          VLOADS(XA, XB)
        }
        VLOADS(XB, 24)
        if (!isO) tnext += TFD;
#pragma unroll 1
        for (int p = isO ? NINO : NINF + 1; p < 6; ++p) {
          pace(tnext, isO ? TPV : TPF);
          dma_piece(ws0, isO ? D : F, smem, L_WB, isO ? wave * 6 + p : wave * 5 + p - 1, fresh(lane0));
        }
      }
#define DA(CTL) "v_add_f32_dpp %[l], %[l], %[l] " CTL " row_mask:0xf bank_mask:0xf\n\t"
#define PW(W) "s_waitcnt vmcnt(6*" #W "+%[nf]) lgkmcnt(" #W ")\n\ts_cbranch_scc1 1f\n\ts_waitcnt vmcnt(6*" #W "+%[no])\n1:\n\t"
#define PX(o, v, a) SM "%[o" #o "], %[a" #a "], v[160+24*" #a "+8*" #o ":167+24*" #a "+8*" #o "], %[ix]\n\t"
      asm volatile("s_cmp_gt_i32 %[y], 19\n\t"
                   PW(3) PX(0, x, 0) DA("quad_perm:[1,0,3,2]") PX(1, y, 0) PX(2, z, 0)
                   PW(2) PX(0, x, 1) DA("quad_perm:[2,3,0,1]") PX(1, y, 1) PX(2, z, 1)
                   PW(1) PX(0, x, 2) DA("row_half_mirror") PX(1, y, 2) PX(2, z, 2)
                   PW(0) PX(0, x, 3) DA("row_ror:8") PX(1, y, 3) PX(2, z, 3) "s_setprio 0"
                   : [o0] "+v"(pv.O0), [o1] "+v"(pv.O1), [o2] "+v"(pv.O2), [a0] "+v"(pv.A0), [a1] "+v"(pv.A1),
                     [a2] "+v"(pv.A2), [a3] "+v"(pv.A3), [l] "+v"(pend)
                   : [ix] "v"(pv.idx), [nf] "n"(5 - NINF), [no] "n"(6 - NINO), [y] "s"(j - 32 * half),
                     "{v[160:191]}"(kv3), "{v[192:223]}"(kv4), "{v[224:255]}"(kv5)
                   : "memory", "scc");
      asm volatile("ds_write2_b32 %[la], %[l], %[mx] offset1:4\n\ts_mov_b64 exec, %[m]\n\ts_nop 2\n\t"
                   "ds_write_b128 %[a], %[o0]\n\tds_write_b128 %[a], %[o1] offset:256\n\t"
                   "ds_write_b128 %[a], %[o2] offset:512\n\t"
                   "s_mov_b64 exec, -1\n\ts_waitcnt lgkmcnt(0)\n\ts_barrier"
                   :
                   : [m] "s"(0xff00ff0000ff00ffull), [a] "v"(pv.oa + smb), [o0] "v"(pv.O0), [o1] "v"(pv.O1), [o2] "v"(pv.O2),
                     [la] "v"(pv.la + smb), [l] "v"(pend), [mx] "v"(pmax)
                   : "memory");
      tref = now();
      if (half == 0) ar = (u32)(g & 3) * AREA + (((u32)(g >> 2) ^ __builtin_amdgcn_readfirstlane(*(volatile LAS u32*)(smem + L_VOTE))) & 1u) * SIDE;
      if (half == 0 && wave == 5 && lane0 == 0) {
        const u32x2 sv = {*(volatile LAS u32*)(smem + L_TOTAL), rtnow()};
        __builtin_amdgcn_raw_buffer_store_b64(sv, rs, WS_END + b * 8, 0, 16);
      }
      if (wave < 4) {
        const float* mp = reinterpret_cast<const float*>(smem + pv.ma);
        float m0 = mp[0], m1 = mp[4 * OM_S];
        f32x4 v = *reinterpret_cast<const f32x4*>(smem + pv.sa), v1 = *reinterpret_cast<const f32x4*>(smem + pv.sa + OM_P * 4);
        f32x4 u = *reinterpret_cast<const f32x4*>(smem + pv.sa + OM_S * 16);
        f32x4 u1 = *reinterpret_cast<const f32x4*>(smem + pv.sa + OM_S * 16 + OM_P * 4);
        asm volatile("" : "+v"(m0), "+v"(m1), "+v"(v), "+v"(v1), "+v"(u), "+v"(u1));
        v += v1;
        u += u1;
        float M = 0.f;
        if (__builtin_expect(__builtin_amdgcn_ballot_w64((__float_as_uint(m0) | __float_as_uint(m1)) != 0u) != 0ull, 0)) {
          M = fmx(m0, m1);
          M = fmx(M, dpp<XOR1>(M));
          M = fmx(M, dpp<XOR2>(M));
          v *= __builtin_amdgcn_exp2f(m0 - M);
          u *= __builtin_amdgcn_exp2f(m1 - M);
        }
        v = sum4x4(v + u);
        u32 p0 = cvt2(v[0], v[1]), p1 = cvt2(v[2], v[3]);
        if (wave == 3) {
          p0 = __float_as_uint(M);
          p1 = __float_as_uint(sum4(v));
        }
        p0 |= 1u;
        p1 |= 1u;
        const u32x4 pk = {p0, p1, dppu<XOR7>(p0), dppu<XOR7>(p1)};
        asm volatile("s_mov_b64 exec, %2\n\tbuffer_store_dwordx4 %0, %1, %3, %4 offen sc1\n\ts_mov_b64 exec, -1"
                     :
                     : "v"(pk), "v"(pv.dst), "s"(wave < 3 ? 0x0101010101010101ull : 0x3ull), "s"(rq), "s"(ar + (u32)half * GO_SZ)
                     : "memory");
      }
    }
    const u32 go = ar + A_GO + (u32)half * GO_SZ;
    const u32 gyw = A_GY + (u32)half * GY_PAIR;
    const u32 gy = gyw + ar;
    const u32 gh = go + A_GH;
    const int land = L_LAND + wave * 2048;
    const u32 landa = (u32)(uintptr_t)(LAS unsigned char*)smem + (u32)land;
    if (isO) {
      {
        const int lane = fresh(lane0);
        const u32 voff = fresh(go + go_slot(wave) + ((lane * 16) ^ ((lane >> 4) * 64)));
        if (half)
          wait_rt(anc + A_O);
        else
          wait_until(tref + 24 * T_O);
        dma_poll<false>(rq, voff, landa, ~0ull, ~0ull);
        const int n = lane & 15, G = lane >> 4;
        const int r0_ = land + (n & 7) * 256 + (n & 3) * 64;
        const int ra = fresh(n < 8 ? r0_ + G * 16 : r0_ ^ 192), rb = fresh((r0_ + G * 16) ^ 64), rc = fresh((r0_ + G * 16) ^ 128);
        const int l0 = fresh(L_WB + wave * 6144 + wlane(lane, 0)), l1 = fresh(L_WB + wave * 6144 + wlane(lane, 1));
        asm volatile("s_waitcnt vmcnt(2)" ::: "memory");
        u32x4 wf[6];
#pragma unroll
        for (int k = 0; k < 3; ++k) {
          wf[2 * k] = wfrag(smem, l0, 2048 * k);
          wf[2 * k + 1] = wfrag(smem, l1, 2048 * k);
        }
        const int rw = fresh(L_RED + wave * 144 + lane * 4);
        const int rr = fresh(L_RED + (lane & 3) * 144 + ((lane >> 2) & 7) * 16);
        const u32 dst = fresh(gyw + gy_rep((lane & 3) + (lane >> 5) * 4) + gy_chunk(4 * j + ((lane >> 3) & 3)) + g * 256);
        f32x4 d0, d1;
        float mc, lc, t1_;
        u32 budget = SPIN_CAP;
#pragma unroll 1
        for (;;) {
          u64 need;
          u32 t;
          asm volatile("s_waitcnt vmcnt(0)\n\t"
                       "ds_read_b128 " F0 ", %[ra]\n\tds_read_b128 " F1 ", %[rb]\n\tds_read_b128 " F2 ", %[rc]\n\t"
                       "s_waitcnt lgkmcnt(2)\n\t"
                       MF "%[d0], " F0 ", %[w0], 0\n\t"
                       "v_add_f32_dpp %[l], v53, v53 quad_perm:[1,0,3,2] row_mask:0xf bank_mask:0xf\n\t"
                       MF "%[d1], " F0 ", %[w1], 0\n\t" AND3("v52", "v53", "v54")
                       "s_waitcnt lgkmcnt(1)\n\t"
                       MF "%[d0], " F1 ", %[w2], %[d0]\n\t"
                       "v_add_f32_dpp %[l], %[l], %[l] quad_perm:[2,3,0,1] row_mask:0xf bank_mask:0xf\n\t" AND3("%[t]", "v55", "v56")
                       MF "%[d1], " F1 ", %[w3], %[d1]\n\t" AND3("%[t]", "v57", "v58")
                       "v_add_f32_dpp %[l], %[l], %[l] row_half_mirror row_mask:0xf bank_mask:0xf\n\t"
                       "s_waitcnt lgkmcnt(0)\n\t"
                       MF "%[d0], " F2 ", %[w4], %[d0]\n\t" AND3("%[t]", "v59", "v60") AND3("%[t]", "v61", "v62")
                       MF "%[d1], " F2 ", %[w5], %[d1]\n\t" AND3("%[t]", "1", "v63")
                       "v_rcp_f32 %[l], %[l]\n\tv_cmp_eq_u32 %[need], 0, %[t]"
                       : [d0] "=&v"(d0), [d1] "=&v"(d1), [t] "=&v"(t), [need] "=&s"(need), [l] "=&v"(lc), "=&{v52}"(mc), "=&{v53}"(t1_)
                       : [ra] "v"(ra), [rb] "v"(rb), [rc] "v"(rc), [w0] "v"(wf[0]), [w1] "v"(wf[1]), [w2] "v"(wf[2]), [w3] "v"(wf[3]),
                         [w4] "v"(wf[4]), [w5] "v"(wf[5])
                       : "memory", "v54", "v55", "v56", "v57", "v58", "v59", "v60", "v61", "v62", "v63");
          if (__builtin_expect(need == 0ull || budget == 0u, 1)) break;
          --budget;
          u64 ea, eb;
          retry_masks<16>(need, lane, ea, eb);
          dma_poll<false>(rq, voff, landa, ea, eb);
        }
        float rl = __uint_as_float(__builtin_amdgcn_readlane(__float_as_uint(lc), 8));
        asm volatile("s_nop 1" : "+v"(d0), "+v"(d1));
        if (__builtin_expect((__builtin_amdgcn_ballot_w64(__float_as_uint(mc) != 1u) & 0xff00ull) != 0ull, 0)) {
          const float fc = __builtin_amdgcn_exp2f(mc - max8(mc));
          const u32 sc = __float_as_uint(fc * __builtin_amdgcn_rcpf(sum8(fc * t1_)));
#pragma unroll
          for (int e = 0; e < 4; ++e) {
            const float se = __uint_as_float(G == 0 ? __builtin_amdgcn_readlane(sc, 8 + e) : __builtin_amdgcn_readlane(sc, 12 + e));
            d0[e] *= se;
            d1[e] *= se;
          }
          rl = 1.0f;
        }
        {
          auto sw = __builtin_amdgcn_permlane16_swap(__float_as_uint(sum4(d0)), __float_as_uint(sum4(d1)), false, false);
          const float r = (__uint_as_float(sw[0]) + __uint_as_float(sw[1])) * rl;
          if (lane < 32) *reinterpret_cast<float*>(smem + rw) = r;
        }
        bar();
        if (wave == 0) {
          const f32x4 y = sum4x4(*reinterpret_cast<const f32x4*>(smem + rr) + *reinterpret_cast<const f32x4*>(smem + rr + 576));
          const u32 p0 = cvt2(y[0], y[1]) | 1u, p1 = cvt2(y[2], y[3]) | 1u;
          const u32x4 pk = {p0, p1, dppu<XOR7>(p0), dppu<XOR7>(p1)};
          asm volatile("s_mov_b64 exec, %2\n\tbuffer_store_dwordx4 %0, %1, %3, 0 offen sc1\n\ts_mov_b64 exec, -1"
                       :
                       : "v"(pk), "v"(dst), "s"(0x0f0f0f0f0f0f0f0full), "s"(rq)
                       : "memory");
        }
      }
      tref = now();
      if (half == 0) {
        if (wave >= 1 && wave < 7) {
          const u16* s0 = karg<56>() + (size_t)(192 * g) * D + 32 * j;
#pragma unroll
          for (int p = 0; p < 2; ++p) dma_piece(s0, D, smem, L_W2, (wave - 1) * 2 + p, fresh(lane0));
          asm volatile("s_waitcnt vmcnt(0)" ::: "memory");
        }
        bar();
        if (wave == 4) {
          wait_until(tref + 24 * T_H);
          stamp_poll(smem, rq, ar + st_line(fresh(lane0) & 7));
        }
        if (wave == 5) {
          wait_until(tref + 24 * T_LAZY);
          x_update<false>(smem, rq, fresh(lane0), gy + gy_row(c), c);
        }
      } else if (g == 0 && wave == 2) {
        const int lane = fresh(lane0);
        const int col = j * 32 + (lane & 7) * 4 + ((lane >> 4) & 1) * 2 + (lane >> 5);
        u32 x0v, dummy;
        u16* const po = karg<64>() + col;
        asm volatile("global_load_ushort %0, %2, off\n\tglobal_load_ushort %1, %3, off\n\ts_waitcnt vmcnt(0)"
                     : "=&v"(x0v), "=&v"(dummy)
                     : "v"(karg<0>() + col), "v"(po)
                     : "memory");
        wait_until(tref + 24 * T_LAZY);
        const u32 o = gy + gy_chunk(4 * j + ((lane >> 1) & 3)) + (lane >> 3) * 256 + (lane & 1) * 8;
        const u32 off[3] = {o - GY_PAIR, o - GY_PAIR + GY_INST, o};
        float y[3];
        col_poll<3>(rq, off, y);
        const float x3 = bf16r(bf16r(bf16r(bflo(x0v) + y[0]) + y[1]) + y[2]);
        const u32 off2[1] = {o + GY_INST};
        wait_rt(anc + A_F);
        float y4[1];
        col_poll<1>(rq, off2, y4);
        const float x4 = x3 + y4[0];
        if ((lane & 8) == 0) asm volatile("global_store_short %0, %1, off" ::"v"(po), "v"(cvt2(x4, x4)) : "memory");
        if (j == 0) {
          const u32 tnow = rtnow();
          u32x4 a, bb;
          asm volatile("buffer_load_dwordx4 %0, %2, %3, 0 offen sc1\n\tbuffer_load_dwordx4 %1, %2, %3, 0 offen offset:16 sc1\n\ts_waitcnt vmcnt(0)"
                       : "=&v"(a), "=&v"(bb) : "v"(WS_END + lane * 32), "s"(rq) : "memory");
          float s0 = (float)(tnow - a[0]), s1 = (float)(tnow - a[2]), s2 = (float)(tnow - bb[0]), s3 = (float)(tnow - bb[2]);
          float m0 = (float)(tnow - a[1]), m1 = (float)(tnow - a[3]), m2 = (float)(tnow - bb[1]), m3 = (float)(tnow - bb[3]);
          auto wmax = [](float v) { return xrow16_max(max_ror8(max8(v))); };
          const float oldest = wmax(fmx(fmx(s0, s1), fmx(s2, s3)));
          float r;
          if (PROBE == 1) r = oldest + wmax(fmx(fmx(-s0, -s1), fmx(-s2, -s3)));
          if (PROBE == 2) r = oldest - wave_sum(s0 + s1 + s2 + s3) * (1.f / 256);
          if (PROBE == 3) r = oldest + wmax(fmx(fmx(-m0, -m1), fmx(-m2, -m3))) - 1700.f;
          if (PROBE == 4) r = oldest - wave_sum(m0 + m1 + m2 + m3) * (1.f / 256) - 1700.f;
          if (PROBE == 5) r = oldest - (float)(tnow - anc) - 1900.f;
          if (PROBE == 6) r = oldest - 2500.f;
          if (PROBE == 7) r = oldest - wmax(fmx(fmx(m0, m1), fmx(m2, m3))) - 1700.f;
          r = __builtin_fminf(__builtin_fmaxf(r, 0.f), 600.f);
          const u32 te = tnow + (u32)__builtin_amdgcn_readfirstlane((int)(r * 10.f));
          while ((int)(rtnow() - te) < 0) __builtin_amdgcn_s_sleep(1);
        }
      }
      if (wave >= 6) {
        const int lane = fresh(lane0), wv = wave - 6;
        const int n = lane & 15, G = lane >> 4;
        const int l0 = fresh(L_W2 + wlane(lane, wv));
        u32x4 wf[6];
#pragma unroll
        for (int k = 0; k < 6; ++k) wf[k] = wfrag(smem, l0, 2048 * k);
        const int l48 = lane < 48 ? lane : 0;
        const u32 off[2] = {gh + (u32)l48 * 16, gh + (u32)l48 * 16 + 16384};
        const int aw = fresh(L_ACT + wv * 384 + l48 * 8);
        const int ab = fresh(L_ACT + wv * 384 + 16 * G);
        const u32 dst = fresh(gyw + GY_INST + gy_rep(n & 7) + gy_chunk(4 * j + wv * 2 + (G >> 1)) + g * 256);
        f32x4 v[2];
        if (half)
          wait_rt(anc + A_H);
        else
          wait_until(tref + 24 * T_H);
        gpoll4<2>(rq, off, v);
        {
          f32x4 a, u;
#pragma unroll
          for (int e = 0; e < 4; ++e) {
            a[e] = bfhi(__float_as_uint(v[0][e]));
            u[e] = bfhi(__float_as_uint(v[1][e]));
          }
          const f32x4 hh = a * u;
          *reinterpret_cast<u32x2*>(smem + aw) = u32x2{cvt2(hh[0], hh[1]), cvt2(hh[2], hh[3])};
        }
        u32x4 af[4];
#pragma unroll
        for (int k = 0; k < 4; ++k) af[k] = afrag(smem, ab + 64 * k);
        asm volatile("" : "+v"(af[0]), "+v"(af[1]), "+v"(af[2]), "+v"(af[3]));
        f32x4 d = {0.f, 0.f, 0.f, 0.f};
        d = mfma32(wf[0], af[0], d);
        d = mfma32(wf[1], af[1], d);
        asm volatile("" : "+v"(d)::"memory");
        af[0] = afrag(smem, ab + 256);
        af[1] = afrag(smem, ab + 320);
        d = mfma32(wf[2], af[2], d);
        d = mfma32(wf[3], af[3], d);
        d = mfma32(wf[4], af[0], d);
        d = mfma32(wf[5], af[1], d);
        const u32 p0 = cvt2(d[0], d[1]) | 1u, p1 = cvt2(d[2], d[3]) | 1u;
        auto s0 = __builtin_amdgcn_permlane16_swap(p0, p0, false, false);
        auto s1 = __builtin_amdgcn_permlane16_swap(p1, p1, false, false);
        if ((lane & (half ? 27 : 24)) == 0) store16(rs, dst, u32x4{s0[0], s1[0], s0[1], s1[1]});
      }
    } else {
      const int lane = fresh(lane0);
      const int n = lane & 15, G = lane >> 4;
      if (half == 1) {
        x_update<false>(smem, rq, lane, gy - GY_PAIR + GY_INST + gy_row(wave), wave);
      }
      asm volatile("s_waitcnt vmcnt(0) lgkmcnt(0)" ::: "memory");
      G80 a;
      g80_load(a, smem, lane, L_WB + wave * 5120, L_HB + 160 * wave, land);
      const u32 voff = fresh(gy + gy_row(wave) + ((lane * 16) ^ ((lane >> 4) * 64)));
      const int rw = fresh(L_RED + n * 144 + wave * 16 + G * 4);
      const int sw_ = fresh(n == 0 ? L_ST + wave * 16 + G * 4 : L_ACT);
      float r0, r1, z;
      if (half)
        wait_rt(anc + A_Y);
      else
        wait_until(tref + 24 * T_YF);
      g80_run<true>(a, smem, rq, lane, voff, landa, r0, r1, z);
      *reinterpret_cast<float*>(smem + rw) = r0;
      *reinterpret_cast<float*>(smem + rw + 16 * 144) = r1;
      *reinterpret_cast<float*>(smem + sw_) = z;
      const int ln = fresh(lane0);
      const int rr = fresh(L_RED + (ln & 31) * 144 + (ln >> 5) * 64), rs_ = fresh(L_ST + 16 * (ln & 7));
      const u32 dst = fresh(gh + fslot + ln * 4);
      bar();
      tref = now();
      if (half == 0 && wave == 1 && j == 20 && ln < 8)
        __builtin_amdgcn_raw_buffer_store_b32((rtnow() << 1) | 1u, rs, gy_rep(ln) + st_line(g), 0, 16);
      if (wave == 0) {
        f32x4 st = *reinterpret_cast<const f32x4*>(smem + rs_);
        f32x4 q0 = *reinterpret_cast<const f32x4*>(smem + rr), q1 = *reinterpret_cast<const f32x4*>(smem + rr + 16);
        f32x4 q2 = *reinterpret_cast<const f32x4*>(smem + rr + 32), q3 = *reinterpret_cast<const f32x4*>(smem + rr + 48);
        asm volatile("" : "+v"(st), "+v"(q0), "+v"(q1), "+v"(q2), "+v"(q3));
        const LnS ls = ln_stats(st[3], st[0] + st[1] + st[2]);
        const f32x4 qs = (q0 + q1) + (q2 + q3);
        float r = fmaf(-ls.mb, qs[3], qs[0] + qs[1] + qs[2]);
        auto sw = __builtin_amdgcn_permlane32_swap(__float_as_uint(r), __float_as_uint(r), false, false);
        r = bf16r((__uint_as_float(sw[0]) + __uint_as_float(sw[1])) * ls.ri);
        if ((j & 1) == 0) r = bf16r(r * __builtin_amdgcn_rcpf(1.0f + __builtin_amdgcn_exp2f(r * -1.4426950408889634f)));
        const f32x4 qd = quad4(r);
        if ((ln & 35) == 0) store_quad(rs, dst, qd);
      }
      if (half == 0) {
        x_update<true>(smem, rq, fresh(lane0), land, wave);
        if (wave == 7) {
          wait_until(tref + 24 * T_AN);
          stamp_poll(smem, rq, ar + st_line(ln & 7));
        }
      }
    }
    if (half == 1) break;
    {
      if (wave < 6) {
        const int lane = fresh(lane0);
        const int n = lane & 15;
        G80 a;
        g80_load(a, smem, lane, L_WQ + wave * 5120, L_HB + 160 * c, land);
        const u32 voff = fresh(gy + GY_INST + gy_row(c) + ((lane * 16) ^ ((lane >> 4) * 64)));
        const float cc = *reinterpret_cast<const float*>(smem + L_CS + wave * 128 + (lane >> 5) * 64 + n * 4);
        const u32 gq = ar + A_GQ + hsub * 16384 + c * 896;
        const u32 dst = fresh(gq + wave * 128 + (lane >> 5) * 64 + n * 4);
        float r0, r1, z;
        bar();
        wait_rt(__builtin_amdgcn_readfirstlane(*(volatile LAS u32*)(smem + L_ANC)) + A_E);
        g80_run<true>(a, smem, rq, lane, voff, landa, r0, r1, z);
        auto sw = __builtin_amdgcn_permlane32_swap(__float_as_uint(r0), __float_as_uint(r1), false, false);
        const float sh = cc * __uint_as_float(__builtin_amdgcn_readlane(__float_as_uint(z), 48));
        float r = __uint_as_float(sw[0]) +
                  __uint_as_float((u32)__builtin_amdgcn_update_dpp((int)sw[1], (int)__float_as_uint(sh), 0xE4, 0xA, 0xf, false));
        auto sx = __builtin_amdgcn_permlane16_swap(__float_as_uint(r), __float_as_uint(r), false, false);
        r = __uint_as_float(sx[0]) + __uint_as_float(sx[1]);
        const f32x4 qd = quad4(r);
        if ((lane & 19) == 0) store_quad(rs, dst, qd);
        if (wave == 5) {
          const u32 zu = __float_as_uint(z);
          const f32x4 zq = {__uint_as_float(__builtin_amdgcn_readlane(zu, 0)), __uint_as_float(__builtin_amdgcn_readlane(zu, 16)),
                            __uint_as_float(__builtin_amdgcn_readlane(zu, 32)), __uint_as_float(__builtin_amdgcn_readlane(zu, 48))};
          if (lane == 0) store_quad(rs, gq + 768, zq);
        }
      } else {
        bar();
      }
    }
  }
  asm volatile(".Lmk_code_end:");
}

}

extern "C" void kernel_launch(void* const* d_in, const int* in_sizes, int n_in, void* d_out, int out_size, void* d_ws,
                              size_t ws_size, hipStream_t stream) {
  Params P;
  P.x0 = (const u16*)d_in[0];
  P.kc = (const u16*)d_in[1];
  P.vc = (const u16*)d_in[2];
  P.wq = (const u16*)d_in[3];
  P.wo = (const u16*)d_in[4];
  P.w1 = (const u16*)d_in[6];
  P.w3 = (const u16*)d_in[7];
  P.w2 = (const u16*)d_in[8];
  P.out = (u16*)d_out;
  P.ws = (u32*)d_ws;
  mega_kernel<<<NWG, NT, 0, stream>>>(P);
}
```

```cpp
#include <hip/hip_runtime.h>
#include <hip/hip_bf16.h>
#include <stdint.h>

namespace {

typedef unsigned int u32;
typedef unsigned short u16;
typedef unsigned long long u64;
using u32x2 = __attribute__((ext_vector_type(2))) unsigned;
using u32x4 = __attribute__((ext_vector_type(4))) unsigned;
using f32x2 = __attribute__((ext_vector_type(2))) float;
using f32x4 = __attribute__((ext_vector_type(4))) float;
using s16x4 = __attribute__((ext_vector_type(4))) short;
using bf16x2 = __attribute__((ext_vector_type(2))) __bf16;
using bf16x8 = __attribute__((ext_vector_type(8))) __bf16;
#define LAS __attribute__((address_space(3)))

constexpr int D = 640, H = 32, DH = 192, WIN = 4096, SK = 32768, F = 1536;
constexpr int NWG = 256, NT = 512;
constexpr u32 SPIN_CAP = 1u << 16;
constexpr float SCL = 0.07216878364870322f * 1.4426950408889634f;

#define T_O 43
#define T_YF 122
#define T_H 120
#define T_AN 35
#define PTHR 60
#define T_LAZY 60

#define A_E 122
#define A_Q (A_E + 84)
#define A_O (A_Q + 146)
#define A_Y (A_O + 96)
#define A_H (A_Y + 94)
#define A_F (A_H + 58)

constexpr u32 SIDE = 8192, AREA = 17 * 16384;
constexpr u32 A_GO = 0;
constexpr u32 A_GY = 2 * 16384;
constexpr u32 A_GQ = 6 * 16384;
constexpr u32 A_GH = 16 * 16384;
constexpr u32 GO_SZ = 10 * 16384, GY_PAIR = GO_SZ, GY_INST = 32768;
constexpr u32 WS_PB = 4 * AREA;
constexpr u32 WS_GOF = WS_PB + (128 << 10), WS_HB = WS_GOF + 4096;
constexpr u32 WS_GQ1 = WS_HB + 256 * 128;
constexpr u32 WS_END = WS_GQ1 + 256 * 2048;
static_assert(WS_END <= (2u << 20), "layout");
__device__ __forceinline__ u32 go_slot(int w) { return (u32)(w >> 2) * 16384u + (u32)(w & 3) * 2048u; }
__device__ __forceinline__ u32 gy_row(int i) { return go_slot(i); }
__device__ __forceinline__ u32 gy_chunk(int u) {
  const int i = (u * 205) >> 11;
  return gy_row(i) + (u32)(u - 10 * i) * 16u;
}
__device__ __forceinline__ u32 st_line(int g) { return A_GH + 3072u + (u32)g * 128u; }
__device__ __forceinline__ u32 gy_rep(int r) { return (u32)(r & 3) * AREA + (u32)(r >> 2) * SIDE; }

constexpr int L_ZERO = 0;
constexpr int L_CNT = 16;
constexpr int L_RI = 28;
constexpr int L_ONE = 32;
constexpr int L_TAG = 48;
constexpr int L_ST = 64;
constexpr int L_VOTE = L_ST;
constexpr int L_ANC = 192;
constexpr int L_Q = 256;
constexpr int L_CS = 640;
constexpr int L_HB = L_CS + 768;
constexpr int L_ACT = L_HB + D * 2;
constexpr int L_RED = L_ACT + 2 * 384;
constexpr int L_P = L_RED + 32 * 144;
constexpr int P_W = 1280, P_U = 592, P_PAR = 288;
constexpr int OM_S = 400, OM_P = 200;
constexpr int L_OM = L_P + 8 * P_W;
constexpr int L_LN = L_OM;
constexpr int L_LAND = (L_OM + 8 * OM_S * 4 + 255) / 256 * 256;
constexpr int L_WQ = L_LAND + 8 * 2048;
constexpr int L_WB = L_WQ + 6 * 5120;
constexpr int L_W2 = L_WB + 768 * 64;
constexpr int L_PF = L_W2 + 192 * 64;
constexpr int L_TAB = L_PF + 256;
constexpr int L_TOTAL = L_TAB + 8 * 2048;
static_assert(L_LAND % 256 == 0 && L_P % 16 == 0 && L_WQ % 16 == 0 && L_HB % 16 == 0 && L_RED % 16 == 0, "alignment");

struct Params {
  const u16* x0;
  const u16* kc;
  const u16* vc;
  const u16* wq;
  const u16* wo;
  const u16* w1;
  const u16* w3;
  const u16* w2;
  u16* out;
  u32* ws;
};

__device__ __forceinline__ u32 cvt2(float lo, float hi) {
  return __builtin_bit_cast(u32, __builtin_convertvector(f32x2{lo, hi}, bf16x2));
}
__device__ __forceinline__ float bflo(u32 w) { return __uint_as_float(w << 16); }
__device__ __forceinline__ float bfhi(u32 w) { return __uint_as_float(w & 0xffff0000u); }
__device__ __forceinline__ float bf16r(float x) { return bflo(cvt2(x, x)); }

template <int CTRL>
__device__ __forceinline__ float dpp(float x) {
  return __builtin_bit_cast(float, __builtin_amdgcn_mov_dpp(__builtin_bit_cast(int, x), CTRL, 0xf, 0xf, true));
}
constexpr int XOR1 = 0xB1, XOR2 = 0x4E, XOR7 = 0x141, ROR8 = 0x128;
__device__ __forceinline__ float fmx(float a, float b) {
  float r;
  asm("v_max_f32 %0, %1, %2" : "=v"(r) : "v"(a), "v"(b));
  return r;
}
__device__ __forceinline__ float xrow16_max(float x) {
  auto s = __builtin_amdgcn_permlane16_swap(__float_as_uint(x), __float_as_uint(x), false, false);
  x = fmx(__uint_as_float(s[0]), __uint_as_float(s[1]));
  auto t = __builtin_amdgcn_permlane32_swap(__float_as_uint(x), __float_as_uint(x), false, false);
  return fmx(__uint_as_float(t[0]), __uint_as_float(t[1]));
}
__device__ __forceinline__ float xrow16_sum(float x) {
  auto s = __builtin_amdgcn_permlane16_swap(__float_as_uint(x), __float_as_uint(x), false, false);
  x = __uint_as_float(s[0]) + __uint_as_float(s[1]);
  auto t = __builtin_amdgcn_permlane32_swap(__float_as_uint(x), __float_as_uint(x), false, false);
  return __uint_as_float(t[0]) + __uint_as_float(t[1]);
}
__device__ __forceinline__ float sum8(float v) {
  v += dpp<XOR1>(v);
  v += dpp<XOR2>(v);
  v += dpp<XOR7>(v);
  return v;
}
__device__ __forceinline__ float max8(float v) {
  asm("s_nop 1\n\tv_max_f32_dpp %0, %0, %0 quad_perm:[1,0,3,2] row_mask:0xf bank_mask:0xf\n\t"
      "s_nop 1\n\tv_max_f32_dpp %0, %0, %0 quad_perm:[2,3,0,1] row_mask:0xf bank_mask:0xf\n\t"
      "s_nop 1\n\tv_max_f32_dpp %0, %0, %0 row_half_mirror row_mask:0xf bank_mask:0xf"
      : "+v"(v));
  return v;
}
__device__ __forceinline__ float max_ror8(float v) {
  asm("s_nop 1\n\tv_max_f32_dpp %0, %0, %0 row_ror:8 row_mask:0xf bank_mask:0xf" : "+v"(v));
  return v;
}
__device__ __forceinline__ f32x4 sum4x4(f32x4 v) {
  float a = v[0], b = v[1], c = v[2], d = v[3];
#define S4(CTL)                                               \
  "v_add_f32_dpp %0, %0, %0 " CTL " row_mask:0xf bank_mask:0xf\n\t" \
  "v_add_f32_dpp %1, %1, %1 " CTL " row_mask:0xf bank_mask:0xf\n\t" \
  "v_add_f32_dpp %2, %2, %2 " CTL " row_mask:0xf bank_mask:0xf\n\t" \
  "v_add_f32_dpp %3, %3, %3 " CTL " row_mask:0xf bank_mask:0xf\n\t"
  asm("s_nop 1\n\t" S4("quad_perm:[1,0,3,2]") S4("quad_perm:[2,3,0,1]") : "+v"(a), "+v"(b), "+v"(c), "+v"(d));
#undef S4
  return f32x4{a, b, c, d};
}
__device__ __forceinline__ float wave_sum(float v) {
  v = sum8(v);
  v += dpp<ROR8>(v);
  return xrow16_sum(v);
}
__device__ __forceinline__ float tsum8(f32x4 a) {
  auto s0 = __builtin_amdgcn_permlane32_swap(__float_as_uint(a[0]), __float_as_uint(a[1]), false, false);
  auto s1 = __builtin_amdgcn_permlane32_swap(__float_as_uint(a[2]), __float_as_uint(a[3]), false, false);
  const float r0 = __uint_as_float(s0[0]) + __uint_as_float(s0[1]);
  const float r1 = __uint_as_float(s1[0]) + __uint_as_float(s1[1]);
  auto t = __builtin_amdgcn_permlane16_swap(__float_as_uint(r0), __float_as_uint(r1), false, false);
  float r = __uint_as_float(t[0]) + __uint_as_float(t[1]);
  r += dpp<ROR8>(r);
  return r;
}

__device__ __forceinline__ u32 tagv(float v) { return __float_as_uint(v) | 1u; }
__device__ __forceinline__ void store_quad(__amdgpu_buffer_rsrc_t rs, u32 off, f32x4 v) {
  const u32x4 s = {tagv(v[0]), tagv(v[1]), tagv(v[2]), tagv(v[3])};
  __builtin_amdgcn_raw_buffer_store_b128(s, rs, off, 0, 16);
}
template <int N>
__device__ __forceinline__ void gpoll4(u32x4 rs, const u32 (&off)[N], f32x4 (&v)[N]) {
  bool need = true;
  u32 budget = SPIN_CAP;
#pragma unroll
  for (int k = 0; k < N; ++k) asm volatile("" : "=v"(v[k]));
#pragma unroll 1
  for (;;) {
    if (need) {
#pragma unroll
      for (int k = 0; k < N; ++k)
        asm volatile("buffer_load_dwordx4 %0, %1, %2, 0 offen sc1" : "+v"(v[k]) : "v"(off[k]), "s"(rs) : "memory");
    }
    asm volatile("s_waitcnt vmcnt(0)" ::: "memory");
    u32 a = 1u;
#pragma unroll
    for (int k = 0; k < N; ++k) {
      asm volatile("" : "+v"(v[k]));
      a &= __float_as_uint(v[k][0]) & __float_as_uint(v[k][1]) & __float_as_uint(v[k][2]) & __float_as_uint(v[k][3]);
    }
    need = a == 0u;
    if (__builtin_amdgcn_ballot_w64(need) == 0ull || budget == 0u) break;
    --budget;
    __builtin_amdgcn_s_sleep(1);
  }
}
__device__ __forceinline__ u32 now() { return (u32)__builtin_amdgcn_s_memtime(); }
__device__ __forceinline__ void wait_until(u32 t) {
#pragma unroll 1
  while ((int)(now() - t) < -160) __builtin_amdgcn_s_sleep(1);
#pragma unroll 1
  while ((int)(now() - t) < 0) {
  }
}
__device__ __forceinline__ u32 rtnow() { return (u32)__builtin_amdgcn_s_memrealtime(); }
__device__ __forceinline__ void wait_rt(u32 t) {
  u32 spins = 0;
#pragma unroll 1
  while ((int)(rtnow() - t) < -8 && ++spins < 400u) __builtin_amdgcn_s_sleep(1);
#pragma unroll 1
  while ((int)(rtnow() - t) < 0 && ++spins < 440u) {
  }
}
template <int BITS>
__device__ __forceinline__ u32 youngest(u32 t);
__device__ __forceinline__ void lds_wait(volatile LAS u32* p, u32 want) {
  u32 spins = 0;
#pragma unroll 1
  while (__builtin_amdgcn_readfirstlane(*p) != want) {
    if (++spins > (SPIN_CAP << 6)) break;
    __builtin_amdgcn_s_sleep(1);
  }
  asm volatile("" ::: "memory");
}
__device__ __forceinline__ u32 lds_wait_nz(volatile LAS u32* p) {
  u32 spins = 0, v;
#pragma unroll 1
  while ((v = __builtin_amdgcn_readfirstlane(*p)) == 0u) {
    if (++spins > (SPIN_CAP << 6)) break;
    __builtin_amdgcn_s_sleep(1);
  }
  asm volatile("" ::: "memory");
  return v;
}
template <int BITS>
__device__ __forceinline__ u32 youngest(u32 t) {
  const u32 nw = rtnow();
  const float age = (float)((nw - t) & ((1u << BITS) - 1u));
  return nw - (u32)__builtin_amdgcn_readfirstlane((int)-xrow16_max(max_ror8(max8(-age))));
}
__device__ __forceinline__ void stamp_poll(unsigned char* smem, u32x4 rq, u32 so) {
  u32 sv, budget = SPIN_CAP;
#pragma unroll 1
  for (;;) {
    asm volatile("buffer_load_dword %0, %1, %2, 0 offen sc1\n\ts_waitcnt vmcnt(0)" : "=&v"(sv) : "v"(so), "s"(rq) : "memory");
    if (__builtin_amdgcn_ballot_w64((sv & 1u) == 0u) == 0ull || budget == 0u) break;
    --budget;
    __builtin_amdgcn_s_sleep(1);
  }
  *(volatile LAS u32*)(smem + L_ANC) = youngest<24>(sv >> 1) | 1u;
}
__device__ __forceinline__ void lds_add(unsigned char* smem, int off, u32 v) {
  __hip_atomic_fetch_add((LAS u32*)(smem + off), v, __ATOMIC_RELAXED, __HIP_MEMORY_SCOPE_WORKGROUP);
}

__device__ __forceinline__ int fresh(int t) {
  asm volatile("" : "+v"(t));
  return t;
}
__device__ __forceinline__ f32x4 zero4() {
  f32x2 a, b;
  asm volatile("v_mov_b64 %0, 0\n\tv_mov_b64 %1, 0" : "=v"(a), "=v"(b));
  return f32x4{a[0], a[1], b[0], b[1]};
}
__device__ __forceinline__ void bar() { asm volatile("s_waitcnt lgkmcnt(0)\n\ts_barrier" ::: "memory"); }

__device__ __forceinline__ u32x2 trread(unsigned char* smem, int off) {
  return __builtin_bit_cast(u32x2, __builtin_amdgcn_ds_read_tr16_b64_v4i16((LAS s16x4*)(smem + off)));
}
__device__ __forceinline__ u32x4 wfrag(unsigned char* smem, int lb, int off) {
  const u32x2 a = trread(smem, lb + off), b = trread(smem, lb + off + 256);
  return u32x4{a[0], a[1], b[0], b[1]};
}
__device__ __forceinline__ int wlane(int lane, int cb) {
  const int i = lane & 15, G = lane >> 4;
  return (8 * G + (i >> 2)) * 64 + (i & 3) * 8 + ((cb ^ (G & 1)) * 32);
}
__device__ __forceinline__ u32x4 afrag(unsigned char* smem, int a) { return *reinterpret_cast<const u32x4*>(smem + a); }
__device__ __forceinline__ f32x4 mfma32(u32x4 w, u32x4 a, f32x4 c) {
  return __builtin_amdgcn_mfma_f32_16x16x32_bf16(__builtin_bit_cast(bf16x8, w), __builtin_bit_cast(bf16x8, a), c, 0, 0, 0);
}

__device__ __forceinline__ void dma_piece(const u16* src, int ld, unsigned char* smem, int ldsoff, int p, int lane) {
  const int row = 16 * p + (lane >> 2), part = lane & 3;
  const int cb = (part >> 1) ^ (lane >> 5);
  const u32 voff = (u32)(row * ld + cb * 16 + (part & 1) * 8) * 2u;
  const u32 dst = (u32)(uintptr_t)(LAS unsigned char*)smem + (u32)(ldsoff + p * 1024);
  u32 keep;
  asm volatile("s_mov_b32 %0, m0\n\ts_mov_b32 m0, %2\n\ts_nop 2\n\tglobal_load_lds_dwordx4 %1, %3 nt\n\ts_mov_b32 m0, %0"
               : "=&s"(keep)
               : "v"(voff), "s"(dst), "s"(src)
               : "memory");
}

template <int CTRL>
__device__ __forceinline__ u32 dppu(u32 x) {
  return (u32)__builtin_amdgcn_mov_dpp((int)x, CTRL, 0xf, 0xf, true);
}
__device__ __forceinline__ float sum4(f32x4 d) { return (d[0] + d[1]) + (d[2] + d[3]); }
__device__ __forceinline__ u32 and4(u32x4 v) { return v[0] & v[1] & v[2] & v[3]; }
__device__ __forceinline__ void store16(__amdgpu_buffer_rsrc_t rs, u32 off, u32x4 v) {
  __builtin_amdgcn_raw_buffer_store_b128(v, rs, off, 0, 16);
}
#define POLL16(dst, voff, imm) \
  asm volatile("buffer_load_dwordx4 %0, %1, %2, 0 offen offset:%3 sc1" : "+v"(dst) : "v"(voff), "s"(rq), "n"(imm) : "memory")

struct LnS {
  float mb, ri;
};
__device__ __forceinline__ LnS ln_stats(float s1, float s2) {
#define S2(CTL)                                                     \
  "v_add_f32_dpp %0, %0, %0 " CTL " row_mask:0xf bank_mask:0xf\n\t" \
  "v_add_f32_dpp %1, %1, %1 " CTL " row_mask:0xf bank_mask:0xf\n\ts_nop 0\n\t"
  asm("s_nop 1\n\t" S2("quad_perm:[1,0,3,2]") S2("quad_perm:[2,3,0,1]") S2("row_half_mirror") : "+v"(s1), "+v"(s2));
#undef S2
  const float dm = s1 * (1.0f / D);
  const u32 mv = cvt2(dm, __builtin_fmaxf(fmaf(-dm, dm, s2 * (1.0f / D)), 0.f));
  LnS r;
  r.mb = bflo(mv);
  r.ri = __builtin_amdgcn_rcpf(bf16r(__builtin_amdgcn_sqrtf(bf16r(bfhi(mv) + 1e-5f))));
  return r;
}

struct G80 {
  u32x4 w[6];
  int ra, rb, rc;
};
__device__ __forceinline__ void g80_load(G80& a, unsigned char* smem, int lane, int wb, int xs, int land) {
  const int m = lane & 15, G = lane >> 4;
  const int l0 = wb + wlane(lane, 0), l1 = wb + wlane(lane, 1), bk = 4096 - (G >> 1) * 1024;
  a.w[0] = wfrag(smem, l0, 0);
  a.w[1] = wfrag(smem, l1, 0);
  a.w[2] = wfrag(smem, l0, 2048);
  a.w[3] = wfrag(smem, l1, 2048);
  a.w[4] = wfrag(smem, l0 + bk, 0);
  a.w[5] = wfrag(smem, l1 + bk, 0);
  const bool vec = m == 8 || (m < 8 && land >= 0);
  const int st = vec ? 64 : 0;
  const int sa = vec ? (m == 8 ? xs : land + 256 * m) + 16 * G : (m == 15 ? L_ONE : L_ZERO);
  const int sw = (vec && m < 8) ? (m & 3) * 64 : 0;
  a.ra = fresh(sa ^ sw);
  a.rb = fresh((sa + st) ^ sw);
  a.rc = fresh(G < 2 ? (sa + 2 * st) ^ sw : L_TAG);
}
template <bool WAIT = true>
__device__ __forceinline__ void dma_poll(u32x4 rq, u32 voff, u32 land, u64 ea, u64 eb) {
  u32 keep;
  asm volatile("s_mov_b32 %0, m0\n\ts_mov_b32 m0, %3\n\ts_mov_b64 exec, %4\n\t"
               "buffer_load_dwordx4 %1, %2, 0 offen sc1 lds\n\t"
               "s_mov_b64 exec, %5\n\t"
               "buffer_load_dwordx4 %1, %2, 0 offen offset:1024 sc1 lds\n\t"
               "s_mov_b64 exec, -1\n\ts_mov_b32 m0, %0"
               : "=&s"(keep)
               : "v"(voff), "s"(rq), "s"(land), "s"(ea), "s"(eb)
               : "memory");
  if (WAIT) asm volatile("s_waitcnt vmcnt(0)" ::: "memory");
}
template <bool X4>
__device__ __forceinline__ void ctl_load(u32x4 rq, u32 voff, u32 lds) {
  u32 keep;
  if (X4)
    asm volatile("s_mov_b32 %0, m0\n\ts_mov_b32 m0, %3\n\ts_nop 2\n\tbuffer_load_dwordx4 %1, %2, 0 offen sc1 lds\n\ts_mov_b32 m0, %0"
                 : "=&s"(keep) : "v"(voff), "s"(rq), "s"(lds) : "memory");
  else
    asm volatile("s_mov_b32 %0, m0\n\ts_mov_b32 m0, %3\n\ts_nop 2\n\tbuffer_load_dword %1, %2, 0 offen sc1 lds\n\ts_mov_b32 m0, %0"
                 : "=&s"(keep) : "v"(voff), "s"(rq), "s"(lds) : "memory");
}
template <int UN>
__device__ __forceinline__ void retry_masks(u64 need, int lane, u64& ea, u64& eb) {
  u32 m = (u32)need | (u32)(need >> 32);
  m |= m >> 16;
  m = (m | (m >> 8)) & 0xffu;
  const int ln = fresh(lane);
  ea = __builtin_amdgcn_ballot_w64(((m >> (ln / UN)) & 1u) != 0u);
  eb = __builtin_amdgcn_ballot_w64(((m >> ((ln + 64) / UN)) & 1u) != 0u);
}
#define MF "v_mfma_f32_16x16x32_bf16 "
#define F0 "v[52:55]"
#define F1 "v[56:59]"
#define F2 "v[60:63]"
#define FCLOB "v52", "v53", "v54", "v55", "v56", "v57", "v58", "v59", "v60", "v61", "v62", "v63"
#define AND3(a, b, c) "v_bitop3_b32 %[t], " a ", " b ", " c " bitop3:0x80\n\t"
template <bool POLL>
__device__ __forceinline__ void g80_run(G80& a, unsigned char* smem, u32x4 rq, int lane, u32 voff, u32 land, float& r0,
                                        float& r1, float& z) {
  f32x4 d0, d1;
  if (POLL) {
    f32x4 d2;
    constexpr u64 USED = 0xff30ff0330ff03ffull;
    u64 ea = USED, eb = USED;
    u32 budget = SPIN_CAP;
#pragma unroll 1
    for (;;) {
      dma_poll(rq, voff, land, ea, eb);
      u64 need;
      u32 t;
      asm volatile("ds_read_b128 " F0 ", %[ra]\n\tds_read_b128 " F1 ", %[rb]\n\tds_read_b128 " F2 ", %[rc]\n\t"
                   "s_waitcnt lgkmcnt(2)\n\t"
                   MF "%[d2], " F0 ", " F0 ", 0\n\t" AND3("v52", "v53", "v54")
                   MF "%[d0], " F0 ", %[w0], 0\n\t" MF "%[d1], " F0 ", %[w1], 0\n\t"
                   "s_waitcnt lgkmcnt(1)\n\t"
                   MF "%[d2], " F1 ", " F1 ", %[d2]\n\t" AND3("%[t]", "v55", "v56")
                   MF "%[d0], " F1 ", %[w2], %[d0]\n\t" AND3("%[t]", "v57", "v58")
                   MF "%[d1], " F1 ", %[w3], %[d1]\n\t"
                   "s_waitcnt lgkmcnt(0)\n\t"
                   MF "%[d2], " F2 ", " F2 ", %[d2]\n\t" AND3("%[t]", "v59", "v60") AND3("%[t]", "v61", "v62")
                   MF "%[d0], " F2 ", %[w4], %[d0]\n\t" AND3("%[t]", "1", "v63")
                   "v_cmp_eq_u32 %[need], 0, %[t]\n\t"
                   MF "%[d1], " F2 ", %[w5], %[d1]"
                   : [d0] "=&v"(d0), [d1] "=&v"(d1), [d2] "=&v"(d2), [t] "=&v"(t), [need] "=&s"(need)
                   : [ra] "v"(a.ra), [rb] "v"(a.rb), [rc] "v"(a.rc), [w0] "v"(a.w[0]), [w1] "v"(a.w[1]), [w2] "v"(a.w[2]),
                     [w3] "v"(a.w[3]), [w4] "v"(a.w[4]), [w5] "v"(a.w[5])
                   : "memory", FCLOB);
      need &= 0x00ff00ff00ff00ffull;
      if (need == 0ull || budget == 0u) break;
      --budget;
      retry_masks<16>(need, lane, ea, eb);
      ea &= USED;
      eb &= USED;
    }
    asm volatile("s_nop 1" : "+v"(d2));
    z = sum4(d2);
    asm volatile("s_nop 1" : "+v"(z), "+v"(d0), "+v"(d1));
    z = (lane & 15) == 15 ? 0.f : z;
    z = sum8(z);
    z += dpp<ROR8>(z);
  } else {
    const u32x4 f0 = afrag(smem, a.ra), f1 = afrag(smem, a.rb), f2 = afrag(smem, a.rc);
    d0 = d1 = f32x4{0.f, 0.f, 0.f, 0.f};
    d0 = mfma32(f0, a.w[0], d0);
    d1 = mfma32(f0, a.w[1], d1);
    d0 = mfma32(f1, a.w[2], d0);
    d1 = mfma32(f1, a.w[3], d1);
    d0 = mfma32(f2, a.w[4], d0);
    d1 = mfma32(f2, a.w[5], d1);
  }
  r0 = sum4(d0);
  r1 = sum4(d1);
}
__device__ __forceinline__ f32x4 quad4(float r) { return f32x4{r, dpp<0x55>(r), dpp<0xAA>(r), dpp<0xFF>(r)}; }

__device__ __forceinline__ int q_addr(int d) {
  const int w = d >> 1;
  return L_Q + ((w & 1) * 48 + (w >> 1)) * 4 + (d & 1) * 2;
}

__device__ __forceinline__ void cs_gather(unsigned char* smem, u32x4 rq, int lane, int wave, int h) {
  f32x4 v;
  asm volatile("buffer_load_dwordx4 %0, %1, %2, 0 offen offset:1024 sc1\n\ts_waitcnt vmcnt(0)"
               : "=&v"(v)
               : "v"(WS_GQ1 + (u32)(h * 8 + (lane >> 3)) * 2048 + wave * 128 + (lane & 7) * 16), "s"(rq)
               : "memory");
  const int e = wave * 32 + (lane & 7) * 4 + ((lane >> 4) & 1) * 2 + (lane >> 5);
  *reinterpret_cast<float*>(smem + L_CS + e * 4) = tsum8(v) * (-1.0f / D);
}
template <bool LDS>
__device__ __forceinline__ void x_update(unsigned char* smem, u32x4 rq, int lane, u32 src, int i) {
  const int l = lane < 40 ? lane : 0;
  const u32 off = src + 4 * l;
  u32 v[8];
  if (LDS) {
#pragma unroll
    for (int k = 0; k < 8; ++k) v[k] = *reinterpret_cast<const u32*>(smem + (off ^ ((k & 3) * 64)) + 256 * k);
  } else {
    u32 budget = SPIN_CAP;
#pragma unroll 1
    for (;;) {
#pragma unroll
      for (int k = 0; k < 8; ++k)
        asm volatile("buffer_load_dword %0, %1, %2, 0 offen offset:%3 sc1" : "=v"(v[k]) : "v"(off), "s"(rq), "n"(256 * k) : "memory");
      asm volatile("s_waitcnt vmcnt(0)"
                   : "+v"(v[0]), "+v"(v[1]), "+v"(v[2]), "+v"(v[3]), "+v"(v[4]), "+v"(v[5]), "+v"(v[6]), "+v"(v[7])::"memory");
      const u32 a = v[0] & v[1] & v[2] & v[3] & v[4] & v[5] & v[6] & v[7] & 1u;
      if (__builtin_amdgcn_ballot_w64(a == 0u) == 0ull || budget == 0u) break;
      --budget;
      __builtin_amdgcn_s_sleep(2);
    }
  }
  float lo = 0.f, hi = 0.f;
#pragma unroll
  for (int k = 0; k < 8; ++k) {
    lo += bflo(v[k]);
    hi += bfhi(v[k]);
  }
  u32* px = reinterpret_cast<u32*>(smem + L_HB + 160 * i + 4 * l);
  const u32 xo = *px, y = cvt2(lo, hi);
  if (lane < 40) *px = cvt2(bflo(xo) + bflo(y), bfhi(xo) + bfhi(y));
}
template <int N>
__device__ __forceinline__ void col_poll(u32x4 rq, const u32 (&off)[N], float (&y)[N]) {
  u32x2 v[N];
  u32 budget = SPIN_CAP;
#pragma unroll 1
  for (;;) {
    u32 a = 1u;
#pragma unroll
    for (int k = 0; k < N; ++k)
      asm volatile("buffer_load_dwordx2 %0, %1, %2, 0 offen sc1" : "=v"(v[k]) : "v"(off[k]), "s"(rq) : "memory");
    asm volatile("s_waitcnt vmcnt(0)" ::: "memory");
#pragma unroll
    for (int k = 0; k < N; ++k) {
      asm volatile("" : "+v"(v[k]));
      a &= v[k][0] & v[k][1];
    }
    if (__builtin_amdgcn_ballot_w64(a == 0u) == 0ull || budget == 0u) break;
    --budget;
  }
#pragma unroll
  for (int k = 0; k < N; ++k) y[k] = bf16r(tsum8(f32x4{bflo(v[k][0]), bfhi(v[k][0]), bflo(v[k][1]), bfhi(v[k][1])}));
}

#define WMAX 6
#define CKL 18
#define CKV 4
#define QACK 5
#define BEAT0 (QI + QACK + 1)
#define CLRI -1
#define CLRX 0
#define XNO 0
#define PROBE 1
#define MAP8 1
#define WBIG 10
#define LANDED asm volatile("s_waitcnt vmcnt(%0)" ::"n"(WMAX - 1) : "memory")
#define TSTEP 230
#define QI 6
#define NINO 3
#define NINF 2
static_assert(5 - NINF >= 6 - NINO, "counts");
#define XCLR 4
#define XPFN "0x140"
#define XTAB 3
#define NPRE 3
#define TPF 220
#define TPV 220
#define TFD 0
#define XA 16
#define XB 18
#define TS_O (TSTEP * (20 * (48 + NINO) + 12 * (48 + NINF)) / (32 * (48 + NINO)))
#define TS_F (TSTEP * (20 * (48 + NINO) + 12 * (48 + NINF)) / (32 * (48 + NINF)))
#define TSQ 256
#define ROT (wave * 3 + j + g * 5)
static_assert(QI >= WMAX && CKL > BEAT0 && BEAT0 > QI + QACK && 23 - CKL + CKV + 1 >= WBIG && CKV + 1 <= XB - WBIG &&
              CKV + 1 + WBIG <= XA && CLRI < BEAT0, "window");
#define PACE_ASM                                                                                                     \
  "1:\n\ts_memrealtime s[98:99]\n\ts_waitcnt lgkmcnt(0)\n\ts_lshl_b32 s98, s98, 3\n\ts_sub_i32 s98, s98, %[tn]\n\t"  \
  "s_cmp_lt_i32 s98, 0\n\ts_cbranch_scc0 2f\n\ts_sleep 1\n\ts_branch 1b\n2:\n\ts_add_i32 %[tn], %[tn], %[ts]\n\t"
__device__ __forceinline__ void pace(u32& tnext, u32 tstep) {
  asm volatile(PACE_ASM : [tn] "+s"(tnext) : [ts] "s"(tstep) : "scc", "s98", "s99");
}
constexpr int kreg(int i) { return i % 3 < 2 ? 64 + 8 * (i / 3) + 4 * (i % 3) : 128 + 4 * (i / 3); }
constexpr int vreg(int i) { return 160 + 24 * (i / 6) + 8 * (i % 3) + 4 * ((i / 3) & 1); }
#define AQ0 3
#define AGAIN 0
#define STR2(x) #x
#define STR(x) STR2(x)
#define ADAPT_ASM
#define LOADW(reg, voff, sbase, imm)                                                                                \
  asm volatile(PACE_ASM "global_load_dwordx4 v[%[r0]:%[r1]], %[p], %[sb] offset:%[im] nt\n\ts_waitcnt vmcnt(%[w])" ADAPT_ASM \
               : [tn] "+s"(tnext)                                                                                   \
               : [r0] "n"(reg), [r1] "n"((reg) + 3), [p] "v"(voff), [sb] "s"(sbase), [im] "n"(imm), [ts] "s"(tstep - AQ0 * AGAIN), \
                 [w] "n"(WBIG - 1)                                                                                  \
               : "scc", "s98", "s99")

#define HS_WAIT(COND)                                                                                \
  _Pragma("unroll 1") for (;;) {                                                                     \
    gd = CTL[512 + lane0] - CTL[576 + lane0];                                                        \
    const u64 ch = __builtin_amdgcn_ballot_w64(gd != 0u) & 0x1ffull;                                 \
    if (ch > 0xffull || (COND) || tmo == 0u) break;                                                  \
    if ((tmo & 3u) == 0u && lane0 == 0)                                                              \
      __builtin_amdgcn_raw_buffer_store_b32(hb, rs, WS_HB + h * 128 + c * 4, 0, 16);                 \
    hb += 4u;                                                                                        \
    --tmo;                                                                                           \
    __builtin_amdgcn_s_sleep(3);                                                                     \
    ctl_load<false>(rq, hso, (u32)(smb + L_CTL + 2048));                                             \
    asm volatile("s_waitcnt vmcnt(0)" ::: "memory");                                                 \
  }

struct QKAddr {
  int qad[6], idxq, wa;
  f32x4 D0, D1;
};
struct PVAddr {
  int ra, idx, oa, sa, la, ma;
  u32 dst;
  f32x4 O0, O1, O2;
  u32x4 A0, A1, A2, A3;
};
__device__ __forceinline__ void att_table(unsigned char* smem, int lane, int wave, u32 go) {
  const int n = lane & 15, G = lane >> 4;
  const int seg = n & 1, pr = (n >> 1) & 1, tq = n >> 2, xy = G >> 1;
  const int qa = L_Q + (pr * 48 + seg * 8 + (G & 1) * 4) * 4;
  u32 t[16];
#pragma unroll
  for (int k = 0; k < 4; ++k) t[k] = tq == k ? qa + xy * 64 : L_ZERO;
#pragma unroll
  for (int k = 0; k < 2; ++k) t[4 + k] = tq == 2 * k + xy ? qa + 128 : L_ZERO;
  t[6] = 0x4444 + pr * 0xAAAA;
  t[7] = L_P + wave * P_W + ((n >> 1) & 1) * P_PAR + ((((G & 1) * 2 + (n >> 3)) * 4 + (n & 1) * 2 + (G >> 1)) * 16) + ((n >> 2) & 1) * 8;
  t[8] = L_P + wave * P_W + ((n >> 2) & 1) * P_U + (n >> 3) * P_PAR + G * 64;
  t[9] = (n & 3) * 0x5555;
  t[10] = L_OM + (wave * OM_S + (n >> 3) * OM_P + (n & 7) * 4 + (G & 1) * 32) * 4;
  t[11] = L_OM + (wave * OM_S + DH + G) * 4;
  t[12] = L_OM + ((lane & 3) * OM_S + (wave & 3) * 64 + (lane >> 3) * 4 + (lane & 4) * 8) * 4;
  t[13] = L_OM + ((lane & 3) * OM_S + DH + 4) * 4;
  const int d0 = wave * 64 + (lane >> 3) * 8;
  t[14] = (go + (wave < 3 ? (d0 >= 96 ? 2048 - 192 : 0) + d0 * 2 : lane * 2048 + 192)) & 0xffffu;
  t[15] = q_addr(wave * 32 + (lane & 7) * 4 + ((lane >> 4) & 1) * 2 + (lane >> 5));
#pragma unroll
  for (int k = 0; k < 2; ++k)
    *reinterpret_cast<u32x4*>(smem + L_TAB + wave * 2048 + k * 1024 + lane * 16) =
        u32x4{t[8 * k] | (t[8 * k + 4] << 16), t[8 * k + 1] | (t[8 * k + 5] << 16), t[8 * k + 2] | (t[8 * k + 6] << 16),
              t[8 * k + 3] | (t[8 * k + 7] << 16)};
}
__device__ __forceinline__ void att_setup(QKAddr& r, PVAddr& v, unsigned char* smem, int lane, int wave, int& qw) {
  const u32x4 a = afrag(smem, L_TAB + wave * 2048 + lane * 16), b = afrag(smem, L_TAB + wave * 2048 + 1024 + lane * 16);
  r.D0 = zero4();
  r.D1 = zero4();
  qw = fresh(b[3] >> 16);
#pragma unroll
  for (int k = 0; k < 4; ++k) r.qad[k] = fresh(a[k] & 0xffffu);
  r.qad[4] = fresh(a[0] >> 16);
  r.qad[5] = fresh(a[1] >> 16);
  r.idxq = fresh(a[2] >> 16);
  r.wa = fresh(a[3] >> 16);
  v.ra = fresh(b[0] & 0xffffu);
  v.idx = fresh(b[1] & 0xffffu);
  v.oa = fresh(b[2] & 0xffffu);
  v.la = fresh(b[3] & 0xffffu);
  v.sa = fresh(b[0] >> 16);
  v.ma = fresh(b[1] >> 16);
  v.dst = fresh(b[2] >> 16);
  asm volatile("" : "+v"(r.D0), "+v"(r.D1));
}
#define SM "v_smfmac_f32_16x16x64_bf16 "
#define P_WRITE_READ(QA)                                                                                              \
  asm volatile("ds_write2_b32 %[wa], %[p], %[p] offset1:%[o1]\n\t"                                                    \
               "ds_read_b128 %[a0], %[ra]\n\tds_read_b128 %[a1], %[ra] offset:16\n\t"                                 \
               "ds_read_b128 %[a2], %[ra] offset:32\n\tds_read_b128 %[a3], %[ra] offset:48"                           \
               : [a0] "=&v"(pv.A0), [a1] "=&v"(pv.A1), [a2] "=&v"(pv.A2), [a3] "=&v"(pv.A3), "+v"(sk)                 \
               : [wa] "v"(QA.wa + smb), [p] "v"(cvt2(pend, 0.f)), [ra] "v"(pv.ra + smb), [o1] "n"(P_U / 4 + 1)        \
               : "memory");
#define QK_SOFTMAX(QA)                                                                                                \
  {                                                                                                                   \
    u32x4 A0, A1, A2, A3, A4, A5;                                                                                     \
    float scl;                                                                                                        \
    asm volatile("ds_read_b128 %[a0], %[q0]\n\tds_read_b128 %[a1], %[q1]\n\tds_read_b128 %[a2], %[q2]\n\t"            \
                 "ds_read_b128 %[a3], %[q3]\n\tds_read_b128 %[a4], %[q4]\n\tds_read_b128 %[a5], %[q5]"                \
                 : [a0] "=&v"(A0), [a1] "=&v"(A1), [a2] "=&v"(A2), [a3] "=&v"(A3), [a4] "=&v"(A4), [a5] "=&v"(A5)     \
                 : [q0] "v"(QA.qad[0] + smb), [q1] "v"(QA.qad[1] + smb), [q2] "v"(QA.qad[2] + smb),                   \
                   [q3] "v"(QA.qad[3] + smb), [q4] "v"(QA.qad[4] + smb), [q5] "v"(QA.qad[5] + smb)                    \
                 : "memory");                                                                                         \
    asm volatile("ds_read_b32 %[s], %[z] offset:%[ri]\n\t"                                                            \
                 "s_waitcnt lgkmcnt(6)\n\t" SM "%[d0], %[a0], v[64:71], %[ix]\n\t" SM "%[d1], %[a0], v[96:103], %[ix]\n\t"   \
                 "s_waitcnt lgkmcnt(5)\n\t" SM "%[d0], %[a1], v[72:79], %[ix]\n\t" SM "%[d1], %[a1], v[104:111], %[ix]\n\t"   \
                 "s_waitcnt lgkmcnt(4)\n\t" SM "%[d0], %[a2], v[80:87], %[ix]\n\t" SM "%[d1], %[a2], v[112:119], %[ix]\n\t"   \
                 "s_waitcnt lgkmcnt(3)\n\t" SM "%[d0], %[a3], v[88:95], %[ix]\n\t" SM "%[d1], %[a3], v[120:127], %[ix]\n\t"   \
                 "s_waitcnt lgkmcnt(2)\n\t" SM "%[d0], %[a4], v[128:135], %[ix]\n\t" SM "%[d1], %[a4], v[144:151], %[ix]\n\t"   \
                 "s_waitcnt lgkmcnt(0)\n\t" SM "%[d0], %[a5], v[136:143], %[ix]\n\t" SM "%[d1], %[a5], v[152:159], %[ix]\n\t"   \
                                      \
                 "ds_read_b128 %[a0], %[z]\n\tds_read_b128 %[a1], %[z]\n\tds_read_b128 %[a2], %[z]\n\ts_nop 4"        \
                 : [d0] "+v"(QA.D0), [d1] "+v"(QA.D1), [s] "=&v"(scl), [a0] "+v"(A0), [a1] "+v"(A1), [a2] "+v"(A2),   \
                   [a3] "+v"(A3), [a4] "+v"(A4), [a5] "+v"(A5)                                                        \
                 : [ix] "v"(QA.idxq), [z] "v"(smb), [ri] "n"(L_RI), "{v[64:95]}"(kv0), "{v[96:127]}"(kv1),           \
                   "{v[128:159]}"(kv2)                                                                                \
                 : "memory");                                                                                         \
    pv.O0 = __builtin_bit_cast(f32x4, A0);                                                                            \
    pv.O1 = __builtin_bit_cast(f32x4, A1);                                                                            \
    pv.O2 = __builtin_bit_cast(f32x4, A2);                                                                            \
    const f32x2 e0 = f32x2{QA.D0[0], QA.D0[1]} + f32x2{QA.D0[2], QA.D0[3]};                                           \
    const f32x2 e1 = f32x2{QA.D1[0], QA.D1[1]} + f32x2{QA.D1[2], QA.D1[3]};                                           \
    const float s0 = e0[0] + dpp<XOR1>(e0[1]), s1 = e1[1] + dpp<XOR1>(e1[0]);                                         \
    const float sc = ((lane0 & 1) ? s1 : s0) * scl;                                                                   \
    pend = __builtin_amdgcn_exp2f(sc);                                                                                \
    pmax = 0.f;                                                                                                       \
    float sk = sc;                                                                                                    \
    P_WRITE_READ(QA)                                                                                                  \
    const bool unsafe =                                                                                               \
        (__builtin_amdgcn_ballot_w64(!(sk <= 24.f)) != 0ull) | (__builtin_amdgcn_ballot_w64(sk >= -24.f) == 0ull);    \
    if (__builtin_expect(unsafe, 0)) {                                                                                \
      pmax = xrow16_max(max_ror8(max8(sk)));                                                                          \
      pend = __builtin_amdgcn_exp2f(sk - pmax);                                                                       \
      asm volatile("s_waitcnt lgkmcnt(0)" : "+v"(pv.A0), "+v"(pv.A1), "+v"(pv.A2), "+v"(pv.A3));                      \
      P_WRITE_READ(QA)                                                                                                \
      asm volatile("s_waitcnt lgkmcnt(0)" : "+v"(pv.A0), "+v"(pv.A1), "+v"(pv.A2), "+v"(pv.A3));                      \
    }                                                                                                                 \
  }

using u32x16 = __attribute__((ext_vector_type(16))) unsigned;
using u32x32 = __attribute__((ext_vector_type(32))) unsigned;
template <int OFF>
__device__ __forceinline__ u16* karg() {
  u64 p;
  asm volatile("s_load_dwordx2 %0, %1, %2\n\ts_waitcnt lgkmcnt(0)" : "=s"(p) : "s"(__builtin_amdgcn_kernarg_segment_ptr()), "n"(OFF));
  return (u16*)p;
}
template <class T>
__device__ __forceinline__ T* mkptr(u32 lo, u32 hi) {
  return (T*)(((u64)hi << 32) | lo);
}

__global__ __launch_bounds__(NT, 2) void mega_kernel(Params) {
  __shared__ __attribute__((aligned(16))) unsigned char smem[L_TOTAL];
  u32x16 ka;
  u32x4 kb4;
  {
    u64 pc;
    u32 keep;
    asm volatile("s_load_dwordx16 %[ka], %[kp], 0x0\n\ts_load_dwordx4 %[kb], %[kp], 0x40\n\t"
                 "s_getpc_b64 %[pc]\n.Lmk_pc:\n\t"
                 "s_mov_b32 %[keep], m0\n\ts_mov_b32 m0, %[dst]\n\t"
                 "v_lshlrev_b32 v1, 7, %[tid]\n\tv_min_u32 v1, .Lmk_code_end-.Lmk_pc-4, v1\n\t"
                 "v_cmp_gt_u32 vcc, " XPFN ", %[tid]\n\ts_mov_b64 exec, vcc\n\t"
                 "global_load_lds_dword v1, %[pc]\n\ts_mov_b64 exec, -1\n\ts_mov_b32 m0, %[keep]\n\t"
                 "v_lshrrev_b32 v1, 2, %[tid]\n\tv_cmp_eq_u32 vcc, 3, v1\n\tv_mov_b32 v3, 0x3f803f80\n\ts_nop 0\n\t"
                 "v_cndmask_b32 v2, 0, 1, vcc\n\tv_cmp_eq_u32 vcc, 2, v1\n\tv_lshlrev_b32 v1, 2, %[tid]\n\ts_nop 0\n\t"
                 "v_cndmask_b32 v2, v2, v3, vcc\n\tv_cmp_eq_u32 vcc, 7, %[tid]\n\tv_mov_b32 v3, %[scl]\n\t"
                 "v_min_u32 v1, 80, v1\n\tv_cndmask_b32 v2, v2, v3, vcc\n\t"
                 "ds_write_b32 v1, v2 offset:%[sm]\n\t"
                 "s_waitcnt lgkmcnt(0)\n\ts_barrier"
                 : [ka] "=&s"(ka), [kb] "=&s"(kb4), [pc] "=&s"(pc), [keep] "=&s"(keep)
                 : [kp] "s"(__builtin_amdgcn_kernarg_segment_ptr()), [tid] "v"(threadIdx.x), [dst] "n"(L_PF),
                   [scl] "n"(__builtin_bit_cast(u32, SCL)), [sm] "n"(0)
                 : "memory", "vcc", "v1", "v2", "v3");
  }
  const u32 tstart = (u32)__builtin_amdgcn_s_memrealtime();
  const int tid = threadIdx.x, lane0 = tid & 63;
  const int wave = __builtin_amdgcn_readfirstlane(tid >> 6);
  const int b = blockIdx.x;
  const int g = b & 7, j = b >> 3, hsub = j & 3, c = j >> 2, h = g * 4 + hsub;
  const bool isO = j < 20;
  const bool leader = b == 0 && wave == 6;
  const u32 fslot = (u32)((j & 1) * 768 + ((j - 20) >> 1) * 128);
  u32 tmo = SPIN_CAP;
  volatile LAS u32* cnt = (volatile LAS u32*)(smem + L_CNT);
  Params P;
  P.x0 = mkptr<const u16>(ka[0], ka[1]);
  P.kc = mkptr<const u16>(ka[2], ka[3]);
  P.vc = mkptr<const u16>(ka[4], ka[5]);
  P.wq = mkptr<const u16>(ka[6], ka[7]);
  P.wo = mkptr<const u16>(ka[8], ka[9]);
  P.w1 = mkptr<const u16>(ka[10], ka[11]);
  P.w3 = mkptr<const u16>(ka[12], ka[13]);
  P.ws = mkptr<u32>(kb4[2], kb4[3]);
  u32* const wsb = (u32*)(((uintptr_t)P.ws + 0x1fffffu) & ~(uintptr_t)0x1fffffu);
  const __amdgpu_buffer_rsrc_t rs = __builtin_amdgcn_make_buffer_rsrc(wsb, 0, (int)WS_END, 0x00020000);
  const u32x4 rq = {(u32)(uintptr_t)wsb, (u32)((uintptr_t)wsb >> 32) & 0xffffu, WS_END, 0x00020000u};

  u32 anc = 0u;
  u32 tref = 0u, ar = 0u;
  u32x32 kv0, kv1, kv2, kv3, kv4, kv5;
  asm volatile(""
               : "={v[64:95]}"(kv0), "={v[96:127]}"(kv1), "={v[128:159]}"(kv2), "={v[160:191]}"(kv3), "={v[192:223]}"(kv4),
                 "={v[224:255]}"(kv5));
  const int smb = (int)(uintptr_t)(LAS unsigned char*)smem;
  const u32 gop = A_GO + go_slot(hsub * 2) + c * 256;
  const u32 tstep = isO ? TS_O : TS_F;
  u32 tnext = tstart * 8u + (u32)wave * (TSTEP / 8) + 5 * TSQ;
  const size_t rowbase = (size_t)h * SK + (SK - WIN) + (wave * 8 + c) * 8;
#define GSTR 512
#undef ROT
#define ROT h
  const u16* vb = P.vc + rowbase * DH;
  const int rot = ROT;
  const u16* ws0 = isO ? P.wo + (size_t)(768 * g) * D + 32 * j : ((j & 1) ? P.w3 : P.w1) + 192 * g + 32 * ((j - 20) >> 1);
  {
    constexpr int L_CTL = L_LAND + 6 * 2048;
    volatile LAS u32* const CTL = (volatile LAS u32*)(smem + L_CTL);
    u32 gd, hb = 0u;
    const u32 lho = WS_HB + (lane0 >> 1) * 128 + (lane0 & 1) * 16;
    const u32 hso = lane0 < 8 ? WS_HB + (u32)h * 128 + lane0 * 4 : WS_GOF + g * 128;
    if (wave < 6) {
      const u16* s0 = P.wq + (size_t)(80 * c) * (H * DH) + h * DH + wave * 32;
      u32 tq = tnext - 5 * TSQ;
#pragma unroll 1
      for (int p = 0; p < 5; ++p) {
        pace(tq, TSQ);
        dma_piece(s0, H * DH, smem, L_WQ + wave * 5120, p, lane0);
      }
    } else {
      if (leader) {
        ctl_load<true>(rq, lho, (u32)(smb + L_CTL));
        ctl_load<false>(rq, WS_GOF + (lane0 & 7) * 128, (u32)(smb + L_CTL + 2560));
      }
      u32 rtt;
      {
        u32 pa = 0u, pb = 0u;
        u64 t1, t2;
        const u32 pbo = WS_PB + (u32)(wave & 1) * (SIDE + 128u) + (u32)((b >> 3) & 7) * 16384u + (u32)((b & 7) * 4 + (b >> 6)) * 256u;
        asm volatile(".p2align 6\n\ts_mov_b64 exec, 1\n\t"
                     "buffer_load_dword %0, off, %4, %5 sc1 nt\n\tbuffer_load_dword %1, off, %4, %6 sc1 nt\n\t"
                     "s_mov_b64 exec, -1\n\t"
                     "s_waitcnt vmcnt(1)\n\ts_memtime %2\n\ts_waitcnt vmcnt(0)\n\ts_memtime %3\n\ts_waitcnt lgkmcnt(0)"
                     : "+v"(pa), "+v"(pb), "=&s"(t1), "=&s"(t2)
                     : "s"(rq), "s"(pbo), "s"(pbo ^ SIDE)
                     : "memory");
        rtt = __builtin_amdgcn_readfirstlane((u32)t2 - (u32)t1) + 1u;
      }
      if (wave == 6) {
        *(volatile LAS u32*)(smem + L_VOTE) = rtt;
        u32 keep;
        asm volatile("s_mov_b32 %0, m0\n\ts_mov_b32 m0, %4\n\ts_nop 2\n\tglobal_load_lds_dwordx4 %1, %3\n\t"
                     "global_load_lds_dword %2, %3 offset:1024\n\ts_mov_b32 m0, %0"
                     : "=&s"(keep)
                     : "v"(lane0 * 16), "v"(lane0 * 4), "s"(P.x0), "s"((u32)(smb + L_HB))
                     : "memory");
      } else {
        ctl_load<false>(rq, hso, (u32)(smb + L_CTL + 2304));
#define CLEAR1                                                                                                      \
  {                                                                                                                 \
    const u32 ca = gy_rep((g + CLRX) & 7) + fresh(lane0) * 16;                                                      \
    store16(rs, ca + (u32)(j >> 3) * 16384u + (u32)(j & 7) * 1024u, u32x4{0u, 0u, 0u, 0u});                         \
    if (lane0 < 8) store16(rs, ca + A_GH + (u32)j * 128u, u32x4{0u, 0u, 0u, 0u});                                   \
  }
        if (CLRI < 0 && !(XNO & 1)) CLEAR1
        const int df = (int)(lds_wait_nz((volatile LAS u32*)(smem + L_VOTE)) - rtt);
        hb = df > PTHR ? 1u << (g >> 2) : (df < -PTHR ? 2u >> (g >> 2) : 0u);
      }
    }
    if (tid == 0) *(volatile LAS u32*)(smem + L_ANC) = 0u;
    *reinterpret_cast<u32x4*>(smem + L_P + wave * P_W + lane0 * 16) = u32x4{0u, 0u, 0u, 0u};
    *reinterpret_cast<u32x4*>(smem + (lane0 < 16 ? L_P + wave * P_W + 1024 + lane0 * 16 : L_OM + (wave * OM_S + OM_P + DH) * 4)) =
        u32x4{0u, 0u, 0u, 0u};
    const u16* kb = P.kc + rowbase * DH;
    const u32 ko = ((lane0 & 15) >> 1) * 384 + 16 * ((lane0 & 1) * 4 + (lane0 >> 4)), vo = (lane0 >> 3) * 384 + 16 * (lane0 & 7);
#pragma unroll
    for (int i = 0; i < 24; ++i) {
      LOADW(kreg(i), ko, kb + ((i / 3 + rot) & 7) * GSTR * DH, 128 * (i % 3));
      if (i == XTAB) att_table(smem, fresh(lane0), wave, gop);
      if (i == 1 && wave == 6) {
        asm volatile("s_waitcnt vmcnt(2)" ::: "memory");
        const u32x4 lv = afrag(smem, L_HB + lane0 * 16);
        const u32 xb = *reinterpret_cast<const u32*>(smem + L_HB + 1024 + lane0 * 4);
        float v[10];
#pragma unroll
        for (int k = 0; k < 4; ++k) {
          v[2 * k] = bflo(lv[k]);
          v[2 * k + 1] = bfhi(lv[k]);
        }
        v[8] = bflo(xb);
        v[9] = bfhi(xb);
        float sm = 0.f;
#pragma unroll
        for (int k = 0; k < 10; ++k) sm += v[k];
        const float mean = wave_sum(sm) * (1.0f / D);
        float q = 0.f;
#pragma unroll
        for (int k = 0; k < 10; ++k) q = fmaf(v[k] - mean, v[k] - mean, q);
        const float var = wave_sum(q) * (1.0f / D);
        const float mb = bf16r(mean);
        const float ri = __builtin_amdgcn_rcpf(bf16r(__builtin_amdgcn_sqrtf(bf16r(bf16r(var) + 1e-5f))));
        u32 hp[5];
#pragma unroll
        for (int k = 0; k < 5; ++k) {
          const u32 cc = cvt2(v[2 * k] - mb, v[2 * k + 1] - mb);
          hp[k] = cvt2(bflo(cc) * ri, bfhi(cc) * ri);
        }
        *reinterpret_cast<u32x4*>(smem + L_LN + lane0 * 16) = u32x4{hp[0], hp[1], hp[2], hp[3]};
        *reinterpret_cast<u32*>(smem + L_LN + 1024 + lane0 * 4) = hp[4];
        asm volatile("s_waitcnt lgkmcnt(0)" ::: "memory");
        *(volatile LAS u32*)(smem + L_VOTE + 4) = 1u;
      }
      if (i == QI && wave < 6) {
        LANDED;
        lds_wait((volatile LAS u32*)(smem + L_VOTE + 4), 1u);
        G80 a;
        g80_load(a, smem, lane0, L_WQ + wave * 5120, L_LN + 160 * c, -1);
        float r0, r1, z;
        g80_run<false>(a, smem, rq, lane0, 0u, 0u, r0, r1, z);
        auto sw = __builtin_amdgcn_permlane32_swap(__float_as_uint(r0), __float_as_uint(r1), false, false);
        const f32x4 qd = quad4(__uint_as_float(sw[1]));
        const int qp = wave * 128 + (lane0 >> 5) * 64 + (lane0 & 15) * 4;
        if ((lane0 & 19) == 0 && !(XNO & 2)) store16(rs, WS_GQ1 + (u32)(h * 8 + c) * 2048 + qp, __builtin_bit_cast(u32x4, qd));
        if ((lane0 & 19) == 16) *reinterpret_cast<f32x4*>(smem + L_CS + qp) = qd;
      }
      if (i == QI + QACK && wave < 6) {
        asm volatile("s_waitcnt vmcnt(%0)" ::"n"(QACK) : "memory");
        if (lane0 == 0) lds_add(smem, L_CNT + 4, 1u);
      }
      if (wave == 7) {
        if (i == CLRI) CLEAR1
        if (i == BEAT0) {
          asm volatile("s_waitcnt vmcnt(%0)" ::"n"(BEAT0 - CLRI) : "memory");
          lds_wait(cnt + 1, 6u);
          hb += (__builtin_amdgcn_readfirstlane(CTL[576 + c]) & ~3u) + 4u;
          if (lane0 == 0) __builtin_amdgcn_raw_buffer_store_b32(hb, rs, WS_HB + h * 128 + c * 4, 0, 16);
          hb += 4u;
        }
        if (i == CKL) ctl_load<false>(rq, hso, (u32)(smb + L_CTL + 2048));
      }
      if (i == CKL && leader) {
        ctl_load<true>(rq, lho, (u32)(smb + L_CTL + 1024));
      }
    }
#define VLOADS(A, B)                                                                           \
  _Pragma("unroll") for (int i = A; i < B; ++i) {                                              \
    LOADW(vreg(i), vo, vb + ((i / 3 + rot) & 7) * GSTR * DH, 128 * (i % 3));                      \
    if (i % 2 == 1 && i / 2 < (isO ? NINO : NINF)) {                                           \
      pace(tnext, tstep);                                                                      \
      dma_piece(ws0, isO ? D : F, smem, L_WB, wave * (isO ? 6 : 5) + i / 2, lane0);            \
    }                                                                                          \
  }
    VLOADS(0, CKV)
    if (wave == 7) {
      HS_WAIT((u32)ch == 0xffu)
      if (lane0 == 0) cnt[1] = 7u;
    }
    if (leader) {
      const u32x4 xa = afrag(smem, L_CTL + lane0 * 16);
      u32x4 lv;
#pragma unroll 1
      for (;;) {
        asm volatile("" ::: "memory");
        lv = afrag(smem, L_CTL + 1024 + lane0 * 16);
        const bool same = lv[0] == xa[0] || lv[1] == xa[1] || lv[2] == xa[2] || lv[3] == xa[3];
        if (__builtin_amdgcn_ballot_w64(same) == 0ull || tmo == 0u) break;
        --tmo;
        __builtin_amdgcn_s_sleep(4);
        ctl_load<true>(rq, lho, (u32)(smb + L_CTL + 1024));
        asm volatile("s_waitcnt vmcnt(0)" ::: "memory");
      }
      u32 nv = 256u;
#pragma unroll
      for (int e = 0; e < 4; ++e)
        nv += (u32)__builtin_popcountll(__builtin_amdgcn_ballot_w64((lv[e] & 2u) != 0u)) -
              (u32)__builtin_popcountll(__builtin_amdgcn_ballot_w64((lv[e] & 1u) != 0u));
      if (lane0 < 8) __builtin_amdgcn_raw_buffer_store_b32(CTL[640 + lane0] + (nv > 256u ? 2u : 1u), rs, WS_GOF + lane0 * 128, 0, 16);
    }
    VLOADS(CKV, CKV + 1)
    if (wave < 6) {
      const int ln = fresh(lane0);
      lds_wait(cnt + 1, 7u);
      ctl_load<true>(rq, WS_GQ1 + (u32)(h * 8 + (ln >> 3)) * 2048 + wave * 128 + (ln & 7) * 16, (u32)(smb + L_LAND + wave * 2048));
    }
    VLOADS(CKV + 1, XB - WBIG)
    if (wave == 7) ctl_load<false>(rq, hso, (u32)(smb + L_CTL + 2048));
    VLOADS(XB - WBIG, CKV + 1 + WBIG)
    if (wave < 6) {
      const int ln = fresh(lane0);
      asm volatile("" ::: "memory");
      const float t = tsum8(*reinterpret_cast<const f32x4*>(smem + L_LAND + wave * 2048 + ln * 16));
      *reinterpret_cast<u16*>(smem + q_addr(wave * 32 + (ln & 7) * 4 + ((ln >> 4) & 1) * 2 + (ln >> 5))) = (u16)cvt2(t, t);
      asm volatile("s_waitcnt lgkmcnt(0)" ::: "memory");
      if (lane0 == 0) lds_add(smem, L_CNT, 1u);
    }
    VLOADS(CKV + 1 + WBIG, XA)
    if (wave >= 4) {
      VLOADS(XA, XB)
      if (wave == 7) {
        HS_WAIT(false)
        if (lane0 == 8) *(volatile LAS u32*)(smem + L_VOTE) = gd >> 1;
      }
    }
  }

#pragma unroll 1
  for (int half = 0;; ++half) {
    {
      float pend, pmax;
      PVAddr pv;
      QKAddr qa;
      const int lane = fresh(lane0);
      const u32 off[1] = {ar + A_GQ + hsub * 16384 + (wave < 6 ? (lane >> 3) * 896 + wave * 128 + (lane & 7) * 16 : (lane & 7) * 896 + 768)};
      f32x4 qv[1];
      asm volatile("" : "=v"(qv[0]));
      if (half == 1) anc = lds_wait_nz((volatile LAS u32*)(smem + L_ANC));
      if (half == 1 && wave < 7) {
        wait_rt(anc + A_Q);
        POLL16(qv[0], off[0], 0);
      }
      int qw;
      att_setup(qa, pv, smem, lane, wave, qw);
      if (half == 0) {
        lds_wait(cnt, 6u);
      } else {
        if (wave < 7) {
          asm volatile("s_waitcnt vmcnt(0)" : "+v"(qv[0])::"memory");
          if (__builtin_expect(__builtin_amdgcn_ballot_w64((and4(__builtin_bit_cast(u32x4, qv[0])) & 1u) == 0u) != 0ull, 0))
            gpoll4<1>(rq, off, qv);
          if (wave < 6) {
            const float t = tsum8(qv[0]);
            *reinterpret_cast<u16*>(smem + qw) = (u16)cvt2(t, t);
          } else {
            const LnS ls = ln_stats(qv[0][3], qv[0][0] + qv[0][1] + qv[0][2]);
            *reinterpret_cast<float*>(smem + L_RI) = ls.ri * SCL;
          }
        }
        if (wave < 4) __builtin_amdgcn_s_setprio(2);
        bar();
      }
      QK_SOFTMAX(qa)
      if (half == 0) {
        const int lv_ = fresh(lane0);
        const u32 vo = (lv_ >> 3) * 384 + 16 * (lv_ & 7);
        if (wave < 4) {
          VLOADS(XA, XB)
        }
        VLOADS(XB, 24)
      }
#define DA(CTL) "v_add_f32_dpp %[l], %[l], %[l] " CTL " row_mask:0xf bank_mask:0xf\n\t"
#define PX(o, v, a) SM "%[o" #o "], %[a" #a "], v[160+24*" #a "+8*" #o ":167+24*" #a "+8*" #o "], %[ix]\n\t"
      asm volatile("s_waitcnt vmcnt(6) lgkmcnt(3)\n\t" PX(0, x, 0) DA("quad_perm:[1,0,3,2]") PX(1, y, 0) PX(2, z, 0)
                   "s_waitcnt lgkmcnt(2)\n\t" PX(0, x, 1) DA("quad_perm:[2,3,0,1]") PX(1, y, 1) PX(2, z, 1)
                   "s_waitcnt lgkmcnt(0)\n\t" PX(0, x, 2) DA("row_half_mirror") PX(1, y, 2) PX(2, z, 2)
                   : [o0] "+v"(pv.O0), [o1] "+v"(pv.O1), [o2] "+v"(pv.O2), [a0] "+v"(pv.A0), [a1] "+v"(pv.A1),
                     [a2] "+v"(pv.A2), [a3] "+v"(pv.A3), [l] "+v"(pend)
                   : [ix] "v"(pv.idx), "{v[160:191]}"(kv3), "{v[192:223]}"(kv4), "{v[224:255]}"(kv5)
                   : "memory");
      static_assert(5 - NINF == 6 - NINO, "same number of pieces behind V");
      if (half == 0) {
        if (!(XNO & 4)) {
          const int ln = fresh(lane0);
          if (wave < 6) {
            if (ln < 8) store16(rs, WS_GQ1 + (u32)(h * 8 + c) * 2048 + 1024 + wave * 128 + ln * 16, afrag(smem, L_CS + wave * 128 + ln * 16));
          } else {
            const int gc = (g + CLRX) & 7, p2 = (isO ? j : j - 8) * 2 + wave - 6;
            if (isO ? j < 12 : (p2 < 40 || (gc & 3) == 0)) {
              const u32 ca = gy_rep(gc) + (u32)(4 + (p2 >> 2)) * 16384u + (u32)(p2 & 3) * 2048u + ln * 16;
              const u32x4 z = __builtin_bit_cast(u32x4, zero4());
              store16(rs, ca, z);
              store16(rs, ca + 1024, z);
            }
          }
        }
        if (!isO) tnext += TFD;
#pragma unroll 1
        for (int p = NINO; p < NINO + NPRE; ++p) {
          pace(tnext, isO ? TPV : TPF);
          dma_piece(ws0, isO ? D : F, smem, L_WB, isO ? wave * 6 + p : wave * 5 + p - 1, fresh(lane0));
        }
      }
      asm volatile("s_waitcnt vmcnt(%[np])\n\t" PX(0, x, 3) DA("row_ror:8") PX(1, y, 3) PX(2, z, 3) "s_setprio 0"
                   : [o0] "+v"(pv.O0), [o1] "+v"(pv.O1), [o2] "+v"(pv.O2), [a3] "+v"(pv.A3), [l] "+v"(pend)
                   : [ix] "v"(pv.idx), [np] "n"(NPRE), "{v[224:255]}"(kv5)
                   : "memory");
      if (half == 0 && NINO + NPRE < 6) {
#pragma unroll 1
        for (int p = NINO + NPRE; p < 6; ++p)
          dma_piece(ws0, isO ? D : F, smem, L_WB, isO ? wave * 6 + p : wave * 5 + p - 1, fresh(lane0));
      }
      asm volatile("ds_write2_b32 %[la], %[l], %[mx] offset1:4\n\ts_mov_b64 exec, %[m]\n\ts_nop 2\n\t"
                   "ds_write_b128 %[a], %[o0]\n\tds_write_b128 %[a], %[o1] offset:256\n\t"
                   "ds_write_b128 %[a], %[o2] offset:512\n\t"
                   "s_mov_b64 exec, -1\n\ts_waitcnt lgkmcnt(0)\n\ts_barrier"
                   :
                   : [m] "s"(0xff00ff0000ff00ffull), [a] "v"(pv.oa + smb), [o0] "v"(pv.O0), [o1] "v"(pv.O1), [o2] "v"(pv.O2),
                     [la] "v"(pv.la + smb), [l] "v"(pend), [mx] "v"(pmax)
                   : "memory");
      tref = now();
      if (half == 0) ar = (u32)(g & 3) * AREA + (((u32)(g >> 2) ^ __builtin_amdgcn_readfirstlane(*(volatile LAS u32*)(smem + L_VOTE))) & 1u) * SIDE;
      if (wave < 4) {
        const float* mp = reinterpret_cast<const float*>(smem + pv.ma);
        float m0 = mp[0], m1 = mp[4 * OM_S];
        f32x4 v = *reinterpret_cast<const f32x4*>(smem + pv.sa), v1 = *reinterpret_cast<const f32x4*>(smem + pv.sa + OM_P * 4);
        f32x4 u = *reinterpret_cast<const f32x4*>(smem + pv.sa + OM_S * 16);
        f32x4 u1 = *reinterpret_cast<const f32x4*>(smem + pv.sa + OM_S * 16 + OM_P * 4);
        asm volatile("" : "+v"(m0), "+v"(m1), "+v"(v), "+v"(v1), "+v"(u), "+v"(u1));
        v += v1;
        u += u1;
        float M = 0.f;
        if (__builtin_expect(__builtin_amdgcn_ballot_w64((__float_as_uint(m0) | __float_as_uint(m1)) != 0u) != 0ull, 0)) {
          M = fmx(m0, m1);
          M = fmx(M, dpp<XOR1>(M));
          M = fmx(M, dpp<XOR2>(M));
          v *= __builtin_amdgcn_exp2f(m0 - M);
          u *= __builtin_amdgcn_exp2f(m1 - M);
        }
        v = sum4x4(v + u);
        u32 p0 = cvt2(v[0], v[1]), p1 = cvt2(v[2], v[3]);
        if (wave == 3) {
          p0 = __float_as_uint(M);
          p1 = __float_as_uint(sum4(v));
        }
        p0 |= 1u;
        p1 |= 1u;
        const u32x4 pk = {p0, p1, dppu<XOR7>(p0), dppu<XOR7>(p1)};
        asm volatile("s_mov_b64 exec, %2\n\tbuffer_store_dwordx4 %0, %1, %3, %4 offen sc1\n\ts_mov_b64 exec, -1"
                     :
                     : "v"(pk), "v"(pv.dst), "s"(wave < 3 ? 0x0101010101010101ull : 0x3ull), "s"(rq), "s"(ar + (u32)half * GO_SZ)
                     : "memory");
      }
    }
    const u32 go = ar + A_GO + (u32)half * GO_SZ;
    const u32 gyw = A_GY + (u32)half * GY_PAIR;
    const u32 gy = gyw + ar;
    const u32 gh = ar + A_GH + (u32)half * 1536u;
    const int land = L_LAND + wave * 2048;
    const u32 landa = (u32)(uintptr_t)(LAS unsigned char*)smem + (u32)land;
    if (isO) {
      {
        const int lane = fresh(lane0);
        const u32 voff = fresh(go + go_slot(wave) + ((lane * 16) ^ ((lane >> 4) * 64)));
        if (half)
          wait_rt(anc + A_O);
        else
          wait_until(tref + 24 * T_O);
        dma_poll<false>(rq, voff, landa, ~0ull, ~0ull);
        const int n = lane & 15, G = lane >> 4;
        const int r0_ = land + (n & 7) * 256 + (n & 3) * 64;
        const int ra = fresh(n < 8 ? r0_ + G * 16 : r0_ ^ 192), rb = fresh((r0_ + G * 16) ^ 64), rc = fresh((r0_ + G * 16) ^ 128);
        const int l0 = fresh(L_WB + wave * 6144 + wlane(lane, 0)), l1 = fresh(L_WB + wave * 6144 + wlane(lane, 1));
        asm volatile("s_waitcnt vmcnt(2)" ::: "memory");
        u32x4 wf[6];
#pragma unroll
        for (int k = 0; k < 3; ++k) {
          wf[2 * k] = wfrag(smem, l0, 2048 * k);
          wf[2 * k + 1] = wfrag(smem, l1, 2048 * k);
        }
        const int rw = fresh(L_RED + wave * 144 + lane * 4);
        const int rr = fresh(L_RED + (lane & 3) * 144 + ((lane >> 2) & 7) * 16);
        const u32 dst = fresh(gyw + gy_rep((lane & 3) + (lane >> 5) * 4) + gy_chunk(4 * j + ((lane >> 3) & 3)) + g * 256);
        f32x4 d0, d1;
        float mc, lc, t1_;
        u32 budget = SPIN_CAP;
#pragma unroll 1
        for (;;) {
          u64 need;
          u32 t;
          asm volatile("s_waitcnt vmcnt(0)\n\t"
                       "ds_read_b128 " F0 ", %[ra]\n\tds_read_b128 " F1 ", %[rb]\n\tds_read_b128 " F2 ", %[rc]\n\t"
                       "s_waitcnt lgkmcnt(2)\n\t"
                       MF "%[d0], " F0 ", %[w0], 0\n\t"
                       "v_add_f32_dpp %[l], v53, v53 quad_perm:[1,0,3,2] row_mask:0xf bank_mask:0xf\n\t"
                       MF "%[d1], " F0 ", %[w1], 0\n\t" AND3("v52", "v53", "v54")
                       "s_waitcnt lgkmcnt(1)\n\t"
                       MF "%[d0], " F1 ", %[w2], %[d0]\n\t"
                       "v_add_f32_dpp %[l], %[l], %[l] quad_perm:[2,3,0,1] row_mask:0xf bank_mask:0xf\n\t" AND3("%[t]", "v55", "v56")
                       MF "%[d1], " F1 ", %[w3], %[d1]\n\t" AND3("%[t]", "v57", "v58")
                       "v_add_f32_dpp %[l], %[l], %[l] row_half_mirror row_mask:0xf bank_mask:0xf\n\t"
                       "s_waitcnt lgkmcnt(0)\n\t"
                       MF "%[d0], " F2 ", %[w4], %[d0]\n\t" AND3("%[t]", "v59", "v60") AND3("%[t]", "v61", "v62")
                       MF "%[d1], " F2 ", %[w5], %[d1]\n\t" AND3("%[t]", "1", "v63")
                       "v_rcp_f32 %[l], %[l]\n\tv_cmp_eq_u32 %[need], 0, %[t]"
                       : [d0] "=&v"(d0), [d1] "=&v"(d1), [t] "=&v"(t), [need] "=&s"(need), [l] "=&v"(lc), "=&{v52}"(mc), "=&{v53}"(t1_)
                       : [ra] "v"(ra), [rb] "v"(rb), [rc] "v"(rc), [w0] "v"(wf[0]), [w1] "v"(wf[1]), [w2] "v"(wf[2]), [w3] "v"(wf[3]),
                         [w4] "v"(wf[4]), [w5] "v"(wf[5])
                       : "memory", "v54", "v55", "v56", "v57", "v58", "v59", "v60", "v61", "v62", "v63");
          if (__builtin_expect(need == 0ull || budget == 0u, 1)) break;
          --budget;
          u64 ea, eb;
          retry_masks<16>(need, lane, ea, eb);
          dma_poll<false>(rq, voff, landa, ea, eb);
        }
        float rl = __uint_as_float(__builtin_amdgcn_readlane(__float_as_uint(lc), 8));
        asm volatile("s_nop 1" : "+v"(d0), "+v"(d1));
        if (__builtin_expect((__builtin_amdgcn_ballot_w64(__float_as_uint(mc) != 1u) & 0xff00ull) != 0ull, 0)) {
          const float fc = __builtin_amdgcn_exp2f(mc - max8(mc));
          const u32 sc = __float_as_uint(fc * __builtin_amdgcn_rcpf(sum8(fc * t1_)));
#pragma unroll
          for (int e = 0; e < 4; ++e) {
            const float se = __uint_as_float(G == 0 ? __builtin_amdgcn_readlane(sc, 8 + e) : __builtin_amdgcn_readlane(sc, 12 + e));
            d0[e] *= se;
            d1[e] *= se;
          }
          rl = 1.0f;
        }
        {
          auto sw = __builtin_amdgcn_permlane16_swap(__float_as_uint(sum4(d0)), __float_as_uint(sum4(d1)), false, false);
          const float r = (__uint_as_float(sw[0]) + __uint_as_float(sw[1])) * rl;
          if (lane < 32) *reinterpret_cast<float*>(smem + rw) = r;
        }
        bar();
        if (wave == 0) {
          const f32x4 y = sum4x4(*reinterpret_cast<const f32x4*>(smem + rr) + *reinterpret_cast<const f32x4*>(smem + rr + 576));
          const u32 p0 = cvt2(y[0], y[1]) | 1u, p1 = cvt2(y[2], y[3]) | 1u;
          const u32x4 pk = {p0, p1, dppu<XOR7>(p0), dppu<XOR7>(p1)};
          asm volatile("s_mov_b64 exec, %2\n\tbuffer_store_dwordx4 %0, %1, %3, 0 offen sc1\n\ts_mov_b64 exec, -1"
                       :
                       : "v"(pk), "v"(dst), "s"(0x0f0f0f0f0f0f0f0full), "s"(rq)
                       : "memory");
        }
      }
      tref = now();
      if (half == 0) {
        if (wave >= 1 && wave < 7) {
          const u16* s0 = karg<56>() + (size_t)(192 * g) * D + 32 * j;
#pragma unroll
          for (int p = 0; p < 2; ++p) dma_piece(s0, D, smem, L_W2, (wave - 1) * 2 + p, fresh(lane0));
          asm volatile("s_waitcnt vmcnt(0)" ::: "memory");
        }
        bar();
        if (wave < 6) cs_gather(smem, rq, fresh(lane0), wave, h);
        if (wave == 4) {
          wait_until(tref + 24 * T_H);
          stamp_poll(smem, rq, ar + st_line(fresh(lane0) & 7));
        }
        if (wave == 5) {
          wait_until(tref + 24 * T_LAZY);
          x_update<false>(smem, rq, fresh(lane0), gy + gy_row(c), c);
        }
      } else if (g == 0 && wave == 2) {
        const int lane = fresh(lane0);
        const int col = j * 32 + (lane & 7) * 4 + ((lane >> 4) & 1) * 2 + (lane >> 5);
        u32 x0v, dummy;
        u16* const po = karg<64>() + col;
        asm volatile("global_load_ushort %0, %2, off\n\tglobal_load_ushort %1, %3, off\n\ts_waitcnt vmcnt(0)"
                     : "=&v"(x0v), "=&v"(dummy)
                     : "v"(karg<0>() + col), "v"(po)
                     : "memory");
        wait_until(tref + 24 * T_LAZY);
        const u32 o = gy + gy_chunk(4 * j + ((lane >> 1) & 3)) + (lane >> 3) * 256 + (lane & 1) * 8;
        const u32 off[3] = {o - GY_PAIR, o - GY_PAIR + GY_INST, o};
        float y[3];
        col_poll<3>(rq, off, y);
        const float x3 = bf16r(bf16r(bf16r(bflo(x0v) + y[0]) + y[1]) + y[2]);
        const u32 off2[1] = {o + GY_INST};
        wait_rt(anc + A_F);
        float y4[1];
        col_poll<1>(rq, off2, y4);
        const float x4 = x3 + y4[0];
        if ((lane & 8) == 0) asm volatile("global_store_short %0, %1, off" ::"v"(po), "v"(cvt2(x4, x4)) : "memory");
      }
      if (wave >= 6) {
        const int lane = fresh(lane0), wv = wave - 6;
        const int n = lane & 15, G = lane >> 4;
        const int l0 = fresh(L_W2 + wlane(lane, wv));
        u32x4 wf[6];
#pragma unroll
        for (int k = 0; k < 6; ++k) wf[k] = wfrag(smem, l0, 2048 * k);
        const int l48 = lane < 48 ? lane : 0;
        const u32 off[2] = {gh + (u32)l48 * 16, gh + (u32)l48 * 16 + 768};
        const int aw = fresh(L_ACT + wv * 384 + l48 * 8);
        const int ab = fresh(L_ACT + wv * 384 + 16 * G);
        const u32 dst = fresh(gyw + GY_INST + gy_rep(n & 7) + gy_chunk(4 * j + wv * 2 + (G >> 1)) + g * 256);
        f32x4 v[2];
        if (half)
          wait_rt(anc + A_H);
        else
          wait_until(tref + 24 * T_H);
        gpoll4<2>(rq, off, v);
        {
          f32x4 a, u;
#pragma unroll
          for (int e = 0; e < 4; ++e) {
            a[e] = bfhi(__float_as_uint(v[0][e]));
            u[e] = bfhi(__float_as_uint(v[1][e]));
          }
          const f32x4 hh = a * u;
          *reinterpret_cast<u32x2*>(smem + aw) = u32x2{cvt2(hh[0], hh[1]), cvt2(hh[2], hh[3])};
        }
        u32x4 af[4];
#pragma unroll
        for (int k = 0; k < 4; ++k) af[k] = afrag(smem, ab + 64 * k);
        asm volatile("" : "+v"(af[0]), "+v"(af[1]), "+v"(af[2]), "+v"(af[3]));
        f32x4 d = {0.f, 0.f, 0.f, 0.f};
        d = mfma32(wf[0], af[0], d);
        d = mfma32(wf[1], af[1], d);
        asm volatile("" : "+v"(d)::"memory");
        af[0] = afrag(smem, ab + 256);
        af[1] = afrag(smem, ab + 320);
        d = mfma32(wf[2], af[2], d);
        d = mfma32(wf[3], af[3], d);
        d = mfma32(wf[4], af[0], d);
        d = mfma32(wf[5], af[1], d);
        const u32 p0 = cvt2(d[0], d[1]) | 1u, p1 = cvt2(d[2], d[3]) | 1u;
        auto s0 = __builtin_amdgcn_permlane16_swap(p0, p0, false, false);
        auto s1 = __builtin_amdgcn_permlane16_swap(p1, p1, false, false);
        if ((lane & (half ? 27 : 24)) == 0) store16(rs, dst, u32x4{s0[0], s1[0], s0[1], s1[1]});
      }
    } else {
      const int lane = fresh(lane0);
      const int n = lane & 15, G = lane >> 4;
      if (half == 1) {
        x_update<false>(smem, rq, lane, gy - GY_PAIR + GY_INST + gy_row(wave), wave);
      }
      asm volatile("s_waitcnt vmcnt(0) lgkmcnt(0)" ::: "memory");
      G80 a;
      g80_load(a, smem, lane, L_WB + wave * 5120, L_HB + 160 * wave, land);
      const u32 voff = fresh(gy + gy_row(wave) + ((lane * 16) ^ ((lane >> 4) * 64)));
      const int rw = fresh(L_RED + n * 144 + wave * 16 + G * 4);
      const int sw_ = fresh(n == 0 ? L_ST + wave * 16 + G * 4 : L_ACT);
      float r0, r1, z;
      if (half)
        wait_rt(anc + A_Y);
      else
        wait_until(tref + 24 * T_YF);
      g80_run<true>(a, smem, rq, lane, voff, landa, r0, r1, z);
      *reinterpret_cast<float*>(smem + rw) = r0;
      *reinterpret_cast<float*>(smem + rw + 16 * 144) = r1;
      *reinterpret_cast<float*>(smem + sw_) = z;
      const int ln = fresh(lane0);
      const int rr = fresh(L_RED + (ln & 31) * 144 + (ln >> 5) * 64), rs_ = fresh(L_ST + 16 * (ln & 7));
      const u32 dst = fresh(gh + fslot + ln * 4);
      bar();
      tref = now();
      if (half == 0 && wave == 1 && j == 20 && ln < 8)
        __builtin_amdgcn_raw_buffer_store_b32((rtnow() << 1) | 1u, rs, gy_rep(ln) + st_line(g), 0, 16);
      if (wave == 0) {
        f32x4 st = *reinterpret_cast<const f32x4*>(smem + rs_);
        f32x4 q0 = *reinterpret_cast<const f32x4*>(smem + rr), q1 = *reinterpret_cast<const f32x4*>(smem + rr + 16);
        f32x4 q2 = *reinterpret_cast<const f32x4*>(smem + rr + 32), q3 = *reinterpret_cast<const f32x4*>(smem + rr + 48);
        asm volatile("" : "+v"(st), "+v"(q0), "+v"(q1), "+v"(q2), "+v"(q3));
        const LnS ls = ln_stats(st[3], st[0] + st[1] + st[2]);
        const f32x4 qs = (q0 + q1) + (q2 + q3);
        float r = fmaf(-ls.mb, qs[3], qs[0] + qs[1] + qs[2]);
        auto sw = __builtin_amdgcn_permlane32_swap(__float_as_uint(r), __float_as_uint(r), false, false);
        r = bf16r((__uint_as_float(sw[0]) + __uint_as_float(sw[1])) * ls.ri);
        if ((j & 1) == 0) r = bf16r(r * __builtin_amdgcn_rcpf(1.0f + __builtin_amdgcn_exp2f(r * -1.4426950408889634f)));
        const f32x4 qd = quad4(r);
        if ((ln & 35) == 0) store_quad(rs, dst, qd);
      }
      if (half == 0) {
        x_update<true>(smem, rq, fresh(lane0), land, wave);
        if (wave < 6) cs_gather(smem, rq, fresh(lane0), wave, h);
        if (wave == 7) {
          wait_until(tref + 24 * T_AN);
          stamp_poll(smem, rq, ar + st_line(ln & 7));
        }
      }
    }
    if (half == 1) break;
    {
      if (wave < 6) {
        const int lane = fresh(lane0);
        const int n = lane & 15;
        G80 a;
        g80_load(a, smem, lane, L_WQ + wave * 5120, L_HB + 160 * c, land);
        const u32 voff = fresh(gy + GY_INST + gy_row(c) + ((lane * 16) ^ ((lane >> 4) * 64)));
        const float cc = *reinterpret_cast<const float*>(smem + L_CS + wave * 128 + (lane >> 5) * 64 + n * 4);
        const u32 gq = ar + A_GQ + hsub * 16384 + c * 896;
        const u32 dst = fresh(gq + wave * 128 + (lane >> 5) * 64 + n * 4);
        float r0, r1, z;
        bar();
        wait_rt(__builtin_amdgcn_readfirstlane(*(volatile LAS u32*)(smem + L_ANC)) + A_E);
        g80_run<true>(a, smem, rq, lane, voff, landa, r0, r1, z);
        auto sw = __builtin_amdgcn_permlane32_swap(__float_as_uint(r0), __float_as_uint(r1), false, false);
        const float sh = cc * __uint_as_float(__builtin_amdgcn_readlane(__float_as_uint(z), 48));
        float r = __uint_as_float(sw[0]) +
                  __uint_as_float((u32)__builtin_amdgcn_update_dpp((int)sw[1], (int)__float_as_uint(sh), 0xE4, 0xA, 0xf, false));
        auto sx = __builtin_amdgcn_permlane16_swap(__float_as_uint(r), __float_as_uint(r), false, false);
        r = __uint_as_float(sx[0]) + __uint_as_float(sx[1]);
        const f32x4 qd = quad4(r);
        if ((lane & 19) == 0) store_quad(rs, dst, qd);
        if (wave == 5) {
          const u32 zu = __float_as_uint(z);
          const f32x4 zq = {__uint_as_float(__builtin_amdgcn_readlane(zu, 0)), __uint_as_float(__builtin_amdgcn_readlane(zu, 16)),
                            __uint_as_float(__builtin_amdgcn_readlane(zu, 32)), __uint_as_float(__builtin_amdgcn_readlane(zu, 48))};
          if (lane == 0) store_quad(rs, gq + 768, zq);
        }
      } else {
        bar();
      }
    }
  }
  asm volatile(".Lmk_code_end:");
}

}

extern "C" void kernel_launch(void* const* d_in, const int* in_sizes, int n_in, void* d_out, int out_size, void* d_ws,
                              size_t ws_size, hipStream_t stream) {
  Params P;
  P.x0 = (const u16*)d_in[0];
  P.kc = (const u16*)d_in[1];
  P.vc = (const u16*)d_in[2];
  P.wq = (const u16*)d_in[3];
  P.wo = (const u16*)d_in[4];
  P.w1 = (const u16*)d_in[6];
  P.w3 = (const u16*)d_in[7];
  P.w2 = (const u16*)d_in[8];
  P.out = (u16*)d_out;
  P.ws = (u32*)d_ws;
  mega_kernel<<<NWG, NT, 0, stream>>>(P);
}
```

```cpp
#define E_QK 300
#define PO0 8
#define NINO 4
#define NPRE 2
#define POS 10
#define NINF 1
#define TSTEP 234
#define A_Q (A_E+96)
#include <hip/hip_runtime.h>
#include <hip/hip_bf16.h>
#include <stdint.h>

namespace {

typedef unsigned int u32;
typedef unsigned short u16;
typedef unsigned long long u64;
using u32x2 = __attribute__((ext_vector_type(2))) unsigned;
using u32x4 = __attribute__((ext_vector_type(4))) unsigned;
using f32x2 = __attribute__((ext_vector_type(2))) float;
using f32x4 = __attribute__((ext_vector_type(4))) float;
using s16x4 = __attribute__((ext_vector_type(4))) short;
using bf16x2 = __attribute__((ext_vector_type(2))) __bf16;
using bf16x8 = __attribute__((ext_vector_type(8))) __bf16;
#define LAS __attribute__((address_space(3)))

constexpr int D = 640, H = 32, DH = 192, WIN = 4096, SK = 32768, F = 1536;
constexpr int NWG = 256, NT = 512;
constexpr u32 SPIN_CAP = 1u << 16;
constexpr float SCL = 0.07216878364870322f * 1.4426950408889634f;

#define T_O 43
#define T_YF 122
#define T_H 120
#define T_AN 35
#define PTHR 60
#define T_LAZY 60

#define A_E 122
#define A_O (A_Q + 146)
#define A_Y (A_O + 96)
#define A_H (A_Y + 94)
#define A_F (A_H + 58)

constexpr u32 SIDE = 8192, AREA = 17 * 16384;
constexpr u32 A_GO = 0;
constexpr u32 A_GY = 2 * 16384;
constexpr u32 A_GQ = 6 * 16384;
constexpr u32 A_GH = 16 * 16384;
constexpr u32 GO_SZ = 10 * 16384, GY_PAIR = GO_SZ, GY_INST = 32768;
constexpr u32 WS_PB = 4 * AREA;
constexpr u32 WS_GOF = WS_PB + (128 << 10), WS_HB = WS_GOF + 4096;
constexpr u32 WS_GQ1 = WS_HB + 256 * 128;
constexpr u32 WS_END = WS_GQ1 + 256 * 2048;
static_assert(WS_END <= (2u << 20), "layout");
__device__ __forceinline__ u32 go_slot(int w) { return (u32)(w >> 2) * 16384u + (u32)(w & 3) * 2048u; }
__device__ __forceinline__ u32 gy_row(int i) { return go_slot(i); }
__device__ __forceinline__ u32 gy_chunk(int u) {
  const int i = (u * 205) >> 11;
  return gy_row(i) + (u32)(u - 10 * i) * 16u;
}
__device__ __forceinline__ u32 st_line(int g) { return A_GH + 3072u + (u32)g * 128u; }
__device__ __forceinline__ u32 gy_rep(int r) { return (u32)(r & 3) * AREA + (u32)(r >> 2) * SIDE; }

constexpr int L_ZERO = 0;
constexpr int L_CNT = 16;
constexpr int L_RI = 28;
constexpr int L_ONE = 32;
constexpr int L_TAG = 48;
constexpr int L_ST = 64;
constexpr int L_VOTE = L_ST;
constexpr int L_ANC = 192;
constexpr int L_Q = 256;
constexpr int L_CS = 640;
constexpr int L_HB = L_CS + 768;
constexpr int L_ACT = L_HB + D * 2;
constexpr int L_RED = L_ACT + 2 * 384;
constexpr int L_P = L_RED + 32 * 144;
constexpr int P_W = 1280, P_U = 592, P_PAR = 288;
constexpr int OM_S = 400, OM_P = 200;
constexpr int L_OM = L_P + 8 * P_W;
constexpr int L_LN = L_OM;
constexpr int L_LAND = (L_OM + 8 * OM_S * 4 + 255) / 256 * 256;
constexpr int L_WQ = L_LAND + 8 * 2048;
constexpr int L_WB = L_WQ + 6 * 5120;
constexpr int L_W2 = L_WB + 768 * 64;
constexpr int L_PF = L_W2 + 192 * 64;
constexpr int L_TAB = L_PF + 256;
constexpr int L_TOTAL = L_TAB + 8 * 2048;
static_assert(L_LAND % 256 == 0 && L_P % 16 == 0 && L_WQ % 16 == 0 && L_HB % 16 == 0 && L_RED % 16 == 0, "alignment");

struct Params {
  const u16* x0;
  const u16* kc;
  const u16* vc;
  const u16* wq;
  const u16* wo;
  const u16* w1;
  const u16* w3;
  const u16* w2;
  u16* out;
  u32* ws;
};

__device__ __forceinline__ u32 cvt2(float lo, float hi) {
  return __builtin_bit_cast(u32, __builtin_convertvector(f32x2{lo, hi}, bf16x2));
}
__device__ __forceinline__ float bflo(u32 w) { return __uint_as_float(w << 16); }
__device__ __forceinline__ float bfhi(u32 w) { return __uint_as_float(w & 0xffff0000u); }
__device__ __forceinline__ float bf16r(float x) { return bflo(cvt2(x, x)); }

template <int CTRL>
__device__ __forceinline__ float dpp(float x) {
  return __builtin_bit_cast(float, __builtin_amdgcn_mov_dpp(__builtin_bit_cast(int, x), CTRL, 0xf, 0xf, true));
}
constexpr int XOR1 = 0xB1, XOR2 = 0x4E, XOR7 = 0x141, ROR8 = 0x128;
__device__ __forceinline__ float fmx(float a, float b) {
  float r;
  asm("v_max_f32 %0, %1, %2" : "=v"(r) : "v"(a), "v"(b));
  return r;
}
__device__ __forceinline__ float xrow16_max(float x) {
  auto s = __builtin_amdgcn_permlane16_swap(__float_as_uint(x), __float_as_uint(x), false, false);
  x = fmx(__uint_as_float(s[0]), __uint_as_float(s[1]));
  auto t = __builtin_amdgcn_permlane32_swap(__float_as_uint(x), __float_as_uint(x), false, false);
  return fmx(__uint_as_float(t[0]), __uint_as_float(t[1]));
}
__device__ __forceinline__ float xrow16_sum(float x) {
  auto s = __builtin_amdgcn_permlane16_swap(__float_as_uint(x), __float_as_uint(x), false, false);
  x = __uint_as_float(s[0]) + __uint_as_float(s[1]);
  auto t = __builtin_amdgcn_permlane32_swap(__float_as_uint(x), __float_as_uint(x), false, false);
  return __uint_as_float(t[0]) + __uint_as_float(t[1]);
}
__device__ __forceinline__ float sum8(float v) {
  v += dpp<XOR1>(v);
  v += dpp<XOR2>(v);
  v += dpp<XOR7>(v);
  return v;
}
__device__ __forceinline__ float max8(float v) {
  asm("s_nop 1\n\tv_max_f32_dpp %0, %0, %0 quad_perm:[1,0,3,2] row_mask:0xf bank_mask:0xf\n\t"
      "s_nop 1\n\tv_max_f32_dpp %0, %0, %0 quad_perm:[2,3,0,1] row_mask:0xf bank_mask:0xf\n\t"
      "s_nop 1\n\tv_max_f32_dpp %0, %0, %0 row_half_mirror row_mask:0xf bank_mask:0xf"
      : "+v"(v));
  return v;
}
__device__ __forceinline__ float max_ror8(float v) {
  asm("s_nop 1\n\tv_max_f32_dpp %0, %0, %0 row_ror:8 row_mask:0xf bank_mask:0xf" : "+v"(v));
  return v;
}
__device__ __forceinline__ f32x4 sum4x4(f32x4 v) {
  float a = v[0], b = v[1], c = v[2], d = v[3];
#define S4(CTL)                                               \
  "v_add_f32_dpp %0, %0, %0 " CTL " row_mask:0xf bank_mask:0xf\n\t" \
  "v_add_f32_dpp %1, %1, %1 " CTL " row_mask:0xf bank_mask:0xf\n\t" \
  "v_add_f32_dpp %2, %2, %2 " CTL " row_mask:0xf bank_mask:0xf\n\t" \
  "v_add_f32_dpp %3, %3, %3 " CTL " row_mask:0xf bank_mask:0xf\n\t"
  asm("s_nop 1\n\t" S4("quad_perm:[1,0,3,2]") S4("quad_perm:[2,3,0,1]") : "+v"(a), "+v"(b), "+v"(c), "+v"(d));
#undef S4
  return f32x4{a, b, c, d};
}
__device__ __forceinline__ float wave_sum(float v) {
  v = sum8(v);
  v += dpp<ROR8>(v);
  return xrow16_sum(v);
}
__device__ __forceinline__ float tsum8(f32x4 a) {
  auto s0 = __builtin_amdgcn_permlane32_swap(__float_as_uint(a[0]), __float_as_uint(a[1]), false, false);
  auto s1 = __builtin_amdgcn_permlane32_swap(__float_as_uint(a[2]), __float_as_uint(a[3]), false, false);
  const float r0 = __uint_as_float(s0[0]) + __uint_as_float(s0[1]);
  const float r1 = __uint_as_float(s1[0]) + __uint_as_float(s1[1]);
  auto t = __builtin_amdgcn_permlane16_swap(__float_as_uint(r0), __float_as_uint(r1), false, false);
  float r = __uint_as_float(t[0]) + __uint_as_float(t[1]);
  r += dpp<ROR8>(r);
  return r;
}

__device__ __forceinline__ u32 tagv(float v) { return __float_as_uint(v) | 1u; }
__device__ __forceinline__ void store_quad(__amdgpu_buffer_rsrc_t rs, u32 off, f32x4 v) {
  const u32x4 s = {tagv(v[0]), tagv(v[1]), tagv(v[2]), tagv(v[3])};
  __builtin_amdgcn_raw_buffer_store_b128(s, rs, off, 0, 16);
}
template <int N>
__device__ __forceinline__ void gpoll4(u32x4 rs, const u32 (&off)[N], f32x4 (&v)[N]) {
  bool need = true;
  u32 budget = SPIN_CAP;
#pragma unroll
  for (int k = 0; k < N; ++k) asm volatile("" : "=v"(v[k]));
#pragma unroll 1
  for (;;) {
    if (need) {
#pragma unroll
      for (int k = 0; k < N; ++k)
        asm volatile("buffer_load_dwordx4 %0, %1, %2, 0 offen sc1" : "+v"(v[k]) : "v"(off[k]), "s"(rs) : "memory");
    }
    asm volatile("s_waitcnt vmcnt(0)" ::: "memory");
    u32 a = 1u;
#pragma unroll
    for (int k = 0; k < N; ++k) {
      asm volatile("" : "+v"(v[k]));
      a &= __float_as_uint(v[k][0]) & __float_as_uint(v[k][1]) & __float_as_uint(v[k][2]) & __float_as_uint(v[k][3]);
    }
    need = a == 0u;
    if (__builtin_amdgcn_ballot_w64(need) == 0ull || budget == 0u) break;
    --budget;
    __builtin_amdgcn_s_sleep(1);
  }
}
__device__ __forceinline__ u32 now() { return (u32)__builtin_amdgcn_s_memtime(); }
__device__ __forceinline__ void wait_until(u32 t) {
#pragma unroll 1
  while ((int)(now() - t) < -160) __builtin_amdgcn_s_sleep(1);
#pragma unroll 1
  while ((int)(now() - t) < 0) {
  }
}
__device__ __forceinline__ u32 rtnow() { return (u32)__builtin_amdgcn_s_memrealtime(); }
__device__ __forceinline__ void wait_rt(u32 t) {
  u32 spins = 0;
#pragma unroll 1
  while ((int)(rtnow() - t) < -8 && ++spins < 400u) __builtin_amdgcn_s_sleep(1);
#pragma unroll 1
  while ((int)(rtnow() - t) < 0 && ++spins < 440u) {
  }
}
template <int BITS>
__device__ __forceinline__ u32 youngest(u32 t);
__device__ __forceinline__ void lds_wait(volatile LAS u32* p, u32 want) {
  u32 spins = 0;
#pragma unroll 1
  while (__builtin_amdgcn_readfirstlane(*p) != want) {
    if (++spins > (SPIN_CAP << 6)) break;
    __builtin_amdgcn_s_sleep(1);
  }
  asm volatile("" ::: "memory");
}
__device__ __forceinline__ u32 lds_wait_nz(volatile LAS u32* p) {
  u32 spins = 0, v;
#pragma unroll 1
  while ((v = __builtin_amdgcn_readfirstlane(*p)) == 0u) {
    if (++spins > (SPIN_CAP << 6)) break;
    __builtin_amdgcn_s_sleep(1);
  }
  asm volatile("" ::: "memory");
  return v;
}
template <int BITS>
__device__ __forceinline__ u32 youngest(u32 t) {
  const u32 nw = rtnow();
  const float age = (float)((nw - t) & ((1u << BITS) - 1u));
  return nw - (u32)__builtin_amdgcn_readfirstlane((int)-xrow16_max(max_ror8(max8(-age))));
}
__device__ __forceinline__ void stamp_poll(unsigned char* smem, u32x4 rq, u32 so) {
  u32 sv, budget = SPIN_CAP;
#pragma unroll 1
  for (;;) {
    asm volatile("buffer_load_dword %0, %1, %2, 0 offen sc1\n\ts_waitcnt vmcnt(0)" : "=&v"(sv) : "v"(so), "s"(rq) : "memory");
    if (__builtin_amdgcn_ballot_w64((sv & 1u) == 0u) == 0ull || budget == 0u) break;
    --budget;
    __builtin_amdgcn_s_sleep(1);
  }
  *(volatile LAS u32*)(smem + L_ANC) = youngest<24>(sv >> 1) | 1u;
}
__device__ __forceinline__ void lds_add(unsigned char* smem, int off, u32 v) {
  __hip_atomic_fetch_add((LAS u32*)(smem + off), v, __ATOMIC_RELAXED, __HIP_MEMORY_SCOPE_WORKGROUP);
}

__device__ __forceinline__ int fresh(int t) {
  asm volatile("" : "+v"(t));
  return t;
}
__device__ __forceinline__ f32x4 zero4() {
  f32x2 a, b;
  asm volatile("v_mov_b64 %0, 0\n\tv_mov_b64 %1, 0" : "=v"(a), "=v"(b));
  return f32x4{a[0], a[1], b[0], b[1]};
}
__device__ __forceinline__ void bar() { asm volatile("s_waitcnt lgkmcnt(0)\n\ts_barrier" ::: "memory"); }

__device__ __forceinline__ u32x2 trread(unsigned char* smem, int off) {
  return __builtin_bit_cast(u32x2, __builtin_amdgcn_ds_read_tr16_b64_v4i16((LAS s16x4*)(smem + off)));
}
__device__ __forceinline__ u32x4 wfrag(unsigned char* smem, int lb, int off) {
  const u32x2 a = trread(smem, lb + off), b = trread(smem, lb + off + 256);
  return u32x4{a[0], a[1], b[0], b[1]};
}
template <int GS = 512>
__device__ __forceinline__ int wlane(int lane, int cb) {
  const int i = lane & 15, G = lane >> 4;
  return G * GS + (i >> 2) * 64 + (i & 3) * 8 + ((cb ^ (G & 1)) * 32);
}
__device__ __forceinline__ u32x4 afrag(unsigned char* smem, int a) { return *reinterpret_cast<const u32x4*>(smem + a); }
__device__ __forceinline__ f32x4 mfma32(u32x4 w, u32x4 a, f32x4 c) {
  return __builtin_amdgcn_mfma_f32_16x16x32_bf16(__builtin_bit_cast(bf16x8, w), __builtin_bit_cast(bf16x8, a), c, 0, 0, 0);
}

__device__ __forceinline__ void dma_kib(const u16* src, u32 voff, unsigned char* smem, int ldsoff) {
  const u32 dst = (u32)(uintptr_t)(LAS unsigned char*)smem + (u32)ldsoff;
  u32 keep;
  asm volatile("s_mov_b32 %0, m0\n\ts_mov_b32 m0, %2\n\ts_nop 2\n\tglobal_load_lds_dwordx4 %1, %3 nt\n\ts_mov_b32 m0, %0"
               : "=&s"(keep)
               : "v"(voff), "s"(dst), "s"(src)
               : "memory");
}
__device__ __forceinline__ void dma_piece(const u16* src, int ld, unsigned char* smem, int ldsoff, int p, int lane) {
  const int row = 16 * p + (lane >> 2), part = lane & 3;
  const int cb = (part >> 1) ^ (lane >> 5);
  dma_kib(src, (u32)(row * ld + cb * 16 + (part & 1) * 8) * 2u, smem, ldsoff + p * 1024);
}

template <int CTRL>
__device__ __forceinline__ u32 dppu(u32 x) {
  return (u32)__builtin_amdgcn_mov_dpp((int)x, CTRL, 0xf, 0xf, true);
}
__device__ __forceinline__ float sum4(f32x4 d) { return (d[0] + d[1]) + (d[2] + d[3]); }
__device__ __forceinline__ u32 and4(u32x4 v) { return v[0] & v[1] & v[2] & v[3]; }
__device__ __forceinline__ void store16(__amdgpu_buffer_rsrc_t rs, u32 off, u32x4 v) {
  __builtin_amdgcn_raw_buffer_store_b128(v, rs, off, 0, 16);
}
#define POLL16(dst, voff, imm) \
  asm volatile("buffer_load_dwordx4 %0, %1, %2, 0 offen offset:%3 sc1" : "+v"(dst) : "v"(voff), "s"(rq), "n"(imm) : "memory")

struct LnS {
  float mb, ri;
};
__device__ __forceinline__ LnS ln_stats(float s1, float s2) {
#define S2(CTL)                                                     \
  "v_add_f32_dpp %0, %0, %0 " CTL " row_mask:0xf bank_mask:0xf\n\t" \
  "v_add_f32_dpp %1, %1, %1 " CTL " row_mask:0xf bank_mask:0xf\n\ts_nop 0\n\t"
  asm("s_nop 1\n\t" S2("quad_perm:[1,0,3,2]") S2("quad_perm:[2,3,0,1]") S2("row_half_mirror") : "+v"(s1), "+v"(s2));
#undef S2
  const float dm = s1 * (1.0f / D);
  const u32 mv = cvt2(dm, __builtin_fmaxf(fmaf(-dm, dm, s2 * (1.0f / D)), 0.f));
  LnS r;
  r.mb = bflo(mv);
  r.ri = __builtin_amdgcn_rcpf(bf16r(__builtin_amdgcn_sqrtf(bf16r(bfhi(mv) + 1e-5f))));
  return r;
}

struct G80 {
  u32x4 w[6];
  int ra, rb, rc;
};
template <int GS>
__device__ __forceinline__ void g80_load(G80& a, unsigned char* smem, int lane, int wb, int xs, int land) {
  const int m = lane & 15, G = lane >> 4;
  const int l0 = wb + wlane<GS>(lane, 0), l1 = wb + wlane<GS>(lane, 1), bk = 8 * GS - (G >> 1) * 2 * GS;
  a.w[0] = wfrag(smem, l0, 0);
  a.w[1] = wfrag(smem, l1, 0);
  a.w[2] = wfrag(smem, l0, 4 * GS);
  a.w[3] = wfrag(smem, l1, 4 * GS);
  a.w[4] = wfrag(smem, l0 + bk, 0);
  a.w[5] = wfrag(smem, l1 + bk, 0);
  const bool vec = m == 8 || (m < 8 && land >= 0);
  const int st = vec ? 64 : 0;
  const int sa = vec ? (m == 8 ? xs : land + 256 * m) + 16 * G : (m == 15 ? L_ONE : L_ZERO);
  const int sw = (vec && m < 8) ? (m & 3) * 64 : 0;
  a.ra = fresh(sa ^ sw);
  a.rb = fresh((sa + st) ^ sw);
  a.rc = fresh(G < 2 ? (sa + 2 * st) ^ sw : L_TAG);
}
template <bool WAIT = true>
__device__ __forceinline__ void dma_poll(u32x4 rq, u32 voff, u32 land, u64 ea, u64 eb) {
  u32 keep;
  asm volatile("s_mov_b32 %0, m0\n\ts_mov_b32 m0, %3\n\ts_mov_b64 exec, %4\n\t"
               "buffer_load_dwordx4 %1, %2, 0 offen sc1 lds\n\t"
               "s_mov_b64 exec, %5\n\t"
               "buffer_load_dwordx4 %1, %2, 0 offen offset:1024 sc1 lds\n\t"
               "s_mov_b64 exec, -1\n\ts_mov_b32 m0, %0"
               : "=&s"(keep)
               : "v"(voff), "s"(rq), "s"(land), "s"(ea), "s"(eb)
               : "memory");
  if (WAIT) asm volatile("s_waitcnt vmcnt(0)" ::: "memory");
}
template <bool X4>
__device__ __forceinline__ void ctl_load(u32x4 rq, u32 voff, u32 lds) {
  u32 keep;
  if (X4)
    asm volatile("s_mov_b32 %0, m0\n\ts_mov_b32 m0, %3\n\ts_nop 2\n\tbuffer_load_dwordx4 %1, %2, 0 offen sc1 lds\n\ts_mov_b32 m0, %0"
                 : "=&s"(keep) : "v"(voff), "s"(rq), "s"(lds) : "memory");
  else
    asm volatile("s_mov_b32 %0, m0\n\ts_mov_b32 m0, %3\n\ts_nop 2\n\tbuffer_load_dword %1, %2, 0 offen sc1 lds\n\ts_mov_b32 m0, %0"
                 : "=&s"(keep) : "v"(voff), "s"(rq), "s"(lds) : "memory");
}
template <int UN>
__device__ __forceinline__ void retry_masks(u64 need, int lane, u64& ea, u64& eb) {
  u32 m = (u32)need | (u32)(need >> 32);
  m |= m >> 16;
  m = (m | (m >> 8)) & 0xffu;
  const int ln = fresh(lane);
  ea = __builtin_amdgcn_ballot_w64(((m >> (ln / UN)) & 1u) != 0u);
  eb = __builtin_amdgcn_ballot_w64(((m >> ((ln + 64) / UN)) & 1u) != 0u);
}
#define MF "v_mfma_f32_16x16x32_bf16 "
#define F0 "v[52:55]"
#define F1 "v[56:59]"
#define F2 "v[60:63]"
#define FCLOB "v52", "v53", "v54", "v55", "v56", "v57", "v58", "v59", "v60", "v61", "v62", "v63"
#define AND3(a, b, c) "v_bitop3_b32 %[t], " a ", " b ", " c " bitop3:0x80\n\t"
template <bool POLL>
__device__ __forceinline__ void g80_run(G80& a, unsigned char* smem, u32x4 rq, int lane, u32 voff, u32 land, float& r0,
                                        float& r1, float& z) {
  f32x4 d0, d1;
  if (POLL) {
    f32x4 d2;
    constexpr u64 USED = 0xff30ff0330ff03ffull;
    u64 ea = USED, eb = USED;
    u32 budget = SPIN_CAP;
#pragma unroll 1
    for (;;) {
      dma_poll(rq, voff, land, ea, eb);
      u64 need;
      u32 t;
      asm volatile("ds_read_b128 " F0 ", %[ra]\n\tds_read_b128 " F1 ", %[rb]\n\tds_read_b128 " F2 ", %[rc]\n\t"
                   "s_waitcnt lgkmcnt(2)\n\t"
                   MF "%[d2], " F0 ", " F0 ", 0\n\t" AND3("v52", "v53", "v54")
                   MF "%[d0], " F0 ", %[w0], 0\n\t" MF "%[d1], " F0 ", %[w1], 0\n\t"
                   "s_waitcnt lgkmcnt(1)\n\t"
                   MF "%[d2], " F1 ", " F1 ", %[d2]\n\t" AND3("%[t]", "v55", "v56")
                   MF "%[d0], " F1 ", %[w2], %[d0]\n\t" AND3("%[t]", "v57", "v58")
                   MF "%[d1], " F1 ", %[w3], %[d1]\n\t"
                   "s_waitcnt lgkmcnt(0)\n\t"
                   MF "%[d2], " F2 ", " F2 ", %[d2]\n\t" AND3("%[t]", "v59", "v60") AND3("%[t]", "v61", "v62")
                   MF "%[d0], " F2 ", %[w4], %[d0]\n\t" AND3("%[t]", "1", "v63")
                   "v_cmp_eq_u32 %[need], 0, %[t]\n\t"
                   MF "%[d1], " F2 ", %[w5], %[d1]"
                   : [d0] "=&v"(d0), [d1] "=&v"(d1), [d2] "=&v"(d2), [t] "=&v"(t), [need] "=&s"(need)
                   : [ra] "v"(a.ra), [rb] "v"(a.rb), [rc] "v"(a.rc), [w0] "v"(a.w[0]), [w1] "v"(a.w[1]), [w2] "v"(a.w[2]),
                     [w3] "v"(a.w[3]), [w4] "v"(a.w[4]), [w5] "v"(a.w[5])
                   : "memory", FCLOB);
      need &= 0x00ff00ff00ff00ffull;
      if (need == 0ull || budget == 0u) break;
      --budget;
      retry_masks<16>(need, lane, ea, eb);
      ea &= USED;
      eb &= USED;
    }
    asm volatile("s_nop 1" : "+v"(d2));
    z = sum4(d2);
    asm volatile("s_nop 1" : "+v"(z), "+v"(d0), "+v"(d1));
    z = (lane & 15) == 15 ? 0.f : z;
    z = sum8(z);
    z += dpp<ROR8>(z);
  } else {
    const u32x4 f0 = afrag(smem, a.ra), f1 = afrag(smem, a.rb), f2 = afrag(smem, a.rc);
    d0 = d1 = f32x4{0.f, 0.f, 0.f, 0.f};
    d0 = mfma32(f0, a.w[0], d0);
    d1 = mfma32(f0, a.w[1], d1);
    d0 = mfma32(f1, a.w[2], d0);
    d1 = mfma32(f1, a.w[3], d1);
    d0 = mfma32(f2, a.w[4], d0);
    d1 = mfma32(f2, a.w[5], d1);
  }
  r0 = sum4(d0);
  r1 = sum4(d1);
}
__device__ __forceinline__ f32x4 quad4(float r) { return f32x4{r, dpp<0x55>(r), dpp<0xAA>(r), dpp<0xFF>(r)}; }

__device__ __forceinline__ int q_addr(int d) {
  const int w = d >> 1;
  return L_Q + ((w & 1) * 48 + (w >> 1)) * 4 + (d & 1) * 2;
}

__device__ __forceinline__ void cs_gather(unsigned char* smem, u32x4 rq, int lane, int wave, int h) {
  f32x4 v;
  asm volatile("buffer_load_dwordx4 %0, %1, %2, 0 offen offset:1024 sc1\n\ts_waitcnt vmcnt(0)"
               : "=&v"(v)
               : "v"(WS_GQ1 + (u32)(h * 8 + (lane >> 3)) * 2048 + wave * 128 + (lane & 7) * 16), "s"(rq)
               : "memory");
  const int e = wave * 32 + (lane & 7) * 4 + ((lane >> 4) & 1) * 2 + (lane >> 5);
  *reinterpret_cast<float*>(smem + L_CS + e * 4) = tsum8(v) * (-1.0f / D);
}
template <bool LDS>
__device__ __forceinline__ void x_update(unsigned char* smem, u32x4 rq, int lane, u32 src, int i) {
  const int l = lane < 40 ? lane : 0;
  const u32 off = src + 4 * l;
  u32 v[8];
  if (LDS) {
#pragma unroll
    for (int k = 0; k < 8; ++k) v[k] = *reinterpret_cast<const u32*>(smem + (off ^ ((k & 3) * 64)) + 256 * k);
  } else {
    u32 budget = SPIN_CAP;
#pragma unroll 1
    for (;;) {
#pragma unroll
      for (int k = 0; k < 8; ++k)
        asm volatile("buffer_load_dword %0, %1, %2, 0 offen offset:%3 sc1" : "=v"(v[k]) : "v"(off), "s"(rq), "n"(256 * k) : "memory");
      asm volatile("s_waitcnt vmcnt(0)"
                   : "+v"(v[0]), "+v"(v[1]), "+v"(v[2]), "+v"(v[3]), "+v"(v[4]), "+v"(v[5]), "+v"(v[6]), "+v"(v[7])::"memory");
      const u32 a = v[0] & v[1] & v[2] & v[3] & v[4] & v[5] & v[6] & v[7] & 1u;
      if (__builtin_amdgcn_ballot_w64(a == 0u) == 0ull || budget == 0u) break;
      --budget;
      __builtin_amdgcn_s_sleep(2);
    }
  }
  float lo = 0.f, hi = 0.f;
#pragma unroll
  for (int k = 0; k < 8; ++k) {
    lo += bflo(v[k]);
    hi += bfhi(v[k]);
  }
  u32* px = reinterpret_cast<u32*>(smem + L_HB + 160 * i + 4 * l);
  const u32 xo = *px, y = cvt2(lo, hi);
  if (lane < 40) *px = cvt2(bflo(xo) + bflo(y), bfhi(xo) + bfhi(y));
}
template <int N>
__device__ __forceinline__ void col_poll(u32x4 rq, const u32 (&off)[N], float (&y)[N]) {
  u32x2 v[N];
  u32 budget = SPIN_CAP;
#pragma unroll 1
  for (;;) {
    u32 a = 1u;
#pragma unroll
    for (int k = 0; k < N; ++k)
      asm volatile("buffer_load_dwordx2 %0, %1, %2, 0 offen sc1" : "=v"(v[k]) : "v"(off[k]), "s"(rq) : "memory");
    asm volatile("s_waitcnt vmcnt(0)" ::: "memory");
#pragma unroll
    for (int k = 0; k < N; ++k) {
      asm volatile("" : "+v"(v[k]));
      a &= v[k][0] & v[k][1];
    }
    if (__builtin_amdgcn_ballot_w64(a == 0u) == 0ull || budget == 0u) break;
    --budget;
  }
#pragma unroll
  for (int k = 0; k < N; ++k) y[k] = bf16r(tsum8(f32x4{bflo(v[k][0]), bfhi(v[k][0]), bflo(v[k][1]), bfhi(v[k][1])}));
}

#define CKL 22
#define CKV 5
#define CW 6
#define CWAIT asm volatile("s_waitcnt vmcnt(%0)" ::"n"(CW) : "memory")
#define QACK 5
#define QP 11
#define BEAT0 (QP + QACK)
#define CLRI -1
#define CLRX 0
#define XNO 0
#define YM 3
#define E_TAB 0
#define E_Q 0
#define E_QN 2
#define PWARM "s_waitcnt vmcnt(0)\n\t"
#define PROBE 1
#define MAP8 1
#define WBIG 10
#define QI 6
#define PF0 25
#define PFS 2
#define TFL 220
#define TFD2 0
static_assert(PO0 + (NINO - 1) * POS < 42 && PF0 + (NINF - 1) * PFS < 42, "pieces");
#define XCLR 4
#define XPFN "0x140"
#define XTAB 3
#define TPF 220
#define TPV 220
#define TFD 0
#define XA 15
#define XB 18
#define TS_O (TSTEP * (20 * (48 + NINO) + 12 * (48 + NINF)) / (32 * (48 + NINO)))
#define TS_F (TSTEP * (20 * (48 + NINO) + 12 * (48 + NINF)) / (32 * (48 + NINF)))
#define TSQ 172
#define ROT (wave * 3 + j + g * 5)
static_assert(CKL > BEAT0 && QP > QI && CLRI < QP && 23 - CKL + CKV >= CW && CKV + 1 + CW <= XB - CW && XB - CW <= XA &&
              CLRI < BEAT0, "window");
#define PACE_ASM                                                                                                     \
  "1:\n\ts_memrealtime s[98:99]\n\ts_waitcnt lgkmcnt(0)\n\ts_lshl_b32 s98, s98, 3\n\ts_sub_i32 s98, s98, %[tn]\n\t"  \
  "s_cmp_lt_i32 s98, 0\n\ts_cbranch_scc0 2f\n\ts_sleep 1\n\ts_branch 1b\n2:\n\ts_add_i32 %[tn], %[tn], %[ts]\n\t"
__device__ __forceinline__ void pace(u32& tnext, u32 tstep) {
  asm volatile(PACE_ASM : [tn] "+s"(tnext) : [ts] "s"(tstep) : "scc", "s98", "s99");
}
constexpr int kreg(int i) { return i % 3 < 2 ? 64 + 8 * (i / 3) + 4 * (i % 3) : 128 + 4 * (i / 3); }
constexpr int vreg(int i) { return 160 + 24 * (i / 6) + 8 * (i % 3) + 4 * ((i / 3) & 1); }
#define AQ0 3
#define AGAIN 0
#define STR2(x) #x
#define STR(x) STR2(x)
#define ADAPT_ASM
#define LOADW(reg, voff, sbase, imm)                                                                                \
  asm volatile(PACE_ASM "global_load_dwordx4 v[%[r0]:%[r1]], %[p], %[sb] offset:%[im] nt\n\ts_waitcnt vmcnt(%[w])" ADAPT_ASM \
               : [tn] "+s"(tnext)                                                                                   \
               : [r0] "n"(reg), [r1] "n"((reg) + 3), [p] "v"(voff), [sb] "s"(sbase), [im] "n"(imm), [ts] "s"(tstep - AQ0 * AGAIN), \
                 [w] "n"(WBIG - 1)                                                                                  \
               : "scc", "s98", "s99")

#define HS_WAIT(COND)                                                                                \
  _Pragma("unroll 1") for (;;) {                                                                     \
    gd = CTL[512 + lane0] - CTL[576 + lane0];                                                        \
    const u64 ch = __builtin_amdgcn_ballot_w64(gd != 0u) & 0x1ffull;                                 \
    if (ch > 0xffull || (COND) || tmo == 0u) break;                                                  \
    if ((tmo & 3u) == 0u && lane0 == 0)                                                              \
      __builtin_amdgcn_raw_buffer_store_b32(hb, rs, WS_HB + h * 128 + c * 4, 0, 16);                 \
    hb += 4u;                                                                                        \
    --tmo;                                                                                           \
    __builtin_amdgcn_s_sleep(3);                                                                     \
    ctl_load<false>(rq, hso, (u32)(smb + L_CTL + 2048));                                             \
    asm volatile("s_waitcnt vmcnt(0)" ::: "memory");                                                 \
  }

struct QKAddr {
  int qad[6], idxq, wa;
  f32x4 D0, D1;
};
struct PVAddr {
  int ra, idx, oa, sa, la, ma;
  u32 dst;
  f32x4 O0, O1, O2;
  u32x4 A0, A1, A2, A3;
};
__device__ __forceinline__ void att_table(unsigned char* smem, int lane, int wave, u32 go) {
  const int n = lane & 15, G = lane >> 4;
  const int seg = n & 1, pr = (n >> 1) & 1, tq = n >> 2, xy = G >> 1;
  const int qa = L_Q + (pr * 48 + seg * 8 + (G & 1) * 4) * 4;
  u32 t[16];
#pragma unroll
  for (int k = 0; k < 4; ++k) t[k] = tq == k ? qa + xy * 64 : L_ZERO;
#pragma unroll
  for (int k = 0; k < 2; ++k) t[4 + k] = tq == 2 * k + xy ? qa + 128 : L_ZERO;
  t[6] = 0x4444 + pr * 0xAAAA;
  t[7] = L_P + wave * P_W + ((n >> 1) & 1) * P_PAR + ((((G & 1) * 2 + (n >> 3)) * 4 + (n & 1) * 2 + (G >> 1)) * 16) + ((n >> 2) & 1) * 8;
  t[8] = L_P + wave * P_W + ((n >> 2) & 1) * P_U + (n >> 3) * P_PAR + G * 64;
  t[9] = (n & 3) * 0x5555;
  t[10] = L_OM + (wave * OM_S + (n >> 3) * OM_P + (n & 7) * 4 + (G & 1) * 32) * 4;
  t[11] = L_OM + (wave * OM_S + DH + G) * 4;
  t[12] = L_OM + ((lane & 3) * OM_S + (wave & 3) * 64 + (lane >> 3) * 4 + (lane & 4) * 8) * 4;
  t[13] = L_OM + ((lane & 3) * OM_S + DH + 4) * 4;
  const int d0 = wave * 64 + (lane >> 3) * 8;
  t[14] = (go + (wave < 3 ? (d0 >= 96 ? 2048 - 192 : 0) + d0 * 2 : lane * 2048 + 192)) & 0xffffu;
  t[15] = q_addr(wave * 32 + (lane & 7) * 4 + ((lane >> 4) & 1) * 2 + (lane >> 5));
#pragma unroll
  for (int k = 0; k < 2; ++k)
    *reinterpret_cast<u32x4*>(smem + L_TAB + wave * 2048 + k * 1024 + lane * 16) =
        u32x4{t[8 * k] | (t[8 * k + 4] << 16), t[8 * k + 1] | (t[8 * k + 5] << 16), t[8 * k + 2] | (t[8 * k + 6] << 16),
              t[8 * k + 3] | (t[8 * k + 7] << 16)};
}
__device__ __forceinline__ void att_setup(QKAddr& r, PVAddr& v, unsigned char* smem, int lane, int wave, int& qw) {
  const u32x4 a = afrag(smem, L_TAB + wave * 2048 + lane * 16), b = afrag(smem, L_TAB + wave * 2048 + 1024 + lane * 16);
  r.D0 = zero4();
  r.D1 = zero4();
  qw = fresh(b[3] >> 16);
#pragma unroll
  for (int k = 0; k < 4; ++k) r.qad[k] = fresh(a[k] & 0xffffu);
  r.qad[4] = fresh(a[0] >> 16);
  r.qad[5] = fresh(a[1] >> 16);
  r.idxq = fresh(a[2] >> 16);
  r.wa = fresh(a[3] >> 16);
  v.ra = fresh(b[0] & 0xffffu);
  v.idx = fresh(b[1] & 0xffffu);
  v.oa = fresh(b[2] & 0xffffu);
  v.la = fresh(b[3] & 0xffffu);
  v.sa = fresh(b[0] >> 16);
  v.ma = fresh(b[1] >> 16);
  v.dst = fresh(b[2] >> 16);
  asm volatile("" : "+v"(r.D0), "+v"(r.D1));
}
#define SM "v_smfmac_f32_16x16x64_bf16 "
#define P_WRITE_READ(QA)                                                                                              \
  asm volatile("ds_write2_b32 %[wa], %[p], %[p] offset1:%[o1]\n\t"                                                    \
               "ds_read_b128 %[a0], %[ra]\n\tds_read_b128 %[a1], %[ra] offset:16\n\t"                                 \
               "ds_read_b128 %[a2], %[ra] offset:32\n\tds_read_b128 %[a3], %[ra] offset:48"                           \
               : [a0] "=&v"(pv.A0), [a1] "=&v"(pv.A1), [a2] "=&v"(pv.A2), [a3] "=&v"(pv.A3), "+v"(sk)                 \
               : [wa] "v"(QA.wa + smb), [p] "v"(cvt2(pend, 0.f)), [ra] "v"(pv.ra + smb), [o1] "n"(P_U / 4 + 1)        \
               : "memory");
#define QK_SOFTMAX(QA)                                                                                                \
  {                                                                                                                   \
    u32x4 A0, A1, A2, A3, A4, A5;                                                                                     \
    float scl;                                                                                                        \
    asm volatile("ds_read_b128 %[a0], %[q0]\n\tds_read_b128 %[a1], %[q1]\n\tds_read_b128 %[a2], %[q2]\n\t"            \
                 "ds_read_b128 %[a3], %[q3]\n\tds_read_b128 %[a4], %[q4]\n\tds_read_b128 %[a5], %[q5]"                \
                 : [a0] "=&v"(A0), [a1] "=&v"(A1), [a2] "=&v"(A2), [a3] "=&v"(A3), [a4] "=&v"(A4), [a5] "=&v"(A5)     \
                 : [q0] "v"(QA.qad[0] + smb), [q1] "v"(QA.qad[1] + smb), [q2] "v"(QA.qad[2] + smb),                   \
                   [q3] "v"(QA.qad[3] + smb), [q4] "v"(QA.qad[4] + smb), [q5] "v"(QA.qad[5] + smb)                    \
                 : "memory");                                                                                         \
    asm volatile("ds_read_b32 %[s], %[z] offset:%[ri]\n\t"                                                            \
                 "s_waitcnt lgkmcnt(6)\n\t" SM "%[d0], %[a0], v[64:71], %[ix]\n\t" SM "%[d1], %[a0], v[96:103], %[ix]\n\t"   \
                 "s_waitcnt lgkmcnt(5)\n\t" SM "%[d0], %[a1], v[72:79], %[ix]\n\t" SM "%[d1], %[a1], v[104:111], %[ix]\n\t"   \
                 "s_waitcnt lgkmcnt(4)\n\t" SM "%[d0], %[a2], v[80:87], %[ix]\n\t" SM "%[d1], %[a2], v[112:119], %[ix]\n\t"   \
                 "s_waitcnt lgkmcnt(3)\n\t" SM "%[d0], %[a3], v[88:95], %[ix]\n\t" SM "%[d1], %[a3], v[120:127], %[ix]\n\t"   \
                 "s_waitcnt lgkmcnt(2)\n\t" SM "%[d0], %[a4], v[128:135], %[ix]\n\t" SM "%[d1], %[a4], v[144:151], %[ix]\n\t"   \
                 "s_waitcnt lgkmcnt(0)\n\t" SM "%[d0], %[a5], v[136:143], %[ix]\n\t" SM "%[d1], %[a5], v[152:159], %[ix]\n\t"   \
                                      \
                 "ds_read_b128 %[a0], %[z]\n\tds_read_b128 %[a1], %[z]\n\tds_read_b128 %[a2], %[z]\n\ts_nop 4"        \
                 : [d0] "+v"(QA.D0), [d1] "+v"(QA.D1), [s] "=&v"(scl), [a0] "+v"(A0), [a1] "+v"(A1), [a2] "+v"(A2),   \
                   [a3] "+v"(A3), [a4] "+v"(A4), [a5] "+v"(A5)                                                        \
                 : [ix] "v"(QA.idxq), [z] "v"(smb), [ri] "n"(L_RI), "{v[64:95]}"(kv0), "{v[96:127]}"(kv1),           \
                   "{v[128:159]}"(kv2)                                                                                \
                 : "memory");                                                                                         \
    pv.O0 = __builtin_bit_cast(f32x4, A0);                                                                            \
    pv.O1 = __builtin_bit_cast(f32x4, A1);                                                                            \
    pv.O2 = __builtin_bit_cast(f32x4, A2);                                                                            \
    const f32x2 e0 = f32x2{QA.D0[0], QA.D0[1]} + f32x2{QA.D0[2], QA.D0[3]};                                           \
    const f32x2 e1 = f32x2{QA.D1[0], QA.D1[1]} + f32x2{QA.D1[2], QA.D1[3]};                                           \
    const float s0 = e0[0] + dpp<XOR1>(e0[1]), s1 = e1[1] + dpp<XOR1>(e1[0]);                                         \
    const float sc = ((lane0 & 1) ? s1 : s0) * scl;                                                                   \
    pend = __builtin_amdgcn_exp2f(sc);                                                                                \
    pmax = 0.f;                                                                                                       \
    float sk = sc;                                                                                                    \
    P_WRITE_READ(QA)                                                                                                  \
    const bool unsafe =                                                                                               \
        (__builtin_amdgcn_ballot_w64(!(sk <= 24.f)) != 0ull) | (__builtin_amdgcn_ballot_w64(sk >= -24.f) == 0ull);    \
    if (__builtin_expect(unsafe, 0)) {                                                                                \
      pmax = xrow16_max(max_ror8(max8(sk)));                                                                          \
      pend = __builtin_amdgcn_exp2f(sk - pmax);                                                                       \
      asm volatile("s_waitcnt lgkmcnt(0)" : "+v"(pv.A0), "+v"(pv.A1), "+v"(pv.A2), "+v"(pv.A3));                      \
      P_WRITE_READ(QA)                                                                                                \
      asm volatile("s_waitcnt lgkmcnt(0)" : "+v"(pv.A0), "+v"(pv.A1), "+v"(pv.A2), "+v"(pv.A3));                      \
    }                                                                                                                 \
  }

using u32x16 = __attribute__((ext_vector_type(16))) unsigned;
using u32x32 = __attribute__((ext_vector_type(32))) unsigned;
template <int OFF>
__device__ __forceinline__ u16* karg() {
  u64 p;
  asm volatile("s_load_dwordx2 %0, %1, %2\n\ts_waitcnt lgkmcnt(0)" : "=s"(p) : "s"(__builtin_amdgcn_kernarg_segment_ptr()), "n"(OFF));
  return (u16*)p;
}
template <class T>
__device__ __forceinline__ T* mkptr(u32 lo, u32 hi) {
  return (T*)(((u64)hi << 32) | lo);
}

__global__ __launch_bounds__(NT, 2) void mega_kernel(Params) {
  __shared__ __attribute__((aligned(16))) unsigned char smem[L_TOTAL];
  u32x16 ka;
  u32x4 kb4;
  {
    u64 pc;
    u32 keep;
    asm volatile("s_load_dwordx16 %[ka], %[kp], 0x0\n\ts_load_dwordx4 %[kb], %[kp], 0x40\n\t"
                 "s_getpc_b64 %[pc]\n.Lmk_pc:\n\t"
                 "s_mov_b32 %[keep], m0\n\ts_mov_b32 m0, %[dst]\n\t"
                 "v_lshlrev_b32 v1, 7, %[tid]\n\tv_min_u32 v1, .Lmk_code_end-.Lmk_pc-4, v1\n\t"
                 "v_cmp_gt_u32 vcc, " XPFN ", %[tid]\n\ts_mov_b64 exec, vcc\n\t"
                 "global_load_lds_dword v1, %[pc]\n\ts_mov_b64 exec, -1\n\ts_mov_b32 m0, %[keep]\n\t"
                 "v_lshrrev_b32 v1, 2, %[tid]\n\tv_cmp_eq_u32 vcc, 3, v1\n\tv_mov_b32 v3, 0x3f803f80\n\ts_nop 0\n\t"
                 "v_cndmask_b32 v2, 0, 1, vcc\n\tv_cmp_eq_u32 vcc, 2, v1\n\tv_lshlrev_b32 v1, 2, %[tid]\n\ts_nop 0\n\t"
                 "v_cndmask_b32 v2, v2, v3, vcc\n\tv_cmp_eq_u32 vcc, 7, %[tid]\n\tv_mov_b32 v3, %[scl]\n\t"
                 "v_min_u32 v1, 80, v1\n\tv_cndmask_b32 v2, v2, v3, vcc\n\t"
                 "ds_write_b32 v1, v2 offset:%[sm]\n\t"
                 "s_waitcnt lgkmcnt(0)\n\ts_barrier"
                 : [ka] "=&s"(ka), [kb] "=&s"(kb4), [pc] "=&s"(pc), [keep] "=&s"(keep)
                 : [kp] "s"(__builtin_amdgcn_kernarg_segment_ptr()), [tid] "v"(threadIdx.x), [dst] "n"(L_PF),
                   [scl] "n"(__builtin_bit_cast(u32, SCL)), [sm] "n"(0)
                 : "memory", "vcc", "v1", "v2", "v3");
  }
  const u32 tstart = (u32)__builtin_amdgcn_s_memrealtime();
  const int tid = threadIdx.x, lane0 = tid & 63;
  const int wave = __builtin_amdgcn_readfirstlane(tid >> 6);
  const int b = blockIdx.x;
  const int g = b & 7, j = b >> 3, hsub = j & 3, c = j >> 2, h = g * 4 + hsub;
  const bool isO = j < 20;
  const bool leader = b == 0 && wave == 6;
  const u32 fslot = (u32)((j & 1) * 768 + ((j - 20) >> 1) * 128);
  u32 tmo = SPIN_CAP;
  volatile LAS u32* cnt = (volatile LAS u32*)(smem + L_CNT);
  Params P;
  P.x0 = mkptr<const u16>(ka[0], ka[1]);
  P.kc = mkptr<const u16>(ka[2], ka[3]);
  P.vc = mkptr<const u16>(ka[4], ka[5]);
  P.wq = mkptr<const u16>(ka[6], ka[7]);
  P.wo = mkptr<const u16>(ka[8], ka[9]);
  P.w1 = mkptr<const u16>(ka[10], ka[11]);
  P.w3 = mkptr<const u16>(ka[12], ka[13]);
  P.ws = mkptr<u32>(kb4[2], kb4[3]);
  u32* const wsb = (u32*)(((uintptr_t)P.ws + 0x1fffffu) & ~(uintptr_t)0x1fffffu);
  const __amdgpu_buffer_rsrc_t rs = __builtin_amdgcn_make_buffer_rsrc(wsb, 0, (int)WS_END, 0x00020000);
  const u32x4 rq = {(u32)(uintptr_t)wsb, (u32)((uintptr_t)wsb >> 32) & 0xffffu, WS_END, 0x00020000u};

  u32 anc = 0u;
  u32 tref = 0u, ar = 0u;
  u32x32 kv0, kv1, kv2, kv3, kv4, kv5;
  asm volatile(""
               : "={v[64:95]}"(kv0), "={v[96:127]}"(kv1), "={v[128:159]}"(kv2), "={v[160:191]}"(kv3), "={v[192:223]}"(kv4),
                 "={v[224:255]}"(kv5));
  const int smb = (int)(uintptr_t)(LAS unsigned char*)smem;
  const u32 gop = A_GO + go_slot(hsub * 2) + c * 256;
  const u32 tstep = isO ? TS_O : TS_F;
  u32 tnext = tstart * 8u + (u32)wave * (TSTEP / 8) + 5 * TSQ;
  const size_t rowbase = (size_t)h * SK + (SK - WIN) + (wave * 8 + c) * 8;
#define GSTR 512
#undef ROT
#define ROT h
  const u16* vb = P.vc + rowbase * DH;
  const int rot = ROT;
  const u16* ws0 = isO ? P.wo + (size_t)(768 * g) * D + 32 * j : ((j & 1) ? P.w3 : P.w1) + 192 * g + 32 * ((j - 20) >> 1);
  {
    constexpr int L_CTL = L_LAND + 6 * 2048;
    volatile LAS u32* const CTL = (volatile LAS u32*)(smem + L_CTL);
    u32 gd, hb = 0u;
    const u32 lho = WS_HB + (lane0 >> 1) * 128 + (lane0 & 1) * 16;
    const u32 hso = lane0 < 8 ? WS_HB + (u32)h * 128 + lane0 * 4 : WS_GOF + g * 128;
    if (wave < 6) {
      const u16* s0 = P.wq + (size_t)(80 * c + 8 * (wave & 1)) * (H * DH) + h * DH + (wave >> 1) * 64;
      const u32 wo_ = (u32)(((lane0 >> 2) & 7) * (H * DH) + (lane0 >> 5) * 32 + (((lane0 >> 1) ^ wave) & 1) * 16 + (lane0 & 1) * 8) * 2u;
      u32 tq = tnext - 5 * TSQ;
#pragma unroll 1
      for (int p = 0; p < 5; ++p) {
        pace(tq, TSQ);
        dma_kib(s0 + (size_t)p * (16 * H * DH), wo_, smem, L_WQ + (wave >> 1) * 10240 + (wave & 1) * 1024 + p * 2048);
      }
    } else {
      if (leader) {
        ctl_load<true>(rq, lho, (u32)(smb + L_CTL));
        ctl_load<false>(rq, WS_GOF + (lane0 & 7) * 128, (u32)(smb + L_CTL + 2560));
      }
      ctl_load<false>(rq, hso, (u32)(smb + L_CTL + (wave == 6 ? 2816 : 2304)));
      if (wave == 6) {
        u32 keep;
        asm volatile("s_mov_b32 %0, m0\n\ts_mov_b32 m0, %4\n\ts_nop 2\n\tglobal_load_lds_dwordx4 %1, %3\n\t"
                     "global_load_lds_dword %2, %3 offset:1024\n\ts_mov_b32 m0, %0"
                     : "=&s"(keep)
                     : "v"(lane0 * 16), "v"(lane0 * 4), "s"(P.x0), "s"((u32)(smb + L_HB))
                     : "memory");
      }
      u32 rtt;
      {
        u32 pa = 0u, pb = 0u;
        u64 t1, t2;
        const u32 pbo = WS_PB + (u32)(wave & 1) * (SIDE + 128u) + (u32)((b >> 3) & 7) * 16384u + (u32)((b & 7) * 4 + (b >> 6)) * 256u;
        asm volatile(PWARM ".p2align 6\n\ts_mov_b64 exec, 1\n\t"
                     "buffer_load_dword %0, off, %4, %5 sc1 nt\n\tbuffer_load_dword %1, off, %4, %6 sc1 nt\n\t"
                     "s_mov_b64 exec, -1\n\t"
                     "s_waitcnt vmcnt(1)\n\ts_memtime %2\n\ts_waitcnt vmcnt(0)\n\ts_memtime %3\n\ts_waitcnt lgkmcnt(0)"
                     : "+v"(pa), "+v"(pb), "=&s"(t1), "=&s"(t2)
                     : "s"(rq), "s"(pbo), "s"(pbo ^ SIDE)
                     : "memory");
        rtt = __builtin_amdgcn_readfirstlane((u32)t2 - (u32)t1) + 1u;
      }
      if (wave == 6) {
        *(volatile LAS u32*)(smem + L_VOTE) = rtt;
      } else {
#define CLEAR1                                                                                                      \
  {                                                                                                                 \
    const u32 ca = gy_rep((g + CLRX) & 7) + fresh(lane0) * 16;                                                      \
    if (j < 16 || (g & 3 & ~YM) == 0)                                                                               \
      store16(rs, ca + (u32)(j >> 3) * 16384u + (u32)(j & 7) * 1024u, u32x4{0u, 0u, 0u, 0u});                       \
    if (lane0 < 8) store16(rs, ca + A_GH + (u32)j * 128u, u32x4{0u, 0u, 0u, 0u});                                   \
  }
        if (CLRI < 0 && !(XNO & 1)) CLEAR1
        hb = rtt;
      }
    }
    if (tid == 0) *(volatile LAS u32*)(smem + L_ANC) = 0u;
    *reinterpret_cast<u32x4*>(smem + L_P + wave * P_W + lane0 * 16) = u32x4{0u, 0u, 0u, 0u};
    *reinterpret_cast<u32x4*>(smem + (lane0 < 16 ? L_P + wave * P_W + 1024 + lane0 * 16 : L_OM + (wave * OM_S + OM_P + DH) * 4)) =
        u32x4{0u, 0u, 0u, 0u};
    const u16* kb = P.kc + rowbase * DH;
    const u32 ko = ((lane0 & 15) >> 1) * 384 + 16 * ((lane0 & 1) * 4 + (lane0 >> 4)), vo = (lane0 >> 3) * 384 + 16 * (lane0 & 7);
#define WPO(n) ((n) >= PO0 && ((n) - PO0) % POS == 0 && ((n) - PO0) / POS < NINO)
#define WPF(n) ((n) >= PF0 && ((n) - PF0) % PFS == 0 && ((n) - PF0) / PFS < NINF)
#define WPIECE(n)                                                                                                   \
  if ((WPO(n) && isO) || (WPF(n) && !isO)) {                                                                        \
    pace(tnext, tstep);                                                                                             \
    dma_piece(ws0, isO ? D : F, smem, L_WB, isO ? wave * 6 + ((n) - PO0) / POS : wave * 5 + ((n) - PF0) / PFS, lane0); \
  }
#pragma unroll
    for (int i = 0; i < 24; ++i) {
      if (i == XTAB) tnext -= E_TAB;
      if (i == QI) tnext -= wave < 6 ? E_Q : 0;
      LOADW(kreg(i), ko, kb + ((i / 3 + rot) & 7) * GSTR * DH, 128 * (i % 3));
      if (i == XTAB) tnext += E_TAB;
      if (i == QI) tnext += wave < 6 ? E_Q : 0;
      WPIECE(i)
      if (i == XTAB) att_table(smem, fresh(lane0), wave, gop);
      if (i == 1 && wave == 6) {
        const u32x4 lv = afrag(smem, L_HB + lane0 * 16);
        const u32 xb = *reinterpret_cast<const u32*>(smem + L_HB + 1024 + lane0 * 4);
        float v[10];
#pragma unroll
        for (int k = 0; k < 4; ++k) {
          v[2 * k] = bflo(lv[k]);
          v[2 * k + 1] = bfhi(lv[k]);
        }
        v[8] = bflo(xb);
        v[9] = bfhi(xb);
        float sm = 0.f;
#pragma unroll
        for (int k = 0; k < 10; ++k) sm += v[k];
        const float mean = wave_sum(sm) * (1.0f / D);
        float q = 0.f;
#pragma unroll
        for (int k = 0; k < 10; ++k) q = fmaf(v[k] - mean, v[k] - mean, q);
        const float var = wave_sum(q) * (1.0f / D);
        const float mb = bf16r(mean);
        const float ri = __builtin_amdgcn_rcpf(bf16r(__builtin_amdgcn_sqrtf(bf16r(bf16r(var) + 1e-5f))));
        u32 hp[5];
#pragma unroll
        for (int k = 0; k < 5; ++k) {
          const u32 cc = cvt2(v[2 * k] - mb, v[2 * k + 1] - mb);
          hp[k] = cvt2(bflo(cc) * ri, bfhi(cc) * ri);
        }
        *reinterpret_cast<u32x4*>(smem + L_LN + lane0 * 16) = u32x4{hp[0], hp[1], hp[2], hp[3]};
        *reinterpret_cast<u32*>(smem + L_LN + 1024 + lane0 * 4) = hp[4];
        asm volatile("s_waitcnt lgkmcnt(0)" ::: "memory");
        if (lane0 == 0) lds_add(smem, L_VOTE + 4, 1u);
      }
      if (i == QI - 1 && wave < 6) {
        asm volatile("s_waitcnt vmcnt(%0)" ::"n"(QI) : "memory");
        if (lane0 == 0) lds_add(smem, L_VOTE + 4, 1u);
      }
      if (i == QI && wave < 6) {
        lds_wait((volatile LAS u32*)(smem + L_VOTE + 4), 7u);
        G80 a;
        g80_load<1024>(a, smem, lane0, L_WQ + (wave >> 1) * 10240 + (wave & 1) * 512, L_LN + 160 * c, -1);
        float r0, r1, z;
        g80_run<false>(a, smem, rq, lane0, 0u, 0u, r0, r1, z);
        auto sw = __builtin_amdgcn_permlane32_swap(__float_as_uint(r0), __float_as_uint(r1), false, false);
        const f32x4 qd = quad4(__uint_as_float(sw[1]));
        const int qp = wave * 128 + (lane0 >> 5) * 64 + (lane0 & 15) * 4;
        if ((lane0 & 19) == 0) *reinterpret_cast<u32x2*>(smem + L_RED + (qp >> 1)) = u32x2{cvt2(qd[0], qd[1]), cvt2(qd[2], qd[3])};
        if ((lane0 & 19) == 16) *reinterpret_cast<f32x4*>(smem + L_CS + qp) = qd;
        if (lane0 == 0) lds_add(smem, L_CNT + 4, 1u);
      }
      if (wave == 7) {
        if (i == CLRI) CLEAR1
        if (i == QP) {
          lds_wait(cnt + 1, 6u);
          const int ln = fresh(lane0);
          if (ln < 24 && !(XNO & 2)) store16(rs, WS_GQ1 + (u32)(h * 8 + c) * 2048 + ln * 16, afrag(smem, L_RED + ln * 16));
        }
        if (i == BEAT0) {
          asm volatile("s_waitcnt vmcnt(%0)" ::"n"(BEAT0 - QP) : "memory");
          const int df = (int)(__builtin_amdgcn_readfirstlane(*(volatile LAS u32*)(smem + L_VOTE)) - hb);
          hb = df > PTHR ? 1u << (g >> 2) : (df < -PTHR ? 2u >> (g >> 2) : 0u);
          hb += (__builtin_amdgcn_readfirstlane(CTL[576 + c]) & ~3u) + 4u;
          if (lane0 == 0) __builtin_amdgcn_raw_buffer_store_b32(hb, rs, WS_HB + h * 128 + c * 4, 0, 16);
          hb += 4u;
        }
        if (i == CKL) ctl_load<false>(rq, hso, (u32)(smb + L_CTL + 2048));
      }
      if (i == CKL && leader) {
        ctl_load<true>(rq, lho, (u32)(smb + L_CTL + 1024));
      }
    }
#define VLOADS(A, B)                                                                           \
  _Pragma("unroll") for (int i = A; i < B; ++i) {                                              \
    if (i == XA - E_QN) tnext -= wave < 4 ? E_QK : 0;                                          \
    if (i == XB - E_QN) tnext -= wave < 4 ? 0 : E_QK;                                          \
    LOADW(vreg(i), vo, vb + ((i / 3 + rot) & 7) * GSTR * DH, 128 * (i % 3));                      \
    WPIECE(24 + i)                                                                             \
  }
    VLOADS(0, CKV)
    if (wave == 7) {
      CWAIT;
      HS_WAIT((u32)ch == 0xffu)
      if (lane0 == 0) cnt[1] = 7u;
    }
    if (leader) {
      CWAIT;
      const u32x4 xa = afrag(smem, L_CTL + lane0 * 16);
      u32x4 lv;
#pragma unroll 1
      for (;;) {
        asm volatile("" ::: "memory");
        lv = afrag(smem, L_CTL + 1024 + lane0 * 16);
        const bool same = lv[0] == xa[0] || lv[1] == xa[1] || lv[2] == xa[2] || lv[3] == xa[3];
        if (__builtin_amdgcn_ballot_w64(same) == 0ull || tmo == 0u) break;
        --tmo;
        __builtin_amdgcn_s_sleep(4);
        ctl_load<true>(rq, lho, (u32)(smb + L_CTL + 1024));
        asm volatile("s_waitcnt vmcnt(0)" ::: "memory");
      }
      u32 nv = 256u;
#pragma unroll
      for (int e = 0; e < 4; ++e)
        nv += (u32)__builtin_popcountll(__builtin_amdgcn_ballot_w64((lv[e] & 2u) != 0u)) -
              (u32)__builtin_popcountll(__builtin_amdgcn_ballot_w64((lv[e] & 1u) != 0u));
      if (lane0 < 8) __builtin_amdgcn_raw_buffer_store_b32(CTL[640 + lane0] + (nv > 256u ? 2u : 1u), rs, WS_GOF + lane0 * 128, 0, 16);
    }
    VLOADS(CKV, CKV + 1)
    if (wave < 3) {
      const int ln = fresh(lane0);
      lds_wait(cnt + 1, 7u);
      ctl_load<true>(rq, WS_GQ1 + (u32)(h * 8 + (ln >> 3)) * 2048 + wave * 128 + (ln & 7) * 16, (u32)(smb + L_LAND + wave * 2048));
    }
    VLOADS(CKV + 1, CKV + 1 + CW)
    if (wave < 3) {
      const int ln = fresh(lane0);
      CWAIT;
      const u32x4 v = afrag(smem, L_LAND + wave * 2048 + ln * 16);
      const float t0 = tsum8(f32x4{bflo(v[0]), bflo(v[1]), bflo(v[2]), bflo(v[3])});
      const float t1 = tsum8(f32x4{bfhi(v[0]), bfhi(v[1]), bfhi(v[2]), bfhi(v[3])});
      *reinterpret_cast<u32*>(smem + q_addr(wave * 64 + (ln & 7) * 8 + ((ln >> 4) & 1) * 4 + (ln >> 5) * 2)) = cvt2(t0, t1);
      asm volatile("s_waitcnt lgkmcnt(0)" ::: "memory");
      if (lane0 == 0) lds_add(smem, L_CNT, 1u);
    }
    VLOADS(CKV + 1 + CW, XB - CW)
    if (wave == 7) ctl_load<false>(rq, hso, (u32)(smb + L_CTL + 2048));
    VLOADS(XB - CW, XA)
    if (wave >= 4) {
      VLOADS(XA, XB)
      if (wave == 7) {
        CWAIT;
        HS_WAIT(false)
        if (lane0 == 8) *(volatile LAS u32*)(smem + L_VOTE) = gd >> 1;
      }
    }
  }

#pragma unroll 1
  for (int half = 0;; ++half) {
    {
      float pend, pmax;
      PVAddr pv;
      QKAddr qa;
      const int lane = fresh(lane0);
      const u32 off[1] = {ar + A_GQ + hsub * 16384 + (wave < 6 ? (lane >> 3) * 896 + wave * 128 + (lane & 7) * 16 : (lane & 7) * 896 + 768)};
      f32x4 qv[1];
      asm volatile("" : "=v"(qv[0]));
      if (half == 1) anc = lds_wait_nz((volatile LAS u32*)(smem + L_ANC));
      if (half == 1 && wave < 7) {
        wait_rt(anc + A_Q);
        POLL16(qv[0], off[0], 0);
      }
      int qw;
      att_setup(qa, pv, smem, lane, wave, qw);
      if (half == 0) {
        lds_wait(cnt, 3u);
      } else {
        if (wave < 7) {
          asm volatile("s_waitcnt vmcnt(0)" : "+v"(qv[0])::"memory");
          if (__builtin_expect(__builtin_amdgcn_ballot_w64((and4(__builtin_bit_cast(u32x4, qv[0])) & 1u) == 0u) != 0ull, 0))
            gpoll4<1>(rq, off, qv);
          if (wave < 6) {
            const float t = tsum8(qv[0]);
            *reinterpret_cast<u16*>(smem + qw) = (u16)cvt2(t, t);
          } else {
            const LnS ls = ln_stats(qv[0][3], qv[0][0] + qv[0][1] + qv[0][2]);
            *reinterpret_cast<float*>(smem + L_RI) = ls.ri * SCL;
          }
        }
        if (wave < 4) __builtin_amdgcn_s_setprio(2);
        bar();
      }
      QK_SOFTMAX(qa)
      if (half == 0) {
        const int lv_ = fresh(lane0);
        const u32 vo = (lv_ >> 3) * 384 + 16 * (lv_ & 7);
        tnext += E_QK;
        if (wave < 4) {
          VLOADS(XA, XB)
        }
        VLOADS(XB, 24)
      }
#define DA(CTL) "v_add_f32_dpp %[l], %[l], %[l] " CTL " row_mask:0xf bank_mask:0xf\n\t"
#define PX(o, v, a) SM "%[o" #o "], %[a" #a "], v[160+24*" #a "+8*" #o ":167+24*" #a "+8*" #o "], %[ix]\n\t"
      asm volatile("s_waitcnt vmcnt(6) lgkmcnt(3)\n\t" PX(0, x, 0) DA("quad_perm:[1,0,3,2]") PX(1, y, 0) PX(2, z, 0)
                   "s_waitcnt lgkmcnt(2)\n\t" PX(0, x, 1) DA("quad_perm:[2,3,0,1]") PX(1, y, 1) PX(2, z, 1)
                   "s_waitcnt lgkmcnt(0)\n\t" PX(0, x, 2) DA("row_half_mirror") PX(1, y, 2) PX(2, z, 2)
                   : [o0] "+v"(pv.O0), [o1] "+v"(pv.O1), [o2] "+v"(pv.O2), [a0] "+v"(pv.A0), [a1] "+v"(pv.A1),
                     [a2] "+v"(pv.A2), [a3] "+v"(pv.A3), [l] "+v"(pend)
                   : [ix] "v"(pv.idx), "{v[160:191]}"(kv3), "{v[192:223]}"(kv4), "{v[224:255]}"(kv5)
                   : "memory");
      static_assert(5 - NINF >= NPRE && 6 - NINO >= NPRE, "same number of pieces behind V");
      if (half == 0) {
        if (!(XNO & 4)) {
          const int ln = fresh(lane0);
          if (wave < 6) {
            if (ln < 8) store16(rs, WS_GQ1 + (u32)(h * 8 + c) * 2048 + 1024 + wave * 128 + ln * 16, afrag(smem, L_CS + wave * 128 + ln * 16));
          } else {
            const int gc = (g + CLRX) & 7, p2 = (isO ? j : j - 8) * 2 + wave - 6;
            const bool isy = p2 < 8 || p2 >= 32;
            if ((isO ? j < 12 : (p2 < 40 || (gc & 3) == 0)) && !(isy && (gc & 3 & ~YM) != 0)) {
              const u32 ca = gy_rep(gc) + (u32)(4 + (p2 >> 2)) * 16384u + (u32)(p2 & 3) * 2048u + ln * 16;
              const u32x4 z = __builtin_bit_cast(u32x4, zero4());
              store16(rs, ca, z);
              store16(rs, ca + 1024, z);
            }
          }
        }
        if (!isO) tnext += TFD;
#pragma unroll 1
        for (int p = 0; p < NPRE; ++p) {
          pace(tnext, isO ? TPV : TPF);
          dma_piece(ws0, isO ? D : F, smem, L_WB, isO ? wave * 6 + NINO + p : wave * 5 + NINF + p, fresh(lane0));
        }
      }
      asm volatile("s_waitcnt vmcnt(%[np])\n\t" PX(0, x, 3) DA("row_ror:8") PX(1, y, 3) PX(2, z, 3) "s_setprio 0"
                   : [o0] "+v"(pv.O0), [o1] "+v"(pv.O1), [o2] "+v"(pv.O2), [a3] "+v"(pv.A3), [l] "+v"(pend)
                   : [ix] "v"(pv.idx), [np] "n"(NPRE), "{v[224:255]}"(kv5)
                   : "memory");
      if (half == 0 && NINO + NPRE < 6 && isO) {
#pragma unroll 1
        for (int p = NINO + NPRE; p < 6; ++p) dma_piece(ws0, D, smem, L_WB, wave * 6 + p, fresh(lane0));
      }
      asm volatile("ds_write2_b32 %[la], %[l], %[mx] offset1:4\n\ts_mov_b64 exec, %[m]\n\ts_nop 2\n\t"
                   "ds_write_b128 %[a], %[o0]\n\tds_write_b128 %[a], %[o1] offset:256\n\t"
                   "ds_write_b128 %[a], %[o2] offset:512\n\t"
                   "s_mov_b64 exec, -1\n\ts_waitcnt lgkmcnt(0)\n\ts_barrier"
                   :
                   : [m] "s"(0xff00ff0000ff00ffull), [a] "v"(pv.oa + smb), [o0] "v"(pv.O0), [o1] "v"(pv.O1), [o2] "v"(pv.O2),
                     [la] "v"(pv.la + smb), [l] "v"(pend), [mx] "v"(pmax)
                   : "memory");
      tref = now();
      if (half == 0) ar = (u32)(g & 3) * AREA + (((u32)(g >> 2) ^ __builtin_amdgcn_readfirstlane(*(volatile LAS u32*)(smem + L_VOTE))) & 1u) * SIDE;
      if (wave < 4) {
        const float* mp = reinterpret_cast<const float*>(smem + pv.ma);
        float m0 = mp[0], m1 = mp[4 * OM_S];
        f32x4 v = *reinterpret_cast<const f32x4*>(smem + pv.sa), v1 = *reinterpret_cast<const f32x4*>(smem + pv.sa + OM_P * 4);
        f32x4 u = *reinterpret_cast<const f32x4*>(smem + pv.sa + OM_S * 16);
        f32x4 u1 = *reinterpret_cast<const f32x4*>(smem + pv.sa + OM_S * 16 + OM_P * 4);
        asm volatile("" : "+v"(m0), "+v"(m1), "+v"(v), "+v"(v1), "+v"(u), "+v"(u1));
        v += v1;
        u += u1;
        float M = 0.f;
        if (__builtin_expect(__builtin_amdgcn_ballot_w64((__float_as_uint(m0) | __float_as_uint(m1)) != 0u) != 0ull, 0)) {
          M = fmx(m0, m1);
          M = fmx(M, dpp<XOR1>(M));
          M = fmx(M, dpp<XOR2>(M));
          v *= __builtin_amdgcn_exp2f(m0 - M);
          u *= __builtin_amdgcn_exp2f(m1 - M);
        }
        v = sum4x4(v + u);
        u32 p0 = cvt2(v[0], v[1]), p1 = cvt2(v[2], v[3]);
        if (wave == 3) {
          p0 = __float_as_uint(M);
          p1 = __float_as_uint(sum4(v));
        }
        p0 |= 1u;
        p1 |= 1u;
        const u32x4 pk = {p0, p1, dppu<XOR7>(p0), dppu<XOR7>(p1)};
        asm volatile("s_mov_b64 exec, %2\n\tbuffer_store_dwordx4 %0, %1, %3, %4 offen sc1\n\ts_mov_b64 exec, -1"
                     :
                     : "v"(pk), "v"(pv.dst), "s"(wave < 3 ? 0x0101010101010101ull : 0x3ull), "s"(rq), "s"(ar + (u32)half * GO_SZ)
                     : "memory");
      }
    }
    const u32 go = ar + A_GO + (u32)half * GO_SZ;
    const u32 gyw = A_GY + (u32)half * GY_PAIR;
    const u32 gy = gyw + ar - (u32)(g & 3 & ~YM) * AREA;
    const u32 gh = ar + A_GH + (u32)half * 1536u;
    const int land = L_LAND + wave * 2048;
    const u32 landa = (u32)(uintptr_t)(LAS unsigned char*)smem + (u32)land;
    if (isO) {
      {
        const int lane = fresh(lane0);
        const u32 voff = fresh(go + go_slot(wave) + ((lane * 16) ^ ((lane >> 4) * 64)));
        if (half)
          wait_rt(anc + A_O);
        else
          wait_until(tref + 24 * T_O);
        dma_poll<false>(rq, voff, landa, ~0ull, ~0ull);
        const int n = lane & 15, G = lane >> 4;
        const int r0_ = land + (n & 7) * 256 + (n & 3) * 64;
        const int ra = fresh(n < 8 ? r0_ + G * 16 : r0_ ^ 192), rb = fresh((r0_ + G * 16) ^ 64), rc = fresh((r0_ + G * 16) ^ 128);
        const int l0 = fresh(L_WB + wave * 6144 + wlane(lane, 0)), l1 = fresh(L_WB + wave * 6144 + wlane(lane, 1));
        asm volatile("s_waitcnt vmcnt(2)" ::: "memory");
        u32x4 wf[6];
#pragma unroll
        for (int k = 0; k < 3; ++k) {
          wf[2 * k] = wfrag(smem, l0, 2048 * k);
          wf[2 * k + 1] = wfrag(smem, l1, 2048 * k);
        }
        const int rw = fresh(L_RED + wave * 144 + lane * 4);
        const int rr = fresh(L_RED + (lane & 3) * 144 + ((lane >> 2) & 7) * 16);
        const u32 dst = fresh(gyw + gy_rep((lane & 3) + (lane >> 5) * 4) + gy_chunk(4 * j + ((lane >> 3) & 3)) + g * 256);
        f32x4 d0, d1;
        float mc, lc, t1_;
        u32 budget = SPIN_CAP;
#pragma unroll 1
        for (;;) {
          u64 need;
          u32 t;
          asm volatile("s_waitcnt vmcnt(0)\n\t"
                       "ds_read_b128 " F0 ", %[ra]\n\tds_read_b128 " F1 ", %[rb]\n\tds_read_b128 " F2 ", %[rc]\n\t"
                       "s_waitcnt lgkmcnt(2)\n\t"
                       MF "%[d0], " F0 ", %[w0], 0\n\t"
                       "v_add_f32_dpp %[l], v53, v53 quad_perm:[1,0,3,2] row_mask:0xf bank_mask:0xf\n\t"
                       MF "%[d1], " F0 ", %[w1], 0\n\t" AND3("v52", "v53", "v54")
                       "s_waitcnt lgkmcnt(1)\n\t"
                       MF "%[d0], " F1 ", %[w2], %[d0]\n\t"
                       "v_add_f32_dpp %[l], %[l], %[l] quad_perm:[2,3,0,1] row_mask:0xf bank_mask:0xf\n\t" AND3("%[t]", "v55", "v56")
                       MF "%[d1], " F1 ", %[w3], %[d1]\n\t" AND3("%[t]", "v57", "v58")
                       "v_add_f32_dpp %[l], %[l], %[l] row_half_mirror row_mask:0xf bank_mask:0xf\n\t"
                       "s_waitcnt lgkmcnt(0)\n\t"
                       MF "%[d0], " F2 ", %[w4], %[d0]\n\t" AND3("%[t]", "v59", "v60") AND3("%[t]", "v61", "v62")
                       MF "%[d1], " F2 ", %[w5], %[d1]\n\t" AND3("%[t]", "1", "v63")
                       "v_rcp_f32 %[l], %[l]\n\tv_cmp_eq_u32 %[need], 0, %[t]"
                       : [d0] "=&v"(d0), [d1] "=&v"(d1), [t] "=&v"(t), [need] "=&s"(need), [l] "=&v"(lc), "=&{v52}"(mc), "=&{v53}"(t1_)
                       : [ra] "v"(ra), [rb] "v"(rb), [rc] "v"(rc), [w0] "v"(wf[0]), [w1] "v"(wf[1]), [w2] "v"(wf[2]), [w3] "v"(wf[3]),
                         [w4] "v"(wf[4]), [w5] "v"(wf[5])
                       : "memory", "v54", "v55", "v56", "v57", "v58", "v59", "v60", "v61", "v62", "v63");
          if (__builtin_expect(need == 0ull || budget == 0u, 1)) break;
          --budget;
          u64 ea, eb;
          retry_masks<16>(need, lane, ea, eb);
          dma_poll<false>(rq, voff, landa, ea, eb);
        }
        float rl = __uint_as_float(__builtin_amdgcn_readlane(__float_as_uint(lc), 8));
        asm volatile("s_nop 1" : "+v"(d0), "+v"(d1));
        if (__builtin_expect((__builtin_amdgcn_ballot_w64(__float_as_uint(mc) != 1u) & 0xff00ull) != 0ull, 0)) {
          const float fc = __builtin_amdgcn_exp2f(mc - max8(mc));
          const u32 sc = __float_as_uint(fc * __builtin_amdgcn_rcpf(sum8(fc * t1_)));
#pragma unroll
          for (int e = 0; e < 4; ++e) {
            const float se = __uint_as_float(G == 0 ? __builtin_amdgcn_readlane(sc, 8 + e) : __builtin_amdgcn_readlane(sc, 12 + e));
            d0[e] *= se;
            d1[e] *= se;
          }
          rl = 1.0f;
        }
        {
          auto sw = __builtin_amdgcn_permlane16_swap(__float_as_uint(sum4(d0)), __float_as_uint(sum4(d1)), false, false);
          const float r = (__uint_as_float(sw[0]) + __uint_as_float(sw[1])) * rl;
          if (lane < 32) *reinterpret_cast<float*>(smem + rw) = r;
        }
        bar();
        if (wave == 0) {
          const f32x4 y = sum4x4(*reinterpret_cast<const f32x4*>(smem + rr) + *reinterpret_cast<const f32x4*>(smem + rr + 576));
          const u32 p0 = cvt2(y[0], y[1]) | 1u, p1 = cvt2(y[2], y[3]) | 1u;
          const u32x4 pk = {p0, p1, dppu<XOR7>(p0), dppu<XOR7>(p1)};
          asm volatile("s_mov_b64 exec, %2\n\tbuffer_store_dwordx4 %0, %1, %3, 0 offen sc1\n\ts_mov_b64 exec, -1"
                       :
                       : "v"(pk), "v"(dst), "s"(0x0101010101010101ull * ((2u << YM) - 1u)), "s"(rq)
                       : "memory");
        }
      }
      tref = now();
      if (half == 0) {
        if (wave >= 1 && wave < 7) {
          const u16* s0 = karg<56>() + (size_t)(192 * g) * D + 32 * j;
#pragma unroll
          for (int p = 0; p < 2; ++p) dma_piece(s0, D, smem, L_W2, (wave - 1) * 2 + p, fresh(lane0));
          asm volatile("s_waitcnt vmcnt(0)" ::: "memory");
        }
        bar();
        if (wave < 6) cs_gather(smem, rq, fresh(lane0), wave, h);
        if (wave == 4) {
          wait_until(tref + 24 * T_H);
          stamp_poll(smem, rq, ar + st_line(fresh(lane0) & 7));
        }
        if (wave == 5) {
          wait_until(tref + 24 * T_LAZY);
          x_update<false>(smem, rq, fresh(lane0), gy + gy_row(c), c);
        }
      } else if (g == 0 && wave == 2) {
        const int lane = fresh(lane0);
        const int col = j * 32 + (lane & 7) * 4 + ((lane >> 4) & 1) * 2 + (lane >> 5);
        u32 x0v, dummy;
        u16* const po = karg<64>() + col;
        asm volatile("global_load_ushort %0, %2, off\n\tglobal_load_ushort %1, %3, off\n\ts_waitcnt vmcnt(0)"
                     : "=&v"(x0v), "=&v"(dummy)
                     : "v"(karg<0>() + col), "v"(po)
                     : "memory");
        wait_until(tref + 24 * T_LAZY);
        const u32 o = gy + gy_chunk(4 * j + ((lane >> 1) & 3)) + (lane >> 3) * 256 + (lane & 1) * 8;
        const u32 off[3] = {o - GY_PAIR, o - GY_PAIR + GY_INST, o};
        float y[3];
        col_poll<3>(rq, off, y);
        const float x3 = bf16r(bf16r(bf16r(bflo(x0v) + y[0]) + y[1]) + y[2]);
        const u32 off2[1] = {o + GY_INST};
        wait_rt(anc + A_F);
        float y4[1];
        col_poll<1>(rq, off2, y4);
        const float x4 = x3 + y4[0];
        if ((lane & 8) == 0) asm volatile("global_store_short %0, %1, off" ::"v"(po), "v"(cvt2(x4, x4)) : "memory");
      }
      if (wave >= 6) {
        const int lane = fresh(lane0), wv = wave - 6;
        const int n = lane & 15, G = lane >> 4;
        const int l0 = fresh(L_W2 + wlane(lane, wv));
        u32x4 wf[6];
#pragma unroll
        for (int k = 0; k < 6; ++k) wf[k] = wfrag(smem, l0, 2048 * k);
        const int l48 = lane < 48 ? lane : 0;
        const u32 off[2] = {gh + (u32)l48 * 16, gh + (u32)l48 * 16 + 768};
        const int aw = fresh(L_ACT + wv * 384 + l48 * 8);
        const int ab = fresh(L_ACT + wv * 384 + 16 * G);
        const u32 dst = fresh(gyw + GY_INST + gy_rep(n & 7) + gy_chunk(4 * j + wv * 2 + (G >> 1)) + g * 256);
        f32x4 v[2];
        if (half)
          wait_rt(anc + A_H);
        else
          wait_until(tref + 24 * T_H);
        gpoll4<2>(rq, off, v);
        {
          f32x4 a, u;
#pragma unroll
          for (int e = 0; e < 4; ++e) {
            a[e] = bfhi(__float_as_uint(v[0][e]));
            u[e] = bfhi(__float_as_uint(v[1][e]));
          }
          const f32x4 hh = a * u;
          *reinterpret_cast<u32x2*>(smem + aw) = u32x2{cvt2(hh[0], hh[1]), cvt2(hh[2], hh[3])};
        }
        u32x4 af[4];
#pragma unroll
        for (int k = 0; k < 4; ++k) af[k] = afrag(smem, ab + 64 * k);
        asm volatile("" : "+v"(af[0]), "+v"(af[1]), "+v"(af[2]), "+v"(af[3]));
        f32x4 d = {0.f, 0.f, 0.f, 0.f};
        d = mfma32(wf[0], af[0], d);
        d = mfma32(wf[1], af[1], d);
        asm volatile("" : "+v"(d)::"memory");
        af[0] = afrag(smem, ab + 256);
        af[1] = afrag(smem, ab + 320);
        d = mfma32(wf[2], af[2], d);
        d = mfma32(wf[3], af[3], d);
        d = mfma32(wf[4], af[0], d);
        d = mfma32(wf[5], af[1], d);
        const u32 p0 = cvt2(d[0], d[1]) | 1u, p1 = cvt2(d[2], d[3]) | 1u;
        auto s0 = __builtin_amdgcn_permlane16_swap(p0, p0, false, false);
        auto s1 = __builtin_amdgcn_permlane16_swap(p1, p1, false, false);
        if ((lane & (half ? 27 : 27 - YM)) == 0) store16(rs, dst, u32x4{s0[0], s1[0], s0[1], s1[1]});
      }
    } else {
      const int lane = fresh(lane0);
      const int n = lane & 15, G = lane >> 4;
      if (half == 1) {
        x_update<false>(smem, rq, lane, gy - GY_PAIR + GY_INST + gy_row(wave), wave);
      } else if (NINF + NPRE < 5) {
        tnext += TFD2;
#pragma unroll 1
        for (int p = NINF + NPRE; p < 5; ++p) {
          pace(tnext, TFL);
          dma_piece(ws0, F, smem, L_WB, wave * 5 + p, fresh(lane0));
        }
      }
      asm volatile("s_waitcnt vmcnt(0) lgkmcnt(0)" ::: "memory");
      G80 a;
      g80_load<512>(a, smem, lane, L_WB + wave * 5120, L_HB + 160 * wave, land);
      const u32 voff = fresh(gy + gy_row(wave) + ((lane * 16) ^ ((lane >> 4) * 64)));
      const int rw = fresh(L_RED + n * 144 + wave * 16 + G * 4);
      const int sw_ = fresh(n == 0 ? L_ST + wave * 16 + G * 4 : L_ACT);
      float r0, r1, z;
      if (half)
        wait_rt(anc + A_Y);
      else
        wait_until(tref + 24 * T_YF);
      g80_run<true>(a, smem, rq, lane, voff, landa, r0, r1, z);
      *reinterpret_cast<float*>(smem + rw) = r0;
      *reinterpret_cast<float*>(smem + rw + 16 * 144) = r1;
      *reinterpret_cast<float*>(smem + sw_) = z;
      const int ln = fresh(lane0);
      const int rr = fresh(L_RED + (ln & 31) * 144 + (ln >> 5) * 64), rs_ = fresh(L_ST + 16 * (ln & 7));
      const u32 dst = fresh(gh + fslot + ln * 4);
      bar();
      tref = now();
      if (half == 0 && wave == 1 && j == 20 && ln < 8)
        __builtin_amdgcn_raw_buffer_store_b32((rtnow() << 1) | 1u, rs, gy_rep(ln) + st_line(g), 0, 16);
      if (wave == 0) {
        f32x4 st = *reinterpret_cast<const f32x4*>(smem + rs_);
        f32x4 q0 = *reinterpret_cast<const f32x4*>(smem + rr), q1 = *reinterpret_cast<const f32x4*>(smem + rr + 16);
        f32x4 q2 = *reinterpret_cast<const f32x4*>(smem + rr + 32), q3 = *reinterpret_cast<const f32x4*>(smem + rr + 48);
        asm volatile("" : "+v"(st), "+v"(q0), "+v"(q1), "+v"(q2), "+v"(q3));
        const LnS ls = ln_stats(st[3], st[0] + st[1] + st[2]);
        const f32x4 qs = (q0 + q1) + (q2 + q3);
        float r = fmaf(-ls.mb, qs[3], qs[0] + qs[1] + qs[2]);
        auto sw = __builtin_amdgcn_permlane32_swap(__float_as_uint(r), __float_as_uint(r), false, false);
        r = bf16r((__uint_as_float(sw[0]) + __uint_as_float(sw[1])) * ls.ri);
        if ((j & 1) == 0) r = bf16r(r * __builtin_amdgcn_rcpf(1.0f + __builtin_amdgcn_exp2f(r * -1.4426950408889634f)));
        const f32x4 qd = quad4(r);
        if ((ln & 35) == 0) store_quad(rs, dst, qd);
      }
      if (half == 0) {
        x_update<true>(smem, rq, fresh(lane0), land, wave);
        if (wave < 6) cs_gather(smem, rq, fresh(lane0), wave, h);
        if (wave == 7) {
          wait_until(tref + 24 * T_AN);
          stamp_poll(smem, rq, ar + st_line(ln & 7));
        }
      }
    }
    if (half == 1) break;
    {
      if (wave < 6) {
        const int lane = fresh(lane0);
        const int n = lane & 15;
        G80 a;
        g80_load<1024>(a, smem, lane, L_WQ + (wave >> 1) * 10240 + (wave & 1) * 512, L_HB + 160 * c, land);
        const u32 voff = fresh(gy + GY_INST + gy_row(c) + ((lane * 16) ^ ((lane >> 4) * 64)));
        const float cc = *reinterpret_cast<const float*>(smem + L_CS + wave * 128 + (lane >> 5) * 64 + n * 4);
        const u32 gq = ar + A_GQ + hsub * 16384 + c * 896;
        const u32 dst = fresh(gq + wave * 128 + (lane >> 5) * 64 + n * 4);
        float r0, r1, z;
        bar();
        wait_rt(__builtin_amdgcn_readfirstlane(*(volatile LAS u32*)(smem + L_ANC)) + A_E);
        g80_run<true>(a, smem, rq, lane, voff, landa, r0, r1, z);
        auto sw = __builtin_amdgcn_permlane32_swap(__float_as_uint(r0), __float_as_uint(r1), false, false);
        const float sh = cc * __uint_as_float(__builtin_amdgcn_readlane(__float_as_uint(z), 48));
        float r = __uint_as_float(sw[0]) +
                  __uint_as_float((u32)__builtin_amdgcn_update_dpp((int)sw[1], (int)__float_as_uint(sh), 0xE4, 0xA, 0xf, false));
        auto sx = __builtin_amdgcn_permlane16_swap(__float_as_uint(r), __float_as_uint(r), false, false);
        r = __uint_as_float(sx[0]) + __uint_as_float(sx[1]);
        const f32x4 qd = quad4(r);
        if ((lane & 19) == 0) store_quad(rs, dst, qd);
        if (wave == 5) {
          const u32 zu = __float_as_uint(z);
          const f32x4 zq = {__uint_as_float(__builtin_amdgcn_readlane(zu, 0)), __uint_as_float(__builtin_amdgcn_readlane(zu, 16)),
                            __uint_as_float(__builtin_amdgcn_readlane(zu, 32)), __uint_as_float(__builtin_amdgcn_readlane(zu, 48))};
          if (lane == 0) store_quad(rs, gq + 768, zq);
        }
      } else {
        bar();
      }
    }
  }
  asm volatile(".Lmk_code_end:");
}

}

extern "C" void kernel_launch(void* const* d_in, const int* in_sizes, int n_in, void* d_out, int out_size, void* d_ws,
                              size_t ws_size, hipStream_t stream) {
  Params P;
  P.x0 = (const u16*)d_in[0];
  P.kc = (const u16*)d_in[1];
  P.vc = (const u16*)d_in[2];
  P.wq = (const u16*)d_in[3];
  P.wo = (const u16*)d_in[4];
  P.w1 = (const u16*)d_in[6];
  P.w3 = (const u16*)d_in[7];
  P.w2 = (const u16*)d_in[8];
  P.out = (u16*)d_out;
  P.ws = (u32*)d_ws;
  mega_kernel<<<NWG, NT, 0, stream>>>(P);
}
```

```cpp
#define TSTEP 235
#include <hip/hip_runtime.h>
#include <hip/hip_bf16.h>
#include <stdint.h>

namespace {

typedef unsigned int u32;
typedef unsigned short u16;
typedef unsigned long long u64;
using u32x2 = __attribute__((ext_vector_type(2))) unsigned;
using u32x4 = __attribute__((ext_vector_type(4))) unsigned;
using f32x2 = __attribute__((ext_vector_type(2))) float;
using f32x4 = __attribute__((ext_vector_type(4))) float;
using s16x4 = __attribute__((ext_vector_type(4))) short;
using bf16x2 = __attribute__((ext_vector_type(2))) __bf16;
using bf16x8 = __attribute__((ext_vector_type(8))) __bf16;
#define LAS __attribute__((address_space(3)))

constexpr int D = 640, H = 32, DH = 192, WIN = 4096, SK = 32768, F = 1536;
constexpr int NWG = 256, NT = 512;
constexpr u32 SPIN_CAP = 1u << 16;
constexpr float SCL = 0.07216878364870322f * 1.4426950408889634f;

#define T_O 12
#define T_YF 88
#define T_H 95
#define T_AN 35
#define PTHR 60
#define T_LAZY 60

#define A_E 104
#define A_Q (A_E + 78)
#define A_O (A_Q + 142)
#define A_Y (A_O + 87)
#define A_H (A_Y + 82)
#define A_F (A_H + 50)

#define X_O 144
#define X_Y (X_O + 93)
#define X_H (X_Y + 85)

constexpr u32 SIDE = 8192, AREA = 17 * 16384;
constexpr u32 A_GO = 0;
constexpr u32 A_GY = 2 * 16384;
constexpr u32 A_GQ = 6 * 16384;
constexpr u32 A_GH = 16 * 16384;
constexpr u32 GO_SZ = 10 * 16384, GY_PAIR = GO_SZ, GY_INST = 32768;
constexpr u32 WS_PB = 4 * AREA;
constexpr u32 WS_GOF = WS_PB + (128 << 10), WS_HB = WS_GOF + 4096;
constexpr u32 WS_GQ1 = WS_HB + 256 * 128;
constexpr u32 WS_END = WS_GQ1 + 256 * 2048;
static_assert(WS_END <= (2u << 20), "layout");
constexpr u32 OOB = 1u << 30;
__device__ __forceinline__ u32 go_slot(int w) { return (u32)(w >> 2) * 16384u + (u32)(w & 3) * 2048u; }
__device__ __forceinline__ u32 gy_row(int i) { return go_slot(i); }
__device__ __forceinline__ u32 gy_chunk(int u) {
  const int i = (u * 205) >> 11;
  return gy_row(i) + (u32)(u - 10 * i) * 16u;
}
__device__ __forceinline__ u32 st_line(int g) { return A_GH + 3072u + (u32)g * 128u; }
__device__ __forceinline__ u32 gy_rep(int r) { return (u32)(r & 3) * AREA + (u32)(r >> 2) * SIDE; }

constexpr int L_ZERO = 0;
constexpr int L_CNT = 16;
constexpr int L_RI = 28;
constexpr int L_ONE = 32;
constexpr int L_TAG = 48;
constexpr int L_ST = 64;
constexpr int L_VOTE = L_ST;
constexpr int L_ANC = 192;
constexpr int L_Q = 256;
constexpr int L_CS = 640;
constexpr int L_HB = L_CS + 768;
constexpr int L_ACT = L_HB + D * 2;
constexpr int L_RED = L_ACT + 2 * 384;
constexpr int L_P = L_RED + 32 * 144;
constexpr int P_W = 1280, P_U = 592, P_PAR = 288;
constexpr int OM_S = 400, OM_P = 200;
constexpr int L_OM = L_P + 8 * P_W;
constexpr int L_LN = L_OM;
constexpr int L_LAND = (L_OM + 8 * OM_S * 4 + 255) / 256 * 256;
constexpr int L_CTL = L_LAND + 6 * 2048;
constexpr int L_WQ = L_LAND + 8 * 2048;
constexpr int L_WB = L_WQ + 6 * 5120;
constexpr int L_W2 = L_WB + 768 * 64;
constexpr int L_PF = L_W2 + 192 * 64;
constexpr int L_TAB = L_PF + 256;
constexpr int L_TOTAL = L_TAB + 8 * 2048;
static_assert(L_LAND % 256 == 0 && L_P % 16 == 0 && L_WQ % 16 == 0 && L_HB % 16 == 0 && L_RED % 16 == 0, "alignment");

struct Params {
  const u16* x0;
  const u16* kc;
  const u16* vc;
  const u16* wq;
  const u16* wo;
  const u16* w1;
  const u16* w3;
  const u16* w2;
  u16* out;
  u32* ws;
};

__device__ __forceinline__ u32 cvt2(float lo, float hi) {
  return __builtin_bit_cast(u32, __builtin_convertvector(f32x2{lo, hi}, bf16x2));
}
__device__ __forceinline__ float bflo(u32 w) { return __uint_as_float(w << 16); }
__device__ __forceinline__ float bfhi(u32 w) { return __uint_as_float(w & 0xffff0000u); }
__device__ __forceinline__ float bf16r(float x) { return bflo(cvt2(x, x)); }

template <int CTRL>
__device__ __forceinline__ float dpp(float x) {
  return __builtin_bit_cast(float, __builtin_amdgcn_mov_dpp(__builtin_bit_cast(int, x), CTRL, 0xf, 0xf, true));
}
constexpr int XOR1 = 0xB1, XOR2 = 0x4E, XOR7 = 0x141, ROR8 = 0x128;
__device__ __forceinline__ float fmx(float a, float b) {
  float r;
  asm("v_max_f32 %0, %1, %2" : "=v"(r) : "v"(a), "v"(b));
  return r;
}
__device__ __forceinline__ float xrow16_max(float x) {
  auto s = __builtin_amdgcn_permlane16_swap(__float_as_uint(x), __float_as_uint(x), false, false);
  x = fmx(__uint_as_float(s[0]), __uint_as_float(s[1]));
  auto t = __builtin_amdgcn_permlane32_swap(__float_as_uint(x), __float_as_uint(x), false, false);
  return fmx(__uint_as_float(t[0]), __uint_as_float(t[1]));
}
__device__ __forceinline__ float xrow16_sum(float x) {
  auto s = __builtin_amdgcn_permlane16_swap(__float_as_uint(x), __float_as_uint(x), false, false);
  x = __uint_as_float(s[0]) + __uint_as_float(s[1]);
  auto t = __builtin_amdgcn_permlane32_swap(__float_as_uint(x), __float_as_uint(x), false, false);
  return __uint_as_float(t[0]) + __uint_as_float(t[1]);
}
__device__ __forceinline__ float sum8(float v) {
  v += dpp<XOR1>(v);
  v += dpp<XOR2>(v);
  v += dpp<XOR7>(v);
  return v;
}
__device__ __forceinline__ float max8(float v) {
  asm("s_nop 1\n\tv_max_f32_dpp %0, %0, %0 quad_perm:[1,0,3,2] row_mask:0xf bank_mask:0xf\n\t"
      "s_nop 1\n\tv_max_f32_dpp %0, %0, %0 quad_perm:[2,3,0,1] row_mask:0xf bank_mask:0xf\n\t"
      "s_nop 1\n\tv_max_f32_dpp %0, %0, %0 row_half_mirror row_mask:0xf bank_mask:0xf"
      : "+v"(v));
  return v;
}
__device__ __forceinline__ float max_ror8(float v) {
  asm("s_nop 1\n\tv_max_f32_dpp %0, %0, %0 row_ror:8 row_mask:0xf bank_mask:0xf" : "+v"(v));
  return v;
}
__device__ __forceinline__ f32x4 sum4x4(f32x4 v) {
  float a = v[0], b = v[1], c = v[2], d = v[3];
#define S4(CTL)                                               \
  "v_add_f32_dpp %0, %0, %0 " CTL " row_mask:0xf bank_mask:0xf\n\t" \
  "v_add_f32_dpp %1, %1, %1 " CTL " row_mask:0xf bank_mask:0xf\n\t" \
  "v_add_f32_dpp %2, %2, %2 " CTL " row_mask:0xf bank_mask:0xf\n\t" \
  "v_add_f32_dpp %3, %3, %3 " CTL " row_mask:0xf bank_mask:0xf\n\t"
  asm("s_nop 1\n\t" S4("quad_perm:[1,0,3,2]") S4("quad_perm:[2,3,0,1]") : "+v"(a), "+v"(b), "+v"(c), "+v"(d));
#undef S4
  return f32x4{a, b, c, d};
}
__device__ __forceinline__ float wave_sum(float v) {
  v = sum8(v);
  v += dpp<ROR8>(v);
  return xrow16_sum(v);
}
__device__ __forceinline__ float tsum8(f32x4 a) {
  auto s0 = __builtin_amdgcn_permlane32_swap(__float_as_uint(a[0]), __float_as_uint(a[1]), false, false);
  auto s1 = __builtin_amdgcn_permlane32_swap(__float_as_uint(a[2]), __float_as_uint(a[3]), false, false);
  const float r0 = __uint_as_float(s0[0]) + __uint_as_float(s0[1]);
  const float r1 = __uint_as_float(s1[0]) + __uint_as_float(s1[1]);
  auto t = __builtin_amdgcn_permlane16_swap(__float_as_uint(r0), __float_as_uint(r1), false, false);
  float r = __uint_as_float(t[0]) + __uint_as_float(t[1]);
  r += dpp<ROR8>(r);
  return r;
}

__device__ __forceinline__ u32 tagv(float v) { return __float_as_uint(v) | 1u; }
__device__ __forceinline__ void store_quad(__amdgpu_buffer_rsrc_t rs, u32 off, f32x4 v) {
  const u32x4 s = {tagv(v[0]), tagv(v[1]), tagv(v[2]), tagv(v[3])};
  __builtin_amdgcn_raw_buffer_store_b128(s, rs, off, 0, 16);
}
template <int N>
__device__ __forceinline__ void gpoll4(u32x4 rs, const u32 (&off)[N], f32x4 (&v)[N]) {
  bool need = true;
  u32 budget = SPIN_CAP;
#pragma unroll
  for (int k = 0; k < N; ++k) asm volatile("" : "=v"(v[k]));
#pragma unroll 1
  for (;;) {
    if (need) {
#pragma unroll
      for (int k = 0; k < N; ++k)
        asm volatile("buffer_load_dwordx4 %0, %1, %2, 0 offen sc1" : "+v"(v[k]) : "v"(off[k]), "s"(rs) : "memory");
    }
    asm volatile("s_waitcnt vmcnt(0)" ::: "memory");
    u32 a = 1u;
#pragma unroll
    for (int k = 0; k < N; ++k) {
      asm volatile("" : "+v"(v[k]));
      a &= __float_as_uint(v[k][0]) & __float_as_uint(v[k][1]) & __float_as_uint(v[k][2]) & __float_as_uint(v[k][3]);
    }
    need = a == 0u;
    if (__builtin_amdgcn_ballot_w64(need) == 0ull || budget == 0u) break;
    --budget;
    __builtin_amdgcn_s_sleep(1);
  }
}
__device__ __forceinline__ u32 now() { return (u32)__builtin_amdgcn_s_memtime(); }
__device__ __forceinline__ void wait_until(u32 t) {
#pragma unroll 1
  while ((int)(now() - t) < -160) __builtin_amdgcn_s_sleep(1);
#pragma unroll 1
  while ((int)(now() - t) < 0) {
  }
}
__device__ __forceinline__ u32 rtnow() { return (u32)__builtin_amdgcn_s_memrealtime(); }
#define WAIT_RT                                                                                                       \
  "s_branch 1f\n0:\n\ts_cmp_lt_i32 s98, -8\n\ts_cbranch_scc0 1f\n\ts_sleep 1\n"                                        \
  "1:\n\ts_memrealtime s[98:99]\n\ts_waitcnt lgkmcnt(0)\n\ts_sub_i32 s98, s98, %[tt]\n\t"                              \
  "s_cmp_ge_u32 s98, 0xfffffc00\n\ts_cbranch_scc1 0b\n\t"
__device__ __forceinline__ void wait_rt(u32 t) { asm volatile(WAIT_RT ::[tt] "s"(t) : "scc", "s98", "s99", "memory"); }
template <int BITS>
__device__ __forceinline__ u32 youngest(u32 t);
__device__ __forceinline__ void lds_wait(volatile LAS u32* p, u32 want) {
  u32 spins = 0;
#pragma unroll 1
  while (__builtin_amdgcn_readfirstlane(*p) != want) {
    if (++spins > (SPIN_CAP << 6)) break;
    __builtin_amdgcn_s_sleep(1);
  }
  asm volatile("" ::: "memory");
}
__device__ __forceinline__ u32 lds_wait_nz(volatile LAS u32* p) {
  u32 spins = 0, v;
#pragma unroll 1
  while ((v = __builtin_amdgcn_readfirstlane(*p)) == 0u) {
    if (++spins > (SPIN_CAP << 6)) break;
    __builtin_amdgcn_s_sleep(1);
  }
  asm volatile("" ::: "memory");
  return v;
}
template <int BITS>
__device__ __forceinline__ u32 youngest(u32 t) {
  const u32 nw = rtnow();
  const float age = (float)((nw - t) & ((1u << BITS) - 1u));
  return nw - (u32)__builtin_amdgcn_readfirstlane((int)-xrow16_max(max_ror8(max8(-age))));
}
__device__ __forceinline__ void stamp_poll(unsigned char* smem, u32x4 rq, u32 so) {
  u32 sv, budget = SPIN_CAP;
#pragma unroll 1
  for (;;) {
    asm volatile("buffer_load_dword %0, %1, %2, 0 offen sc1\n\ts_waitcnt vmcnt(0)" : "=&v"(sv) : "v"(so), "s"(rq) : "memory");
    if (__builtin_amdgcn_ballot_w64((sv & 1u) == 0u) == 0ull || budget == 0u) break;
    --budget;
    __builtin_amdgcn_s_sleep(1);
  }
  *(volatile LAS u32*)(smem + L_ANC) = youngest<24>(sv >> 1) | 1u;
}
__device__ __forceinline__ void lds_add(unsigned char* smem, int off, u32 v) {
  __hip_atomic_fetch_add((LAS u32*)(smem + off), v, __ATOMIC_RELAXED, __HIP_MEMORY_SCOPE_WORKGROUP);
}

__device__ __forceinline__ int fresh(int t) {
  asm volatile("" : "+v"(t));
  return t;
}
__device__ __forceinline__ f32x4 zero4() {
  f32x2 a, b;
  asm volatile("v_mov_b64 %0, 0\n\tv_mov_b64 %1, 0" : "=v"(a), "=v"(b));
  return f32x4{a[0], a[1], b[0], b[1]};
}
__device__ __forceinline__ void bar() { asm volatile("s_waitcnt lgkmcnt(0)\n\ts_barrier" ::: "memory"); }

__device__ __forceinline__ u32x2 trread(unsigned char* smem, int off) {
  return __builtin_bit_cast(u32x2, __builtin_amdgcn_ds_read_tr16_b64_v4i16((LAS s16x4*)(smem + off)));
}
__device__ __forceinline__ u32x4 wfrag(unsigned char* smem, int lb, int off) {
  const u32x2 a = trread(smem, lb + off), b = trread(smem, lb + off + 256);
  return u32x4{a[0], a[1], b[0], b[1]};
}
template <int GS = 512>
__device__ __forceinline__ int wlane(int lane, int cb) {
  const int i = lane & 15, G = lane >> 4;
  return G * GS + (i >> 2) * 64 + (i & 3) * 8 + ((cb ^ (G & 1)) * 32);
}
__device__ __forceinline__ u32x4 afrag(unsigned char* smem, int a) { return *reinterpret_cast<const u32x4*>(smem + a); }
__device__ __forceinline__ f32x4 mfma32(u32x4 w, u32x4 a, f32x4 c) {
  return __builtin_amdgcn_mfma_f32_16x16x32_bf16(__builtin_bit_cast(bf16x8, w), __builtin_bit_cast(bf16x8, a), c, 0, 0, 0);
}

__device__ __forceinline__ void dma_kib(const u16* src, u32 voff, unsigned char* smem, int ldsoff) {
  const u32 dst = (u32)(uintptr_t)(LAS unsigned char*)smem + (u32)ldsoff;
  u32 keep;
  asm volatile("s_mov_b32 %0, m0\n\ts_mov_b32 m0, %2\n\ts_nop 2\n\tglobal_load_lds_dwordx4 %1, %3 nt\n\ts_mov_b32 m0, %0"
               : "=&s"(keep)
               : "v"(voff), "s"(dst), "s"(src)
               : "memory");
}
__device__ __forceinline__ void dma_piece(const u16* src, int ld, unsigned char* smem, int ldsoff, int p, int lane) {
  const int row = 16 * p + (lane >> 2), part = lane & 3;
  const int cb = (part >> 1) ^ (lane >> 5);
  dma_kib(src, (u32)(row * ld + cb * 16 + (part & 1) * 8) * 2u, smem, ldsoff + p * 1024);
}

template <int CTRL>
__device__ __forceinline__ u32 dppu(u32 x) {
  return (u32)__builtin_amdgcn_mov_dpp((int)x, CTRL, 0xf, 0xf, true);
}
__device__ __forceinline__ float sum4(f32x4 d) { return (d[0] + d[1]) + (d[2] + d[3]); }
__device__ __forceinline__ u32 and4(u32x4 v) { return v[0] & v[1] & v[2] & v[3]; }
__device__ __forceinline__ void store16(__amdgpu_buffer_rsrc_t rs, u32 off, u32x4 v) {
  __builtin_amdgcn_raw_buffer_store_b128(v, rs, off, 0, 16);
}
#define POLL16(dst, voff, imm) \
  asm volatile("buffer_load_dwordx4 %0, %1, %2, 0 offen offset:%3 sc1" : "+v"(dst) : "v"(voff), "s"(rq), "n"(imm) : "memory")

struct LnS {
  float mb, ri;
};
__device__ __forceinline__ LnS ln_stats(float s1, float s2) {
#define S2(CTL)                                                     \
  "v_add_f32_dpp %0, %0, %0 " CTL " row_mask:0xf bank_mask:0xf\n\t" \
  "v_add_f32_dpp %1, %1, %1 " CTL " row_mask:0xf bank_mask:0xf\n\ts_nop 0\n\t"
  asm("s_nop 1\n\t" S2("quad_perm:[1,0,3,2]") S2("quad_perm:[2,3,0,1]") S2("row_half_mirror") : "+v"(s1), "+v"(s2));
#undef S2
  const float dm = s1 * (1.0f / D);
  const u32 mv = cvt2(dm, __builtin_fmaxf(fmaf(-dm, dm, s2 * (1.0f / D)), 0.f));
  LnS r;
  r.mb = bflo(mv);
  r.ri = __builtin_amdgcn_rcpf(bf16r(__builtin_amdgcn_sqrtf(bf16r(bfhi(mv) + 1e-5f))));
  return r;
}

struct G80 {
  u32x4 w[6];
  int ra, rb, rc;
};
template <int GS>
__device__ __forceinline__ void g80_load(G80& a, unsigned char* smem, int lane, int wb, int xs, int land) {
  const int m = lane & 15, G = lane >> 4;
  const int l0 = wb + wlane<GS>(lane, 0), l1 = wb + wlane<GS>(lane, 1), bk = 8 * GS - (G >> 1) * 2 * GS;
  a.w[0] = wfrag(smem, l0, 0);
  a.w[1] = wfrag(smem, l1, 0);
  a.w[2] = wfrag(smem, l0, 4 * GS);
  a.w[3] = wfrag(smem, l1, 4 * GS);
  a.w[4] = wfrag(smem, l0 + bk, 0);
  a.w[5] = wfrag(smem, l1 + bk, 0);
  const bool vec = m == 8 || (m < 8 && land >= 0);
  const int st = vec ? 64 : 0;
  const int sa = vec ? (m == 8 ? xs : land + 256 * m) + 16 * G : (m == 15 ? L_ONE : L_ZERO);
  const int sw = (vec && m < 8) ? (m & 3) * 64 : 0;
  a.ra = fresh(sa ^ sw);
  a.rb = fresh((sa + st) ^ sw);
  a.rc = fresh(G < 2 ? (sa + 2 * st) ^ sw : L_TAG);
}
template <bool WAIT = true>
__device__ __forceinline__ void dma_poll(u32x4 rq, u32 voff, u32 land, u64 ea, u64 eb) {
  u32 keep;
  asm volatile("s_mov_b32 %0, m0\n\ts_mov_b32 m0, %3\n\ts_mov_b64 exec, %4\n\t"
               "buffer_load_dwordx4 %1, %2, 0 offen sc1 lds\n\t"
               "s_mov_b64 exec, %5\n\t"
               "buffer_load_dwordx4 %1, %2, 0 offen offset:1024 sc1 lds\n\t"
               "s_mov_b64 exec, -1\n\ts_mov_b32 m0, %0"
               : "=&s"(keep)
               : "v"(voff), "s"(rq), "s"(land), "s"(ea), "s"(eb)
               : "memory");
  if (WAIT) asm volatile("s_waitcnt vmcnt(0)" ::: "memory");
}
template <bool X4>
__device__ __forceinline__ void ctl_load(u32x4 rq, u32 voff, u32 lds) {
  u32 keep;
  if (X4)
    asm volatile("s_mov_b32 %0, m0\n\ts_mov_b32 m0, %3\n\ts_nop 2\n\tbuffer_load_dwordx4 %1, %2, 0 offen sc1 lds\n\ts_mov_b32 m0, %0"
                 : "=&s"(keep) : "v"(voff), "s"(rq), "s"(lds) : "memory");
  else
    asm volatile("s_mov_b32 %0, m0\n\ts_mov_b32 m0, %3\n\ts_nop 2\n\tbuffer_load_dword %1, %2, 0 offen sc1 lds\n\ts_mov_b32 m0, %0"
                 : "=&s"(keep) : "v"(voff), "s"(rq), "s"(lds) : "memory");
}
template <int UN>
__device__ __forceinline__ void retry_masks(u64 need, int lane, u64& ea, u64& eb) {
  u32 m = (u32)need | (u32)(need >> 32);
  m |= m >> 16;
  m = (m | (m >> 8)) & 0xffu;
  const int ln = fresh(lane);
  ea = __builtin_amdgcn_ballot_w64(((m >> (ln / UN)) & 1u) != 0u);
  eb = __builtin_amdgcn_ballot_w64(((m >> ((ln + 64) / UN)) & 1u) != 0u);
}
#define MF "v_mfma_f32_16x16x32_bf16 "
#define F0 "v[52:55]"
#define F1 "v[56:59]"
#define F2 "v[60:63]"
#define FCLOB "v52", "v53", "v54", "v55", "v56", "v57", "v58", "v59", "v60", "v61", "v62", "v63"
#define AND3(a, b, c) "v_bitop3_b32 %[t], " a ", " b ", " c " bitop3:0x80\n\t"
#define BREP "s_bitreplicate_b64_b32 "
#define USEX "s_and_b32 exec_lo, exec_lo, 0x30ff03ff\n\ts_and_b32 exec_hi, exec_hi, 0xff30ff03\n\t"
constexpr u64 USED = 0xff30ff0330ff03ffull;
#define POLL_HEAD(FOLD, MASK)                                                                                        \
  "s_mov_b32 %[bud], 0x10000\n\ts_branch 2f\n"                                                                       \
  "1:\n\ts_sub_u32 %[bud], %[bud], 1\n\ts_cbranch_scc1 3f\n\t"                                                       \
  "s_or_b32 s98, vcc_lo, vcc_hi\n\ts_lshr_b32 s99, s98, 16\n\ts_or_b32 s98, s98, s99\n\t" FOLD                       \
  BREP "s[98:99], s98\n\t" BREP "s[98:99], s98\n\t" BREP "s[98:99], s98\n\t"                                         \
  "s_mov_b32 vcc_lo, m0\n\ts_mov_b32 m0, %[land]\n\t" BREP "exec, s98\n\t" MASK                                      \
  "buffer_load_dwordx4 %[vo], %[rq], 0 offen sc1 lds\n\t" BREP "exec, s99\n\t" MASK                                  \
  "buffer_load_dwordx4 %[vo], %[rq], 0 offen offset:1024 sc1 lds\n\t"                                                \
  "s_mov_b64 exec, -1\n\ts_mov_b32 m0, vcc_lo\n"                                                                     \
  "2:\n\ts_waitcnt vmcnt(0)\n\t"                                                                                     \
  "ds_read_b128 " F0 ", %[ra]\n\tds_read_b128 " F1 ", %[rb]\n\tds_read_b128 " F2 ", %[rc]\n\t"
#define R0 "v[40:43]"
#define R1 "v[44:47]"
#define R2 "v[48:51]"
#define DCLOB "v40", "v41", "v42", "v43", "v44", "v45", "v46", "v47", "v48", "v49", "v50", "v51"
#define ZD(CTL, BANK) "v_add_f32_dpp v48, v48, v48 " CTL " row_mask:0xf bank_mask:" BANK "\n\t"
#define G80_LOOP(FILL)                                                                                               \
  POLL_HEAD("", USEX)                                                                                                \
  "s_waitcnt lgkmcnt(2)\n\t"                                                                                         \
  MF R2 ", " F0 ", " F0 ", 0\n\t" AND3("v52", "v53", "v54")                                                          \
  MF R0 ", " F0 ", %[w0], 0\n\t" MF R1 ", " F0 ", %[w1], 0\n\t"                                                      \
  "s_waitcnt lgkmcnt(1)\n\t"                                                                                         \
  MF R2 ", " F1 ", " F1 ", " R2 "\n\t" AND3("%[t]", "v55", "v56")                                                    \
  MF R0 ", " F1 ", %[w2], " R0 "\n\t" AND3("%[t]", "v57", "v58")                                                     \
  MF R1 ", " F1 ", %[w3], " R1 "\n\t"                                                                                \
  "s_waitcnt lgkmcnt(0)\n\t"                                                                                         \
  MF R2 ", " F2 ", " F2 ", " R2 "\n\t" AND3("%[t]", "v59", "v60") AND3("%[t]", "v61", "v62")                         \
  MF R0 ", " F2 ", %[w4], " R0 "\n\t" AND3("%[t]", "1", "v63")                                                       \
  "v_cmp_eq_u32 vcc, 0, %[t]\n\t"                                                                                    \
  MF R1 ", " F2 ", %[w5], " R1 "\n\t"                                                                                \
  "s_and_b64 vcc, vcc, %[nm]\n\ts_cbranch_scc1 1b\n"                                                                 \
  "3:\n\tv_pk_add_f32 v[48:49], v[48:49], v[50:51]\n\tv_add_f32 v48, v48, v49\n\t" FILL                              \
  "v_pk_add_f32 v[40:41], v[40:41], v[42:43]\n\t" ZD("row_ror:8", "0x1") "v_add_f32 v40, v40, v41\n\t"               \
  "v_pk_add_f32 v[44:45], v[44:45], v[46:47]\n\t" ZD("quad_perm:[1,0,3,2]", "0xf") "v_add_f32 v44, v44, v45\n\t"
#define SD(R, CTL) "v_add_f32_dpp " R ", " R ", " R " " CTL " row_mask:0xf bank_mask:0xf\n\t"
#define PKA(A, B) "v_pk_add_f32 " A ", " A ", " B "\n\t"
#define RND(R, X) "v_cvt_pk_bf16_f32 " R ", 0, " X "\n\t"
#define G80_IN(A)                                                                                                    \
  [ra] "v"(A.ra), [rb] "v"(A.rb), [rc] "v"(A.rc), [w0] "v"(A.w[0]), [w1] "v"(A.w[1]), [w2] "v"(A.w[2]),              \
      [w3] "v"(A.w[3]), [w4] "v"(A.w[4]), [w5] "v"(A.w[5]), [vo] "v"(voff), [rq] "s"(rq), [land] "s"(landa)
__device__ __forceinline__ void g80_run(G80& a, unsigned char* smem, float& r0, float& r1) {
  const u32x4 f0 = afrag(smem, a.ra), f1 = afrag(smem, a.rb), f2 = afrag(smem, a.rc);
  f32x4 d0 = {0.f, 0.f, 0.f, 0.f}, d1 = d0;
  d0 = mfma32(f0, a.w[0], d0);
  d1 = mfma32(f0, a.w[1], d1);
  d0 = mfma32(f1, a.w[2], d0);
  d1 = mfma32(f1, a.w[3], d1);
  d0 = mfma32(f2, a.w[4], d0);
  d1 = mfma32(f2, a.w[5], d1);
  r0 = sum4(d0);
  r1 = sum4(d1);
}
__device__ __forceinline__ f32x4 quad4(float r) { return f32x4{r, dpp<0x55>(r), dpp<0xAA>(r), dpp<0xFF>(r)}; }

__device__ __forceinline__ int q_addr(int d) {
  const int w = d >> 1;
  return L_Q + ((w & 1) * 48 + (w >> 1)) * 4 + (d & 1) * 2;
}

__device__ __forceinline__ void cs_gather(unsigned char* smem, u32x4 rq, int lane, int wave, int h) {
  f32x4 v;
  asm volatile("buffer_load_dwordx4 %0, %1, %2, 0 offen offset:1024 sc1\n\ts_waitcnt vmcnt(0)"
               : "=&v"(v)
               : "v"(WS_GQ1 + (u32)(h * 8 + (lane >> 3)) * 2048 + wave * 128 + (lane & 7) * 16), "s"(rq)
               : "memory");
  const int e = wave * 32 + (lane & 7) * 4 + ((lane >> 4) & 1) * 2 + (lane >> 5);
  *reinterpret_cast<float*>(smem + L_CS + e * 4) = tsum8(v) * (-1.0f / D);
}
template <bool LDS>
__device__ __forceinline__ void x_update(unsigned char* smem, u32x4 rq, int lane, u32 src, int i) {
  const int l = lane < 40 ? lane : 0;
  const u32 off = src + 4 * l;
  u32 v[8];
  if (LDS) {
#pragma unroll
    for (int k = 0; k < 8; ++k) v[k] = *reinterpret_cast<const u32*>(smem + (off ^ ((k & 3) * 64)) + 256 * k);
  } else {
    u32 budget = SPIN_CAP;
#pragma unroll 1
    for (;;) {
#pragma unroll
      for (int k = 0; k < 8; ++k)
        asm volatile("buffer_load_dword %0, %1, %2, 0 offen offset:%3 sc1" : "=v"(v[k]) : "v"(off), "s"(rq), "n"(256 * k) : "memory");
      asm volatile("s_waitcnt vmcnt(0)"
                   : "+v"(v[0]), "+v"(v[1]), "+v"(v[2]), "+v"(v[3]), "+v"(v[4]), "+v"(v[5]), "+v"(v[6]), "+v"(v[7])::"memory");
      const u32 a = v[0] & v[1] & v[2] & v[3] & v[4] & v[5] & v[6] & v[7] & 1u;
      if (__builtin_amdgcn_ballot_w64(a == 0u) == 0ull || budget == 0u) break;
      --budget;
      __builtin_amdgcn_s_sleep(2);
    }
  }
  float lo = 0.f, hi = 0.f;
#pragma unroll
  for (int k = 0; k < 8; ++k) {
    lo += bflo(v[k]);
    hi += bfhi(v[k]);
  }
  u32* px = reinterpret_cast<u32*>(smem + L_HB + 160 * i + 4 * l);
  const u32 xo = *px, y = cvt2(lo, hi);
  if (lane < 40) *px = cvt2(bflo(xo) + bflo(y), bfhi(xo) + bfhi(y));
}
template <int N>
__device__ __forceinline__ void col_poll(u32x4 rq, const u32 (&off)[N], float (&y)[N]) {
  u32x2 v[N];
  u32 budget = SPIN_CAP;
#pragma unroll 1
  for (;;) {
    u32 a = 1u;
#pragma unroll
    for (int k = 0; k < N; ++k)
      asm volatile("buffer_load_dwordx2 %0, %1, %2, 0 offen sc1" : "=v"(v[k]) : "v"(off[k]), "s"(rq) : "memory");
    asm volatile("s_waitcnt vmcnt(0)" ::: "memory");
#pragma unroll
    for (int k = 0; k < N; ++k) {
      asm volatile("" : "+v"(v[k]));
      a &= v[k][0] & v[k][1];
    }
    if (__builtin_amdgcn_ballot_w64(a == 0u) == 0ull || budget == 0u) break;
    --budget;
  }
#pragma unroll
  for (int k = 0; k < N; ++k) y[k] = bf16r(tsum8(f32x4{bflo(v[k][0]), bfhi(v[k][0]), bflo(v[k][1]), bfhi(v[k][1])}));
}

#define CKL 22
#define CKV 5
#define CW 6
#define CWAIT asm volatile("s_waitcnt vmcnt(%0)" ::"n"(CW) : "memory")
#define QACK 5
#define QP 11
#define BEAT0 (QP + QACK)
#define CLRI -1
#define PRE 50
#define CLRW 5
#define CLRX 0
#define XNO 0
#define YM 3
#define DRY 1
#define CLRL 1
#define ALN 1
#define FB 0
#define FB_T {16, 20, 23, 26}, {27, 30, 33, 35}, {34, 37, 41, 42}
#define FB_B 72, 66, 62
#define FB_G 16, 16, 64
#define FB_I 0
#define FB_MISS 250
constexpr int fb_t[3][4] = {FB_T}, fb_b[3] = {FB_B}, fb_g[3] = {FB_G};
constexpr int fb_at(int n, int k) { return (FB & 1) && fb_t[0][k] == n ? 0 : ((FB & 2) && fb_t[1][k] == n ? 1 : ((FB & 4) && fb_t[2][k] == n ? 2 : -1)); }
#define AL_R 3
#define AL_L 8
#define AL_A 11
#define AL_OFF 8
#define QKV 0
#define RLATE 1
#define E_TAB 0
#define E_Q 0
#define E_QK 300
#define E_QN 2
#define PWARM "s_waitcnt vmcnt(0)\n\t"
#define PROBE 1
#define MAP8 1
#define WBIG 10
#define QI 7
#define NINO 4
#define NINF 0
#define PO0 8
#define POS 10
#define PF0 25
#define PFS 2
#define TFL 220
#define TFD2 0
static_assert(QI <= PO0 && PO0 + (NINO - 1) * POS < 42 && PF0 + (NINF - 1) * PFS < 42, "pieces");
#define XCLR 4
#define XPFN "0x140"
#define XTAB 12
#define NPRE 2
#define HOT 1
#define FEARLY 1
#define NPF NPRE
#define TPF 220
#define TPV 220
#define TFD 0
#define XA 15
#define XB 18
#define TS_O (TSTEP * (20 * (48 + NINO) + 12 * (48 + NINF)) / (32 * (48 + NINO)))
#define TS_F (TSTEP * (20 * (48 + NINO) + 12 * (48 + NINF)) / (32 * (48 + NINF)))
#define TSQ 172
#define ROT (wave * 3 + j + g * 5)
static_assert(CKL > BEAT0 && QP > QI && CLRI < QP && 23 - CKL + CKV >= CW && CKV + 1 + CW <= XB - CW && XB - CW <= XA &&
              CLRI < BEAT0, "window");
#define PACE_ASM                                                                                                     \
  "1:\n\ts_memrealtime s[98:99]\n\ts_waitcnt lgkmcnt(0)\n\ts_lshl_b32 s98, s98, 3\n\ts_sub_i32 s98, s98, %[tn]\n\t"  \
  "s_cmp_lt_i32 s98, 0\n\ts_cbranch_scc0 2f\n\ts_sleep 1\n\ts_branch 1b\n2:\n\ts_add_i32 %[tn], %[tn], %[ts]\n\t"
__device__ __forceinline__ void pace(u32& tnext, u32 tstep) {
  asm volatile(PACE_ASM : [tn] "+s"(tnext) : [ts] "s"(tstep) : "scc", "s98", "s99");
}
constexpr int kreg(int i) { return i % 3 < 2 ? 64 + 8 * (i / 3) + 4 * (i % 3) : 128 + 4 * (i / 3); }
constexpr int vreg(int i) { return 160 + 24 * (i / 6) + 8 * (i % 3) + 4 * ((i / 3) & 1); }
#define AQ0 3
#define AGAIN 0
#define STR2(x) #x
#define STR(x) STR2(x)
#define ADAPT_ASM
#define LOADW(reg, voff, sbase, imm)                                                                                \
  asm volatile(PACE_ASM "global_load_dwordx4 v[%[r0]:%[r1]], %[p], %[sb] offset:%[im] nt\n\ts_waitcnt vmcnt(%[w])" ADAPT_ASM \
               : [tn] "+s"(tnext)                                                                                   \
               : [r0] "n"(reg), [r1] "n"((reg) + 3), [p] "v"(voff), [sb] "s"(sbase), [im] "n"(imm), [ts] "s"(tstep - AQ0 * AGAIN), \
                 [w] "n"(WBIG - 1)                                                                                  \
               : "scc", "s98", "s99")

#define HS_WAIT(COND)                                                                                \
  _Pragma("unroll 1") for (;;) {                                                                     \
    gd = CTL[512 + lane0] - CTL[576 + lane0];                                                        \
    const u64 ch = __builtin_amdgcn_ballot_w64(gd != 0u) & 0x1ffull;                                 \
    if (ch > 0xffull || (COND) || tmo == 0u) break;                                                  \
    if ((tmo & 3u) == 0u && lane0 == 0)                                                              \
      __builtin_amdgcn_raw_buffer_store_b32(hb, rs, WS_HB + h * 128 + c * 4, 0, 16);                 \
    hb += 4u;                                                                                        \
    --tmo;                                                                                           \
    __builtin_amdgcn_s_sleep(3);                                                                     \
    ctl_load<false>(rq, hso, (u32)(smb + L_CTL + 2048));                                             \
    asm volatile("s_waitcnt vmcnt(0)" ::: "memory");                                                 \
  }

struct QKAddr {
  int qad[6], idxq, wa;
  f32x4 D0, D1;
};
struct PVAddr {
  int ra, idx, oa, sa, la, ma;
  u32 dst;
  f32x4 O0, O1, O2;
  u32x4 A0, A1, A2, A3;
};
__device__ __forceinline__ void att_table(unsigned char* smem, int lane, int wave, u32 go) {
  const int n = lane & 15, G = lane >> 4;
  const int seg = n & 1, pr = (n >> 1) & 1, tq = n >> 2, xy = G >> 1;
  const int qa = L_Q + (pr * 48 + seg * 8 + (G & 1) * 4) * 4;
  u32 t[16];
#pragma unroll
  for (int k = 0; k < 4; ++k) t[k] = tq == k ? qa + xy * 64 : L_ZERO;
#pragma unroll
  for (int k = 0; k < 2; ++k) t[4 + k] = tq == 2 * k + xy ? qa + 128 : L_ZERO;
  t[6] = 0x4444 + pr * 0xAAAA;
  t[7] = L_P + wave * P_W + ((n >> 1) & 1) * P_PAR + ((((G & 1) * 2 + (n >> 3)) * 4 + (n & 1) * 2 + (G >> 1)) * 16) + ((n >> 2) & 1) * 8;
  t[8] = L_P + wave * P_W + ((n >> 2) & 1) * P_U + (n >> 3) * P_PAR + G * 64;
  t[9] = (n & 3) * 0x5555;
  t[10] = L_OM + (wave * OM_S + (n >> 3) * OM_P + (n & 7) * 4 + (G & 1) * 32) * 4;
  t[11] = L_OM + (wave * OM_S + DH + G) * 4;
  const int src = wave < 6 ? lane >> 3 : lane & 7;
  t[12] = L_OM + (src * OM_S + (wave < 6 ? (wave >> 1) * 64 + (lane & 1) * 32 + (wave & 1) * 16 + (lane & 6) * 2 : DH)) * 4;
  t[13] = L_OM + (src * OM_S + DH + 4) * 4;
  t[14] = lane >= (wave < 6 ? 16 : (wave == 6 ? 2 : 0)) ? 0x8000u
          : go + (wave < 6 ? (wave >= 3 ? 2048 - 192 : 0) + wave * 64 + (lane & 7) * 8 + (lane >> 3) * 4 : lane * 2048 + 192);
  t[15] = wave < 6 ? q_addr(wave * 32 + (lane & 7) * 4 + (lane >> 4)) : (wave == 6 ? L_RI : L_ACT);
#pragma unroll
  for (int k = 0; k < 2; ++k)
    *reinterpret_cast<u32x4*>(smem + L_TAB + wave * 2048 + k * 1024 + lane * 16) =
        u32x4{t[8 * k] | (t[8 * k + 4] << 16), t[8 * k + 1] | (t[8 * k + 5] << 16), t[8 * k + 2] | (t[8 * k + 6] << 16),
              t[8 * k + 3] | (t[8 * k + 7] << 16)};
}
__device__ __forceinline__ void att_setup(QKAddr& r, PVAddr& v, unsigned char* smem, int lane, int wave, int& qw) {
  const u32x4 a = afrag(smem, L_TAB + wave * 2048 + lane * 16), b = afrag(smem, L_TAB + wave * 2048 + 1024 + lane * 16);
  r.D0 = zero4();
  r.D1 = zero4();
  qw = fresh(b[3] >> 16);
#pragma unroll
  for (int k = 0; k < 4; ++k) r.qad[k] = fresh(a[k] & 0xffffu);
  r.qad[4] = fresh(a[0] >> 16);
  r.qad[5] = fresh(a[1] >> 16);
  r.idxq = fresh(a[2] >> 16);
  r.wa = fresh(a[3] >> 16);
  v.ra = fresh(b[0] & 0xffffu);
  v.idx = fresh(b[1] & 0xffffu);
  v.oa = fresh(b[2] & 0xffffu);
  v.la = fresh(b[3] & 0xffffu);
  v.sa = fresh(b[0] >> 16);
  v.ma = fresh(b[1] >> 16);
  v.dst = fresh((int)b[2] >> 16);
  asm volatile("" : "+v"(r.D0), "+v"(r.D1));
}
#define SM "v_smfmac_f32_16x16x64_bf16 "
#define P_WRITE_READ(QA)                                                                                              \
  asm volatile("ds_write2_b32 %[wa], %[p], %[p] offset1:%[o1]\n\t"                                                    \
               "ds_read_b128 %[a0], %[ra]\n\tds_read_b128 %[a1], %[ra] offset:16\n\t"                                 \
               "ds_read_b128 %[a2], %[ra] offset:32\n\tds_read_b128 %[a3], %[ra] offset:48"                           \
               : [a0] "=&v"(pv.A0), [a1] "=&v"(pv.A1), [a2] "=&v"(pv.A2), [a3] "=&v"(pv.A3), "+v"(sk)                 \
               : [wa] "v"(QA.wa + smb), [p] "v"(pb), [ra] "v"(pv.ra + smb), [o1] "n"(P_U / 4 + 1)        \
               : "memory");
#define QS(D, A, K) SM "v[" #D ":" #D "+3], v[" #A ":" #A "+3], v[" #K ":" #K "+7], %[ix]\n\t"
#define SH1(D, A, N) "v_mov_b32_dpp v[" D "], v[" A "] row_shr:" #N " row_mask:0xf bank_mask:0xf bound_ctrl:1\n\t"
#define SH(D, A, N) SH1(#D, #A, N) SH1(#D "+1", #A "+1", N)
#define RDS "ds_read_b32 %[s], %[z] offset:%[ri]\n\t"
#define LW(N) "s_waitcnt lgkmcnt(" #N ")\n\t"
#define QK_BODY                                                                                                       \
  "ds_read_b128 v[40:43], %[q0]\n\tds_read_b128 v[44:47], %[q1]\n\tds_read_b128 v[48:51], %[q2]\n\t"                   \
  "ds_read_b128 v[52:55], %[q3]\n\tds_read_b128 v[56:59], %[q4]\n\tds_read_b128 v[60:63], %[q5]\n\t"                   \
  LW(5) QS(32, 40, 64) QS(36, 40, 96) LW(4) QS(32, 44, 72) QS(36, 44, 104) LW(3) QS(32, 48, 80) QS(36, 48, 112)       \
  LW(2) QS(32, 52, 88) QS(36, 52, 120) LW(0) RDS1 QS(32, 56, 128) QS(36, 56, 144) RDS2 QS(32, 60, 136) QS(36, 60, 152)
#define QK_IN(QA) [q0] "v"(QA.qad[0]), [q1] "v"(QA.qad[1]), [q2] "v"(QA.qad[2]), [q3] "v"(QA.qad[3]), [q4] "v"(QA.qad[4]), [q5] "v"(QA.qad[5]),
#define RDS1
#define RDS2 RDS
#define UNSAFE(X) ((__builtin_amdgcn_ballot_w64(!(X <= 24.f)) != 0ull) | (__builtin_amdgcn_ballot_w64(X >= -24.f) == 0ull))
#define QK_SOFTMAX(QA)                                                                                                \
  {                                                                                                                   \
    float scl, raw, sk;                                                                                               \
    u32 pb;                                                                                                           \
                                                                      \
    asm volatile(QK_BODY                                                                                              \
                 "v_mov_b64 v[52:53], 0\n\tv_mov_b64 v[54:55], 0\n\tv_mov_b64 v[56:57], 0\n\tv_mov_b64 v[58:59], 0\n\t" \
                 "v_mov_b64 v[60:61], 0\n\tv_mov_b64 v[62:63], 0\n\ts_waitcnt lgkmcnt(0)\n\t"                         \
                 PKA("v[32:33]", "v[34:35]") PKA("v[36:37]", "v[38:39]") "s_nop 0\n\t"                                \
                 "v_add_f32_dpp v32, v33, v32 quad_perm:[1,0,3,2] row_mask:0xf bank_mask:0xf\n\t"                     \
                 "v_add_f32_dpp v36, v36, v37 quad_perm:[1,0,3,2] row_mask:0xf bank_mask:0xf\n\t"                     \
                 "v_cndmask_b32 %[raw], v32, v36, %[od]\n\tv_mul_f32 %[sk], %[s], %[raw]\n\tv_exp_f32 %[p], %[sk]\n\t" \
                 "s_nop 0\n\tv_cvt_pk_bf16_f32 %[pb], %[p], 0"                                                        \
                 : "+{v[32:35]}"(QA.D0), "+{v[36:39]}"(QA.D1), [s] "=&v"(scl), [raw] "=&v"(raw), [sk] "=&v"(sk),      \
                   [p] "=&v"(pend), [pb] "=&v"(pb), "=&{v[52:55]}"(pv.O0), "=&{v[56:59]}"(pv.O1), "=&{v[60:63]}"(pv.O2) \
                 : QK_IN(QA) [ix] "v"(QA.idxq), [z] "v"(smb), [ri] "n"(L_RI),                                        \
                   [od] "s"(0xaaaaaaaaaaaaaaaaull), "{v[64:95]}"(kv0), "{v[96:127]}"(kv1), "{v[128:159]}"(kv2)        \
                 : "memory", DCLOB);                                                                                  \
    pmax = 0.f;                                                                                                       \
    P_WRITE_READ(QA)                                                                                                  \
    if (__builtin_expect(UNSAFE(sk) | (__builtin_amdgcn_ballot_w64(__float_as_uint(scl) == 0u) != 0ull), 0)) {        \
      sk = raw * __uint_as_float(lds_wait_nz((volatile LAS u32*)(smem + L_RI)));                                      \
      if (UNSAFE(sk)) pmax = xrow16_max(max_ror8(max8(sk)));                                                          \
      pend = __builtin_amdgcn_exp2f(sk - pmax);                                                                       \
      pb = cvt2(pend, 0.f);                                                                                           \
      asm volatile("s_waitcnt lgkmcnt(0)" : "+v"(pv.A0), "+v"(pv.A1), "+v"(pv.A2), "+v"(pv.A3));                      \
      P_WRITE_READ(QA)                                                                                                \
      asm volatile("s_waitcnt lgkmcnt(0)" : "+v"(pv.A0), "+v"(pv.A1), "+v"(pv.A2), "+v"(pv.A3));                      \
    }                                                                                                                 \
  }

using u32x16 = __attribute__((ext_vector_type(16))) unsigned;
using u32x32 = __attribute__((ext_vector_type(32))) unsigned;
template <int OFF>
__device__ __forceinline__ u16* karg() {
  u64 p;
  asm volatile("s_load_dwordx2 %0, %1, %2\n\ts_waitcnt lgkmcnt(0)" : "=s"(p) : "s"(__builtin_amdgcn_kernarg_segment_ptr()), "n"(OFF));
  return (u16*)p;
}
template <class T>
__device__ __forceinline__ T* mkptr(u32 lo, u32 hi) {
  return (T*)(((u64)hi << 32) | lo);
}

template <int I>
struct IC {
  static constexpr int value = I;
};
template <int A, int B, class Fn>
__device__ __forceinline__ void sfor(Fn&& f) {
  if constexpr (A < B) {
    f(IC<A>{});
    sfor<A + 1, B>(f);
  }
}

__global__ __launch_bounds__(NT, 2) void mega_kernel(Params) {
  __shared__ __attribute__((aligned(16))) unsigned char smem[L_TOTAL];
  u32x16 ka;
  u32x4 kb4;
  {
    u64 pc;
    u32 keep;
    asm volatile("s_load_dwordx16 %[ka], %[kp], 0x0\n\ts_load_dwordx4 %[kb], %[kp], 0x40\n\t"
                 "s_getpc_b64 %[pc]\n.Lmk_pc:\n\t"
                 "s_mov_b32 %[keep], m0\n\ts_mov_b32 m0, %[dst]\n\t"
                 "v_lshlrev_b32 v1, 7, %[tid]\n\tv_min_u32 v1, .Lmk_code_end-.Lmk_pc-4, v1\n\t"
                 "v_cmp_gt_u32 vcc, " XPFN ", %[tid]\n\ts_mov_b64 exec, vcc\n\t"
                 "global_load_lds_dword v1, %[pc]\n\ts_mov_b64 exec, -1\n\ts_mov_b32 m0, %[keep]\n\t"
                 "v_lshrrev_b32 v1, 2, %[tid]\n\tv_cmp_eq_u32 vcc, 3, v1\n\tv_mov_b32 v3, 0x3f803f80\n\ts_nop 0\n\t"
                 "v_cndmask_b32 v2, 0, 1, vcc\n\tv_cmp_eq_u32 vcc, 2, v1\n\tv_lshlrev_b32 v1, 2, %[tid]\n\ts_nop 0\n\t"
                 "v_cndmask_b32 v2, v2, v3, vcc\n\tv_cmp_eq_u32 vcc, 7, %[tid]\n\tv_mov_b32 v3, %[scl]\n\t"
                 "v_min_u32 v1, 80, v1\n\tv_cndmask_b32 v2, v2, v3, vcc\n\t"
                 "ds_write_b32 v1, v2 offset:%[sm]\n\t"
                 "s_waitcnt lgkmcnt(0)\n\ts_barrier"
                 : [ka] "=&s"(ka), [kb] "=&s"(kb4), [pc] "=&s"(pc), [keep] "=&s"(keep)
                 : [kp] "s"(__builtin_amdgcn_kernarg_segment_ptr()), [tid] "v"(threadIdx.x), [dst] "n"(L_PF),
                   [scl] "n"(__builtin_bit_cast(u32, SCL)), [sm] "n"(0)
                 : "memory", "vcc", "v1", "v2", "v3");
  }
  const u32 tstart = (u32)__builtin_amdgcn_s_memrealtime();
  const int tid = threadIdx.x, lane0 = tid & 63;
  const int wave = __builtin_amdgcn_readfirstlane(tid >> 6);
  const int b = blockIdx.x;
  const int g = b & 7, j = b >> 3, hsub = j & 3, c = j >> 2, h = g * 4 + hsub;
  const bool isO = j < 20;
  const bool leader = b == 0 && wave == 6;
  const u32 fslot = (u32)((j & 1) * 768 + ((j - 20) >> 1) * 128);
  u32 tmo = SPIN_CAP;
  volatile LAS u32* cnt = (volatile LAS u32*)(smem + L_CNT);
  Params P;
  P.x0 = mkptr<const u16>(ka[0], ka[1]);
  P.kc = mkptr<const u16>(ka[2], ka[3]);
  P.vc = mkptr<const u16>(ka[4], ka[5]);
  P.wq = mkptr<const u16>(ka[6], ka[7]);
  P.wo = mkptr<const u16>(ka[8], ka[9]);
  P.w1 = mkptr<const u16>(ka[10], ka[11]);
  P.w3 = mkptr<const u16>(ka[12], ka[13]);
  P.ws = mkptr<u32>(kb4[2], kb4[3]);
  u32* const wsb = (u32*)(((uintptr_t)P.ws + 0x1fffffu) & ~(uintptr_t)0x1fffffu);
  const __amdgpu_buffer_rsrc_t rs = __builtin_amdgcn_make_buffer_rsrc(wsb, 0, (int)WS_END, 0x00020000);
  const u32x4 rq = {(u32)(uintptr_t)wsb, (u32)((uintptr_t)wsb >> 32) & 0xffffu, WS_END, 0x00020000u};

  u32 anc = 0u;
  u32 tref = 0u, ar = 0u;
  u32x32 kv0, kv1, kv2, kv3, kv4, kv5;
  asm volatile(""
               : "={v[64:95]}"(kv0), "={v[96:127]}"(kv1), "={v[128:159]}"(kv2), "={v[160:191]}"(kv3), "={v[192:223]}"(kv4),
                 "={v[224:255]}"(kv5));
  const int smb = (int)(uintptr_t)(LAS unsigned char*)smem;
  const u32 gop = A_GO + go_slot(hsub * 2) + c * 256;
  u32 tnext = tstart * 8u + (u32)wave * (TSTEP / 8) + 5 * TSQ;
  const size_t rowbase = (size_t)h * SK + (SK - WIN) + (wave * 8 + c) * 8;
#define GSTR 512
#undef ROT
#define ROT h
  const u16* vb = P.vc + rowbase * DH;
  const int rot = ROT;
  const u16* ws0 = isO ? P.wo + (size_t)(768 * g) * D + 32 * j : ((j & 1) ? P.w3 : P.w1) + 192 * g + 32 * ((j - 20) >> 1);
  const bool sO = isO || (HOT && ((u32)(uintptr_t)ws0 & 0x380u) == 0x180u);
  u32 tstep = sO ? TS_O : TS_F;
  int pn = isO ? wave * 6 : wave * 5;
  {
    volatile LAS u32* const CTL = (volatile LAS u32*)(smem + L_CTL);
    u32 gd, hb = 0u;
    const u32 lho = WS_HB + (lane0 >> 1) * 128 + (lane0 & 1) * 16;
    const u32 hso = lane0 < 8 ? WS_HB + (u32)h * 128 + lane0 * 4 : WS_GOF + g * 128;
#define CLEAR1                                                                                                      \
  {                                                                                                                 \
    const u32 ca = gy_rep((g + CLRX) & 7) + fresh(lane0) * 16;                                                      \
    if (j < 16 || (g & 3 & ~YM) == 0)                                                                               \
      store16(rs, ca + (u32)(j >> 3) * 16384u + (u32)(j & 7) * 1024u, u32x4{0u, 0u, 0u, 0u});                       \
    if (lane0 < 8) store16(rs, ca + A_GH + (u32)j * 128u, u32x4{0u, 0u, 0u, 0u});                                   \
  }
    if (CLRI == -2 && wave == CLRW) CLEAR1
    if (wave < 6) {
      const u16* s0 = P.wq + (size_t)(80 * c + 8 * (wave & 1)) * (H * DH) + h * DH + (wave >> 1) * 64;
      const u32 wo_ = (u32)(((lane0 >> 2) & 7) * (H * DH) + (lane0 >> 5) * 32 + (((lane0 >> 1) ^ wave) & 1) * 16 + (lane0 & 1) * 8) * 2u;
      u32 tq = tnext - 5 * TSQ;
#pragma unroll 1
      for (int p = 0; p < 5; ++p) {
        pace(tq, TSQ);
        dma_kib(s0 + (size_t)p * (16 * H * DH), wo_, smem, L_WQ + (wave >> 1) * 10240 + (wave & 1) * 1024 + p * 2048);
        if (CLRI == -3 && wave == CLRW && p == 0) CLEAR1
      }
    } else {
      if (ALN && b < 8 && wave == 7 && lane0 < 32)
        __builtin_amdgcn_raw_buffer_store_b32(lane0 ? (tstart - 4096u) << 12 : tstart, rs, WS_GOF + 2048 + b * 128 + lane0 * 4, 0, 16);
      if (leader) {
        ctl_load<true>(rq, lho, (u32)(smb + L_CTL));
        ctl_load<false>(rq, WS_GOF + (lane0 & 7) * 128, (u32)(smb + L_CTL + 2560));
      }
      ctl_load<false>(rq, hso, (u32)(smb + L_CTL + (wave == 6 ? 2816 : 2304)));
      if (wave == 6) {
        u32 keep;
        asm volatile("s_mov_b32 %0, m0\n\ts_mov_b32 m0, %4\n\ts_nop 2\n\tglobal_load_lds_dwordx4 %1, %3\n\t"
                     "global_load_lds_dword %2, %3 offset:1024\n\ts_mov_b32 m0, %0"
                     : "=&s"(keep)
                     : "v"(lane0 * 16), "v"(lane0 * 4), "s"(P.x0), "s"((u32)(smb + L_HB))
                     : "memory");
      }
      u32 rtt;
      {
        u32 pa = 0u, pb = 0u;
        u64 t1, t2;
        const u32 pbo = WS_PB + (u32)(wave & 1) * (SIDE + 128u) + (u32)((b >> 3) & 7) * 16384u + (u32)((b & 7) * 4 + (b >> 6)) * 256u;
        asm volatile(PWARM ".p2align 6\n\ts_mov_b64 exec, 1\n\t"
                     "buffer_load_dword %0, off, %4, %5 sc1 nt\n\tbuffer_load_dword %1, off, %4, %6 sc1 nt\n\t"
                     "s_mov_b64 exec, -1\n\t"
                     "s_waitcnt vmcnt(1)\n\ts_memtime %2\n\ts_waitcnt vmcnt(0)\n\ts_memtime %3\n\ts_waitcnt lgkmcnt(0)"
                     : "+v"(pa), "+v"(pb), "=&s"(t1), "=&s"(t2)
                     : "s"(rq), "s"(pbo), "s"(pbo ^ SIDE)
                     : "memory");
        rtt = __builtin_amdgcn_readfirstlane((u32)t2 - (u32)t1) + 1u;
      }
      if (wave == 6) {
        *(volatile LAS u32*)(smem + L_VOTE) = rtt;
      } else {
        if (CLRI == -1 && !(XNO & 1)) CLEAR1
        hb = rtt;
      }
    }
    if (tid == 0) *(volatile LAS u32*)(smem + L_ANC) = 0u;
    *reinterpret_cast<u32x4*>(smem + L_P + wave * P_W + lane0 * 16) = u32x4{0u, 0u, 0u, 0u};
    *reinterpret_cast<u32x4*>(smem + (lane0 < 16 ? L_P + wave * P_W + 1024 + lane0 * 16 : L_OM + (wave * OM_S + OM_P + DH) * 4)) =
        u32x4{0u, 0u, 0u, 0u};
    const u16* kb = P.kc + rowbase * DH;
    const u32 ko = ((lane0 & 15) >> 1) * 384 + 16 * ((lane0 & 1) * 4 + (lane0 >> 4)), vo = (lane0 >> 3) * 384 + 16 * (lane0 & 7);
#define WPO(n) ((n) >= PO0 && ((n) - PO0) % POS == 0 && ((n) - PO0) / POS < NINO)
#define WPF(n) ((n) >= PF0 && ((n) - PF0) % PFS == 0 && ((n) - PF0) / PFS < NINF)
#define WPIECE(n)                                                                                                   \
  if ((WPO(n) && sO) || (WPF(n) && !sO)) {                                                                          \
    pace(tnext, tstep);                                                                                             \
    if (XNO & 8) {                                                                                                  \
      dma_kib(vb - (size_t)WIN * DH + (n) * 64 * DH, lane0 * 16, smem, L_WB + pn * 1024);                           \
      ++pn;                                                                                                         \
    } else if ((n) != PO0 || isO) dma_piece(ws0, isO ? D : F, smem, L_WB, pn++, lane0);                             \
  }
#define FBB(r) (float)fb_b[r]
#define FBG(r) (float)fb_g[r]
#define FBI (float)FB_I
#define FBACK(n)                                                                                                    \
  if (fb_at(n, 0) >= 0 && j == 4 * wave + 1) {                                                                      \
    u32 q;                                                                                                          \
    asm volatile("s_getreg_b32 %0, hwreg(HW_REG_IB_STS, 0, 4)" : "=s"(q));                                          \
    const u32 nw = rtnow(), e = (nw * 8u - tnext + q * tstep) >> 3;                                                 \
    if (lane0 == 0)                                                                                                 \
      __builtin_amdgcn_raw_buffer_store_b32((nw << 12) | (e < 4095u ? e : 0u), rs,                                  \
                                            WS_GOF + 2048 + 32 * (fb_at(n, 0) + 1) + g * 128 + wave * 4, 0, 16);    \
  }                                                                                                                 \
  if (fb_at(n, 1) >= 0 && wave == 6) {                                                                              \
    const int ln = fresh(lane0);                                                                                    \
    ctl_load<false>(rq, WS_GOF + 2048 + 32 * (fb_at(n, 1) + 1) + (ln >> 3) * 128 + (ln & 7) * 4, (u32)(smb + L_CTL + 3072)); \
  }                                                                                                                 \
  if (fb_at(n, 2) >= 0 && wave == 6) {                                                                              \
    const int r = fb_at(n, 2) < 0 ? 0 : fb_at(n, 2);                                                                \
    asm volatile("s_waitcnt vmcnt(%0)" ::"n"(fb_t[r][2] - fb_t[r][1]) : "memory");                                  \
    const u32 w = *(volatile LAS u32*)(smem + L_CTL + 3072 + fresh(lane0) * 4);                                     \
    const float m = wave_sum((rtnow() << 12) - w < (600u << 12) ? (float)(w & 0xfffu) : (float)FB_MISS) * (1.f / 64); \
    volatile LAS int* const pw = (volatile LAS int*)(smem + L_VOTE + 12);                                           \
    *pw = (int)__builtin_fminf(__builtin_fmaxf((m - FBB(r)) * FBG(r) * (1.f / 16), 0.f) + FBI * (1.f / 16) * (float)*pw, 400.f); \
  }                                                                                                                 \
  if (fb_at(n, 3) >= 0 || (ALN && (n) == AL_A + 15))                                                                \
    tstep = (sO ? TS_O : TS_F) + __builtin_amdgcn_readfirstlane(*(volatile LAS u32*)(smem + L_VOTE + 12));
    const u32 pre = wave < 6 ? PRE : 0;
    tstep -= pre;
    sfor<0, 24>([&](auto ic) __attribute__((always_inline)) {
      constexpr int i = ic.value;
      if (i == XTAB) tnext -= E_TAB;
      if (i == QI) tnext -= wave < 6 ? E_Q : 0;
      LOADW(kreg(i), ko, kb + ((i / 3 + rot) & 7) * GSTR * DH, 128 * (i % 3));
      if (i == XTAB) tnext += E_TAB;
      if (i == QI) tnext += wave < 6 ? E_Q : 0;
      if (i == QI) {
        tstep += pre;
        tnext += (QI + 1) * pre;
      }
      FBACK(i)
      WPIECE(i)
      if (i == XTAB) att_table(smem, fresh(lane0), wave, gop);
      if (i == 1 && wave == 6) {
        const u32x4 lv = afrag(smem, L_HB + lane0 * 16);
        const u32 xb = *reinterpret_cast<const u32*>(smem + L_HB + 1024 + lane0 * 4);
        float v[10];
#pragma unroll
        for (int k = 0; k < 4; ++k) {
          v[2 * k] = bflo(lv[k]);
          v[2 * k + 1] = bfhi(lv[k]);
        }
        v[8] = bflo(xb);
        v[9] = bfhi(xb);
        float sm = 0.f;
#pragma unroll
        for (int k = 0; k < 10; ++k) sm += v[k];
        const float mean = wave_sum(sm) * (1.0f / D);
        float q = 0.f;
#pragma unroll
        for (int k = 0; k < 10; ++k) q = fmaf(v[k] - mean, v[k] - mean, q);
        const float var = wave_sum(q) * (1.0f / D);
        const float mb = bf16r(mean);
        const float ri = __builtin_amdgcn_rcpf(bf16r(__builtin_amdgcn_sqrtf(bf16r(bf16r(var) + 1e-5f))));
        u32 hp[5];
#pragma unroll
        for (int k = 0; k < 5; ++k) {
          const u32 cc = cvt2(v[2 * k] - mb, v[2 * k + 1] - mb);
          hp[k] = cvt2(bflo(cc) * ri, bfhi(cc) * ri);
        }
        *reinterpret_cast<u32x4*>(smem + L_LN + lane0 * 16) = u32x4{hp[0], hp[1], hp[2], hp[3]};
        *reinterpret_cast<u32*>(smem + L_LN + 1024 + lane0 * 4) = hp[4];
        asm volatile("s_waitcnt lgkmcnt(0)" ::: "memory");
        if (lane0 == 0) lds_add(smem, L_VOTE + 4, 1u);
      }
      if (ALN && wave == 6) {
        if (i == AL_R) ctl_load<false>(rq, WS_GOF + 2048 + (fresh(lane0) & 7) * 128, (u32)(smb + L_CTL + 1024));
        if (i == AL_L) {
          asm volatile("s_waitcnt vmcnt(%0)" ::"n"(AL_L - AL_R) : "memory");
          const u32 d = CTL[256 + lane0] - tstart + AL_OFF;
          *(volatile LAS int*)(smem + L_VOTE + 8) = (int)sum8(d + 128u < 256u ? (float)(int)d : 0.f) >> 4;
        }
      }
      if (ALN && i == AL_A) tstep += __builtin_amdgcn_readfirstlane(*(volatile LAS u32*)(smem + L_VOTE + 8));
      if (i == QI - 1 && wave < 6) {
        asm volatile("s_waitcnt vmcnt(%0)" ::"n"(QI) : "memory");
        if (lane0 == 0) lds_add(smem, L_VOTE + 4, 1u);
      }
      if (i == QI && wave < 6) {
        lds_wait((volatile LAS u32*)(smem + L_VOTE + 4), 7u);
        G80 a;
        g80_load<1024>(a, smem, lane0, L_WQ + (wave >> 1) * 10240 + (wave & 1) * 512, L_LN + 160 * c, -1);
        float r0, r1;
        g80_run(a, smem, r0, r1);
        auto sw = __builtin_amdgcn_permlane32_swap(__float_as_uint(r0), __float_as_uint(r1), false, false);
        const f32x4 qd = quad4(__uint_as_float(sw[1]));
        const int qp = wave * 128 + (lane0 >> 5) * 64 + (lane0 & 15) * 4;
        if ((lane0 & 19) == 0) *reinterpret_cast<u32x2*>(smem + L_RED + (qp >> 1)) = u32x2{cvt2(qd[0], qd[1]), cvt2(qd[2], qd[3])};
        if ((lane0 & 19) == 16) *reinterpret_cast<f32x4*>(smem + L_CS + qp) = qd;
        if (lane0 == 0) lds_add(smem, L_CNT + 4, 1u);
      }
      if (wave == 7) {
        if (i == CLRI) CLEAR1
        if (i == QP) {
          lds_wait(cnt + 1, 6u);
          const int ln = fresh(lane0);
          if (ln < 24 && !(XNO & 2)) store16(rs, WS_GQ1 + (u32)(h * 8 + c) * 2048 + ln * 16, afrag(smem, L_RED + ln * 16));
        }
        if (i == BEAT0) {
          asm volatile("s_waitcnt vmcnt(%0)" ::"n"(BEAT0 - QP) : "memory");
          const int df = (int)(__builtin_amdgcn_readfirstlane(*(volatile LAS u32*)(smem + L_VOTE)) - hb);
          hb = df > PTHR ? 1u << (g >> 2) : (df < -PTHR ? 2u >> (g >> 2) : 0u);
          hb += (__builtin_amdgcn_readfirstlane(CTL[576 + c]) & ~3u) + 4u;
          if (lane0 == 0) __builtin_amdgcn_raw_buffer_store_b32(hb, rs, WS_HB + h * 128 + c * 4, 0, 16);
          hb += 4u;
        }
        if (i == CKL) ctl_load<false>(rq, hso, (u32)(smb + L_CTL + 2048));
      }
      if (i == CKL && leader) {
        ctl_load<true>(rq, lho, (u32)(smb + L_CTL + 1024));
      }
    });
#define VLOADS(A, B)                                                                           \
  _Pragma("unroll") for (int i = A; i < B; ++i) {                                              \
    if (i == XA - E_QN) tnext -= wave < 4 ? E_QK : 0;                                          \
    if (i == XB - E_QN) tnext -= wave < 4 ? 0 : E_QK;                                          \
    LOADW(vreg(i), vo, vb + ((i / 3 + rot) & 7) * GSTR * DH, 128 * (i % 3));                      \
    FBACK(24 + i)                                                                              \
    WPIECE(24 + i)                                                                             \
  }
    VLOADS(0, CKV)
    if (wave == 7) {
      CWAIT;
      HS_WAIT((u32)ch == 0xffu)
      if (lane0 == 0) cnt[1] = 7u;
    }
    if (leader) {
      CWAIT;
      const u32x4 xa = afrag(smem, L_CTL + lane0 * 16);
      u32x4 lv;
#pragma unroll 1
      for (;;) {
        asm volatile("" ::: "memory");
        lv = afrag(smem, L_CTL + 1024 + lane0 * 16);
        const bool same = lv[0] == xa[0] || lv[1] == xa[1] || lv[2] == xa[2] || lv[3] == xa[3];
        if (__builtin_amdgcn_ballot_w64(same) == 0ull || tmo == 0u) break;
        --tmo;
        __builtin_amdgcn_s_sleep(4);
        ctl_load<true>(rq, lho, (u32)(smb + L_CTL + 1024));
        asm volatile("s_waitcnt vmcnt(0)" ::: "memory");
      }
      u32 nv = 256u;
#pragma unroll
      for (int e = 0; e < 4; ++e)
        nv += (u32)__builtin_popcountll(__builtin_amdgcn_ballot_w64((lv[e] & 2u) != 0u)) -
              (u32)__builtin_popcountll(__builtin_amdgcn_ballot_w64((lv[e] & 1u) != 0u));
      if (lane0 < 8) __builtin_amdgcn_raw_buffer_store_b32(CTL[640 + lane0] + (nv > 256u ? 2u : 1u), rs, WS_GOF + lane0 * 128, 0, 16);
    }
    VLOADS(CKV, CKV + 1)
    if (wave < 3) {
      const int ln = fresh(lane0);
      lds_wait(cnt + 1, 7u);
      ctl_load<true>(rq, WS_GQ1 + (u32)(h * 8 + (ln >> 3)) * 2048 + wave * 128 + (ln & 7) * 16, (u32)(smb + L_LAND + wave * 2048));
    }
    VLOADS(CKV + 1, CKV + 1 + CW)
    if (wave < 3) {
      const int ln = fresh(lane0);
      CWAIT;
      const u32x4 v = afrag(smem, L_LAND + wave * 2048 + ln * 16);
      const float t0 = tsum8(f32x4{bflo(v[0]), bflo(v[1]), bflo(v[2]), bflo(v[3])});
      const float t1 = tsum8(f32x4{bfhi(v[0]), bfhi(v[1]), bfhi(v[2]), bfhi(v[3])});
      *reinterpret_cast<u32*>(smem + q_addr(wave * 64 + (ln & 7) * 8 + ((ln >> 4) & 1) * 4 + (ln >> 5) * 2)) = cvt2(t0, t1);
      asm volatile("s_waitcnt lgkmcnt(0)" ::: "memory");
      if (lane0 == 0) lds_add(smem, L_CNT, 1u);
    }
    VLOADS(CKV + 1 + CW, XB - CW)
    if (wave == 7) ctl_load<false>(rq, hso, (u32)(smb + L_CTL + 2048));
    VLOADS(XB - CW, XA)
    if (wave >= 4) {
      VLOADS(XA, XB)
      if (wave == 7) {
        CWAIT;
        HS_WAIT(false)
        if (lane0 == 8) cnt[2] = (gd >> 1) | 2u;
      }
    }
  }

#pragma unroll 1
  for (int half = 0;; ++half) {
    {
      float pend, pmax;
      PVAddr pv;
      QKAddr qa;
      const int lane = fresh(lane0);
      const u32 off = ar + A_GQ + hsub * 16384 + (wave < 6 ? (lane >> 3) * 896 + wave * 128 + (lane & 7) * 16 : (lane & 7) * 896 + 768);
      u32x4 qv;
      asm volatile("v_mov_b64 v[56:57], -1\n\tv_mov_b64 v[58:59], -1" : "={v[56:59]}"(qv));
      if (half == 1) {
        anc = lds_wait_nz((volatile LAS u32*)(smem + L_ANC));
        if (wave == 6) *(volatile LAS u32*)(smem + L_RI) = 0u;
        asm volatile("s_cmp_lt_u32 %[wv], 7\n\ts_cselect_b64 exec, -1, 0\n\t" WAIT_RT
                     "buffer_load_dwordx4 v[56:59], %[off], %[rq], 0 offen sc1\n\ts_mov_b64 exec, -1"
                     : "+{v[56:59]}"(qv)
                     : [off] "v"(off), [rq] "s"(rq), [wv] "s"(wave), [tt] "s"(anc + (wave < 7 ? A_Q : 0))
                     : "memory", "scc", "s98", "s99");
        if (wave < 4) __builtin_amdgcn_s_setprio(2);
      }
      int qw;
      att_setup(qa, pv, smem, lane, wave, qw);
      {
        u32 t, bud;
#define QRETRY(L) "s_sub_u32 %[bud], %[bud], 1\n\ts_cbranch_scc1 " L "\n\ts_sleep 1\n\tbuffer_load_dwordx4 v[56:59], %[off], %[rq], 0 offen sc1\n"
        asm volatile("s_mov_b32 %[bud], 0x10000\n\ts_cmp_eq_u32 %[hf], 0\n\ts_cbranch_scc1 0f\n\t"
                     "s_cmp_eq_u32 %[wv], 6\n\ts_cbranch_scc0 2f\n\ts_branch 5f\n"
                     "0:\n\ts_mov_b32 %[bud], 0x400000\n"
                     "0:\n\tds_read_b32 %[t], %[z] offset:%[cn]\n\ts_sub_u32 %[bud], %[bud], 1\n\ts_cbranch_scc1 8f\n\ts_waitcnt lgkmcnt(0)\n\t"
                     "v_cmp_eq_u32 vcc, 3, %[t]\n\ts_cbranch_vccnz 8f\n\ts_sleep 1\n\ts_branch 0b\n"
                     "4:\n\t" QRETRY("6f")
                     "5:\n\ts_waitcnt vmcnt(0)\n\t" AND3("v56", "v57", "v58") AND3("%[t]", "1", "v59")
                     "v_cmp_eq_u32 vcc, 0, %[t]\n\ts_cbranch_vccnz 4b\n"
                     "6:\n\tv_add_f32 v56, v56, v57\n\t" SD("v59", "quad_perm:[1,0,3,2]") "v_add_f32 v56, v56, v58\n\t"
                     "s_cmp_eq_u32 %[wv], 7\n\t" SD("v59", "quad_perm:[2,3,0,1]") SD("v56", "quad_perm:[1,0,3,2]")
                     "s_nop 0\n\t" SD("v59", "row_half_mirror") SD("v56", "quad_perm:[2,3,0,1]")
                     "v_mul_f32 v59, 0x3acccccd, v59\n\tv_mul_f32 v57, -v59, v59\n\t" SD("v56", "row_half_mirror")
                     "s_waitcnt lgkmcnt(0)\n\ts_barrier\n\ts_cbranch_scc1 8f\n"
                     "7:\n\t"
                     "v_fmac_f32 v57, 0x3acccccd, v56\n\t" RND("v57", "|v57|") "v_add_f32 v57, 0x3727c5ac, v57\n\t" RND("v57", "v57")
                     "v_sqrt_f32 v57, v57\n\ts_nop 0\n\t" RND("v57", "v57") "v_rcp_f32 v57, v57\n\ts_nop 0\n\t"
                     "v_mul_f32 v57, %[scl], v57\n\tv_or_b32 v57, 1, v57\n\tds_write_b32 %[qw], v57\n\t"
                     "s_cbranch_scc1 6b\n\ts_branch 8f\n"
                     "1:\n\t" QRETRY("3f")
                     "2:\n\ts_cmp_eq_u32 %[wv], 7\n\ts_waitcnt vmcnt(0)\n\t"
                     "v_permlane32_swap_b32 v56, v58\n\tv_permlane32_swap_b32 v57, v59\n\t"
                     "v_pk_add_f32 v[60:61], v[56:57], v[58:59]\n\t" AND3("v56", "v57", "v58") AND3("%[t]", "1", "v59")
                     "v_permlane16_swap_b32 v60, v61\n\tv_add_f32 v60, v60, v61\n\t"
                     "v_cmp_eq_u32 vcc, 0, %[t]\n\ts_nop 0\n\t" SD("v60", "row_ror:8")
                     "v_cvt_pk_bf16_f32 v60, v60, v60\n\tds_write_b16 %[qw], v60\n\ts_cbranch_vccnz 1b\n\ts_cbranch_scc1 7b\n"
                     "3:\n\ts_waitcnt lgkmcnt(0)\n\ts_barrier\n"
                     "8:"
                     : [t] "=&v"(t), [bud] "=&s"(bud), "+{v[56:59]}"(qv)
                     : [hf] "s"(half), [wv] "s"(wave), [z] "v"(smb), [cn] "n"(L_CNT), [off] "v"(off), [rq] "s"(rq), [qw] "v"(qw),
                       [scl] "n"(__builtin_bit_cast(u32, SCL))
                     : "memory", "vcc", "scc", "v60", "v61");
      }
      QK_SOFTMAX(qa)
      if (half == 0) {
        const int lv_ = fresh(lane0);
        const u32 vo = (lv_ >> 3) * 384 + 16 * (lv_ & 7);
        tnext += E_QK;
        if (wave < 4) {
          VLOADS(XA, XB)
        }
        VLOADS(XB, 24)
      }
#define DA(CTL) "v_add_f32_dpp %[l], %[l], %[l] " CTL " row_mask:0xf bank_mask:0xf\n\t"
#define PX(o, v, a) SM "%[o" #o "], %[a" #a "], v[160+24*" #a "+8*" #o ":167+24*" #a "+8*" #o "], %[ix]\n\t"
      asm volatile("s_waitcnt vmcnt(6) lgkmcnt(3)\n\t" PX(0, x, 0) DA("quad_perm:[1,0,3,2]") PX(1, y, 0) PX(2, z, 0)
                   "s_waitcnt lgkmcnt(2)\n\t" PX(0, x, 1) DA("quad_perm:[2,3,0,1]") PX(1, y, 1) PX(2, z, 1)
                   "s_waitcnt lgkmcnt(0)\n\t" PX(0, x, 2) DA("row_half_mirror") PX(1, y, 2) PX(2, z, 2)
                   : [o0] "+v"(pv.O0), [o1] "+v"(pv.O1), [o2] "+v"(pv.O2), [a0] "+v"(pv.A0), [a1] "+v"(pv.A1),
                     [a2] "+v"(pv.A2), [a3] "+v"(pv.A3), [l] "+v"(pend)
                   : [ix] "v"(pv.idx), "{v[160:191]}"(kv3), "{v[192:223]}"(kv4), "{v[224:255]}"(kv5)
                   : "memory");
#define CLEAR2                                                                                                         \
  {                                                                                                                    \
    const int p2 = (isO ? j : j - 8) * 2 + (wave & 1);                                                                 \
    const u32 ca = ((isO ? j < 12 : (p2 < 40 || (g & 3) == 0)) ? gy_rep(g) : OOB) + (u32)(4 + (p2 >> 2)) * 16384u +    \
                   (u32)(p2 & 3) * 2048u + fresh(lane0) * 16;                                                          \
    const u32x4 z = __builtin_bit_cast(u32x4, zero4());                                                                \
    store16(rs, ca, z);                                                                                                \
    store16(rs, ca + 1024, z);                                                                                         \
  }
      static_assert(5 - NINF >= NPF && 6 - NINO >= NPRE && NPF >= NPRE, "pieces behind V");
      if (half == 0) {
        if (!(XNO & 4)) {
          const int ln = fresh(lane0);
          if (wave < 6) {
            if (ln < 8) store16(rs, WS_GQ1 + (u32)(h * 8 + c) * 2048 + 1024 + wave * 128 + ln * 16, afrag(smem, L_CS + wave * 128 + ln * 16));
          } else if (CLRL == 0) {
            CLEAR2
          }
        }
        anc = rtnow();
        anc += (int)(tnext - (u32)wave * (TSTEP / 8) + (sO ? 0 : (48 + NINO) * TS_O - (48 + NINF) * TS_F) - anc * 8u) >> 3;
        if (!isO) tnext += TFD;
#pragma unroll 1
        for (int p = 0; p < (sO ? NPRE : NPF); ++p) {
          pace(tnext, sO ? TPV : TPF);
          dma_piece(ws0, isO ? D : F, smem, L_WB, pn++, fresh(lane0));
        }
        if (CLRL == 2 && wave < 2) CLEAR2
        ar = (u32)(g & 3) * AREA + (((u32)(g >> 2) ^ lds_wait_nz(cnt + 2)) & 1u) * SIDE;
      }
      const u32 gob = ar + (u32)half * GO_SZ;
      if (NPF != NPRE && sO) asm volatile("s_waitcnt vmcnt(%0)" ::"n"(NPRE) : "memory");
      static_assert(CLRL != 2 || NPF == NPRE, "counts");
      if (CLRL == 2 && wave >= 2) asm volatile("s_waitcnt vmcnt(%0)" ::"n"(NPRE) : "memory");
      asm volatile("s_waitcnt vmcnt(%[np])\n\t" PX(0, x, 3) DA("row_ror:8") PX(1, y, 3) PX(2, z, 3) "s_setprio 0"
                   : [o0] "+v"(pv.O0), [o1] "+v"(pv.O1), [o2] "+v"(pv.O2), [a3] "+v"(pv.A3), [l] "+v"(pend)
                   : [ix] "v"(pv.idx), [np] "n"(NPF + (CLRL == 2 ? 2 : 0)), "{v[224:255]}"(kv5)
                   : "memory");
      static_assert(NINO + NPRE == 6 && PF0 != PO0, "pieces");
      if (half == 0 && FEARLY && !sO) {
#pragma unroll 1
        for (int p = NINF + NPF; p < NINF + NPF + FEARLY; ++p) dma_piece(ws0, F, smem, L_WB, wave * 5 + p, fresh(lane0));
      }
#define MX(CTL) "s_nop 1\n\tv_max_f32_dpp v49, v49, v49 " CTL " row_mask:0xf bank_mask:0xf\n\t"
#define MXP(W) "v_mov_b32 v50, v49\n\ts_nop 1\n\tv_permlane" W "_swap_b32 v49, v50\n\tv_max_f32 v49, v49, v50\n\t"
#define RB(D, A, BANK) "v_add_f32_dpp " D ", " A ", " A " row_ror:8 row_mask:0xf bank_mask:" BANK "\n\t"
#define SWP(W) "v_permlane" W "_swap_b32 v40, v42\n\tv_permlane" W "_swap_b32 v41, v43\n\t" PKA("v[40:41]", "v[42:43]")
#define SC2(R) "v_pk_mul_f32 " R ", " R ", v[50:51] op_sel_hi:[1,0]\n\t"
      asm volatile("ds_write2_b32 %[la], %[l], %[mx] offset1:4\n\ts_mov_b64 exec, %[m]\n\ts_cmp_lt_u32 %[wv], 6\n\ts_nop 1\n\t"
                   "ds_write_b128 %[a], %[o0]\n\tds_write_b128 %[a], %[o1] offset:256\n\t"
                   "ds_write_b128 %[a], %[o2] offset:512\n\t"
                   "s_mov_b64 exec, -1\n\ts_waitcnt lgkmcnt(0)\n\ts_barrier\n\t"
                   "ds_read_b32 v48, %[ma]\n\tds_read_b128 v[40:43], %[sa]\n\tds_read_b128 v[44:47], %[sa] offset:%[op]\n\t"
                   "s_waitcnt lgkmcnt(2)\n\tv_cmp_ne_u32 vcc, 0, v48\n\ts_cbranch_vccnz 5f\n"
                   "4:\n\ts_cbranch_scc0 6f\n\ts_waitcnt lgkmcnt(0)\n\t"
                   PKA("v[40:41]", "v[44:45]") PKA("v[42:43]", "v[46:47]") "s_nop 0\n\t"
                   RB("v40", "v40", "0x3") RB("v41", "v41", "0x3") RB("v40", "v42", "0xc") RB("v41", "v43", "0xc")
                   "s_nop 0\n\t" SWP("16") "s_nop 1\n\t" SWP("32")
                   "v_cvt_pk_bf16_f32 v40, v40, v41\n\tv_or_b32 v40, 1, v40\n\t"
                   "buffer_store_dword v40, %[dst], %[rq], %[so] offen sc1\n\ts_branch 9f\n"
                   "5:\n\tv_mov_b32 v49, v48\n\t" MX("quad_perm:[1,0,3,2]") MX("quad_perm:[2,3,0,1]") MX("row_half_mirror") MX("row_ror:8")
                   MXP("16") MXP("32") "v_sub_f32 v50, v48, v49\n\tv_exp_f32 v50, v50\n\tv_mov_b32 v48, v49\n\ts_waitcnt lgkmcnt(0)\n\t"
                   SC2("v[40:41]") SC2("v[42:43]") SC2("v[44:45]") SC2("v[46:47]") "s_branch 4b\n"
                   "6:\n\ts_waitcnt lgkmcnt(0)\n\t" PKA("v[42:43]", "v[40:41]") "v_add_f32 v41, v42, v43\n\t"
                   "v_or_b32 v40, 1, v48\n\tv_or_b32 v42, 1, v48\n\t" SD("v41", "quad_perm:[1,0,3,2]")
                   "v_mov_b32 v43, 1\n\ts_nop 0\n\t" SD("v41", "quad_perm:[2,3,0,1]") "s_nop 1\n\t" SD("v41", "row_half_mirror")
                   "v_or_b32 v41, 1, v41\n\tbuffer_store_dwordx4 v[40:43], %[dst], %[rq], %[so] offen sc1\n\ts_nop 1\n"
                   "9:"
                   :
                   : [m] "s"(0xff00ff0000ff00ffull), [a] "v"(pv.oa + smb), [o0] "v"(pv.O0), [o1] "v"(pv.O1), [o2] "v"(pv.O2),
                     [la] "v"(pv.la + smb), [l] "v"(pend), [mx] "v"(pmax), [wv] "s"(wave), [ma] "v"(pv.ma), [sa] "v"(pv.sa),
                     [op] "n"(OM_P * 4), [dst] "v"(pv.dst), [rq] "s"(rq), [so] "s"(gob)
                   : "memory", "vcc", "scc", DCLOB);
      tref = now();
      if (CLRL == 1 && half == 0 && wave >= 6) CLEAR2
    }
    const u32 go = ar + A_GO + (u32)half * GO_SZ;
    const u32 gyw = A_GY + (u32)half * GY_PAIR;
    const u32 gy = gyw + ar - (u32)(g & 3 & ~YM) * AREA;
    const u32 gh = ar + A_GH + (u32)half * 1536u;
    const int land = L_LAND + wave * 2048;
    const u32 landa = (u32)(uintptr_t)(LAS unsigned char*)smem + (u32)land;
    if (isO) {
      {
        const int lane = fresh(lane0);
        const u32 voff = fresh(go + go_slot(wave) + ((lane * 16) ^ ((lane >> 4) * 64)));
        if (!half) wait_until(tref + 24 * T_O);
        wait_rt(anc + (half ? A_O : X_O));
        dma_poll<false>(rq, voff, landa, ~0ull, ~0ull);
        const int n = lane & 15, G = lane >> 4;
        const int r0_ = land + (n & 7) * 256 + (n & 3) * 64;
        const int ra = fresh(n < 8 ? r0_ + G * 16 : r0_ ^ 192), rb = fresh((r0_ + G * 16) ^ 64), rc = fresh((r0_ + G * 16) ^ 128);
        const int l0 = fresh(L_WB + wave * 6144 + wlane(lane, 0)), l1 = fresh(L_WB + wave * 6144 + wlane(lane, 1));
        asm volatile("s_waitcnt vmcnt(2)" ::: "memory");
        u32x4 wf[6];
#pragma unroll
        for (int k = 0; k < 3; ++k) {
          wf[2 * k] = wfrag(smem, l0, 2048 * k);
          wf[2 * k + 1] = wfrag(smem, l1, 2048 * k);
        }
        const int rw = fresh(lane < 32 ? L_RED + wave * 160 + lane * 4 : L_ACT);
        const int rr = fresh(L_RED + (lane & 8) * 80 + (lane & 7) * 16);
        const u32 dst = fresh((((lane >> 3) & 3 & ~YM) ? OOB : gyw) + gy_rep(lane >> 3) + gy_chunk(4 * j + ((lane >> 1) & 3)) + g * 256 + (lane & 1) * 8);
        f32x4 d0, d1;
        float mc, t1_, rl;
        u32 t, bud, rare;
        asm volatile(POLL_HEAD("s_lshr_b32 s99, s98, 8\n\ts_or_b32 s98, s98, s99\n\t", "")
                     "s_waitcnt lgkmcnt(2)\n\t"
                     MF R0 ", " F0 ", %[w0], 0\n\t"
                     "v_add_f32_dpp v48, v53, v53 quad_perm:[1,0,3,2] row_mask:0xf bank_mask:0xf\n\t"
                     MF R1 ", " F0 ", %[w1], 0\n\t" AND3("v52", "v53", "v54") "v_cmp_ne_u32 s[98:99], 1, v52\n\t"
                     "s_waitcnt lgkmcnt(1)\n\t"
                     MF R0 ", " F1 ", %[w2], " R0 "\n\t" ZD("quad_perm:[2,3,0,1]", "0xf") AND3("%[t]", "v55", "v56")
                     MF R1 ", " F1 ", %[w3], " R1 "\n\t" ZD("row_half_mirror", "0xf") AND3("%[t]", "v57", "v58")
                     "v_rcp_f32 v48, v48\n\t"
                     "s_waitcnt lgkmcnt(0)\n\t"
                     MF R0 ", " F2 ", %[w4], " R0 "\n\t" AND3("%[t]", "v59", "v60") AND3("%[t]", "v61", "v62") AND3("%[t]", "1", "v63")
                     MF R1 ", " F2 ", %[w5], " R1 "\n\t"
                     "v_cmp_eq_u32 vcc, 0, %[t]\n\ts_cbranch_vccnz 1b\n"
                     "3:\n\tv_readlane_b32 %[rl], v48, 8\n\ts_and_b32 %[rare], s98, 0xff00"
                     : "=&{v[40:43]}"(d0), "=&{v[44:47]}"(d1), [t] "=&v"(t), [bud] "=&s"(bud), "=&{v52}"(mc), "=&{v53}"(t1_),
                       [rl] "=&s"(rl), [rare] "=&s"(rare)
                     : [ra] "v"(ra), [rb] "v"(rb), [rc] "v"(rc), [w0] "v"(wf[0]), [w1] "v"(wf[1]), [w2] "v"(wf[2]), [w3] "v"(wf[3]),
                       [w4] "v"(wf[4]), [w5] "v"(wf[5]), [vo] "v"(voff), [rq] "s"(rq), [land] "s"(landa)
                     : "memory", "vcc", "scc", "s98", "s99", "v48", "v54", "v55", "v56", "v57", "v58", "v59", "v60", "v61", "v62",
                       "v63");
        if (__builtin_expect(rare != 0u, 0)) {
          asm volatile("s_nop 7" : "+v"(d0), "+v"(d1));
          const float fc = __builtin_amdgcn_exp2f(mc - max8(mc));
          const u32 sc = __float_as_uint(fc * __builtin_amdgcn_rcpf(sum8(fc * t1_)));
#pragma unroll
          for (int e = 0; e < 4; ++e) {
            const float se = __uint_as_float(G == 0 ? __builtin_amdgcn_readlane(sc, 8 + e) : __builtin_amdgcn_readlane(sc, 12 + e));
            d0[e] *= se;
            d1[e] *= se;
          }
          rl = 1.0f;
        }
        asm volatile("v_pk_add_f32 v[40:41], v[40:41], v[42:43]\n\tv_add_f32 v40, v40, v41\n\t"
                     "v_pk_add_f32 v[44:45], v[44:45], v[46:47]\n\tv_add_f32 v44, v44, v45\n\t"
                     "s_cmp_eq_u32 %[wv], 0\n\ts_nop 0\n\t"
                     "v_permlane16_swap_b32 v40, v44\n\tv_add_f32 v40, v40, v44\n\tv_mul_f32 v40, %[rl], v40\n\t"
                     "ds_write_b32 %[rw], v40\n\ts_waitcnt lgkmcnt(0)\n\ts_barrier\n\ts_cbranch_scc0 9f\n\t"
                     "ds_read_b128 v[40:43], %[rr]\n\tds_read_b128 v[44:47], %[rr] offset:160\n\t"
                     "ds_read_b128 v[48:51], %[rr] offset:320\n\tds_read_b128 v[52:55], %[rr] offset:480\n\t"
                     "s_waitcnt lgkmcnt(2)\n\t" PKA("v[40:41]", "v[44:45]") PKA("v[42:43]", "v[46:47]")
                     "s_waitcnt lgkmcnt(0)\n\t" PKA("v[48:49]", "v[52:53]") PKA("v[50:51]", "v[54:55]")
                     PKA("v[40:41]", "v[48:49]") PKA("v[42:43]", "v[50:51]") "s_nop 0\n\t"
                     SD("v40", "row_ror:8") SD("v41", "row_ror:8") SD("v42", "row_ror:8") SD("v43", "row_ror:8")
                     "v_cvt_pk_bf16_f32 v40, v40, v41\n\tv_cvt_pk_bf16_f32 v41, v42, v43\n\t"
                     "v_or_b32 v40, 1, v40\n\tv_or_b32 v41, 1, v41\n\t"
                     "buffer_store_dwordx2 v[40:41], %[dst], %[rq], 0 offen sc1\n"
                     "9:"
                     : "+{v[40:43]}"(d0), "+{v[44:47]}"(d1)
                     : [wv] "s"(wave), [rl] "s"(rl), [rw] "v"(rw), [rr] "v"(rr), [dst] "v"(dst), [rq] "s"(rq)
                     : "memory", "scc", "v48", "v49", "v50", "v51", "v52", "v53", "v54", "v55");
      }
      tref = now();
      if (half == 0) {
        if (wave >= 1 && wave < 7) {
          const u16* s0 = karg<56>() + (size_t)(192 * g) * D + 32 * j;
#pragma unroll
          for (int p = 0; p < 2; ++p) dma_piece(s0, D, smem, L_W2, (wave - 1) * 2 + p, fresh(lane0));
          asm volatile("s_waitcnt vmcnt(0)" ::: "memory");
        }
        bar();
        if (wave < 6) cs_gather(smem, rq, fresh(lane0), wave, h);
        if (wave == 4) {
          wait_until(tref + 24 * T_H);
          wait_rt(anc + X_H);
          stamp_poll(smem, rq, ar + st_line(fresh(lane0) & 7));
        }
        if (wave == 5) {
          wait_until(tref + 24 * T_LAZY);
          x_update<false>(smem, rq, fresh(lane0), gy + gy_row(c), c);
        }
      } else if (g == 0 && wave == 2) {
        const int lane = fresh(lane0);
        const int col = j * 32 + (lane & 7) * 4 + ((lane >> 4) & 1) * 2 + (lane >> 5);
        u32 x0v, dummy;
        u16* const po = karg<64>() + col;
        asm volatile("global_load_ushort %0, %2, off\n\tglobal_load_ushort %1, %3, off\n\ts_waitcnt vmcnt(0)"
                     : "=&v"(x0v), "=&v"(dummy)
                     : "v"(karg<0>() + col), "v"(po)
                     : "memory");
        wait_until(tref + 24 * T_LAZY);
        const u32 o = gy + gy_chunk(4 * j + ((lane >> 1) & 3)) + (lane >> 3) * 256 + (lane & 1) * 8;
        const u32 off[3] = {o - GY_PAIR, o - GY_PAIR + GY_INST, o};
        float y[3];
        col_poll<3>(rq, off, y);
        float x3 = bf16r(bf16r(bf16r(bflo(x0v) + y[0]) + y[1]) + y[2]);
        const u32 off2 = fresh(o + GY_INST);
        asm volatile("" : "+v"(x3));
        wait_rt(anc + A_F);
        u32 t, bud;
        asm volatile("s_mov_b32 %[bud], 0x10000\n\ts_branch 2f\n"
                     "1:\n\ts_sub_u32 %[bud], %[bud], 1\n\ts_cbranch_scc1 3f\n"
                     "2:\n\tbuffer_load_dwordx2 v[40:41], %[off], %[rq], 0 offen sc1\n\ts_waitcnt vmcnt(0)\n\t"
                     "v_lshlrev_b32 v42, 16, v40\n\tv_and_b32 v44, 0xffff0000, v40\n\t"
                     "v_lshlrev_b32 v43, 16, v41\n\tv_and_b32 v45, 0xffff0000, v41\n\t"
                     "v_permlane32_swap_b32 v42, v44\n\t" AND3("v40", "1", "v41") "v_permlane32_swap_b32 v43, v45\n\t"
                     PKA("v[42:43]", "v[44:45]") "v_cmp_eq_u32 vcc, 0, %[t]\n\ts_cbranch_vccnz 1b\n"
                     "3:\n\tv_permlane16_swap_b32 v42, v43\n\tv_add_f32 v42, v42, v43\n\ts_nop 1\n\t" SD("v42", "row_ror:8")
                     RND("v42", "v42") "v_add_f32 v42, v42, %[x3]\n\tv_cvt_pk_bf16_f32 v42, v42, v42\n\t"
                     "global_store_short %[po], v42, off"
                     : [t] "=&v"(t), [bud] "=&s"(bud)
                     : [off] "v"(off2), [rq] "s"(rq), [x3] "v"(x3), [po] "v"(po)
                     : "memory", "vcc", "scc", "s98", "s99", DCLOB);
      }
      if (wave >= 6) {
        const int lane = fresh(lane0), wv = wave - 6;
        const int n = lane & 15, G = lane >> 4;
        const int l0 = fresh(L_W2 + wlane(lane, wv));
        u32x4 wf[6];
#pragma unroll
        for (int k = 0; k < 6; ++k) wf[k] = wfrag(smem, l0, 2048 * k);
        const int l48 = lane < 48 ? lane : 0;
        const u32 off = fresh(gh + (u32)l48 * 16);
        const int aw = fresh(L_ACT + wv * 384 + l48 * 8);
        const int ab = fresh(L_ACT + wv * 384 + 16 * G);
        const u32 dst = fresh(((G & (half ? 3 : ~YM)) ? OOB : gyw) + GY_INST + gy_rep((n & 1) * 4 + G) + gy_chunk(4 * j + wv * 2 + (n >> 3)) +
                              g * 256 + (n & 6) * 2);
        if (!half) wait_until(tref + 24 * T_H);
        wait_rt(anc + (half ? A_H : X_H));
        u32 t, bud;
        asm volatile("s_mov_b32 %[bud], 0x10000\n\ts_branch 2f\n"
                     "1:\n\ts_sub_u32 %[bud], %[bud], 1\n\ts_cbranch_scc1 3f\n\ts_waitcnt lgkmcnt(0)\n\ts_sleep 1\n\ts_mov_b64 exec, vcc\n"
                     "2:\n\tbuffer_load_dwordx4 v[32:35], %[off], %[rq], 0 offen sc1\n\t"
                     "buffer_load_dwordx4 v[36:39], %[off], %[rq], 0 offen offset:768 sc1\n\t"
                     "s_mov_b64 exec, -1\n\ts_waitcnt vmcnt(0)\n\t"
                     "v_pk_mul_f32 v[40:41], v[32:33], v[36:37]\n\tv_pk_mul_f32 v[42:43], v[34:35], v[38:39]\n\t"
                     "v_cvt_pk_bf16_f32 v40, v40, v41\n\tv_cvt_pk_bf16_f32 v41, v42, v43\n\t"
                     "ds_write_b64 %[aw], v[40:41]\n\t"
                     "ds_read_b128 v[40:43], %[ab]\n\tds_read_b128 v[44:47], %[ab] offset:64\n\t"
                     "ds_read_b128 v[48:51], %[ab] offset:128\n\tds_read_b128 v[52:55], %[ab] offset:192\n\t"
                     "ds_read_b128 v[56:59], %[ab] offset:256\n\tds_read_b128 v[60:63], %[ab] offset:320\n\t"
                     AND3("v32", "v33", "v34") AND3("%[t]", "v35", "v36") AND3("%[t]", "v37", "v38") AND3("%[t]", "1", "v39")
                     "v_cmp_eq_u32 vcc, 0, %[t]\n\ts_cbranch_vccnz 1b\n"
                     "3:\n\ts_waitcnt lgkmcnt(5)\n\t" MF "v[32:35], v[40:43], %[w0], 0\n\t"
                     "s_waitcnt lgkmcnt(4)\n\t" MF "v[32:35], v[44:47], %[w1], v[32:35]\n\t"
                     "s_waitcnt lgkmcnt(3)\n\t" MF "v[32:35], v[48:51], %[w2], v[32:35]\n\t"
                     "s_waitcnt lgkmcnt(2)\n\t" MF "v[32:35], v[52:55], %[w3], v[32:35]\n\t"
                     "s_waitcnt lgkmcnt(1)\n\t" MF "v[32:35], v[56:59], %[w4], v[32:35]\n\t"
                     "s_waitcnt lgkmcnt(0)\n\t" MF "v[32:35], v[60:63], %[w5], v[32:35]\n\ts_nop 7\n\t"
                     "v_mov_b32_dpp v36, v32 quad_perm:[0,0,2,2] row_mask:0xf bank_mask:0xf\n\t"
                     "v_mov_b32_dpp v37, v32 quad_perm:[1,1,3,3] row_mask:0xf bank_mask:0xf\n\t"
                     "v_cvt_pk_bf16_f32 v36, v36, v37\n\tv_or_b32 v36, 1, v36\n\t"
                     "buffer_store_dword v36, %[dst], %[rq], 0 offen sc1"
                     : [t] "=&v"(t), [bud] "=&s"(bud)
                     : [off] "v"(off), [aw] "v"(aw), [ab] "v"(ab), [dst] "v"(dst), [rq] "s"(rq), [w0] "v"(wf[0]), [w1] "v"(wf[1]),
                       [w2] "v"(wf[2]), [w3] "v"(wf[3]), [w4] "v"(wf[4]), [w5] "v"(wf[5])
                     : "memory", "vcc", "scc", "v32", "v33", "v34", "v35", "v36", "v37", "v38", "v39", FCLOB, DCLOB);
      }
    } else {
      const int lane = fresh(lane0);
      const int n = lane & 15, G = lane >> 4;
      if (half == 1) {
        x_update<false>(smem, rq, lane, gy - GY_PAIR + GY_INST + gy_row(wave), wave);
      } else if (NINF + NPF + FEARLY < 5 && !sO) {
        tnext += TFD2;
#pragma unroll 1
        for (int p = NINF + NPF + FEARLY; p < 5; ++p) {
          pace(tnext, TFL);
          dma_piece(ws0, F, smem, L_WB, wave * 5 + p, fresh(lane0));
        }
      }
      asm volatile("s_waitcnt vmcnt(0) lgkmcnt(0)" ::: "memory");
      G80 a;
      g80_load<512>(a, smem, lane, L_WB + wave * 5120, L_HB + 160 * wave, land);
      const u32 voff = fresh(gy + gy_row(wave) + ((lane * 16) ^ ((lane >> 4) * 64)));
      const int rw = fresh(L_RED + n * 144 + wave * 16 + G * 4);
      const int sw_ = fresh(n == 0 ? L_ST + wave * 16 + G * 4 : L_ACT);
      if (!half) wait_until(tref + 24 * T_YF);
      wait_rt(anc + (half ? A_Y : X_Y));
      dma_poll<false>(rq, voff, landa, USED, USED);
      {
        u32 t, bud;
        asm volatile(G80_LOOP("s_nop 0\n\t")
                     "ds_write2st64_b32 %[rw], v40, v44 offset1:9\n\t" ZD("quad_perm:[2,3,0,1]", "0xf") "s_nop 1\n\t"
                     ZD("row_half_mirror", "0xf") "ds_write_b32 %[sw], v48"
                     : [t] "=&v"(t), [bud] "=&s"(bud)
                     : G80_IN(a), [nm] "s"(0x00ff00ff00ff00ffull), [rw] "v"(rw), [sw] "v"(sw_)
                     : "memory", "vcc", "scc", "s98", "s99", FCLOB, DCLOB);
      }
      const int ln = fresh(lane0);
      const int rr = fresh(L_RED + (ln & 31) * 144 + (ln >> 5) * 64), rs_ = fresh(L_ST + 16 * (ln & 7));
      const u32 dst = fresh(gh + fslot + ln * 4);
      asm volatile("s_cmp_eq_u32 %[wv], 0\n\ts_waitcnt lgkmcnt(0)\n\ts_barrier\n\ts_cbranch_scc0 9f\n\t"
                   "ds_read_b128 v[40:43], %[rs]\n\tds_read_b128 v[44:47], %[rr]\n\tds_read_b128 v[48:51], %[rr] offset:16\n\t"
                   "ds_read_b128 v[52:55], %[rr] offset:32\n\tds_read_b128 v[56:59], %[rr] offset:48\n\t"
                   "s_bitcmp1_b32 %[j], 0\n\ts_mov_b32 s98, 0x10000\n\t"
                   "s_waitcnt lgkmcnt(4)\n\t"
                   "v_add_f32 v40, v40, v41\n\t" SD("v43", "quad_perm:[1,0,3,2]") "v_add_f32 v40, v40, v42\n\t"
                   "s_waitcnt lgkmcnt(2)\n\t" SD("v43", "quad_perm:[2,3,0,1]") SD("v40", "quad_perm:[1,0,3,2]")
                   PKA("v[44:45]", "v[48:49]") SD("v43", "row_half_mirror") SD("v40", "quad_perm:[2,3,0,1]")
                   "v_mul_f32 v43, 0x3acccccd, v43\n\tv_mul_f32 v41, -v43, v43\n\t" SD("v40", "row_half_mirror")
                   "v_fmac_f32 v41, 0x3acccccd, v40\n\t" RND("v41", "|v41|") "v_add_f32 v41, 0x3727c5ac, v41\n\t" RND("v41", "v41")
                   "v_sqrt_f32 v41, v41\n\t" PKA("v[46:47]", "v[50:51]") RND("v41", "v41") "v_rcp_f32 v41, v41\n\t"
                   "s_waitcnt lgkmcnt(0)\n\t"
                   PKA("v[52:53]", "v[56:57]") PKA("v[54:55]", "v[58:59]") PKA("v[44:45]", "v[52:53]") PKA("v[46:47]", "v[54:55]")
                   RND("v43", "v43") "v_add_f32 v44, v44, v45\n\tv_fma_f32 v46, -v43, v47, v46\n\tv_add_f32 v44, v44, v46\n\t"
                   "s_nop 1\n\tv_permlane32_swap_b32 v44, v45\n\tv_add_f32 v44, v44, v45\n\tv_mul_f32 v44, v44, v41\n\t"
                   "s_cbranch_scc1 8f\n\t" RND("v44", "v44")
                   "v_mul_f32 v45, 0xbfb8aa3b, v44\n\tv_exp_f32 v45, v45\n\ts_nop 0\n\tv_add_f32 v45, 1.0, v45\n\t"
                   "v_rcp_f32 v45, v45\n\ts_nop 0\n\tv_mul_f32 v44, v44, v45\n"
                   "8:\n\ts_mov_b32 exec_hi, 0\n\tv_cvt_pk_bf16_f32 v44, s98, v44\n\t"
                   "buffer_store_dword v44, %[dst], %[rq], 0 offen sc1\n\ts_mov_b32 exec_hi, -1\n"
                   "9:"
                   :
                   : [wv] "s"(wave), [j] "s"(j), [rs] "v"(rs_), [rr] "v"(rr), [dst] "v"(dst), [rq] "s"(rq)
                   : "memory", "scc", "s98", FCLOB, DCLOB);
      tref = now();
      if (half == 0 && wave == 1 && j == 20 && ln < 8)
        __builtin_amdgcn_raw_buffer_store_b32((rtnow() << 1) | 1u, rs, gy_rep(ln) + st_line(g), 0, 16);
      if (half == 0) {
        x_update<true>(smem, rq, fresh(lane0), land, wave);
        if (wave < 6) cs_gather(smem, rq, fresh(lane0), wave, h);
        if (wave == 7) {
          wait_until(tref + 24 * T_AN);
          stamp_poll(smem, rq, ar + st_line(ln & 7));
        }
      }
    }
    if (half == 1) break;
    {
      if (wave < 6 || DRY) {
        const int lane = fresh(lane0);
        const int n = lane & 15;
        G80 a;
        g80_load<1024>(a, smem, lane, L_WQ + (wave >> 1) * 10240 + (wave & 1) * 512, L_HB + 160 * c, land);
        const u32 voff = fresh((wave < 6 ? gy : OOB) + GY_INST + gy_row(c) + ((lane * 16) ^ ((lane >> 4) * 64)));
        const float cc = *reinterpret_cast<const float*>(smem + L_CS + wave * 128 + (lane >> 5) * 64 + n * 4);
        const u32 gq = ar + A_GQ + hsub * 16384 + c * 896;
        const u32 dst = fresh((wave < 6 ? gq : OOB) + wave * 128 + (lane >> 5) * 64 + n * 4);
        const u32 dsz = fresh((wave == 5 ? gq : OOB) + 768 + (lane >> 4) * 4);
        bar();
        wait_rt(__builtin_amdgcn_readfirstlane(*(volatile LAS u32*)(smem + L_ANC)) + (wave < 6 ? A_E : 0));
        dma_poll<false>(rq, voff, landa, USED, USED);
        u32 t, bud;
        asm volatile(G80_LOOP("s_mov_b32 vcc_lo, 0xffff\n\t")
                     "s_mov_b32 vcc_hi, 0xffff\n\t" ZD("quad_perm:[2,3,0,1]", "0xf")
                     "v_permlane32_swap_b32 v40, v44\n\tv_add_f32 v44, v40, v44\n\t"
                     ZD("row_half_mirror", "0xf") "v_permlane16_swap_b32 v40, v41\n\tv_readlane_b32 s98, v48, 48\n\t"
                     "v_add_f32 v44, v44, v41\n\ts_mov_b64 exec, vcc\n\t"
                     "v_fmac_f32 v44, s98, %[cc]\n\tv_or_b32 v44, 1, v44\n\t"
                     "buffer_store_dword v44, %[dst], %[rq], 0 offen sc1\n\t"
                     "s_mov_b32 exec_lo, 0x10001\n\ts_mov_b32 exec_hi, 0x10001\n\tv_or_b32 v48, 1, v48\n\t"
                     "buffer_store_dword v48, %[dsz], %[rq], 0 offen sc1\n\ts_mov_b64 exec, -1"
                     : [t] "=&v"(t), [bud] "=&s"(bud)
                     : G80_IN(a), [nm] "s"(wave < 6 ? 0x00ff00ff00ff00ffull : 0ull), [cc] "v"(cc), [dst] "v"(dst), [dsz] "v"(dsz)
                     : "memory", "vcc", "scc", "s98", "s99", FCLOB, DCLOB);
      } else {
        bar();
      }
    }
  }
  asm volatile(".Lmk_code_end:");
}

}

extern "C" void kernel_launch(void* const* d_in, const int* in_sizes, int n_in, void* d_out, int out_size, void* d_ws,
                              size_t ws_size, hipStream_t stream) {
  Params P;
  P.x0 = (const u16*)d_in[0];
  P.kc = (const u16*)d_in[1];
  P.vc = (const u16*)d_in[2];
  P.wq = (const u16*)d_in[3];
  P.wo = (const u16*)d_in[4];
  P.w1 = (const u16*)d_in[6];
  P.w3 = (const u16*)d_in[7];
  P.w2 = (const u16*)d_in[8];
  P.out = (u16*)d_out;
  P.ws = (u32*)d_ws;
  mega_kernel<<<NWG, NT, 0, stream>>>(P);
}
```

```cpp
#define PRB 2
#define TSTEP 235
#include <hip/hip_runtime.h>
#include <hip/hip_bf16.h>
#include <stdint.h>

namespace {

typedef unsigned int u32;
typedef unsigned short u16;
typedef unsigned long long u64;
using u32x2 = __attribute__((ext_vector_type(2))) unsigned;
using u32x4 = __attribute__((ext_vector_type(4))) unsigned;
using f32x2 = __attribute__((ext_vector_type(2))) float;
using f32x4 = __attribute__((ext_vector_type(4))) float;
using s16x4 = __attribute__((ext_vector_type(4))) short;
using bf16x2 = __attribute__((ext_vector_type(2))) __bf16;
using bf16x8 = __attribute__((ext_vector_type(8))) __bf16;
#define LAS __attribute__((address_space(3)))

constexpr int D = 640, H = 32, DH = 192, WIN = 4096, SK = 32768, F = 1536;
constexpr int NWG = 256, NT = 512;
constexpr u32 SPIN_CAP = 1u << 16;
constexpr float SCL = 0.07216878364870322f * 1.4426950408889634f;

#define T_O 12
#define T_YF 88
#define T_H 95
#define T_AN 35
#define PTHR 60
#define T_LAZY 60

#define A_E 104
#define A_Q (A_E + 78)
#define A_O (A_Q + 142)
#define A_Y (A_O + 87)
#define A_H (A_Y + 82)
#define A_F (A_H + 50)

#define X_O 144
#define X_Y (X_O + 93)
#define X_H (X_Y + 85)

constexpr u32 SIDE = 8192, AREA = 17 * 16384;
constexpr u32 A_GO = 0;
constexpr u32 A_GY = 2 * 16384;
constexpr u32 A_GQ = 6 * 16384;
constexpr u32 A_GH = 16 * 16384;
constexpr u32 GO_SZ = 10 * 16384, GY_PAIR = GO_SZ, GY_INST = 32768;
constexpr u32 WS_PB = 4 * AREA;
constexpr u32 WS_GOF = WS_PB + (128 << 10), WS_HB = WS_GOF + 4096;
constexpr u32 WS_GQ1 = WS_HB + 256 * 128;
constexpr u32 WS_END = WS_GQ1 + 256 * 2048;
static_assert(WS_END <= (2u << 20), "layout");
constexpr u32 OOB = 1u << 30;
__device__ __forceinline__ u32 go_slot(int w) { return (u32)(w >> 2) * 16384u + (u32)(w & 3) * 2048u; }
__device__ __forceinline__ u32 gy_row(int i) { return go_slot(i); }
__device__ __forceinline__ u32 gy_chunk(int u) {
  const int i = (u * 205) >> 11;
  return gy_row(i) + (u32)(u - 10 * i) * 16u;
}
__device__ __forceinline__ u32 st_line(int g) { return A_GH + 3072u + (u32)g * 128u; }
__device__ __forceinline__ u32 gy_rep(int r) { return (u32)(r & 3) * AREA + (u32)(r >> 2) * SIDE; }

constexpr int L_ZERO = 0;
constexpr int L_CNT = 16;
constexpr int L_RI = 28;
constexpr int L_ONE = 32;
constexpr int L_TAG = 48;
constexpr int L_ST = 64;
constexpr int L_VOTE = L_ST;
constexpr int L_ANC = 192;
constexpr int L_Q = 256;
constexpr int L_CS = 640;
constexpr int L_HB = L_CS + 768;
constexpr int L_ACT = L_HB + D * 2;
constexpr int L_RED = L_ACT + 2 * 384;
constexpr int L_P = L_RED + 32 * 144;
constexpr int P_W = 1280, P_U = 592, P_PAR = 288;
constexpr int OM_S = 400, OM_P = 200;
constexpr int L_OM = L_P + 8 * P_W;
constexpr int L_LN = L_OM;
constexpr int L_LAND = (L_OM + 8 * OM_S * 4 + 255) / 256 * 256;
constexpr int L_CTL = L_LAND + 6 * 2048;
constexpr int L_WQ = L_LAND + 8 * 2048;
constexpr int L_WB = L_WQ + 6 * 5120;
constexpr int L_W2 = L_WB + 768 * 64;
constexpr int L_PF = L_W2 + 192 * 64;
constexpr int L_TAB = L_PF + 256;
constexpr int L_TOTAL = L_TAB + 8 * 2048;
static_assert(L_LAND % 256 == 0 && L_P % 16 == 0 && L_WQ % 16 == 0 && L_HB % 16 == 0 && L_RED % 16 == 0, "alignment");

struct Params {
  const u16* x0;
  const u16* kc;
  const u16* vc;
  const u16* wq;
  const u16* wo;
  const u16* w1;
  const u16* w3;
  const u16* w2;
  u16* out;
  u32* ws;
};

__device__ __forceinline__ u32 cvt2(float lo, float hi) {
  return __builtin_bit_cast(u32, __builtin_convertvector(f32x2{lo, hi}, bf16x2));
}
__device__ __forceinline__ float bflo(u32 w) { return __uint_as_float(w << 16); }
__device__ __forceinline__ float bfhi(u32 w) { return __uint_as_float(w & 0xffff0000u); }
__device__ __forceinline__ float bf16r(float x) { return bflo(cvt2(x, x)); }

template <int CTRL>
__device__ __forceinline__ float dpp(float x) {
  return __builtin_bit_cast(float, __builtin_amdgcn_mov_dpp(__builtin_bit_cast(int, x), CTRL, 0xf, 0xf, true));
}
constexpr int XOR1 = 0xB1, XOR2 = 0x4E, XOR7 = 0x141, ROR8 = 0x128;
__device__ __forceinline__ float fmx(float a, float b) {
  float r;
  asm("v_max_f32 %0, %1, %2" : "=v"(r) : "v"(a), "v"(b));
  return r;
}
__device__ __forceinline__ float xrow16_max(float x) {
  auto s = __builtin_amdgcn_permlane16_swap(__float_as_uint(x), __float_as_uint(x), false, false);
  x = fmx(__uint_as_float(s[0]), __uint_as_float(s[1]));
  auto t = __builtin_amdgcn_permlane32_swap(__float_as_uint(x), __float_as_uint(x), false, false);
  return fmx(__uint_as_float(t[0]), __uint_as_float(t[1]));
}
__device__ __forceinline__ float xrow16_sum(float x) {
  auto s = __builtin_amdgcn_permlane16_swap(__float_as_uint(x), __float_as_uint(x), false, false);
  x = __uint_as_float(s[0]) + __uint_as_float(s[1]);
  auto t = __builtin_amdgcn_permlane32_swap(__float_as_uint(x), __float_as_uint(x), false, false);
  return __uint_as_float(t[0]) + __uint_as_float(t[1]);
}
__device__ __forceinline__ float sum8(float v) {
  v += dpp<XOR1>(v);
  v += dpp<XOR2>(v);
  v += dpp<XOR7>(v);
  return v;
}
__device__ __forceinline__ float max8(float v) {
  asm("s_nop 1\n\tv_max_f32_dpp %0, %0, %0 quad_perm:[1,0,3,2] row_mask:0xf bank_mask:0xf\n\t"
      "s_nop 1\n\tv_max_f32_dpp %0, %0, %0 quad_perm:[2,3,0,1] row_mask:0xf bank_mask:0xf\n\t"
      "s_nop 1\n\tv_max_f32_dpp %0, %0, %0 row_half_mirror row_mask:0xf bank_mask:0xf"
      : "+v"(v));
  return v;
}
__device__ __forceinline__ float max_ror8(float v) {
  asm("s_nop 1\n\tv_max_f32_dpp %0, %0, %0 row_ror:8 row_mask:0xf bank_mask:0xf" : "+v"(v));
  return v;
}
__device__ __forceinline__ f32x4 sum4x4(f32x4 v) {
  float a = v[0], b = v[1], c = v[2], d = v[3];
#define S4(CTL)                                               \
  "v_add_f32_dpp %0, %0, %0 " CTL " row_mask:0xf bank_mask:0xf\n\t" \
  "v_add_f32_dpp %1, %1, %1 " CTL " row_mask:0xf bank_mask:0xf\n\t" \
  "v_add_f32_dpp %2, %2, %2 " CTL " row_mask:0xf bank_mask:0xf\n\t" \
  "v_add_f32_dpp %3, %3, %3 " CTL " row_mask:0xf bank_mask:0xf\n\t"
  asm("s_nop 1\n\t" S4("quad_perm:[1,0,3,2]") S4("quad_perm:[2,3,0,1]") : "+v"(a), "+v"(b), "+v"(c), "+v"(d));
#undef S4
  return f32x4{a, b, c, d};
}
__device__ __forceinline__ float wave_sum(float v) {
  v = sum8(v);
  v += dpp<ROR8>(v);
  return xrow16_sum(v);
}
__device__ __forceinline__ float tsum8(f32x4 a) {
  auto s0 = __builtin_amdgcn_permlane32_swap(__float_as_uint(a[0]), __float_as_uint(a[1]), false, false);
  auto s1 = __builtin_amdgcn_permlane32_swap(__float_as_uint(a[2]), __float_as_uint(a[3]), false, false);
  const float r0 = __uint_as_float(s0[0]) + __uint_as_float(s0[1]);
  const float r1 = __uint_as_float(s1[0]) + __uint_as_float(s1[1]);
  auto t = __builtin_amdgcn_permlane16_swap(__float_as_uint(r0), __float_as_uint(r1), false, false);
  float r = __uint_as_float(t[0]) + __uint_as_float(t[1]);
  r += dpp<ROR8>(r);
  return r;
}

__device__ __forceinline__ u32 tagv(float v) { return __float_as_uint(v) | 1u; }
__device__ __forceinline__ void store_quad(__amdgpu_buffer_rsrc_t rs, u32 off, f32x4 v) {
  const u32x4 s = {tagv(v[0]), tagv(v[1]), tagv(v[2]), tagv(v[3])};
  __builtin_amdgcn_raw_buffer_store_b128(s, rs, off, 0, 16);
}
template <int N>
__device__ __forceinline__ void gpoll4(u32x4 rs, const u32 (&off)[N], f32x4 (&v)[N]) {
  bool need = true;
  u32 budget = SPIN_CAP;
#pragma unroll
  for (int k = 0; k < N; ++k) asm volatile("" : "=v"(v[k]));
#pragma unroll 1
  for (;;) {
    if (need) {
#pragma unroll
      for (int k = 0; k < N; ++k)
        asm volatile("buffer_load_dwordx4 %0, %1, %2, 0 offen sc1" : "+v"(v[k]) : "v"(off[k]), "s"(rs) : "memory");
    }
    asm volatile("s_waitcnt vmcnt(0)" ::: "memory");
    u32 a = 1u;
#pragma unroll
    for (int k = 0; k < N; ++k) {
      asm volatile("" : "+v"(v[k]));
      a &= __float_as_uint(v[k][0]) & __float_as_uint(v[k][1]) & __float_as_uint(v[k][2]) & __float_as_uint(v[k][3]);
    }
    need = a == 0u;
    if (__builtin_amdgcn_ballot_w64(need) == 0ull || budget == 0u) break;
    --budget;
    __builtin_amdgcn_s_sleep(1);
  }
}
__device__ __forceinline__ u32 now() { return (u32)__builtin_amdgcn_s_memtime(); }
__device__ __forceinline__ void wait_until(u32 t) {
#pragma unroll 1
  while ((int)(now() - t) < -160) __builtin_amdgcn_s_sleep(1);
#pragma unroll 1
  while ((int)(now() - t) < 0) {
  }
}
__device__ __forceinline__ u32 rtnow() { return (u32)__builtin_amdgcn_s_memrealtime(); }
#define WAIT_RT                                                                                                       \
  "s_branch 1f\n0:\n\ts_cmp_lt_i32 s98, -8\n\ts_cbranch_scc0 1f\n\ts_sleep 1\n"                                        \
  "1:\n\ts_memrealtime s[98:99]\n\ts_waitcnt lgkmcnt(0)\n\ts_sub_i32 s98, s98, %[tt]\n\t"                              \
  "s_cmp_ge_u32 s98, 0xfffffc00\n\ts_cbranch_scc1 0b\n\t"
__device__ __forceinline__ void wait_rt(u32 t) { asm volatile(WAIT_RT ::[tt] "s"(t) : "scc", "s98", "s99", "memory"); }
template <int BITS>
__device__ __forceinline__ u32 youngest(u32 t);
__device__ __forceinline__ void lds_wait(volatile LAS u32* p, u32 want) {
  u32 spins = 0;
#pragma unroll 1
  while (__builtin_amdgcn_readfirstlane(*p) != want) {
    if (++spins > (SPIN_CAP << 6)) break;
    __builtin_amdgcn_s_sleep(1);
  }
  asm volatile("" ::: "memory");
}
__device__ __forceinline__ u32 lds_wait_nz(volatile LAS u32* p) {
  u32 spins = 0, v;
#pragma unroll 1
  while ((v = __builtin_amdgcn_readfirstlane(*p)) == 0u) {
    if (++spins > (SPIN_CAP << 6)) break;
    __builtin_amdgcn_s_sleep(1);
  }
  asm volatile("" ::: "memory");
  return v;
}
template <int BITS>
__device__ __forceinline__ u32 youngest(u32 t) {
  const u32 nw = rtnow();
  const float age = (float)((nw - t) & ((1u << BITS) - 1u));
  return nw - (u32)__builtin_amdgcn_readfirstlane((int)-xrow16_max(max_ror8(max8(-age))));
}
__device__ __forceinline__ void stamp_poll(unsigned char* smem, u32x4 rq, u32 so) {
  u32 sv, budget = SPIN_CAP;
#pragma unroll 1
  for (;;) {
    asm volatile("buffer_load_dword %0, %1, %2, 0 offen sc1\n\ts_waitcnt vmcnt(0)" : "=&v"(sv) : "v"(so), "s"(rq) : "memory");
    if (__builtin_amdgcn_ballot_w64((sv & 1u) == 0u) == 0ull || budget == 0u) break;
    --budget;
    __builtin_amdgcn_s_sleep(1);
  }
  *(volatile LAS u32*)(smem + L_ANC) = youngest<24>(sv >> 1) | 1u;
}
__device__ __forceinline__ void lds_add(unsigned char* smem, int off, u32 v) {
  __hip_atomic_fetch_add((LAS u32*)(smem + off), v, __ATOMIC_RELAXED, __HIP_MEMORY_SCOPE_WORKGROUP);
}

__device__ __forceinline__ int fresh(int t) {
  asm volatile("" : "+v"(t));
  return t;
}
__device__ __forceinline__ f32x4 zero4() {
  f32x2 a, b;
  asm volatile("v_mov_b64 %0, 0\n\tv_mov_b64 %1, 0" : "=v"(a), "=v"(b));
  return f32x4{a[0], a[1], b[0], b[1]};
}
__device__ __forceinline__ void bar() { asm volatile("s_waitcnt lgkmcnt(0)\n\ts_barrier" ::: "memory"); }

__device__ __forceinline__ u32x2 trread(unsigned char* smem, int off) {
  return __builtin_bit_cast(u32x2, __builtin_amdgcn_ds_read_tr16_b64_v4i16((LAS s16x4*)(smem + off)));
}
__device__ __forceinline__ u32x4 wfrag(unsigned char* smem, int lb, int off) {
  const u32x2 a = trread(smem, lb + off), b = trread(smem, lb + off + 256);
  return u32x4{a[0], a[1], b[0], b[1]};
}
template <int GS = 512>
__device__ __forceinline__ int wlane(int lane, int cb) {
  const int i = lane & 15, G = lane >> 4;
  return G * GS + (i >> 2) * 64 + (i & 3) * 8 + ((cb ^ (G & 1)) * 32);
}
__device__ __forceinline__ u32x4 afrag(unsigned char* smem, int a) { return *reinterpret_cast<const u32x4*>(smem + a); }
__device__ __forceinline__ f32x4 mfma32(u32x4 w, u32x4 a, f32x4 c) {
  return __builtin_amdgcn_mfma_f32_16x16x32_bf16(__builtin_bit_cast(bf16x8, w), __builtin_bit_cast(bf16x8, a), c, 0, 0, 0);
}

__device__ __forceinline__ void dma_kib(const u16* src, u32 voff, unsigned char* smem, int ldsoff) {
  const u32 dst = (u32)(uintptr_t)(LAS unsigned char*)smem + (u32)ldsoff;
  u32 keep;
  asm volatile("s_mov_b32 %0, m0\n\ts_mov_b32 m0, %2\n\ts_nop 2\n\tglobal_load_lds_dwordx4 %1, %3 nt\n\ts_mov_b32 m0, %0"
               : "=&s"(keep)
               : "v"(voff), "s"(dst), "s"(src)
               : "memory");
}
__device__ __forceinline__ void dma_piece(const u16* src, int ld, unsigned char* smem, int ldsoff, int p, int lane) {
  const int row = 16 * p + (lane >> 2), part = lane & 3;
  const int cb = (part >> 1) ^ (lane >> 5);
  dma_kib(src, (u32)(row * ld + cb * 16 + (part & 1) * 8) * 2u, smem, ldsoff + p * 1024);
}

template <int CTRL>
__device__ __forceinline__ u32 dppu(u32 x) {
  return (u32)__builtin_amdgcn_mov_dpp((int)x, CTRL, 0xf, 0xf, true);
}
__device__ __forceinline__ float sum4(f32x4 d) { return (d[0] + d[1]) + (d[2] + d[3]); }
__device__ __forceinline__ u32 and4(u32x4 v) { return v[0] & v[1] & v[2] & v[3]; }
__device__ __forceinline__ void store16(__amdgpu_buffer_rsrc_t rs, u32 off, u32x4 v) {
  __builtin_amdgcn_raw_buffer_store_b128(v, rs, off, 0, 16);
}
#define POLL16(dst, voff, imm) \
  asm volatile("buffer_load_dwordx4 %0, %1, %2, 0 offen offset:%3 sc1" : "+v"(dst) : "v"(voff), "s"(rq), "n"(imm) : "memory")

struct LnS {
  float mb, ri;
};
__device__ __forceinline__ LnS ln_stats(float s1, float s2) {
#define S2(CTL)                                                     \
  "v_add_f32_dpp %0, %0, %0 " CTL " row_mask:0xf bank_mask:0xf\n\t" \
  "v_add_f32_dpp %1, %1, %1 " CTL " row_mask:0xf bank_mask:0xf\n\ts_nop 0\n\t"
  asm("s_nop 1\n\t" S2("quad_perm:[1,0,3,2]") S2("quad_perm:[2,3,0,1]") S2("row_half_mirror") : "+v"(s1), "+v"(s2));
#undef S2
  const float dm = s1 * (1.0f / D);
  const u32 mv = cvt2(dm, __builtin_fmaxf(fmaf(-dm, dm, s2 * (1.0f / D)), 0.f));
  LnS r;
  r.mb = bflo(mv);
  r.ri = __builtin_amdgcn_rcpf(bf16r(__builtin_amdgcn_sqrtf(bf16r(bfhi(mv) + 1e-5f))));
  return r;
}

struct G80 {
  u32x4 w[6];
  int ra, rb, rc;
};
template <int GS>
__device__ __forceinline__ void g80_load(G80& a, unsigned char* smem, int lane, int wb, int xs, int land) {
  const int m = lane & 15, G = lane >> 4;
  const int l0 = wb + wlane<GS>(lane, 0), l1 = wb + wlane<GS>(lane, 1), bk = 8 * GS - (G >> 1) * 2 * GS;
  a.w[0] = wfrag(smem, l0, 0);
  a.w[1] = wfrag(smem, l1, 0);
  a.w[2] = wfrag(smem, l0, 4 * GS);
  a.w[3] = wfrag(smem, l1, 4 * GS);
  a.w[4] = wfrag(smem, l0 + bk, 0);
  a.w[5] = wfrag(smem, l1 + bk, 0);
  const bool vec = m == 8 || (m < 8 && land >= 0);
  const int st = vec ? 64 : 0;
  const int sa = vec ? (m == 8 ? xs : land + 256 * m) + 16 * G : (m == 15 ? L_ONE : L_ZERO);
  const int sw = (vec && m < 8) ? (m & 3) * 64 : 0;
  a.ra = fresh(sa ^ sw);
  a.rb = fresh((sa + st) ^ sw);
  a.rc = fresh(G < 2 ? (sa + 2 * st) ^ sw : L_TAG);
}
template <bool WAIT = true>
__device__ __forceinline__ void dma_poll(u32x4 rq, u32 voff, u32 land, u64 ea, u64 eb) {
  u32 keep;
  asm volatile("s_mov_b32 %0, m0\n\ts_mov_b32 m0, %3\n\ts_mov_b64 exec, %4\n\t"
               "buffer_load_dwordx4 %1, %2, 0 offen sc1 lds\n\t"
               "s_mov_b64 exec, %5\n\t"
               "buffer_load_dwordx4 %1, %2, 0 offen offset:1024 sc1 lds\n\t"
               "s_mov_b64 exec, -1\n\ts_mov_b32 m0, %0"
               : "=&s"(keep)
               : "v"(voff), "s"(rq), "s"(land), "s"(ea), "s"(eb)
               : "memory");
  if (WAIT) asm volatile("s_waitcnt vmcnt(0)" ::: "memory");
}
template <bool X4>
__device__ __forceinline__ void ctl_load(u32x4 rq, u32 voff, u32 lds) {
  u32 keep;
  if (X4)
    asm volatile("s_mov_b32 %0, m0\n\ts_mov_b32 m0, %3\n\ts_nop 2\n\tbuffer_load_dwordx4 %1, %2, 0 offen sc1 lds\n\ts_mov_b32 m0, %0"
                 : "=&s"(keep) : "v"(voff), "s"(rq), "s"(lds) : "memory");
  else
    asm volatile("s_mov_b32 %0, m0\n\ts_mov_b32 m0, %3\n\ts_nop 2\n\tbuffer_load_dword %1, %2, 0 offen sc1 lds\n\ts_mov_b32 m0, %0"
                 : "=&s"(keep) : "v"(voff), "s"(rq), "s"(lds) : "memory");
}
template <int UN>
__device__ __forceinline__ void retry_masks(u64 need, int lane, u64& ea, u64& eb) {
  u32 m = (u32)need | (u32)(need >> 32);
  m |= m >> 16;
  m = (m | (m >> 8)) & 0xffu;
  const int ln = fresh(lane);
  ea = __builtin_amdgcn_ballot_w64(((m >> (ln / UN)) & 1u) != 0u);
  eb = __builtin_amdgcn_ballot_w64(((m >> ((ln + 64) / UN)) & 1u) != 0u);
}
#define MF "v_mfma_f32_16x16x32_bf16 "
#define F0 "v[52:55]"
#define F1 "v[56:59]"
#define F2 "v[60:63]"
#define FCLOB "v52", "v53", "v54", "v55", "v56", "v57", "v58", "v59", "v60", "v61", "v62", "v63"
#define AND3(a, b, c) "v_bitop3_b32 %[t], " a ", " b ", " c " bitop3:0x80\n\t"
#define BREP "s_bitreplicate_b64_b32 "
#define USEX "s_and_b32 exec_lo, exec_lo, 0x30ff03ff\n\ts_and_b32 exec_hi, exec_hi, 0xff30ff03\n\t"
constexpr u64 USED = 0xff30ff0330ff03ffull;
#define POLL_HEAD(FOLD, MASK)                                                                                        \
  "s_mov_b32 %[bud], 0x10000\n\ts_branch 2f\n"                                                                       \
  "1:\n\ts_sub_u32 %[bud], %[bud], 1\n\ts_cbranch_scc1 3f\n\t"                                                       \
  "s_or_b32 s98, vcc_lo, vcc_hi\n\ts_lshr_b32 s99, s98, 16\n\ts_or_b32 s98, s98, s99\n\t" FOLD                       \
  BREP "s[98:99], s98\n\t" BREP "s[98:99], s98\n\t" BREP "s[98:99], s98\n\t"                                         \
  "s_mov_b32 vcc_lo, m0\n\ts_mov_b32 m0, %[land]\n\t" BREP "exec, s98\n\t" MASK                                      \
  "buffer_load_dwordx4 %[vo], %[rq], 0 offen sc1 lds\n\t" BREP "exec, s99\n\t" MASK                                  \
  "buffer_load_dwordx4 %[vo], %[rq], 0 offen offset:1024 sc1 lds\n\t"                                                \
  "s_mov_b64 exec, -1\n\ts_mov_b32 m0, vcc_lo\n"                                                                     \
  "2:\n\ts_waitcnt vmcnt(0)\n\t"                                                                                     \
  "ds_read_b128 " F0 ", %[ra]\n\tds_read_b128 " F1 ", %[rb]\n\tds_read_b128 " F2 ", %[rc]\n\t"
#define R0 "v[40:43]"
#define R1 "v[44:47]"
#define R2 "v[48:51]"
#define DCLOB "v40", "v41", "v42", "v43", "v44", "v45", "v46", "v47", "v48", "v49", "v50", "v51"
#define ZD(CTL, BANK) "v_add_f32_dpp v48, v48, v48 " CTL " row_mask:0xf bank_mask:" BANK "\n\t"
#define G80_LOOP(FILL)                                                                                               \
  POLL_HEAD("", USEX)                                                                                                \
  "s_waitcnt lgkmcnt(2)\n\t"                                                                                         \
  MF R2 ", " F0 ", " F0 ", 0\n\t" AND3("v52", "v53", "v54")                                                          \
  MF R0 ", " F0 ", %[w0], 0\n\t" MF R1 ", " F0 ", %[w1], 0\n\t"                                                      \
  "s_waitcnt lgkmcnt(1)\n\t"                                                                                         \
  MF R2 ", " F1 ", " F1 ", " R2 "\n\t" AND3("%[t]", "v55", "v56")                                                    \
  MF R0 ", " F1 ", %[w2], " R0 "\n\t" AND3("%[t]", "v57", "v58")                                                     \
  MF R1 ", " F1 ", %[w3], " R1 "\n\t"                                                                                \
  "s_waitcnt lgkmcnt(0)\n\t"                                                                                         \
  MF R2 ", " F2 ", " F2 ", " R2 "\n\t" AND3("%[t]", "v59", "v60") AND3("%[t]", "v61", "v62")                         \
  MF R0 ", " F2 ", %[w4], " R0 "\n\t" AND3("%[t]", "1", "v63")                                                       \
  "v_cmp_eq_u32 vcc, 0, %[t]\n\t"                                                                                    \
  MF R1 ", " F2 ", %[w5], " R1 "\n\t"                                                                                \
  "s_and_b64 vcc, vcc, %[nm]\n\ts_cbranch_scc1 1b\n"                                                                 \
  "3:\n\tv_pk_add_f32 v[48:49], v[48:49], v[50:51]\n\tv_add_f32 v48, v48, v49\n\t" FILL                              \
  "v_pk_add_f32 v[40:41], v[40:41], v[42:43]\n\t" ZD("row_ror:8", "0x1") "v_add_f32 v40, v40, v41\n\t"               \
  "v_pk_add_f32 v[44:45], v[44:45], v[46:47]\n\t" ZD("quad_perm:[1,0,3,2]", "0xf") "v_add_f32 v44, v44, v45\n\t"
#define SD(R, CTL) "v_add_f32_dpp " R ", " R ", " R " " CTL " row_mask:0xf bank_mask:0xf\n\t"
#define PKA(A, B) "v_pk_add_f32 " A ", " A ", " B "\n\t"
#define RND(R, X) "v_cvt_pk_bf16_f32 " R ", 0, " X "\n\t"
#define G80_IN(A)                                                                                                    \
  [ra] "v"(A.ra), [rb] "v"(A.rb), [rc] "v"(A.rc), [w0] "v"(A.w[0]), [w1] "v"(A.w[1]), [w2] "v"(A.w[2]),              \
      [w3] "v"(A.w[3]), [w4] "v"(A.w[4]), [w5] "v"(A.w[5]), [vo] "v"(voff), [rq] "s"(rq), [land] "s"(landa)
__device__ __forceinline__ void g80_run(G80& a, unsigned char* smem, float& r0, float& r1) {
  const u32x4 f0 = afrag(smem, a.ra), f1 = afrag(smem, a.rb), f2 = afrag(smem, a.rc);
  f32x4 d0 = {0.f, 0.f, 0.f, 0.f}, d1 = d0;
  d0 = mfma32(f0, a.w[0], d0);
  d1 = mfma32(f0, a.w[1], d1);
  d0 = mfma32(f1, a.w[2], d0);
  d1 = mfma32(f1, a.w[3], d1);
  d0 = mfma32(f2, a.w[4], d0);
  d1 = mfma32(f2, a.w[5], d1);
  r0 = sum4(d0);
  r1 = sum4(d1);
}
__device__ __forceinline__ f32x4 quad4(float r) { return f32x4{r, dpp<0x55>(r), dpp<0xAA>(r), dpp<0xFF>(r)}; }

__device__ __forceinline__ int q_addr(int d) {
  const int w = d >> 1;
  return L_Q + ((w & 1) * 48 + (w >> 1)) * 4 + (d & 1) * 2;
}

__device__ __forceinline__ void cs_gather(unsigned char* smem, u32x4 rq, int lane, int wave, int h) {
  f32x4 v;
  asm volatile("buffer_load_dwordx4 %0, %1, %2, 0 offen offset:1024 sc1\n\ts_waitcnt vmcnt(0)"
               : "=&v"(v)
               : "v"(WS_GQ1 + (u32)(h * 8 + (lane >> 3)) * 2048 + wave * 128 + (lane & 7) * 16), "s"(rq)
               : "memory");
  const int e = wave * 32 + (lane & 7) * 4 + ((lane >> 4) & 1) * 2 + (lane >> 5);
  *reinterpret_cast<float*>(smem + L_CS + e * 4) = tsum8(v) * (-1.0f / D);
}
template <bool LDS>
__device__ __forceinline__ void x_update(unsigned char* smem, u32x4 rq, int lane, u32 src, int i) {
  const int l = lane < 40 ? lane : 0;
  const u32 off = src + 4 * l;
  u32 v[8];
  if (LDS) {
#pragma unroll
    for (int k = 0; k < 8; ++k) v[k] = *reinterpret_cast<const u32*>(smem + (off ^ ((k & 3) * 64)) + 256 * k);
  } else {
    u32 budget = SPIN_CAP;
#pragma unroll 1
    for (;;) {
#pragma unroll
      for (int k = 0; k < 8; ++k)
        asm volatile("buffer_load_dword %0, %1, %2, 0 offen offset:%3 sc1" : "=v"(v[k]) : "v"(off), "s"(rq), "n"(256 * k) : "memory");
      asm volatile("s_waitcnt vmcnt(0)"
                   : "+v"(v[0]), "+v"(v[1]), "+v"(v[2]), "+v"(v[3]), "+v"(v[4]), "+v"(v[5]), "+v"(v[6]), "+v"(v[7])::"memory");
      const u32 a = v[0] & v[1] & v[2] & v[3] & v[4] & v[5] & v[6] & v[7] & 1u;
      if (__builtin_amdgcn_ballot_w64(a == 0u) == 0ull || budget == 0u) break;
      --budget;
      __builtin_amdgcn_s_sleep(2);
    }
  }
  float lo = 0.f, hi = 0.f;
#pragma unroll
  for (int k = 0; k < 8; ++k) {
    lo += bflo(v[k]);
    hi += bfhi(v[k]);
  }
  u32* px = reinterpret_cast<u32*>(smem + L_HB + 160 * i + 4 * l);
  const u32 xo = *px, y = cvt2(lo, hi);
  if (lane < 40) *px = cvt2(bflo(xo) + bflo(y), bfhi(xo) + bfhi(y));
}
template <int N>
__device__ __forceinline__ void col_poll(u32x4 rq, const u32 (&off)[N], float (&y)[N]) {
  u32x2 v[N];
  u32 budget = SPIN_CAP;
#pragma unroll 1
  for (;;) {
    u32 a = 1u;
#pragma unroll
    for (int k = 0; k < N; ++k)
      asm volatile("buffer_load_dwordx2 %0, %1, %2, 0 offen sc1" : "=v"(v[k]) : "v"(off[k]), "s"(rq) : "memory");
    asm volatile("s_waitcnt vmcnt(0)" ::: "memory");
#pragma unroll
    for (int k = 0; k < N; ++k) {
      asm volatile("" : "+v"(v[k]));
      a &= v[k][0] & v[k][1];
    }
    if (__builtin_amdgcn_ballot_w64(a == 0u) == 0ull || budget == 0u) break;
    --budget;
  }
#pragma unroll
  for (int k = 0; k < N; ++k) y[k] = bf16r(tsum8(f32x4{bflo(v[k][0]), bfhi(v[k][0]), bflo(v[k][1]), bfhi(v[k][1])}));
}

#define CKL 22
#define CKV 5
#define CW 6
#define CWAIT asm volatile("s_waitcnt vmcnt(%0)" ::"n"(CW) : "memory")
#define QACK 5
#define QP 11
#define BEAT0 (QP + QACK)
#define CLRI -1
#define PRE 50
#define CLRW 5
#define CLRX 0
#define XNO 0
#define YM 3
#define DRY 1
#define CLRL 1
#define ALN 1
#define FB 0
#define FB_T {16, 20, 23, 26}, {27, 30, 33, 35}, {34, 37, 41, 42}
#define FB_B 72, 66, 62
#define FB_G 16, 16, 64
#define FB_I 0
#define FB_MISS 250
constexpr int fb_t[3][4] = {FB_T}, fb_b[3] = {FB_B}, fb_g[3] = {FB_G};
constexpr int fb_at(int n, int k) { return (FB & 1) && fb_t[0][k] == n ? 0 : ((FB & 2) && fb_t[1][k] == n ? 1 : ((FB & 4) && fb_t[2][k] == n ? 2 : -1)); }
#define AL_R 3
#define AL_L 8
#define AL_A 11
#define AL_OFF 8
#define QKV 0
#define RLATE 1
#define E_TAB 0
#define E_Q 0
#define E_QK 300
#define E_QN 2
#define PWARM "s_waitcnt vmcnt(0)\n\t"
#define PROBE 1
#define MAP8 1
#define WBIG 10
#define QI 7
#define NINO 4
#define NINF 0
#define PO0 8
#define POS 10
#define PF0 25
#define PFS 2
#define TFL 220
#define TFD2 0
static_assert(QI <= PO0 && PO0 + (NINO - 1) * POS < 42 && PF0 + (NINF - 1) * PFS < 42, "pieces");
#define XCLR 4
#define XPFN "0x140"
#define XTAB 12
#define NPRE 2
#define HOT 1
#define FEARLY 1
#define NPF NPRE
#define TPF 220
#define TPV 220
#define TFD 0
#define XA 15
#define XB 18
#define TS_O (TSTEP * (20 * (48 + NINO) + 12 * (48 + NINF)) / (32 * (48 + NINO)))
#define TS_F (TSTEP * (20 * (48 + NINO) + 12 * (48 + NINF)) / (32 * (48 + NINF)))
#define TSQ 172
#define ROT (wave * 3 + j + g * 5)
static_assert(CKL > BEAT0 && QP > QI && CLRI < QP && 23 - CKL + CKV >= CW && CKV + 1 + CW <= XB - CW && XB - CW <= XA &&
              CLRI < BEAT0, "window");
#define PACE_ASM                                                                                                     \
  "1:\n\ts_memrealtime s[98:99]\n\ts_waitcnt lgkmcnt(0)\n\ts_lshl_b32 s98, s98, 3\n\ts_sub_i32 s98, s98, %[tn]\n\t"  \
  "s_cmp_lt_i32 s98, 0\n\ts_cbranch_scc0 2f\n\ts_sleep 1\n\ts_branch 1b\n2:\n\ts_add_i32 %[tn], %[tn], %[ts]\n\t"
__device__ __forceinline__ void pace(u32& tnext, u32 tstep) {
  asm volatile(PACE_ASM : [tn] "+s"(tnext) : [ts] "s"(tstep) : "scc", "s98", "s99");
}
constexpr int kreg(int i) { return i % 3 < 2 ? 64 + 8 * (i / 3) + 4 * (i % 3) : 128 + 4 * (i / 3); }
constexpr int vreg(int i) { return 160 + 24 * (i / 6) + 8 * (i % 3) + 4 * ((i / 3) & 1); }
#define AQ0 3
#define AGAIN 0
#define STR2(x) #x
#define STR(x) STR2(x)
#define ADAPT_ASM
#define LOADW(reg, voff, sbase, imm)                                                                                \
  asm volatile(PACE_ASM "global_load_dwordx4 v[%[r0]:%[r1]], %[p], %[sb] offset:%[im] nt\n\ts_waitcnt vmcnt(%[w])" ADAPT_ASM \
               : [tn] "+s"(tnext)                                                                                   \
               : [r0] "n"(reg), [r1] "n"((reg) + 3), [p] "v"(voff), [sb] "s"(sbase), [im] "n"(imm), [ts] "s"(tstep - AQ0 * AGAIN), \
                 [w] "n"(WBIG - 1)                                                                                  \
               : "scc", "s98", "s99")

#define HS_WAIT(COND)                                                                                \
  _Pragma("unroll 1") for (;;) {                                                                     \
    gd = CTL[512 + lane0] - CTL[576 + lane0];                                                        \
    const u64 ch = __builtin_amdgcn_ballot_w64(gd != 0u) & 0x1ffull;                                 \
    if (ch > 0xffull || (COND) || tmo == 0u) break;                                                  \
    if ((tmo & 3u) == 0u && lane0 == 0)                                                              \
      __builtin_amdgcn_raw_buffer_store_b32(hb, rs, WS_HB + h * 128 + c * 4, 0, 16);                 \
    hb += 4u;                                                                                        \
    --tmo;                                                                                           \
    __builtin_amdgcn_s_sleep(3);                                                                     \
    ctl_load<false>(rq, hso, (u32)(smb + L_CTL + 2048));                                             \
    asm volatile("s_waitcnt vmcnt(0)" ::: "memory");                                                 \
  }

struct QKAddr {
  int qad[6], idxq, wa;
  f32x4 D0, D1;
};
struct PVAddr {
  int ra, idx, oa, sa, la, ma;
  u32 dst;
  f32x4 O0, O1, O2;
  u32x4 A0, A1, A2, A3;
};
__device__ __forceinline__ void att_table(unsigned char* smem, int lane, int wave, u32 go) {
  const int n = lane & 15, G = lane >> 4;
  const int seg = n & 1, pr = (n >> 1) & 1, tq = n >> 2, xy = G >> 1;
  const int qa = L_Q + (pr * 48 + seg * 8 + (G & 1) * 4) * 4;
  u32 t[16];
#pragma unroll
  for (int k = 0; k < 4; ++k) t[k] = tq == k ? qa + xy * 64 : L_ZERO;
#pragma unroll
  for (int k = 0; k < 2; ++k) t[4 + k] = tq == 2 * k + xy ? qa + 128 : L_ZERO;
  t[6] = 0x4444 + pr * 0xAAAA;
  t[7] = L_P + wave * P_W + ((n >> 1) & 1) * P_PAR + ((((G & 1) * 2 + (n >> 3)) * 4 + (n & 1) * 2 + (G >> 1)) * 16) + ((n >> 2) & 1) * 8;
  t[8] = L_P + wave * P_W + ((n >> 2) & 1) * P_U + (n >> 3) * P_PAR + G * 64;
  t[9] = (n & 3) * 0x5555;
  t[10] = L_OM + (wave * OM_S + (n >> 3) * OM_P + (n & 7) * 4 + (G & 1) * 32) * 4;
  t[11] = L_OM + (wave * OM_S + DH + G) * 4;
  const int src = wave < 6 ? lane >> 3 : lane & 7;
  t[12] = L_OM + (src * OM_S + (wave < 6 ? (wave >> 1) * 64 + (lane & 1) * 32 + (wave & 1) * 16 + (lane & 6) * 2 : DH)) * 4;
  t[13] = L_OM + (src * OM_S + DH + 4) * 4;
  t[14] = lane >= (wave < 6 ? 16 : (wave == 6 ? 2 : 0)) ? 0x8000u
          : go + (wave < 6 ? (wave >= 3 ? 2048 - 192 : 0) + wave * 64 + (lane & 7) * 8 + (lane >> 3) * 4 : lane * 2048 + 192);
  t[15] = wave < 6 ? q_addr(wave * 32 + (lane & 7) * 4 + (lane >> 4)) : (wave == 6 ? L_RI : L_ACT);
#pragma unroll
  for (int k = 0; k < 2; ++k)
    *reinterpret_cast<u32x4*>(smem + L_TAB + wave * 2048 + k * 1024 + lane * 16) =
        u32x4{t[8 * k] | (t[8 * k + 4] << 16), t[8 * k + 1] | (t[8 * k + 5] << 16), t[8 * k + 2] | (t[8 * k + 6] << 16),
              t[8 * k + 3] | (t[8 * k + 7] << 16)};
}
__device__ __forceinline__ void att_setup(QKAddr& r, PVAddr& v, unsigned char* smem, int lane, int wave, int& qw) {
  const u32x4 a = afrag(smem, L_TAB + wave * 2048 + lane * 16), b = afrag(smem, L_TAB + wave * 2048 + 1024 + lane * 16);
  r.D0 = zero4();
  r.D1 = zero4();
  qw = fresh(b[3] >> 16);
#pragma unroll
  for (int k = 0; k < 4; ++k) r.qad[k] = fresh(a[k] & 0xffffu);
  r.qad[4] = fresh(a[0] >> 16);
  r.qad[5] = fresh(a[1] >> 16);
  r.idxq = fresh(a[2] >> 16);
  r.wa = fresh(a[3] >> 16);
  v.ra = fresh(b[0] & 0xffffu);
  v.idx = fresh(b[1] & 0xffffu);
  v.oa = fresh(b[2] & 0xffffu);
  v.la = fresh(b[3] & 0xffffu);
  v.sa = fresh(b[0] >> 16);
  v.ma = fresh(b[1] >> 16);
  v.dst = fresh((int)b[2] >> 16);
  asm volatile("" : "+v"(r.D0), "+v"(r.D1));
}
#define SM "v_smfmac_f32_16x16x64_bf16 "
#define P_WRITE_READ(QA)                                                                                              \
  asm volatile("ds_write2_b32 %[wa], %[p], %[p] offset1:%[o1]\n\t"                                                    \
               "ds_read_b128 %[a0], %[ra]\n\tds_read_b128 %[a1], %[ra] offset:16\n\t"                                 \
               "ds_read_b128 %[a2], %[ra] offset:32\n\tds_read_b128 %[a3], %[ra] offset:48"                           \
               : [a0] "=&v"(pv.A0), [a1] "=&v"(pv.A1), [a2] "=&v"(pv.A2), [a3] "=&v"(pv.A3), "+v"(sk)                 \
               : [wa] "v"(QA.wa + smb), [p] "v"(pb), [ra] "v"(pv.ra + smb), [o1] "n"(P_U / 4 + 1)        \
               : "memory");
#define QS(D, A, K) SM "v[" #D ":" #D "+3], v[" #A ":" #A "+3], v[" #K ":" #K "+7], %[ix]\n\t"
#define SH1(D, A, N) "v_mov_b32_dpp v[" D "], v[" A "] row_shr:" #N " row_mask:0xf bank_mask:0xf bound_ctrl:1\n\t"
#define SH(D, A, N) SH1(#D, #A, N) SH1(#D "+1", #A "+1", N)
#define RDS "ds_read_b32 %[s], %[z] offset:%[ri]\n\t"
#define LW(N) "s_waitcnt lgkmcnt(" #N ")\n\t"
#define QK_BODY                                                                                                       \
  "ds_read_b128 v[40:43], %[q0]\n\tds_read_b128 v[44:47], %[q1]\n\tds_read_b128 v[48:51], %[q2]\n\t"                   \
  "ds_read_b128 v[52:55], %[q3]\n\tds_read_b128 v[56:59], %[q4]\n\tds_read_b128 v[60:63], %[q5]\n\t"                   \
  LW(5) QS(32, 40, 64) QS(36, 40, 96) LW(4) QS(32, 44, 72) QS(36, 44, 104) LW(3) QS(32, 48, 80) QS(36, 48, 112)       \
  LW(2) QS(32, 52, 88) QS(36, 52, 120) LW(0) RDS1 QS(32, 56, 128) QS(36, 56, 144) RDS2 QS(32, 60, 136) QS(36, 60, 152)
#define QK_IN(QA) [q0] "v"(QA.qad[0]), [q1] "v"(QA.qad[1]), [q2] "v"(QA.qad[2]), [q3] "v"(QA.qad[3]), [q4] "v"(QA.qad[4]), [q5] "v"(QA.qad[5]),
#define RDS1
#define RDS2 RDS
#define UNSAFE(X) ((__builtin_amdgcn_ballot_w64(!(X <= 24.f)) != 0ull) | (__builtin_amdgcn_ballot_w64(X >= -24.f) == 0ull))
#define QK_SOFTMAX(QA)                                                                                                \
  {                                                                                                                   \
    float scl, raw, sk;                                                                                               \
    u32 pb;                                                                                                           \
                                                                      \
    asm volatile(QK_BODY                                                                                              \
                 "v_mov_b64 v[52:53], 0\n\tv_mov_b64 v[54:55], 0\n\tv_mov_b64 v[56:57], 0\n\tv_mov_b64 v[58:59], 0\n\t" \
                 "v_mov_b64 v[60:61], 0\n\tv_mov_b64 v[62:63], 0\n\ts_waitcnt lgkmcnt(0)\n\t"                         \
                 PKA("v[32:33]", "v[34:35]") PKA("v[36:37]", "v[38:39]") "s_nop 0\n\t"                                \
                 "v_add_f32_dpp v32, v33, v32 quad_perm:[1,0,3,2] row_mask:0xf bank_mask:0xf\n\t"                     \
                 "v_add_f32_dpp v36, v36, v37 quad_perm:[1,0,3,2] row_mask:0xf bank_mask:0xf\n\t"                     \
                 "v_cndmask_b32 %[raw], v32, v36, %[od]\n\tv_mul_f32 %[sk], %[s], %[raw]\n\tv_exp_f32 %[p], %[sk]\n\t" \
                 "s_nop 0\n\tv_cvt_pk_bf16_f32 %[pb], %[p], 0"                                                        \
                 : "+{v[32:35]}"(QA.D0), "+{v[36:39]}"(QA.D1), [s] "=&v"(scl), [raw] "=&v"(raw), [sk] "=&v"(sk),      \
                   [p] "=&v"(pend), [pb] "=&v"(pb), "=&{v[52:55]}"(pv.O0), "=&{v[56:59]}"(pv.O1), "=&{v[60:63]}"(pv.O2) \
                 : QK_IN(QA) [ix] "v"(QA.idxq), [z] "v"(smb), [ri] "n"(L_RI),                                        \
                   [od] "s"(0xaaaaaaaaaaaaaaaaull), "{v[64:95]}"(kv0), "{v[96:127]}"(kv1), "{v[128:159]}"(kv2)        \
                 : "memory", DCLOB);                                                                                  \
    pmax = 0.f;                                                                                                       \
    P_WRITE_READ(QA)                                                                                                  \
    if (__builtin_expect(UNSAFE(sk) | (__builtin_amdgcn_ballot_w64(__float_as_uint(scl) == 0u) != 0ull), 0)) {        \
      sk = raw * __uint_as_float(lds_wait_nz((volatile LAS u32*)(smem + L_RI)));                                      \
      if (UNSAFE(sk)) pmax = xrow16_max(max_ror8(max8(sk)));                                                          \
      pend = __builtin_amdgcn_exp2f(sk - pmax);                                                                       \
      pb = cvt2(pend, 0.f);                                                                                           \
      asm volatile("s_waitcnt lgkmcnt(0)" : "+v"(pv.A0), "+v"(pv.A1), "+v"(pv.A2), "+v"(pv.A3));                      \
      P_WRITE_READ(QA)                                                                                                \
      asm volatile("s_waitcnt lgkmcnt(0)" : "+v"(pv.A0), "+v"(pv.A1), "+v"(pv.A2), "+v"(pv.A3));                      \
    }                                                                                                                 \
  }

using u32x16 = __attribute__((ext_vector_type(16))) unsigned;
using u32x32 = __attribute__((ext_vector_type(32))) unsigned;
template <int OFF>
__device__ __forceinline__ u16* karg() {
  u64 p;
  asm volatile("s_load_dwordx2 %0, %1, %2\n\ts_waitcnt lgkmcnt(0)" : "=s"(p) : "s"(__builtin_amdgcn_kernarg_segment_ptr()), "n"(OFF));
  return (u16*)p;
}
template <class T>
__device__ __forceinline__ T* mkptr(u32 lo, u32 hi) {
  return (T*)(((u64)hi << 32) | lo);
}

template <int I>
struct IC {
  static constexpr int value = I;
};
template <int A, int B, class Fn>
__device__ __forceinline__ void sfor(Fn&& f) {
  if constexpr (A < B) {
    f(IC<A>{});
    sfor<A + 1, B>(f);
  }
}

__global__ __launch_bounds__(NT, 2) void mega_kernel(Params) {
  __shared__ __attribute__((aligned(16))) unsigned char smem[L_TOTAL + 16];
  u32x16 ka;
  u32x4 kb4;
  {
    u64 pc;
    u32 keep;
    asm volatile("s_load_dwordx16 %[ka], %[kp], 0x0\n\ts_load_dwordx4 %[kb], %[kp], 0x40\n\t"
                 "s_getpc_b64 %[pc]\n.Lmk_pc:\n\t"
                 "s_mov_b32 %[keep], m0\n\ts_mov_b32 m0, %[dst]\n\t"
                 "v_lshlrev_b32 v1, 7, %[tid]\n\tv_min_u32 v1, .Lmk_code_end-.Lmk_pc-4, v1\n\t"
                 "v_cmp_gt_u32 vcc, " XPFN ", %[tid]\n\ts_mov_b64 exec, vcc\n\t"
                 "global_load_lds_dword v1, %[pc]\n\ts_mov_b64 exec, -1\n\ts_mov_b32 m0, %[keep]\n\t"
                 "v_lshrrev_b32 v1, 2, %[tid]\n\tv_cmp_eq_u32 vcc, 3, v1\n\tv_mov_b32 v3, 0x3f803f80\n\ts_nop 0\n\t"
                 "v_cndmask_b32 v2, 0, 1, vcc\n\tv_cmp_eq_u32 vcc, 2, v1\n\tv_lshlrev_b32 v1, 2, %[tid]\n\ts_nop 0\n\t"
                 "v_cndmask_b32 v2, v2, v3, vcc\n\tv_cmp_eq_u32 vcc, 7, %[tid]\n\tv_mov_b32 v3, %[scl]\n\t"
                 "v_min_u32 v1, 80, v1\n\tv_cndmask_b32 v2, v2, v3, vcc\n\t"
                 "ds_write_b32 v1, v2 offset:%[sm]\n\t"
                 "s_waitcnt lgkmcnt(0)\n\ts_barrier"
                 : [ka] "=&s"(ka), [kb] "=&s"(kb4), [pc] "=&s"(pc), [keep] "=&s"(keep)
                 : [kp] "s"(__builtin_amdgcn_kernarg_segment_ptr()), [tid] "v"(threadIdx.x), [dst] "n"(L_PF),
                   [scl] "n"(__builtin_bit_cast(u32, SCL)), [sm] "n"(0)
                 : "memory", "vcc", "v1", "v2", "v3");
  }
  const u32 tstart = (u32)__builtin_amdgcn_s_memrealtime();
  if (threadIdx.x == 128) {
    *(volatile LAS u32*)(smem + L_TOTAL) = tstart;
    *(volatile LAS u32*)(smem + L_TOTAL + 4) = now();
  }
  const int tid = threadIdx.x, lane0 = tid & 63;
  const int wave = __builtin_amdgcn_readfirstlane(tid >> 6);
  const int b = blockIdx.x;
  const int g = b & 7, j = b >> 3, hsub = j & 3, c = j >> 2, h = g * 4 + hsub;
  const bool isO = j < 20;
  const bool leader = b == 0 && wave == 6;
  const u32 fslot = (u32)((j & 1) * 768 + ((j - 20) >> 1) * 128);
  u32 tmo = SPIN_CAP;
  volatile LAS u32* cnt = (volatile LAS u32*)(smem + L_CNT);
  Params P;
  P.x0 = mkptr<const u16>(ka[0], ka[1]);
  P.kc = mkptr<const u16>(ka[2], ka[3]);
  P.vc = mkptr<const u16>(ka[4], ka[5]);
  P.wq = mkptr<const u16>(ka[6], ka[7]);
  P.wo = mkptr<const u16>(ka[8], ka[9]);
  P.w1 = mkptr<const u16>(ka[10], ka[11]);
  P.w3 = mkptr<const u16>(ka[12], ka[13]);
  P.ws = mkptr<u32>(kb4[2], kb4[3]);
  u32* const wsb = (u32*)(((uintptr_t)P.ws + 0x1fffffu) & ~(uintptr_t)0x1fffffu);
  const __amdgpu_buffer_rsrc_t rs = __builtin_amdgcn_make_buffer_rsrc(wsb, 0, (int)WS_END, 0x00020000);
  const u32x4 rq = {(u32)(uintptr_t)wsb, (u32)((uintptr_t)wsb >> 32) & 0xffffu, WS_END, 0x00020000u};

  u32 anc = 0u;
  u32 tref = 0u, ar = 0u;
  u32x32 kv0, kv1, kv2, kv3, kv4, kv5;
  asm volatile(""
               : "={v[64:95]}"(kv0), "={v[96:127]}"(kv1), "={v[128:159]}"(kv2), "={v[160:191]}"(kv3), "={v[192:223]}"(kv4),
                 "={v[224:255]}"(kv5));
  const int smb = (int)(uintptr_t)(LAS unsigned char*)smem;
  const u32 gop = A_GO + go_slot(hsub * 2) + c * 256;
  u32 tnext = tstart * 8u + (u32)wave * (TSTEP / 8) + 5 * TSQ;
  const size_t rowbase = (size_t)h * SK + (SK - WIN) + (wave * 8 + c) * 8;
#define GSTR 512
#undef ROT
#define ROT h
  const u16* vb = P.vc + rowbase * DH;
  const int rot = ROT;
  const u16* ws0 = isO ? P.wo + (size_t)(768 * g) * D + 32 * j : ((j & 1) ? P.w3 : P.w1) + 192 * g + 32 * ((j - 20) >> 1);
  const bool sO = isO || (HOT && ((u32)(uintptr_t)ws0 & 0x380u) == 0x180u);
  u32 tstep = sO ? TS_O : TS_F;
  int pn = isO ? wave * 6 : wave * 5;
  {
    volatile LAS u32* const CTL = (volatile LAS u32*)(smem + L_CTL);
    u32 gd, hb = 0u;
    const u32 lho = WS_HB + (lane0 >> 1) * 128 + (lane0 & 1) * 16;
    const u32 hso = lane0 < 8 ? WS_HB + (u32)h * 128 + lane0 * 4 : WS_GOF + g * 128;
#define CLEAR1                                                                                                      \
  {                                                                                                                 \
    const u32 ca = gy_rep((g + CLRX) & 7) + fresh(lane0) * 16;                                                      \
    if (j < 16 || (g & 3 & ~YM) == 0)                                                                               \
      store16(rs, ca + (u32)(j >> 3) * 16384u + (u32)(j & 7) * 1024u, u32x4{0u, 0u, 0u, 0u});                       \
    if (lane0 < 8) store16(rs, ca + A_GH + (u32)j * 128u, u32x4{0u, 0u, 0u, 0u});                                   \
  }
    if (CLRI == -2 && wave == CLRW) CLEAR1
    if (wave < 6) {
      const u16* s0 = P.wq + (size_t)(80 * c + 8 * (wave & 1)) * (H * DH) + h * DH + (wave >> 1) * 64;
      const u32 wo_ = (u32)(((lane0 >> 2) & 7) * (H * DH) + (lane0 >> 5) * 32 + (((lane0 >> 1) ^ wave) & 1) * 16 + (lane0 & 1) * 8) * 2u;
      u32 tq = tnext - 5 * TSQ;
#pragma unroll 1
      for (int p = 0; p < 5; ++p) {
        pace(tq, TSQ);
        dma_kib(s0 + (size_t)p * (16 * H * DH), wo_, smem, L_WQ + (wave >> 1) * 10240 + (wave & 1) * 1024 + p * 2048);
        if (CLRI == -3 && wave == CLRW && p == 0) CLEAR1
      }
    } else {
      if (ALN && b < 8 && wave == 7 && lane0 < 32)
        __builtin_amdgcn_raw_buffer_store_b32(lane0 ? (tstart - 4096u) << 12 : tstart, rs, WS_GOF + 2048 + b * 128 + lane0 * 4, 0, 16);
      if (leader) {
        ctl_load<true>(rq, lho, (u32)(smb + L_CTL));
        ctl_load<false>(rq, WS_GOF + (lane0 & 7) * 128, (u32)(smb + L_CTL + 2560));
      }
      ctl_load<false>(rq, hso, (u32)(smb + L_CTL + (wave == 6 ? 2816 : 2304)));
      if (wave == 6) {
        u32 keep;
        asm volatile("s_mov_b32 %0, m0\n\ts_mov_b32 m0, %4\n\ts_nop 2\n\tglobal_load_lds_dwordx4 %1, %3\n\t"
                     "global_load_lds_dword %2, %3 offset:1024\n\ts_mov_b32 m0, %0"
                     : "=&s"(keep)
                     : "v"(lane0 * 16), "v"(lane0 * 4), "s"(P.x0), "s"((u32)(smb + L_HB))
                     : "memory");
      }
      u32 rtt;
      {
        u32 pa = 0u, pb = 0u;
        u64 t1, t2;
        const u32 pbo = WS_PB + (u32)(wave & 1) * (SIDE + 128u) + (u32)((b >> 3) & 7) * 16384u + (u32)((b & 7) * 4 + (b >> 6)) * 256u;
        asm volatile(PWARM ".p2align 6\n\ts_mov_b64 exec, 1\n\t"
                     "buffer_load_dword %0, off, %4, %5 sc1 nt\n\tbuffer_load_dword %1, off, %4, %6 sc1 nt\n\t"
                     "s_mov_b64 exec, -1\n\t"
                     "s_waitcnt vmcnt(1)\n\ts_memtime %2\n\ts_waitcnt vmcnt(0)\n\ts_memtime %3\n\ts_waitcnt lgkmcnt(0)"
                     : "+v"(pa), "+v"(pb), "=&s"(t1), "=&s"(t2)
                     : "s"(rq), "s"(pbo), "s"(pbo ^ SIDE)
                     : "memory");
        rtt = __builtin_amdgcn_readfirstlane((u32)t2 - (u32)t1) + 1u;
      }
      if (wave == 6) {
        *(volatile LAS u32*)(smem + L_VOTE) = rtt;
      } else {
        if (CLRI == -1 && !(XNO & 1)) CLEAR1
        hb = rtt;
      }
    }
    if (tid == 0) *(volatile LAS u32*)(smem + L_ANC) = 0u;
    *reinterpret_cast<u32x4*>(smem + L_P + wave * P_W + lane0 * 16) = u32x4{0u, 0u, 0u, 0u};
    *reinterpret_cast<u32x4*>(smem + (lane0 < 16 ? L_P + wave * P_W + 1024 + lane0 * 16 : L_OM + (wave * OM_S + OM_P + DH) * 4)) =
        u32x4{0u, 0u, 0u, 0u};
    const u16* kb = P.kc + rowbase * DH;
    const u32 ko = ((lane0 & 15) >> 1) * 384 + 16 * ((lane0 & 1) * 4 + (lane0 >> 4)), vo = (lane0 >> 3) * 384 + 16 * (lane0 & 7);
#define WPO(n) ((n) >= PO0 && ((n) - PO0) % POS == 0 && ((n) - PO0) / POS < NINO)
#define WPF(n) ((n) >= PF0 && ((n) - PF0) % PFS == 0 && ((n) - PF0) / PFS < NINF)
#define WPIECE(n)                                                                                                   \
  if ((WPO(n) && sO) || (WPF(n) && !sO)) {                                                                          \
    pace(tnext, tstep);                                                                                             \
    if (XNO & 8) {                                                                                                  \
      dma_kib(vb - (size_t)WIN * DH + (n) * 64 * DH, lane0 * 16, smem, L_WB + pn * 1024);                           \
      ++pn;                                                                                                         \
    } else if ((n) != PO0 || isO) dma_piece(ws0, isO ? D : F, smem, L_WB, pn++, lane0);                             \
  }
#define FBB(r) (float)fb_b[r]
#define FBG(r) (float)fb_g[r]
#define FBI (float)FB_I
#define FBACK(n)                                                                                                    \
  if (fb_at(n, 0) >= 0 && j == 4 * wave + 1) {                                                                      \
    u32 q;                                                                                                          \
    asm volatile("s_getreg_b32 %0, hwreg(HW_REG_IB_STS, 0, 4)" : "=s"(q));                                          \
    const u32 nw = rtnow(), e = (nw * 8u - tnext + q * tstep) >> 3;                                                 \
    if (lane0 == 0)                                                                                                 \
      __builtin_amdgcn_raw_buffer_store_b32((nw << 12) | (e < 4095u ? e : 0u), rs,                                  \
                                            WS_GOF + 2048 + 32 * (fb_at(n, 0) + 1) + g * 128 + wave * 4, 0, 16);    \
  }                                                                                                                 \
  if (fb_at(n, 1) >= 0 && wave == 6) {                                                                              \
    const int ln = fresh(lane0);                                                                                    \
    ctl_load<false>(rq, WS_GOF + 2048 + 32 * (fb_at(n, 1) + 1) + (ln >> 3) * 128 + (ln & 7) * 4, (u32)(smb + L_CTL + 3072)); \
  }                                                                                                                 \
  if (fb_at(n, 2) >= 0 && wave == 6) {                                                                              \
    const int r = fb_at(n, 2) < 0 ? 0 : fb_at(n, 2);                                                                \
    asm volatile("s_waitcnt vmcnt(%0)" ::"n"(fb_t[r][2] - fb_t[r][1]) : "memory");                                  \
    const u32 w = *(volatile LAS u32*)(smem + L_CTL + 3072 + fresh(lane0) * 4);                                     \
    const float m = wave_sum((rtnow() << 12) - w < (600u << 12) ? (float)(w & 0xfffu) : (float)FB_MISS) * (1.f / 64); \
    volatile LAS int* const pw = (volatile LAS int*)(smem + L_VOTE + 12);                                           \
    *pw = (int)__builtin_fminf(__builtin_fmaxf((m - FBB(r)) * FBG(r) * (1.f / 16), 0.f) + FBI * (1.f / 16) * (float)*pw, 400.f); \
  }                                                                                                                 \
  if (fb_at(n, 3) >= 0 || (ALN && (n) == AL_A + 15))                                                                \
    tstep = (sO ? TS_O : TS_F) + __builtin_amdgcn_readfirstlane(*(volatile LAS u32*)(smem + L_VOTE + 12));
    const u32 pre = wave < 6 ? PRE : 0;
    tstep -= pre;
    sfor<0, 24>([&](auto ic) __attribute__((always_inline)) {
      constexpr int i = ic.value;
      if (i == XTAB) tnext -= E_TAB;
      if (i == QI) tnext -= wave < 6 ? E_Q : 0;
      LOADW(kreg(i), ko, kb + ((i / 3 + rot) & 7) * GSTR * DH, 128 * (i % 3));
      if (i == XTAB) tnext += E_TAB;
      if (i == QI) tnext += wave < 6 ? E_Q : 0;
      if (i == QI) {
        tstep += pre;
        tnext += (QI + 1) * pre;
      }
      FBACK(i)
      WPIECE(i)
      if (i == XTAB) att_table(smem, fresh(lane0), wave, gop);
      if (i == 1 && wave == 6) {
        const u32x4 lv = afrag(smem, L_HB + lane0 * 16);
        const u32 xb = *reinterpret_cast<const u32*>(smem + L_HB + 1024 + lane0 * 4);
        float v[10];
#pragma unroll
        for (int k = 0; k < 4; ++k) {
          v[2 * k] = bflo(lv[k]);
          v[2 * k + 1] = bfhi(lv[k]);
        }
        v[8] = bflo(xb);
        v[9] = bfhi(xb);
        float sm = 0.f;
#pragma unroll
        for (int k = 0; k < 10; ++k) sm += v[k];
        const float mean = wave_sum(sm) * (1.0f / D);
        float q = 0.f;
#pragma unroll
        for (int k = 0; k < 10; ++k) q = fmaf(v[k] - mean, v[k] - mean, q);
        const float var = wave_sum(q) * (1.0f / D);
        const float mb = bf16r(mean);
        const float ri = __builtin_amdgcn_rcpf(bf16r(__builtin_amdgcn_sqrtf(bf16r(bf16r(var) + 1e-5f))));
        u32 hp[5];
#pragma unroll
        for (int k = 0; k < 5; ++k) {
          const u32 cc = cvt2(v[2 * k] - mb, v[2 * k + 1] - mb);
          hp[k] = cvt2(bflo(cc) * ri, bfhi(cc) * ri);
        }
        *reinterpret_cast<u32x4*>(smem + L_LN + lane0 * 16) = u32x4{hp[0], hp[1], hp[2], hp[3]};
        *reinterpret_cast<u32*>(smem + L_LN + 1024 + lane0 * 4) = hp[4];
        asm volatile("s_waitcnt lgkmcnt(0)" ::: "memory");
        if (lane0 == 0) lds_add(smem, L_VOTE + 4, 1u);
      }
      if (ALN && wave == 6) {
        if (i == AL_R) ctl_load<false>(rq, WS_GOF + 2048 + (fresh(lane0) & 7) * 128, (u32)(smb + L_CTL + 1024));
        if (i == AL_L) {
          asm volatile("s_waitcnt vmcnt(%0)" ::"n"(AL_L - AL_R) : "memory");
          const u32 d = CTL[256 + lane0] - tstart + AL_OFF;
          *(volatile LAS int*)(smem + L_VOTE + 8) = (int)sum8(d + 128u < 256u ? (float)(int)d : 0.f) >> 4;
        }
      }
      if (ALN && i == AL_A) tstep += __builtin_amdgcn_readfirstlane(*(volatile LAS u32*)(smem + L_VOTE + 8));
      if (i == QI - 1 && wave < 6) {
        asm volatile("s_waitcnt vmcnt(%0)" ::"n"(QI) : "memory");
        if (lane0 == 0) lds_add(smem, L_VOTE + 4, 1u);
      }
      if (i == QI && wave < 6) {
        lds_wait((volatile LAS u32*)(smem + L_VOTE + 4), 7u);
        G80 a;
        g80_load<1024>(a, smem, lane0, L_WQ + (wave >> 1) * 10240 + (wave & 1) * 512, L_LN + 160 * c, -1);
        float r0, r1;
        g80_run(a, smem, r0, r1);
        auto sw = __builtin_amdgcn_permlane32_swap(__float_as_uint(r0), __float_as_uint(r1), false, false);
        const f32x4 qd = quad4(__uint_as_float(sw[1]));
        const int qp = wave * 128 + (lane0 >> 5) * 64 + (lane0 & 15) * 4;
        if ((lane0 & 19) == 0) *reinterpret_cast<u32x2*>(smem + L_RED + (qp >> 1)) = u32x2{cvt2(qd[0], qd[1]), cvt2(qd[2], qd[3])};
        if ((lane0 & 19) == 16) *reinterpret_cast<f32x4*>(smem + L_CS + qp) = qd;
        if (lane0 == 0) lds_add(smem, L_CNT + 4, 1u);
      }
      if (wave == 7) {
        if (i == CLRI) CLEAR1
        if (i == QP) {
          lds_wait(cnt + 1, 6u);
          const int ln = fresh(lane0);
          if (ln < 24 && !(XNO & 2)) store16(rs, WS_GQ1 + (u32)(h * 8 + c) * 2048 + ln * 16, afrag(smem, L_RED + ln * 16));
        }
        if (i == BEAT0) {
          asm volatile("s_waitcnt vmcnt(%0)" ::"n"(BEAT0 - QP) : "memory");
          const int df = (int)(__builtin_amdgcn_readfirstlane(*(volatile LAS u32*)(smem + L_VOTE)) - hb);
          hb = df > PTHR ? 1u << (g >> 2) : (df < -PTHR ? 2u >> (g >> 2) : 0u);
          hb += (__builtin_amdgcn_readfirstlane(CTL[576 + c]) & ~3u) + 4u;
          if (lane0 == 0) __builtin_amdgcn_raw_buffer_store_b32(hb, rs, WS_HB + h * 128 + c * 4, 0, 16);
          hb += 4u;
        }
        if (i == CKL) ctl_load<false>(rq, hso, (u32)(smb + L_CTL + 2048));
      }
      if (i == CKL && leader) {
        ctl_load<true>(rq, lho, (u32)(smb + L_CTL + 1024));
      }
    });
#define VLOADS(A, B)                                                                           \
  _Pragma("unroll") for (int i = A; i < B; ++i) {                                              \
    if (i == XA - E_QN) tnext -= wave < 4 ? E_QK : 0;                                          \
    if (i == XB - E_QN) tnext -= wave < 4 ? 0 : E_QK;                                          \
    LOADW(vreg(i), vo, vb + ((i / 3 + rot) & 7) * GSTR * DH, 128 * (i % 3));                      \
    FBACK(24 + i)                                                                              \
    WPIECE(24 + i)                                                                             \
  }
    VLOADS(0, CKV)
    if (wave == 7) {
      CWAIT;
      HS_WAIT((u32)ch == 0xffu)
      if (lane0 == 0) cnt[1] = 7u;
    }
    if (leader) {
      CWAIT;
      const u32x4 xa = afrag(smem, L_CTL + lane0 * 16);
      u32x4 lv;
#pragma unroll 1
      for (;;) {
        asm volatile("" ::: "memory");
        lv = afrag(smem, L_CTL + 1024 + lane0 * 16);
        const bool same = lv[0] == xa[0] || lv[1] == xa[1] || lv[2] == xa[2] || lv[3] == xa[3];
        if (__builtin_amdgcn_ballot_w64(same) == 0ull || tmo == 0u) break;
        --tmo;
        __builtin_amdgcn_s_sleep(4);
        ctl_load<true>(rq, lho, (u32)(smb + L_CTL + 1024));
        asm volatile("s_waitcnt vmcnt(0)" ::: "memory");
      }
      u32 nv = 256u;
#pragma unroll
      for (int e = 0; e < 4; ++e)
        nv += (u32)__builtin_popcountll(__builtin_amdgcn_ballot_w64((lv[e] & 2u) != 0u)) -
              (u32)__builtin_popcountll(__builtin_amdgcn_ballot_w64((lv[e] & 1u) != 0u));
      if (lane0 < 8) __builtin_amdgcn_raw_buffer_store_b32(CTL[640 + lane0] + (nv > 256u ? 2u : 1u), rs, WS_GOF + lane0 * 128, 0, 16);
    }
    VLOADS(CKV, CKV + 1)
    if (wave < 3) {
      const int ln = fresh(lane0);
      lds_wait(cnt + 1, 7u);
      ctl_load<true>(rq, WS_GQ1 + (u32)(h * 8 + (ln >> 3)) * 2048 + wave * 128 + (ln & 7) * 16, (u32)(smb + L_LAND + wave * 2048));
    }
    VLOADS(CKV + 1, CKV + 1 + CW)
    if (wave < 3) {
      const int ln = fresh(lane0);
      CWAIT;
      const u32x4 v = afrag(smem, L_LAND + wave * 2048 + ln * 16);
      const float t0 = tsum8(f32x4{bflo(v[0]), bflo(v[1]), bflo(v[2]), bflo(v[3])});
      const float t1 = tsum8(f32x4{bfhi(v[0]), bfhi(v[1]), bfhi(v[2]), bfhi(v[3])});
      *reinterpret_cast<u32*>(smem + q_addr(wave * 64 + (ln & 7) * 8 + ((ln >> 4) & 1) * 4 + (ln >> 5) * 2)) = cvt2(t0, t1);
      asm volatile("s_waitcnt lgkmcnt(0)" ::: "memory");
      if (lane0 == 0) lds_add(smem, L_CNT, 1u);
    }
    VLOADS(CKV + 1 + CW, XB - CW)
    if (wave == 7) ctl_load<false>(rq, hso, (u32)(smb + L_CTL + 2048));
    VLOADS(XB - CW, XA)
    if (wave >= 4) {
      VLOADS(XA, XB)
      if (wave == 7) {
        CWAIT;
        HS_WAIT(false)
        if (lane0 == 8) cnt[2] = (gd >> 1) | 2u;
      }
    }
  }

#pragma unroll 1
  for (int half = 0;; ++half) {
    {
      float pend, pmax;
      PVAddr pv;
      QKAddr qa;
      const int lane = fresh(lane0);
      const u32 off = ar + A_GQ + hsub * 16384 + (wave < 6 ? (lane >> 3) * 896 + wave * 128 + (lane & 7) * 16 : (lane & 7) * 896 + 768);
      u32x4 qv;
      asm volatile("v_mov_b64 v[56:57], -1\n\tv_mov_b64 v[58:59], -1" : "={v[56:59]}"(qv));
      if (half == 1) {
        anc = lds_wait_nz((volatile LAS u32*)(smem + L_ANC));
        if (wave == 6) *(volatile LAS u32*)(smem + L_RI) = 0u;
        asm volatile("s_cmp_lt_u32 %[wv], 7\n\ts_cselect_b64 exec, -1, 0\n\t" WAIT_RT
                     "buffer_load_dwordx4 v[56:59], %[off], %[rq], 0 offen sc1\n\ts_mov_b64 exec, -1"
                     : "+{v[56:59]}"(qv)
                     : [off] "v"(off), [rq] "s"(rq), [wv] "s"(wave), [tt] "s"(anc + (wave < 7 ? A_Q : 0))
                     : "memory", "scc", "s98", "s99");
        if (wave < 4) __builtin_amdgcn_s_setprio(2);
      }
      int qw;
      att_setup(qa, pv, smem, lane, wave, qw);
      {
        u32 t, bud;
#define QRETRY(L) "s_sub_u32 %[bud], %[bud], 1\n\ts_cbranch_scc1 " L "\n\ts_sleep 1\n\tbuffer_load_dwordx4 v[56:59], %[off], %[rq], 0 offen sc1\n"
        asm volatile("s_mov_b32 %[bud], 0x10000\n\ts_cmp_eq_u32 %[hf], 0\n\ts_cbranch_scc1 0f\n\t"
                     "s_cmp_eq_u32 %[wv], 6\n\ts_cbranch_scc0 2f\n\ts_branch 5f\n"
                     "0:\n\ts_mov_b32 %[bud], 0x400000\n"
                     "0:\n\tds_read_b32 %[t], %[z] offset:%[cn]\n\ts_sub_u32 %[bud], %[bud], 1\n\ts_cbranch_scc1 8f\n\ts_waitcnt lgkmcnt(0)\n\t"
                     "v_cmp_eq_u32 vcc, 3, %[t]\n\ts_cbranch_vccnz 8f\n\ts_sleep 1\n\ts_branch 0b\n"
                     "4:\n\t" QRETRY("6f")
                     "5:\n\ts_waitcnt vmcnt(0)\n\t" AND3("v56", "v57", "v58") AND3("%[t]", "1", "v59")
                     "v_cmp_eq_u32 vcc, 0, %[t]\n\ts_cbranch_vccnz 4b\n"
                     "6:\n\tv_add_f32 v56, v56, v57\n\t" SD("v59", "quad_perm:[1,0,3,2]") "v_add_f32 v56, v56, v58\n\t"
                     "s_cmp_eq_u32 %[wv], 7\n\t" SD("v59", "quad_perm:[2,3,0,1]") SD("v56", "quad_perm:[1,0,3,2]")
                     "s_nop 0\n\t" SD("v59", "row_half_mirror") SD("v56", "quad_perm:[2,3,0,1]")
                     "v_mul_f32 v59, 0x3acccccd, v59\n\tv_mul_f32 v57, -v59, v59\n\t" SD("v56", "row_half_mirror")
                     "s_waitcnt lgkmcnt(0)\n\ts_barrier\n\ts_cbranch_scc1 8f\n"
                     "7:\n\t"
                     "v_fmac_f32 v57, 0x3acccccd, v56\n\t" RND("v57", "|v57|") "v_add_f32 v57, 0x3727c5ac, v57\n\t" RND("v57", "v57")
                     "v_sqrt_f32 v57, v57\n\ts_nop 0\n\t" RND("v57", "v57") "v_rcp_f32 v57, v57\n\ts_nop 0\n\t"
                     "v_mul_f32 v57, %[scl], v57\n\tv_or_b32 v57, 1, v57\n\tds_write_b32 %[qw], v57\n\t"
                     "s_cbranch_scc1 6b\n\ts_branch 8f\n"
                     "1:\n\t" QRETRY("3f")
                     "2:\n\ts_cmp_eq_u32 %[wv], 7\n\ts_waitcnt vmcnt(0)\n\t"
                     "v_permlane32_swap_b32 v56, v58\n\tv_permlane32_swap_b32 v57, v59\n\t"
                     "v_pk_add_f32 v[60:61], v[56:57], v[58:59]\n\t" AND3("v56", "v57", "v58") AND3("%[t]", "1", "v59")
                     "v_permlane16_swap_b32 v60, v61\n\tv_add_f32 v60, v60, v61\n\t"
                     "v_cmp_eq_u32 vcc, 0, %[t]\n\ts_nop 0\n\t" SD("v60", "row_ror:8")
                     "v_cvt_pk_bf16_f32 v60, v60, v60\n\tds_write_b16 %[qw], v60\n\ts_cbranch_vccnz 1b\n\ts_cbranch_scc1 7b\n"
                     "3:\n\ts_waitcnt lgkmcnt(0)\n\ts_barrier\n"
                     "8:"
                     : [t] "=&v"(t), [bud] "=&s"(bud), "+{v[56:59]}"(qv)
                     : [hf] "s"(half), [wv] "s"(wave), [z] "v"(smb), [cn] "n"(L_CNT), [off] "v"(off), [rq] "s"(rq), [qw] "v"(qw),
                       [scl] "n"(__builtin_bit_cast(u32, SCL))
                     : "memory", "vcc", "scc", "v60", "v61");
      }
      QK_SOFTMAX(qa)
      if (half == 0) {
        const int lv_ = fresh(lane0);
        const u32 vo = (lv_ >> 3) * 384 + 16 * (lv_ & 7);
        tnext += E_QK;
        if (wave < 4) {
          VLOADS(XA, XB)
        }
        VLOADS(XB, 24)
      }
#define DA(CTL) "v_add_f32_dpp %[l], %[l], %[l] " CTL " row_mask:0xf bank_mask:0xf\n\t"
#define PX(o, v, a) SM "%[o" #o "], %[a" #a "], v[160+24*" #a "+8*" #o ":167+24*" #a "+8*" #o "], %[ix]\n\t"
      asm volatile("s_waitcnt vmcnt(6) lgkmcnt(3)\n\t" PX(0, x, 0) DA("quad_perm:[1,0,3,2]") PX(1, y, 0) PX(2, z, 0)
                   "s_waitcnt lgkmcnt(2)\n\t" PX(0, x, 1) DA("quad_perm:[2,3,0,1]") PX(1, y, 1) PX(2, z, 1)
                   "s_waitcnt lgkmcnt(0)\n\t" PX(0, x, 2) DA("row_half_mirror") PX(1, y, 2) PX(2, z, 2)
                   : [o0] "+v"(pv.O0), [o1] "+v"(pv.O1), [o2] "+v"(pv.O2), [a0] "+v"(pv.A0), [a1] "+v"(pv.A1),
                     [a2] "+v"(pv.A2), [a3] "+v"(pv.A3), [l] "+v"(pend)
                   : [ix] "v"(pv.idx), "{v[160:191]}"(kv3), "{v[192:223]}"(kv4), "{v[224:255]}"(kv5)
                   : "memory");
#define CLEAR2                                                                                                         \
  {                                                                                                                    \
    const int p2 = (isO ? j : j - 8) * 2 + (wave & 1);                                                                 \
    const u32 ca = ((isO ? j < 12 : (p2 < 40 || (g & 3) == 0)) ? gy_rep(g) : OOB) + (u32)(4 + (p2 >> 2)) * 16384u +    \
                   (u32)(p2 & 3) * 2048u + fresh(lane0) * 16;                                                          \
    const u32x4 z = __builtin_bit_cast(u32x4, zero4());                                                                \
    store16(rs, ca, z);                                                                                                \
    store16(rs, ca + 1024, z);                                                                                         \
  }
      static_assert(5 - NINF >= NPF && 6 - NINO >= NPRE && NPF >= NPRE, "pieces behind V");
      if (half == 0) {
        if (!(XNO & 4)) {
          const int ln = fresh(lane0);
          if (wave < 6) {
            if (ln < 8) store16(rs, WS_GQ1 + (u32)(h * 8 + c) * 2048 + 1024 + wave * 128 + ln * 16, afrag(smem, L_CS + wave * 128 + ln * 16));
          } else if (CLRL == 0) {
            CLEAR2
          }
        }
        anc = rtnow();
        anc += (int)(tnext - (u32)wave * (TSTEP / 8) + (sO ? 0 : (48 + NINO) * TS_O - (48 + NINF) * TS_F) - anc * 8u) >> 3;
        if (!isO) tnext += TFD;
#pragma unroll 1
        for (int p = 0; p < (sO ? NPRE : NPF); ++p) {
          pace(tnext, sO ? TPV : TPF);
          dma_piece(ws0, isO ? D : F, smem, L_WB, pn++, fresh(lane0));
        }
        if (CLRL == 2 && wave < 2) CLEAR2
        ar = (u32)(g & 3) * AREA + (((u32)(g >> 2) ^ lds_wait_nz(cnt + 2)) & 1u) * SIDE;
      }
      const u32 gob = ar + (u32)half * GO_SZ;
      if (NPF != NPRE && sO) asm volatile("s_waitcnt vmcnt(%0)" ::"n"(NPRE) : "memory");
      static_assert(CLRL != 2 || NPF == NPRE, "counts");
      if (CLRL == 2 && wave >= 2) asm volatile("s_waitcnt vmcnt(%0)" ::"n"(NPRE) : "memory");
      asm volatile("s_waitcnt vmcnt(%[np])\n\t" PX(0, x, 3) DA("row_ror:8") PX(1, y, 3) PX(2, z, 3) "s_setprio 0"
                   : [o0] "+v"(pv.O0), [o1] "+v"(pv.O1), [o2] "+v"(pv.O2), [a3] "+v"(pv.A3), [l] "+v"(pend)
                   : [ix] "v"(pv.idx), [np] "n"(NPF + (CLRL == 2 ? 2 : 0)), "{v[224:255]}"(kv5)
                   : "memory");
      static_assert(NINO + NPRE == 6 && PF0 != PO0, "pieces");
      if (half == 0 && FEARLY && !sO) {
#pragma unroll 1
        for (int p = NINF + NPF; p < NINF + NPF + FEARLY; ++p) dma_piece(ws0, F, smem, L_WB, wave * 5 + p, fresh(lane0));
      }
#define MX(CTL) "s_nop 1\n\tv_max_f32_dpp v49, v49, v49 " CTL " row_mask:0xf bank_mask:0xf\n\t"
#define MXP(W) "v_mov_b32 v50, v49\n\ts_nop 1\n\tv_permlane" W "_swap_b32 v49, v50\n\tv_max_f32 v49, v49, v50\n\t"
#define RB(D, A, BANK) "v_add_f32_dpp " D ", " A ", " A " row_ror:8 row_mask:0xf bank_mask:" BANK "\n\t"
#define SWP(W) "v_permlane" W "_swap_b32 v40, v42\n\tv_permlane" W "_swap_b32 v41, v43\n\t" PKA("v[40:41]", "v[42:43]")
#define SC2(R) "v_pk_mul_f32 " R ", " R ", v[50:51] op_sel_hi:[1,0]\n\t"
      asm volatile("ds_write2_b32 %[la], %[l], %[mx] offset1:4\n\ts_mov_b64 exec, %[m]\n\ts_cmp_lt_u32 %[wv], 6\n\ts_nop 1\n\t"
                   "ds_write_b128 %[a], %[o0]\n\tds_write_b128 %[a], %[o1] offset:256\n\t"
                   "ds_write_b128 %[a], %[o2] offset:512\n\t"
                   "s_mov_b64 exec, -1\n\ts_waitcnt lgkmcnt(0)\n\ts_barrier\n\t"
                   "ds_read_b32 v48, %[ma]\n\tds_read_b128 v[40:43], %[sa]\n\tds_read_b128 v[44:47], %[sa] offset:%[op]\n\t"
                   "s_waitcnt lgkmcnt(2)\n\tv_cmp_ne_u32 vcc, 0, v48\n\ts_cbranch_vccnz 5f\n"
                   "4:\n\ts_cbranch_scc0 6f\n\ts_waitcnt lgkmcnt(0)\n\t"
                   PKA("v[40:41]", "v[44:45]") PKA("v[42:43]", "v[46:47]") "s_nop 0\n\t"
                   RB("v40", "v40", "0x3") RB("v41", "v41", "0x3") RB("v40", "v42", "0xc") RB("v41", "v43", "0xc")
                   "s_nop 0\n\t" SWP("16") "s_nop 1\n\t" SWP("32")
                   "v_cvt_pk_bf16_f32 v40, v40, v41\n\tv_or_b32 v40, 1, v40\n\t"
                   "buffer_store_dword v40, %[dst], %[rq], %[so] offen sc1\n\ts_branch 9f\n"
                   "5:\n\tv_mov_b32 v49, v48\n\t" MX("quad_perm:[1,0,3,2]") MX("quad_perm:[2,3,0,1]") MX("row_half_mirror") MX("row_ror:8")
                   MXP("16") MXP("32") "v_sub_f32 v50, v48, v49\n\tv_exp_f32 v50, v50\n\tv_mov_b32 v48, v49\n\ts_waitcnt lgkmcnt(0)\n\t"
                   SC2("v[40:41]") SC2("v[42:43]") SC2("v[44:45]") SC2("v[46:47]") "s_branch 4b\n"
                   "6:\n\ts_waitcnt lgkmcnt(0)\n\t" PKA("v[42:43]", "v[40:41]") "v_add_f32 v41, v42, v43\n\t"
                   "v_or_b32 v40, 1, v48\n\tv_or_b32 v42, 1, v48\n\t" SD("v41", "quad_perm:[1,0,3,2]")
                   "v_mov_b32 v43, 1\n\ts_nop 0\n\t" SD("v41", "quad_perm:[2,3,0,1]") "s_nop 1\n\t" SD("v41", "row_half_mirror")
                   "v_or_b32 v41, 1, v41\n\tbuffer_store_dwordx4 v[40:43], %[dst], %[rq], %[so] offen sc1\n\ts_nop 1\n"
                   "9:"
                   :
                   : [m] "s"(0xff00ff0000ff00ffull), [a] "v"(pv.oa + smb), [o0] "v"(pv.O0), [o1] "v"(pv.O1), [o2] "v"(pv.O2),
                     [la] "v"(pv.la + smb), [l] "v"(pend), [mx] "v"(pmax), [wv] "s"(wave), [ma] "v"(pv.ma), [sa] "v"(pv.sa),
                     [op] "n"(OM_P * 4), [dst] "v"(pv.dst), [rq] "s"(rq), [so] "s"(gob)
                   : "memory", "vcc", "scc", DCLOB);
      tref = now();
      if (CLRL == 1 && half == 0 && wave >= 6) CLEAR2
    }
    if (half == 0 && threadIdx.x == 128) {
      *(volatile LAS u32*)(smem + L_TOTAL + 8) = rtnow();
      *(volatile LAS u32*)(smem + L_TOTAL + 12) = now();
    }
    const u32 go = ar + A_GO + (u32)half * GO_SZ;
    const u32 gyw = A_GY + (u32)half * GY_PAIR;
    const u32 gy = gyw + ar - (u32)(g & 3 & ~YM) * AREA;
    const u32 gh = ar + A_GH + (u32)half * 1536u;
    const int land = L_LAND + wave * 2048;
    const u32 landa = (u32)(uintptr_t)(LAS unsigned char*)smem + (u32)land;
    if (isO) {
      {
        const int lane = fresh(lane0);
        const u32 voff = fresh(go + go_slot(wave) + ((lane * 16) ^ ((lane >> 4) * 64)));
        if (!half) wait_until(tref + 24 * T_O);
        wait_rt(anc + (half ? A_O : X_O));
        dma_poll<false>(rq, voff, landa, ~0ull, ~0ull);
        const int n = lane & 15, G = lane >> 4;
        const int r0_ = land + (n & 7) * 256 + (n & 3) * 64;
        const int ra = fresh(n < 8 ? r0_ + G * 16 : r0_ ^ 192), rb = fresh((r0_ + G * 16) ^ 64), rc = fresh((r0_ + G * 16) ^ 128);
        const int l0 = fresh(L_WB + wave * 6144 + wlane(lane, 0)), l1 = fresh(L_WB + wave * 6144 + wlane(lane, 1));
        asm volatile("s_waitcnt vmcnt(2)" ::: "memory");
        u32x4 wf[6];
#pragma unroll
        for (int k = 0; k < 3; ++k) {
          wf[2 * k] = wfrag(smem, l0, 2048 * k);
          wf[2 * k + 1] = wfrag(smem, l1, 2048 * k);
        }
        const int rw = fresh(lane < 32 ? L_RED + wave * 160 + lane * 4 : L_ACT);
        const int rr = fresh(L_RED + (lane & 8) * 80 + (lane & 7) * 16);
        const u32 dst = fresh((((lane >> 3) & 3 & ~YM) ? OOB : gyw) + gy_rep(lane >> 3) + gy_chunk(4 * j + ((lane >> 1) & 3)) + g * 256 + (lane & 1) * 8);
        f32x4 d0, d1;
        float mc, t1_, rl;
        u32 t, bud, rare;
        asm volatile(POLL_HEAD("s_lshr_b32 s99, s98, 8\n\ts_or_b32 s98, s98, s99\n\t", "")
                     "s_waitcnt lgkmcnt(2)\n\t"
                     MF R0 ", " F0 ", %[w0], 0\n\t"
                     "v_add_f32_dpp v48, v53, v53 quad_perm:[1,0,3,2] row_mask:0xf bank_mask:0xf\n\t"
                     MF R1 ", " F0 ", %[w1], 0\n\t" AND3("v52", "v53", "v54") "v_cmp_ne_u32 s[98:99], 1, v52\n\t"
                     "s_waitcnt lgkmcnt(1)\n\t"
                     MF R0 ", " F1 ", %[w2], " R0 "\n\t" ZD("quad_perm:[2,3,0,1]", "0xf") AND3("%[t]", "v55", "v56")
                     MF R1 ", " F1 ", %[w3], " R1 "\n\t" ZD("row_half_mirror", "0xf") AND3("%[t]", "v57", "v58")
                     "v_rcp_f32 v48, v48\n\t"
                     "s_waitcnt lgkmcnt(0)\n\t"
                     MF R0 ", " F2 ", %[w4], " R0 "\n\t" AND3("%[t]", "v59", "v60") AND3("%[t]", "v61", "v62") AND3("%[t]", "1", "v63")
                     MF R1 ", " F2 ", %[w5], " R1 "\n\t"
                     "v_cmp_eq_u32 vcc, 0, %[t]\n\ts_cbranch_vccnz 1b\n"
                     "3:\n\tv_readlane_b32 %[rl], v48, 8\n\ts_and_b32 %[rare], s98, 0xff00"
                     : "=&{v[40:43]}"(d0), "=&{v[44:47]}"(d1), [t] "=&v"(t), [bud] "=&s"(bud), "=&{v52}"(mc), "=&{v53}"(t1_),
                       [rl] "=&s"(rl), [rare] "=&s"(rare)
                     : [ra] "v"(ra), [rb] "v"(rb), [rc] "v"(rc), [w0] "v"(wf[0]), [w1] "v"(wf[1]), [w2] "v"(wf[2]), [w3] "v"(wf[3]),
                       [w4] "v"(wf[4]), [w5] "v"(wf[5]), [vo] "v"(voff), [rq] "s"(rq), [land] "s"(landa)
                     : "memory", "vcc", "scc", "s98", "s99", "v48", "v54", "v55", "v56", "v57", "v58", "v59", "v60", "v61", "v62",
                       "v63");
        if (__builtin_expect(rare != 0u, 0)) {
          asm volatile("s_nop 7" : "+v"(d0), "+v"(d1));
          const float fc = __builtin_amdgcn_exp2f(mc - max8(mc));
          const u32 sc = __float_as_uint(fc * __builtin_amdgcn_rcpf(sum8(fc * t1_)));
#pragma unroll
          for (int e = 0; e < 4; ++e) {
            const float se = __uint_as_float(G == 0 ? __builtin_amdgcn_readlane(sc, 8 + e) : __builtin_amdgcn_readlane(sc, 12 + e));
            d0[e] *= se;
            d1[e] *= se;
          }
          rl = 1.0f;
        }
        asm volatile("v_pk_add_f32 v[40:41], v[40:41], v[42:43]\n\tv_add_f32 v40, v40, v41\n\t"
                     "v_pk_add_f32 v[44:45], v[44:45], v[46:47]\n\tv_add_f32 v44, v44, v45\n\t"
                     "s_cmp_eq_u32 %[wv], 0\n\ts_nop 0\n\t"
                     "v_permlane16_swap_b32 v40, v44\n\tv_add_f32 v40, v40, v44\n\tv_mul_f32 v40, %[rl], v40\n\t"
                     "ds_write_b32 %[rw], v40\n\ts_waitcnt lgkmcnt(0)\n\ts_barrier\n\ts_cbranch_scc0 9f\n\t"
                     "ds_read_b128 v[40:43], %[rr]\n\tds_read_b128 v[44:47], %[rr] offset:160\n\t"
                     "ds_read_b128 v[48:51], %[rr] offset:320\n\tds_read_b128 v[52:55], %[rr] offset:480\n\t"
                     "s_waitcnt lgkmcnt(2)\n\t" PKA("v[40:41]", "v[44:45]") PKA("v[42:43]", "v[46:47]")
                     "s_waitcnt lgkmcnt(0)\n\t" PKA("v[48:49]", "v[52:53]") PKA("v[50:51]", "v[54:55]")
                     PKA("v[40:41]", "v[48:49]") PKA("v[42:43]", "v[50:51]") "s_nop 0\n\t"
                     SD("v40", "row_ror:8") SD("v41", "row_ror:8") SD("v42", "row_ror:8") SD("v43", "row_ror:8")
                     "v_cvt_pk_bf16_f32 v40, v40, v41\n\tv_cvt_pk_bf16_f32 v41, v42, v43\n\t"
                     "v_or_b32 v40, 1, v40\n\tv_or_b32 v41, 1, v41\n\t"
                     "buffer_store_dwordx2 v[40:41], %[dst], %[rq], 0 offen sc1\n"
                     "9:"
                     : "+{v[40:43]}"(d0), "+{v[44:47]}"(d1)
                     : [wv] "s"(wave), [rl] "s"(rl), [rw] "v"(rw), [rr] "v"(rr), [dst] "v"(dst), [rq] "s"(rq)
                     : "memory", "scc", "v48", "v49", "v50", "v51", "v52", "v53", "v54", "v55");
      }
      tref = now();
      if (half == 0) {
        if (wave >= 1 && wave < 7) {
          const u16* s0 = karg<56>() + (size_t)(192 * g) * D + 32 * j;
#pragma unroll
          for (int p = 0; p < 2; ++p) dma_piece(s0, D, smem, L_W2, (wave - 1) * 2 + p, fresh(lane0));
          asm volatile("s_waitcnt vmcnt(0)" ::: "memory");
        }
        bar();
        if (wave < 6) cs_gather(smem, rq, fresh(lane0), wave, h);
        if (wave == 4) {
          wait_until(tref + 24 * T_H);
          wait_rt(anc + X_H);
          stamp_poll(smem, rq, ar + st_line(fresh(lane0) & 7));
        }
        if (wave == 5) {
          wait_until(tref + 24 * T_LAZY);
          x_update<false>(smem, rq, fresh(lane0), gy + gy_row(c), c);
        }
      } else if (g == 0 && wave == 2) {
        const int lane = fresh(lane0);
        const int col = j * 32 + (lane & 7) * 4 + ((lane >> 4) & 1) * 2 + (lane >> 5);
        u32 x0v, dummy;
        u16* const po = karg<64>() + col;
        asm volatile("global_load_ushort %0, %2, off\n\tglobal_load_ushort %1, %3, off\n\ts_waitcnt vmcnt(0)"
                     : "=&v"(x0v), "=&v"(dummy)
                     : "v"(karg<0>() + col), "v"(po)
                     : "memory");
        wait_until(tref + 24 * T_LAZY);
        const u32 o = gy + gy_chunk(4 * j + ((lane >> 1) & 3)) + (lane >> 3) * 256 + (lane & 1) * 8;
        const u32 off[3] = {o - GY_PAIR, o - GY_PAIR + GY_INST, o};
        float y[3];
        col_poll<3>(rq, off, y);
        float x3 = bf16r(bf16r(bf16r(bflo(x0v) + y[0]) + y[1]) + y[2]);
        const u32 off2 = fresh(o + GY_INST);
        asm volatile("" : "+v"(x3));
        wait_rt(anc + A_F);
        u32 t, bud;
        asm volatile("s_mov_b32 %[bud], 0x10000\n\ts_branch 2f\n"
                     "1:\n\ts_sub_u32 %[bud], %[bud], 1\n\ts_cbranch_scc1 3f\n"
                     "2:\n\tbuffer_load_dwordx2 v[40:41], %[off], %[rq], 0 offen sc1\n\ts_waitcnt vmcnt(0)\n\t"
                     "v_lshlrev_b32 v42, 16, v40\n\tv_and_b32 v44, 0xffff0000, v40\n\t"
                     "v_lshlrev_b32 v43, 16, v41\n\tv_and_b32 v45, 0xffff0000, v41\n\t"
                     "v_permlane32_swap_b32 v42, v44\n\t" AND3("v40", "1", "v41") "v_permlane32_swap_b32 v43, v45\n\t"
                     PKA("v[42:43]", "v[44:45]") "v_cmp_eq_u32 vcc, 0, %[t]\n\ts_cbranch_vccnz 1b\n"
                     "3:\n\tv_permlane16_swap_b32 v42, v43\n\tv_add_f32 v42, v42, v43\n\ts_nop 1\n\t" SD("v42", "row_ror:8")
                     RND("v42", "v42") "v_add_f32 v42, v42, %[x3]\n\tv_cvt_pk_bf16_f32 v42, v42, v42\n\t"
                     "global_store_short %[po], v42, off"
                     : [t] "=&v"(t), [bud] "=&s"(bud)
                     : [off] "v"(off2), [rq] "s"(rq), [x3] "v"(x3), [po] "v"(po)
                     : "memory", "vcc", "scc", "s98", "s99", DCLOB);
        if (j == 0) {
          const u32 tnow = rtnow(), cnow = now();
          const u32x4 z = afrag(smem, L_TOTAL);
          float r;
          if (PRB == 1) r = (float)(tnow - z[0]) - 2300.f;
          if (PRB == 2) r = (float)(anc - z[0]) - 1800.f;
          if (PRB == 3) r = (float)(tnow - anc) - 500.f;
          if (PRB == 4) r = (float)(z[3] - z[1]) * 10.f / (float)(z[2] - z[0]) - 100.f;
          if (PRB == 5) r = (float)(cnow - z[3]) * 10.f / (float)(tnow - z[2]) - 100.f;
          if (PRB == 6) r = (float)(z[2] - z[0]) - 1600.f;
          r = __builtin_fminf(__builtin_fmaxf(r, 0.f), 600.f);
          const u32 te = tnow + (u32)__builtin_amdgcn_readfirstlane((int)(r * 10.f));
          while ((int)(rtnow() - te) < 0) __builtin_amdgcn_s_sleep(1);
        }
      }
      if (wave >= 6) {
        const int lane = fresh(lane0), wv = wave - 6;
        const int n = lane & 15, G = lane >> 4;
        const int l0 = fresh(L_W2 + wlane(lane, wv));
        u32x4 wf[6];
#pragma unroll
        for (int k = 0; k < 6; ++k) wf[k] = wfrag(smem, l0, 2048 * k);
        const int l48 = lane < 48 ? lane : 0;
        const u32 off = fresh(gh + (u32)l48 * 16);
        const int aw = fresh(L_ACT + wv * 384 + l48 * 8);
        const int ab = fresh(L_ACT + wv * 384 + 16 * G);
        const u32 dst = fresh(((G & (half ? 3 : ~YM)) ? OOB : gyw) + GY_INST + gy_rep((n & 1) * 4 + G) + gy_chunk(4 * j + wv * 2 + (n >> 3)) +
                              g * 256 + (n & 6) * 2);
        if (!half) wait_until(tref + 24 * T_H);
        wait_rt(anc + (half ? A_H : X_H));
        u32 t, bud;
        asm volatile("s_mov_b32 %[bud], 0x10000\n\ts_branch 2f\n"
                     "1:\n\ts_sub_u32 %[bud], %[bud], 1\n\ts_cbranch_scc1 3f\n\ts_waitcnt lgkmcnt(0)\n\ts_sleep 1\n\ts_mov_b64 exec, vcc\n"
                     "2:\n\tbuffer_load_dwordx4 v[32:35], %[off], %[rq], 0 offen sc1\n\t"
                     "buffer_load_dwordx4 v[36:39], %[off], %[rq], 0 offen offset:768 sc1\n\t"
                     "s_mov_b64 exec, -1\n\ts_waitcnt vmcnt(0)\n\t"
                     "v_pk_mul_f32 v[40:41], v[32:33], v[36:37]\n\tv_pk_mul_f32 v[42:43], v[34:35], v[38:39]\n\t"
                     "v_cvt_pk_bf16_f32 v40, v40, v41\n\tv_cvt_pk_bf16_f32 v41, v42, v43\n\t"
                     "ds_write_b64 %[aw], v[40:41]\n\t"
                     "ds_read_b128 v[40:43], %[ab]\n\tds_read_b128 v[44:47], %[ab] offset:64\n\t"
                     "ds_read_b128 v[48:51], %[ab] offset:128\n\tds_read_b128 v[52:55], %[ab] offset:192\n\t"
                     "ds_read_b128 v[56:59], %[ab] offset:256\n\tds_read_b128 v[60:63], %[ab] offset:320\n\t"
                     AND3("v32", "v33", "v34") AND3("%[t]", "v35", "v36") AND3("%[t]", "v37", "v38") AND3("%[t]", "1", "v39")
                     "v_cmp_eq_u32 vcc, 0, %[t]\n\ts_cbranch_vccnz 1b\n"
                     "3:\n\ts_waitcnt lgkmcnt(5)\n\t" MF "v[32:35], v[40:43], %[w0], 0\n\t"
                     "s_waitcnt lgkmcnt(4)\n\t" MF "v[32:35], v[44:47], %[w1], v[32:35]\n\t"
                     "s_waitcnt lgkmcnt(3)\n\t" MF "v[32:35], v[48:51], %[w2], v[32:35]\n\t"
                     "s_waitcnt lgkmcnt(2)\n\t" MF "v[32:35], v[52:55], %[w3], v[32:35]\n\t"
                     "s_waitcnt lgkmcnt(1)\n\t" MF "v[32:35], v[56:59], %[w4], v[32:35]\n\t"
                     "s_waitcnt lgkmcnt(0)\n\t" MF "v[32:35], v[60:63], %[w5], v[32:35]\n\ts_nop 7\n\t"
                     "v_mov_b32_dpp v36, v32 quad_perm:[0,0,2,2] row_mask:0xf bank_mask:0xf\n\t"
                     "v_mov_b32_dpp v37, v32 quad_perm:[1,1,3,3] row_mask:0xf bank_mask:0xf\n\t"
                     "v_cvt_pk_bf16_f32 v36, v36, v37\n\tv_or_b32 v36, 1, v36\n\t"
                     "buffer_store_dword v36, %[dst], %[rq], 0 offen sc1"
                     : [t] "=&v"(t), [bud] "=&s"(bud)
                     : [off] "v"(off), [aw] "v"(aw), [ab] "v"(ab), [dst] "v"(dst), [rq] "s"(rq), [w0] "v"(wf[0]), [w1] "v"(wf[1]),
                       [w2] "v"(wf[2]), [w3] "v"(wf[3]), [w4] "v"(wf[4]), [w5] "v"(wf[5])
                     : "memory", "vcc", "scc", "v32", "v33", "v34", "v35", "v36", "v37", "v38", "v39", FCLOB, DCLOB);
      }
    } else {
      const int lane = fresh(lane0);
      const int n = lane & 15, G = lane >> 4;
      if (half == 1) {
        x_update<false>(smem, rq, lane, gy - GY_PAIR + GY_INST + gy_row(wave), wave);
      } else if (NINF + NPF + FEARLY < 5 && !sO) {
        tnext += TFD2;
#pragma unroll 1
        for (int p = NINF + NPF + FEARLY; p < 5; ++p) {
          pace(tnext, TFL);
          dma_piece(ws0, F, smem, L_WB, wave * 5 + p, fresh(lane0));
        }
      }
      asm volatile("s_waitcnt vmcnt(0) lgkmcnt(0)" ::: "memory");
      G80 a;
      g80_load<512>(a, smem, lane, L_WB + wave * 5120, L_HB + 160 * wave, land);
      const u32 voff = fresh(gy + gy_row(wave) + ((lane * 16) ^ ((lane >> 4) * 64)));
      const int rw = fresh(L_RED + n * 144 + wave * 16 + G * 4);
      const int sw_ = fresh(n == 0 ? L_ST + wave * 16 + G * 4 : L_ACT);
      if (!half) wait_until(tref + 24 * T_YF);
      wait_rt(anc + (half ? A_Y : X_Y));
      dma_poll<false>(rq, voff, landa, USED, USED);
      {
        u32 t, bud;
        asm volatile(G80_LOOP("s_nop 0\n\t")
                     "ds_write2st64_b32 %[rw], v40, v44 offset1:9\n\t" ZD("quad_perm:[2,3,0,1]", "0xf") "s_nop 1\n\t"
                     ZD("row_half_mirror", "0xf") "ds_write_b32 %[sw], v48"
                     : [t] "=&v"(t), [bud] "=&s"(bud)
                     : G80_IN(a), [nm] "s"(0x00ff00ff00ff00ffull), [rw] "v"(rw), [sw] "v"(sw_)
                     : "memory", "vcc", "scc", "s98", "s99", FCLOB, DCLOB);
      }
      const int ln = fresh(lane0);
      const int rr = fresh(L_RED + (ln & 31) * 144 + (ln >> 5) * 64), rs_ = fresh(L_ST + 16 * (ln & 7));
      const u32 dst = fresh(gh + fslot + ln * 4);
      asm volatile("s_cmp_eq_u32 %[wv], 0\n\ts_waitcnt lgkmcnt(0)\n\ts_barrier\n\ts_cbranch_scc0 9f\n\t"
                   "ds_read_b128 v[40:43], %[rs]\n\tds_read_b128 v[44:47], %[rr]\n\tds_read_b128 v[48:51], %[rr] offset:16\n\t"
                   "ds_read_b128 v[52:55], %[rr] offset:32\n\tds_read_b128 v[56:59], %[rr] offset:48\n\t"
                   "s_bitcmp1_b32 %[j], 0\n\ts_mov_b32 s98, 0x10000\n\t"
                   "s_waitcnt lgkmcnt(4)\n\t"
                   "v_add_f32 v40, v40, v41\n\t" SD("v43", "quad_perm:[1,0,3,2]") "v_add_f32 v40, v40, v42\n\t"
                   "s_waitcnt lgkmcnt(2)\n\t" SD("v43", "quad_perm:[2,3,0,1]") SD("v40", "quad_perm:[1,0,3,2]")
                   PKA("v[44:45]", "v[48:49]") SD("v43", "row_half_mirror") SD("v40", "quad_perm:[2,3,0,1]")
                   "v_mul_f32 v43, 0x3acccccd, v43\n\tv_mul_f32 v41, -v43, v43\n\t" SD("v40", "row_half_mirror")
                   "v_fmac_f32 v41, 0x3acccccd, v40\n\t" RND("v41", "|v41|") "v_add_f32 v41, 0x3727c5ac, v41\n\t" RND("v41", "v41")
                   "v_sqrt_f32 v41, v41\n\t" PKA("v[46:47]", "v[50:51]") RND("v41", "v41") "v_rcp_f32 v41, v41\n\t"
                   "s_waitcnt lgkmcnt(0)\n\t"
                   PKA("v[52:53]", "v[56:57]") PKA("v[54:55]", "v[58:59]") PKA("v[44:45]", "v[52:53]") PKA("v[46:47]", "v[54:55]")
                   RND("v43", "v43") "v_add_f32 v44, v44, v45\n\tv_fma_f32 v46, -v43, v47, v46\n\tv_add_f32 v44, v44, v46\n\t"
                   "s_nop 1\n\tv_permlane32_swap_b32 v44, v45\n\tv_add_f32 v44, v44, v45\n\tv_mul_f32 v44, v44, v41\n\t"
                   "s_cbranch_scc1 8f\n\t" RND("v44", "v44")
                   "v_mul_f32 v45, 0xbfb8aa3b, v44\n\tv_exp_f32 v45, v45\n\ts_nop 0\n\tv_add_f32 v45, 1.0, v45\n\t"
                   "v_rcp_f32 v45, v45\n\ts_nop 0\n\tv_mul_f32 v44, v44, v45\n"
                   "8:\n\ts_mov_b32 exec_hi, 0\n\tv_cvt_pk_bf16_f32 v44, s98, v44\n\t"
                   "buffer_store_dword v44, %[dst], %[rq], 0 offen sc1\n\ts_mov_b32 exec_hi, -1\n"
                   "9:"
                   :
                   : [wv] "s"(wave), [j] "s"(j), [rs] "v"(rs_), [rr] "v"(rr), [dst] "v"(dst), [rq] "s"(rq)
                   : "memory", "scc", "s98", FCLOB, DCLOB);
      tref = now();
      if (half == 0 && wave == 1 && j == 20 && ln < 8)
        __builtin_amdgcn_raw_buffer_store_b32((rtnow() << 1) | 1u, rs, gy_rep(ln) + st_line(g), 0, 16);
      if (half == 0) {
        x_update<true>(smem, rq, fresh(lane0), land, wave);
        if (wave < 6) cs_gather(smem, rq, fresh(lane0), wave, h);
        if (wave == 7) {
          wait_until(tref + 24 * T_AN);
          stamp_poll(smem, rq, ar + st_line(ln & 7));
        }
      }
    }
    if (half == 1) break;
    {
      if (wave < 6 || DRY) {
        const int lane = fresh(lane0);
        const int n = lane & 15;
        G80 a;
        g80_load<1024>(a, smem, lane, L_WQ + (wave >> 1) * 10240 + (wave & 1) * 512, L_HB + 160 * c, land);
        const u32 voff = fresh((wave < 6 ? gy : OOB) + GY_INST + gy_row(c) + ((lane * 16) ^ ((lane >> 4) * 64)));
        const float cc = *reinterpret_cast<const float*>(smem + L_CS + wave * 128 + (lane >> 5) * 64 + n * 4);
        const u32 gq = ar + A_GQ + hsub * 16384 + c * 896;
        const u32 dst = fresh((wave < 6 ? gq : OOB) + wave * 128 + (lane >> 5) * 64 + n * 4);
        const u32 dsz = fresh((wave == 5 ? gq : OOB) + 768 + (lane >> 4) * 4);
        bar();
        wait_rt(__builtin_amdgcn_readfirstlane(*(volatile LAS u32*)(smem + L_ANC)) + (wave < 6 ? A_E : 0));
        dma_poll<false>(rq, voff, landa, USED, USED);
        u32 t, bud;
        asm volatile(G80_LOOP("s_mov_b32 vcc_lo, 0xffff\n\t")
                     "s_mov_b32 vcc_hi, 0xffff\n\t" ZD("quad_perm:[2,3,0,1]", "0xf")
                     "v_permlane32_swap_b32 v40, v44\n\tv_add_f32 v44, v40, v44\n\t"
                     ZD("row_half_mirror", "0xf") "v_permlane16_swap_b32 v40, v41\n\tv_readlane_b32 s98, v48, 48\n\t"
                     "v_add_f32 v44, v44, v41\n\ts_mov_b64 exec, vcc\n\t"
                     "v_fmac_f32 v44, s98, %[cc]\n\tv_or_b32 v44, 1, v44\n\t"
                     "buffer_store_dword v44, %[dst], %[rq], 0 offen sc1\n\t"
                     "s_mov_b32 exec_lo, 0x10001\n\ts_mov_b32 exec_hi, 0x10001\n\tv_or_b32 v48, 1, v48\n\t"
                     "buffer_store_dword v48, %[dsz], %[rq], 0 offen sc1\n\ts_mov_b64 exec, -1"
                     : [t] "=&v"(t), [bud] "=&s"(bud)
                     : G80_IN(a), [nm] "s"(wave < 6 ? 0x00ff00ff00ff00ffull : 0ull), [cc] "v"(cc), [dst] "v"(dst), [dsz] "v"(dsz)
                     : "memory", "vcc", "scc", "s98", "s99", FCLOB, DCLOB);
      } else {
        bar();
      }
    }
  }
  asm volatile(".Lmk_code_end:");
}

}

extern "C" void kernel_launch(void* const* d_in, const int* in_sizes, int n_in, void* d_out, int out_size, void* d_ws,
                              size_t ws_size, hipStream_t stream) {
  Params P;
  P.x0 = (const u16*)d_in[0];
  P.kc = (const u16*)d_in[1];
  P.vc = (const u16*)d_in[2];
  P.wq = (const u16*)d_in[3];
  P.wo = (const u16*)d_in[4];
  P.w1 = (const u16*)d_in[6];
  P.w3 = (const u16*)d_in[7];
  P.w2 = (const u16*)d_in[8];
  P.out = (u16*)d_out;
  P.ws = (u32*)d_ws;
  mega_kernel<<<NWG, NT, 0, stream>>>(P);
}
```

```cpp
#include <hip/hip_runtime.h>
#include <hip/hip_bf16.h>
#include <stdint.h>

namespace {

typedef unsigned int u32;
typedef unsigned short u16;
typedef unsigned long long u64;
using u32x2 = __attribute__((ext_vector_type(2))) unsigned;
using u32x4 = __attribute__((ext_vector_type(4))) unsigned;
using f32x2 = __attribute__((ext_vector_type(2))) float;
using f32x4 = __attribute__((ext_vector_type(4))) float;
using s16x4 = __attribute__((ext_vector_type(4))) short;
using bf16x2 = __attribute__((ext_vector_type(2))) __bf16;
using bf16x8 = __attribute__((ext_vector_type(8))) __bf16;
#define LAS __attribute__((address_space(3)))

constexpr int D = 640, H = 32, DH = 192, WIN = 4096, SK = 32768, F = 1536;
constexpr int NWG = 256, NT = 512;
constexpr u32 SPIN_CAP = 1u << 16;
constexpr float SCL = 0.07216878364870322f * 1.4426950408889634f;

#define T_O 12
#define T_YF 88
#define T_H 95
#define T_AN 28
#define PTHR 60
#define T_LAZY 60

#define A_E 100
#define A_Q (A_E + 78)
#define A_O (A_Q + 142)
#define A_Y (A_O + 87)
#define A_H (A_Y + 82)
#define A_F (A_H + 50)

#define X_O 150
#define X_Y (X_O + 93)
#define X_H (X_Y + 85)
#define X_L (X_H - 16)
#define X_E (X_H + 56)
#define X_S (X_E - A_E)

constexpr u32 SIDE = 8192, AREA = 17 * 16384;
constexpr u32 A_GO = 0;
constexpr u32 A_GY = 2 * 16384;
constexpr u32 A_GQ = 6 * 16384;
constexpr u32 A_GH = 16 * 16384;
constexpr u32 GO_SZ = 10 * 16384, GY_PAIR = GO_SZ, GY_INST = 32768;
constexpr u32 WS_PB = 4 * AREA;
constexpr u32 WS_GOF = WS_PB + (128 << 10), WS_HB = WS_GOF + 4096;
constexpr u32 WS_GQ1 = WS_HB + 256 * 128;
constexpr u32 WS_END = WS_GQ1 + 256 * 2048;
static_assert(WS_END <= (2u << 20), "layout");
constexpr u32 OOB = 1u << 30;
__device__ __forceinline__ u32 go_slot(int w) { return (u32)(w >> 2) * 16384u + (u32)(w & 3) * 2048u; }
__device__ __forceinline__ u32 gy_row(int i) { return go_slot(i); }
__device__ __forceinline__ u32 gy_chunk(int u) {
  const int i = (u * 205) >> 11;
  return gy_row(i) + (u32)(u - 10 * i) * 16u;
}
__device__ __forceinline__ u32 st_line(int g) { return A_GH + 3072u + (u32)g * 128u; }
__device__ __forceinline__ u32 gy_rep(int r) { return (u32)(r & 3) * AREA + (u32)(r >> 2) * SIDE; }

constexpr int L_ZERO = 0;
constexpr int L_CNT = 16;
constexpr int L_RI = 28;
constexpr int L_ONE = 32;
constexpr int L_TAG = 48;
constexpr int L_ST = 64;
constexpr int L_VOTE = L_ST;
constexpr int L_ANC = 192;
constexpr int L_Q = 256;
constexpr int L_CS = 640;
constexpr int L_HB = L_CS + 768;
constexpr int L_ACT = L_HB + D * 2;
constexpr int L_RED = L_ACT + 2 * 384;
constexpr int L_P = L_RED + 32 * 144;
constexpr int P_W = 1280, P_U = 592, P_PAR = 288;
constexpr int OM_S = 400, OM_P = 200;
constexpr int L_OM = L_P + 8 * P_W;
constexpr int L_LN = L_OM;
constexpr int L_LAND = (L_OM + 8 * OM_S * 4 + 255) / 256 * 256;
constexpr int L_CTL = L_LAND + 6 * 2048;
constexpr int L_WQ = L_LAND + 8 * 2048;
constexpr int L_WB = L_WQ + 6 * 5120;
constexpr int L_W2 = L_WB + 768 * 64;
constexpr int L_PF = L_W2 + 192 * 64;
constexpr int L_TAB = L_PF + 256;
constexpr int L_TOTAL = L_TAB + 8 * 2048;
static_assert(L_LAND % 256 == 0 && L_P % 16 == 0 && L_WQ % 16 == 0 && L_HB % 16 == 0 && L_RED % 16 == 0, "alignment");

struct Params {
  const u16* x0;
  const u16* kc;
  const u16* vc;
  const u16* wq;
  u32* ws;
  const u16* w1;
  const u16* w3;
  const u16* wo;
  const u16* w2;
  u16* out;
};

__device__ __forceinline__ u32 cvt2(float lo, float hi) {
  return __builtin_bit_cast(u32, __builtin_convertvector(f32x2{lo, hi}, bf16x2));
}
__device__ __forceinline__ float bflo(u32 w) { return __uint_as_float(w << 16); }
__device__ __forceinline__ float bfhi(u32 w) { return __uint_as_float(w & 0xffff0000u); }
__device__ __forceinline__ float bf16r(float x) { return bflo(cvt2(x, x)); }

template <int CTRL>
__device__ __forceinline__ float dpp(float x) {
  return __builtin_bit_cast(float, __builtin_amdgcn_mov_dpp(__builtin_bit_cast(int, x), CTRL, 0xf, 0xf, true));
}
constexpr int XOR1 = 0xB1, XOR2 = 0x4E, XOR7 = 0x141, ROR8 = 0x128;
__device__ __forceinline__ float fmx(float a, float b) {
  float r;
  asm("v_max_f32 %0, %1, %2" : "=v"(r) : "v"(a), "v"(b));
  return r;
}
__device__ __forceinline__ float xrow16_max(float x) {
  auto s = __builtin_amdgcn_permlane16_swap(__float_as_uint(x), __float_as_uint(x), false, false);
  x = fmx(__uint_as_float(s[0]), __uint_as_float(s[1]));
  auto t = __builtin_amdgcn_permlane32_swap(__float_as_uint(x), __float_as_uint(x), false, false);
  return fmx(__uint_as_float(t[0]), __uint_as_float(t[1]));
}
__device__ __forceinline__ float xrow16_sum(float x) {
  auto s = __builtin_amdgcn_permlane16_swap(__float_as_uint(x), __float_as_uint(x), false, false);
  x = __uint_as_float(s[0]) + __uint_as_float(s[1]);
  auto t = __builtin_amdgcn_permlane32_swap(__float_as_uint(x), __float_as_uint(x), false, false);
  return __uint_as_float(t[0]) + __uint_as_float(t[1]);
}
__device__ __forceinline__ float sum8(float v) {
  v += dpp<XOR1>(v);
  v += dpp<XOR2>(v);
  v += dpp<XOR7>(v);
  return v;
}
__device__ __forceinline__ float max8(float v) {
  asm("s_nop 1\n\tv_max_f32_dpp %0, %0, %0 quad_perm:[1,0,3,2] row_mask:0xf bank_mask:0xf\n\t"
      "s_nop 1\n\tv_max_f32_dpp %0, %0, %0 quad_perm:[2,3,0,1] row_mask:0xf bank_mask:0xf\n\t"
      "s_nop 1\n\tv_max_f32_dpp %0, %0, %0 row_half_mirror row_mask:0xf bank_mask:0xf"
      : "+v"(v));
  return v;
}
__device__ __forceinline__ float max_ror8(float v) {
  asm("s_nop 1\n\tv_max_f32_dpp %0, %0, %0 row_ror:8 row_mask:0xf bank_mask:0xf" : "+v"(v));
  return v;
}
__device__ __forceinline__ f32x4 sum4x4(f32x4 v) {
  float a = v[0], b = v[1], c = v[2], d = v[3];
#define S4(CTL)                                               \
  "v_add_f32_dpp %0, %0, %0 " CTL " row_mask:0xf bank_mask:0xf\n\t" \
  "v_add_f32_dpp %1, %1, %1 " CTL " row_mask:0xf bank_mask:0xf\n\t" \
  "v_add_f32_dpp %2, %2, %2 " CTL " row_mask:0xf bank_mask:0xf\n\t" \
  "v_add_f32_dpp %3, %3, %3 " CTL " row_mask:0xf bank_mask:0xf\n\t"
  asm("s_nop 1\n\t" S4("quad_perm:[1,0,3,2]") S4("quad_perm:[2,3,0,1]") : "+v"(a), "+v"(b), "+v"(c), "+v"(d));
#undef S4
  return f32x4{a, b, c, d};
}
__device__ __forceinline__ float wave_sum(float v) {
  v = sum8(v);
  v += dpp<ROR8>(v);
  return xrow16_sum(v);
}
__device__ __forceinline__ float tsum8(f32x4 a) {
  auto s0 = __builtin_amdgcn_permlane32_swap(__float_as_uint(a[0]), __float_as_uint(a[1]), false, false);
  auto s1 = __builtin_amdgcn_permlane32_swap(__float_as_uint(a[2]), __float_as_uint(a[3]), false, false);
  const float r0 = __uint_as_float(s0[0]) + __uint_as_float(s0[1]);
  const float r1 = __uint_as_float(s1[0]) + __uint_as_float(s1[1]);
  auto t = __builtin_amdgcn_permlane16_swap(__float_as_uint(r0), __float_as_uint(r1), false, false);
  float r = __uint_as_float(t[0]) + __uint_as_float(t[1]);
  r += dpp<ROR8>(r);
  return r;
}

__device__ __forceinline__ u32 tagv(float v) { return __float_as_uint(v) | 1u; }
__device__ __forceinline__ void store_quad(__amdgpu_buffer_rsrc_t rs, u32 off, f32x4 v) {
  const u32x4 s = {tagv(v[0]), tagv(v[1]), tagv(v[2]), tagv(v[3])};
  __builtin_amdgcn_raw_buffer_store_b128(s, rs, off, 0, 16);
}
template <int N>
__device__ __forceinline__ void gpoll4(u32x4 rs, const u32 (&off)[N], f32x4 (&v)[N]) {
  bool need = true;
  u32 budget = SPIN_CAP;
#pragma unroll
  for (int k = 0; k < N; ++k) asm volatile("" : "=v"(v[k]));
#pragma unroll 1
  for (;;) {
    if (need) {
#pragma unroll
      for (int k = 0; k < N; ++k)
        asm volatile("buffer_load_dwordx4 %0, %1, %2, 0 offen sc1" : "+v"(v[k]) : "v"(off[k]), "s"(rs) : "memory");
    }
    asm volatile("s_waitcnt vmcnt(0)" ::: "memory");
    u32 a = 1u;
#pragma unroll
    for (int k = 0; k < N; ++k) {
      asm volatile("" : "+v"(v[k]));
      a &= __float_as_uint(v[k][0]) & __float_as_uint(v[k][1]) & __float_as_uint(v[k][2]) & __float_as_uint(v[k][3]);
    }
    need = a == 0u;
    if (__builtin_amdgcn_ballot_w64(need) == 0ull || budget == 0u) break;
    --budget;
    __builtin_amdgcn_s_sleep(1);
  }
}
__device__ __forceinline__ u32 now() { return (u32)__builtin_amdgcn_s_memtime(); }
__device__ __forceinline__ void wait_until(u32 t) {
#pragma unroll 1
  while ((int)(now() - t) < -160) __builtin_amdgcn_s_sleep(1);
#pragma unroll 1
  while ((int)(now() - t) < 0) {
  }
}
__device__ __forceinline__ u32 rtnow() { return (u32)__builtin_amdgcn_s_memrealtime(); }
#define WAIT_RT                                                                                                       \
  "s_branch 1f\n0:\n\ts_cmp_lt_i32 s98, -8\n\ts_cbranch_scc0 1f\n\ts_sleep 1\n"                                        \
  "1:\n\ts_memrealtime s[98:99]\n\ts_waitcnt lgkmcnt(0)\n\ts_sub_i32 s98, s98, %[tt]\n\t"                              \
  "s_cmp_ge_u32 s98, 0xfffffc00\n\ts_cbranch_scc1 0b\n\t"
__device__ __forceinline__ void wait_rt(u32 t) { asm volatile(WAIT_RT ::[tt] "s"(t) : "scc", "s98", "s99", "memory"); }
template <int BITS>
__device__ __forceinline__ u32 youngest(u32 t);
__device__ __forceinline__ void lds_wait(volatile LAS u32* p, u32 want) {
  u32 spins = 0;
#pragma unroll 1
  while (__builtin_amdgcn_readfirstlane(*p) != want) {
    if (++spins > (SPIN_CAP << 6)) break;
    __builtin_amdgcn_s_sleep(1);
  }
  asm volatile("" ::: "memory");
}
__device__ __forceinline__ u32 lds_wait_nz(volatile LAS u32* p) {
  u32 spins = 0, v;
#pragma unroll 1
  while ((v = __builtin_amdgcn_readfirstlane(*p)) == 0u) {
    if (++spins > (SPIN_CAP << 6)) break;
    __builtin_amdgcn_s_sleep(1);
  }
  asm volatile("" ::: "memory");
  return v;
}
template <int BITS>
__device__ __forceinline__ u32 youngest(u32 t) {
  const u32 nw = rtnow();
  const float age = (float)((nw - t) & ((1u << BITS) - 1u));
  return nw - (u32)__builtin_amdgcn_readfirstlane((int)-xrow16_max(max_ror8(max8(-age))));
}
__device__ __forceinline__ void stamp_poll(unsigned char* smem, u32x4 rq, u32 so) {
  u32 sv, budget = SPIN_CAP;
#pragma unroll 1
  for (;;) {
    asm volatile("buffer_load_dword %0, %1, %2, 0 offen sc1\n\ts_waitcnt vmcnt(0)" : "=&v"(sv) : "v"(so), "s"(rq) : "memory");
    if (__builtin_amdgcn_ballot_w64((sv & 1u) == 0u) == 0ull || budget == 0u) break;
    --budget;
    __builtin_amdgcn_s_sleep(1);
  }
  *(volatile LAS u32*)(smem + L_ANC) = youngest<24>(sv >> 1) | 1u;
}
__device__ __forceinline__ void lds_add(unsigned char* smem, int off, u32 v) {
  __hip_atomic_fetch_add((LAS u32*)(smem + off), v, __ATOMIC_RELAXED, __HIP_MEMORY_SCOPE_WORKGROUP);
}

__device__ __forceinline__ int fresh(int t) {
  asm volatile("" : "+v"(t));
  return t;
}
__device__ __forceinline__ int lane_id() {
  int t;
  asm volatile("v_mbcnt_lo_u32_b32 %0, -1, 0\n\tv_mbcnt_hi_u32_b32 %0, -1, %0" : "=v"(t));
  return t;
}
__device__ __forceinline__ f32x4 zero4() {
  f32x2 a, b;
  asm volatile("v_mov_b64 %0, 0\n\tv_mov_b64 %1, 0" : "=v"(a), "=v"(b));
  return f32x4{a[0], a[1], b[0], b[1]};
}
__device__ __forceinline__ void bar() { asm volatile("s_waitcnt lgkmcnt(0)\n\ts_barrier" ::: "memory"); }

__device__ __forceinline__ u32x2 trread(unsigned char* smem, int off) {
  return __builtin_bit_cast(u32x2, __builtin_amdgcn_ds_read_tr16_b64_v4i16((LAS s16x4*)(smem + off)));
}
__device__ __forceinline__ u32x4 wfrag(unsigned char* smem, int lb, int off) {
  const u32x2 a = trread(smem, lb + off), b = trread(smem, lb + off + 256);
  return u32x4{a[0], a[1], b[0], b[1]};
}
template <int GS = 512>
__device__ __forceinline__ int wlane(int lane, int cb) {
  const int i = lane & 15, G = lane >> 4;
  return G * GS + (i >> 2) * 64 + (i & 3) * 8 + ((cb ^ (G & 1)) * 32);
}
__device__ __forceinline__ u32x4 afrag(unsigned char* smem, int a) { return *reinterpret_cast<const u32x4*>(smem + a); }
__device__ __forceinline__ f32x4 mfma32(u32x4 w, u32x4 a, f32x4 c) {
  return __builtin_amdgcn_mfma_f32_16x16x32_bf16(__builtin_bit_cast(bf16x8, w), __builtin_bit_cast(bf16x8, a), c, 0, 0, 0);
}

__device__ __forceinline__ void dma_kib(const u16* src, u32 voff, unsigned char* smem, int ldsoff) {
  const u32 dst = (u32)(uintptr_t)(LAS unsigned char*)smem + (u32)ldsoff;
  u32 keep;
  asm volatile("s_mov_b32 %0, m0\n\ts_mov_b32 m0, %2\n\ts_nop 2\n\tglobal_load_lds_dwordx4 %1, %3 nt\n\ts_mov_b32 m0, %0"
               : "=&s"(keep)
               : "v"(voff), "s"(dst), "s"(src)
               : "memory");
}
__device__ __forceinline__ void dma_piece(const u16* src, int ld, unsigned char* smem, int ldsoff, int p, int lane) {
  const int row = 16 * p + (lane >> 2), part = lane & 3;
  const int cb = (part >> 1) ^ (lane >> 5);
  dma_kib(src, (u32)(row * ld + cb * 16 + (part & 1) * 8) * 2u, smem, ldsoff + p * 1024);
}

template <int CTRL>
__device__ __forceinline__ u32 dppu(u32 x) {
  return (u32)__builtin_amdgcn_mov_dpp((int)x, CTRL, 0xf, 0xf, true);
}
__device__ __forceinline__ float sum4(f32x4 d) { return (d[0] + d[1]) + (d[2] + d[3]); }
__device__ __forceinline__ u32 and4(u32x4 v) { return v[0] & v[1] & v[2] & v[3]; }
__device__ __forceinline__ void store16(__amdgpu_buffer_rsrc_t rs, u32 off, u32x4 v) {
  __builtin_amdgcn_raw_buffer_store_b128(v, rs, off, 0, 16);
}
#define POLL16(dst, voff, imm) \
  asm volatile("buffer_load_dwordx4 %0, %1, %2, 0 offen offset:%3 sc1" : "+v"(dst) : "v"(voff), "s"(rq), "n"(imm) : "memory")

struct LnS {
  float mb, ri;
};
__device__ __forceinline__ LnS ln_stats(float s1, float s2) {
#define S2(CTL)                                                     \
  "v_add_f32_dpp %0, %0, %0 " CTL " row_mask:0xf bank_mask:0xf\n\t" \
  "v_add_f32_dpp %1, %1, %1 " CTL " row_mask:0xf bank_mask:0xf\n\ts_nop 0\n\t"
  asm("s_nop 1\n\t" S2("quad_perm:[1,0,3,2]") S2("quad_perm:[2,3,0,1]") S2("row_half_mirror") : "+v"(s1), "+v"(s2));
#undef S2
  const float dm = s1 * (1.0f / D);
  const u32 mv = cvt2(dm, __builtin_fmaxf(fmaf(-dm, dm, s2 * (1.0f / D)), 0.f));
  LnS r;
  r.mb = bflo(mv);
  r.ri = __builtin_amdgcn_rcpf(bf16r(__builtin_amdgcn_sqrtf(bf16r(bfhi(mv) + 1e-5f))));
  return r;
}

struct G80 {
  u32x4 w[6];
  int ra, rb, rc;
};
template <int GS>
__device__ __forceinline__ void g80_w(G80& a, unsigned char* smem, int lane, int wb) {
  const int l0 = wb + wlane<GS>(lane, 0), l1 = wb + wlane<GS>(lane, 1), bk = 8 * GS - (lane >> 5) * 2 * GS;
  a.w[0] = wfrag(smem, l0, 0);
  a.w[1] = wfrag(smem, l1, 0);
  a.w[2] = wfrag(smem, l0, 4 * GS);
  a.w[3] = wfrag(smem, l1, 4 * GS);
  a.w[4] = wfrag(smem, l0 + bk, 0);
  a.w[5] = wfrag(smem, l1 + bk, 0);
}
__device__ __forceinline__ void g80_a(G80& a, int lane, int xs, int land) {
  const int m = lane & 15, G = lane >> 4;
  const bool vec = m == 8 || (m < 8 && land >= 0);
  const int st = vec ? 64 : 0;
  const int sa = vec ? (m == 8 ? xs : land + 256 * m) + 16 * G : (m == 15 ? L_ONE : L_ZERO);
  const int sw = (vec && m < 8) ? (m & 3) * 64 : 0;
  a.ra = fresh(sa ^ sw);
  a.rb = fresh((sa + st) ^ sw);
  a.rc = fresh(G < 2 ? (sa + 2 * st) ^ sw : L_TAG);
}
template <bool WAIT = true>
__device__ __forceinline__ void dma_poll(u32x4 rq, u32 voff, u32 land, u64 ea, u64 eb) {
  u32 keep;
  asm volatile("s_mov_b32 %0, m0\n\ts_mov_b32 m0, %3\n\ts_mov_b64 exec, %4\n\t"
               "buffer_load_dwordx4 %1, %2, 0 offen sc1 lds\n\t"
               "s_mov_b64 exec, %5\n\t"
               "buffer_load_dwordx4 %1, %2, 0 offen offset:1024 sc1 lds\n\t"
               "s_mov_b64 exec, -1\n\ts_mov_b32 m0, %0"
               : "=&s"(keep)
               : "v"(voff), "s"(rq), "s"(land), "s"(ea), "s"(eb)
               : "memory");
  if (WAIT) asm volatile("s_waitcnt vmcnt(0)" ::: "memory");
}
template <bool X4>
__device__ __forceinline__ void ctl_load(u32x4 rq, u32 voff, u32 lds) {
  u32 keep;
  if (X4)
    asm volatile("s_mov_b32 %0, m0\n\ts_mov_b32 m0, %3\n\ts_nop 2\n\tbuffer_load_dwordx4 %1, %2, 0 offen sc1 lds\n\ts_mov_b32 m0, %0"
                 : "=&s"(keep) : "v"(voff), "s"(rq), "s"(lds) : "memory");
  else
    asm volatile("s_mov_b32 %0, m0\n\ts_mov_b32 m0, %3\n\ts_nop 2\n\tbuffer_load_dword %1, %2, 0 offen sc1 lds\n\ts_mov_b32 m0, %0"
                 : "=&s"(keep) : "v"(voff), "s"(rq), "s"(lds) : "memory");
}
template <int UN>
__device__ __forceinline__ void retry_masks(u64 need, int lane, u64& ea, u64& eb) {
  u32 m = (u32)need | (u32)(need >> 32);
  m |= m >> 16;
  m = (m | (m >> 8)) & 0xffu;
  const int ln = fresh(lane);
  ea = __builtin_amdgcn_ballot_w64(((m >> (ln / UN)) & 1u) != 0u);
  eb = __builtin_amdgcn_ballot_w64(((m >> ((ln + 64) / UN)) & 1u) != 0u);
}
#define MF "v_mfma_f32_16x16x32_bf16 "
#define F0 "v[52:55]"
#define F1 "v[56:59]"
#define F2 "v[60:63]"
#define FCLOB "v52", "v53", "v54", "v55", "v56", "v57", "v58", "v59", "v60", "v61", "v62", "v63"
#define AND3(a, b, c) "v_bitop3_b32 %[t], " a ", " b ", " c " bitop3:0x80\n\t"
#define BREP "s_bitreplicate_b64_b32 "
#define USEX "s_and_b32 exec_lo, exec_lo, 0x30ff03ff\n\ts_and_b32 exec_hi, exec_hi, 0xff30ff03\n\t"
constexpr u64 USED = 0xff30ff0330ff03ffull;
#define POLL_HEAD(FOLD, MASK)                                                                                        \
  "s_mov_b32 %[bud], 0x10000\n\ts_branch 2f\n"                                                                       \
  "1:\n\ts_sub_u32 %[bud], %[bud], 1\n\ts_cbranch_scc1 3f\n\t"                                                       \
  "s_or_b32 s98, vcc_lo, vcc_hi\n\ts_lshr_b32 s99, s98, 16\n\ts_or_b32 s98, s98, s99\n\t" FOLD                       \
  BREP "s[98:99], s98\n\t" BREP "s[98:99], s98\n\t" BREP "s[98:99], s98\n\t"                                         \
  "s_mov_b32 vcc_lo, m0\n\ts_mov_b32 m0, %[land]\n\t" BREP "exec, s98\n\t" MASK                                      \
  "buffer_load_dwordx4 %[vo], %[rq], 0 offen sc1 lds\n\t" BREP "exec, s99\n\t" MASK                                  \
  "buffer_load_dwordx4 %[vo], %[rq], 0 offen offset:1024 sc1 lds\n\t"                                                \
  "s_mov_b64 exec, -1\n\ts_mov_b32 m0, vcc_lo\n"                                                                     \
  "2:\n\ts_waitcnt vmcnt(0)\n\t"                                                                                     \
  "ds_read_b128 " F0 ", %[ra]\n\tds_read_b128 " F1 ", %[rb]\n\tds_read_b128 " F2 ", %[rc]\n\t"
#define R0 "v[40:43]"
#define R1 "v[44:47]"
#define R2 "v[48:51]"
#define DCLOB "v40", "v41", "v42", "v43", "v44", "v45", "v46", "v47", "v48", "v49", "v50", "v51"
#define ZD(CTL, BANK) "v_add_f32_dpp v48, v48, v48 " CTL " row_mask:0xf bank_mask:" BANK "\n\t"
#define G80_LOOP(FILL)                                                                                               \
  POLL_HEAD("", USEX)                                                                                                \
  "s_waitcnt lgkmcnt(2)\n\t"                                                                                         \
  MF R2 ", " F0 ", " F0 ", 0\n\t" AND3("v52", "v53", "v54")                                                          \
  MF R0 ", " F0 ", %[w0], 0\n\t" MF R1 ", " F0 ", %[w1], 0\n\t"                                                      \
  "s_waitcnt lgkmcnt(1)\n\t"                                                                                         \
  MF R2 ", " F1 ", " F1 ", " R2 "\n\t" AND3("%[t]", "v55", "v56")                                                    \
  MF R0 ", " F1 ", %[w2], " R0 "\n\t" AND3("%[t]", "v57", "v58")                                                     \
  MF R1 ", " F1 ", %[w3], " R1 "\n\t"                                                                                \
  "s_waitcnt lgkmcnt(0)\n\t"                                                                                         \
  MF R2 ", " F2 ", " F2 ", " R2 "\n\t" AND3("%[t]", "v59", "v60") AND3("%[t]", "v61", "v62")                         \
  MF R0 ", " F2 ", %[w4], " R0 "\n\t" AND3("%[t]", "1", "v63")                                                       \
  "v_cmp_eq_u32 vcc, 0, %[t]\n\t"                                                                                    \
  MF R1 ", " F2 ", %[w5], " R1 "\n\t"                                                                                \
  "s_and_b64 vcc, vcc, %[nm]\n\ts_cbranch_scc1 1b\n"                                                                 \
  "3:\n\tv_pk_add_f32 v[48:49], v[48:49], v[50:51]\n\tv_add_f32 v48, v48, v49\n\t" FILL                              \
  "v_pk_add_f32 v[40:41], v[40:41], v[42:43]\n\t" ZD("row_ror:8", "0x1") "v_add_f32 v40, v40, v41\n\t"               \
  "v_pk_add_f32 v[44:45], v[44:45], v[46:47]\n\t" ZD("quad_perm:[1,0,3,2]", "0xf") "v_add_f32 v44, v44, v45\n\t"
#define SD(R, CTL) "v_add_f32_dpp " R ", " R ", " R " " CTL " row_mask:0xf bank_mask:0xf\n\t"
#define PKA(A, B) "v_pk_add_f32 " A ", " A ", " B "\n\t"
#define RND(R, X) "v_cvt_pk_bf16_f32 " R ", 0, " X "\n\t"
#define G80_IN(A)                                                                                                    \
  [ra] "v"(A.ra), [rb] "v"(A.rb), [rc] "v"(A.rc), [w0] "v"(A.w[0]), [w1] "v"(A.w[1]), [w2] "v"(A.w[2]),              \
      [w3] "v"(A.w[3]), [w4] "v"(A.w[4]), [w5] "v"(A.w[5]), [vo] "v"(voff), [rq] "s"(rq), [land] "s"(landa)
__device__ __forceinline__ void g80_run(G80& a, unsigned char* smem, float& r0, float& r1) {
  const u32x4 f0 = afrag(smem, a.ra), f1 = afrag(smem, a.rb), f2 = afrag(smem, a.rc);
  f32x4 d0 = {0.f, 0.f, 0.f, 0.f}, d1 = d0;
  d0 = mfma32(f0, a.w[0], d0);
  d1 = mfma32(f0, a.w[1], d1);
  d0 = mfma32(f1, a.w[2], d0);
  d1 = mfma32(f1, a.w[3], d1);
  d0 = mfma32(f2, a.w[4], d0);
  d1 = mfma32(f2, a.w[5], d1);
  r0 = sum4(d0);
  r1 = sum4(d1);
}
__device__ __forceinline__ f32x4 quad4(float r) { return f32x4{r, dpp<0x55>(r), dpp<0xAA>(r), dpp<0xFF>(r)}; }

__device__ __forceinline__ int q_addr(int d) {
  const int w = d >> 1;
  return L_Q + ((w & 1) * 48 + (w >> 1)) * 4 + (d & 1) * 2;
}

template <bool LDS>
__device__ __forceinline__ void x_update(unsigned char* smem, u32x4 rq, int lane, u32 src, int i) {
  const int l = lane < 40 ? lane : 0;
  const u32 off = src + 4 * l;
  u32 v[8];
  if (LDS) {
#pragma unroll
    for (int k = 0; k < 8; ++k) v[k] = *reinterpret_cast<const u32*>(smem + (off ^ ((k & 3) * 64)) + 256 * k);
  } else {
    u32 budget = SPIN_CAP;
#pragma unroll 1
    for (;;) {
#pragma unroll
      for (int k = 0; k < 8; ++k)
        asm volatile("buffer_load_dword %0, %1, %2, 0 offen offset:%3 sc1" : "=v"(v[k]) : "v"(off), "s"(rq), "n"(256 * k) : "memory");
      asm volatile("s_waitcnt vmcnt(0)"
                   : "+v"(v[0]), "+v"(v[1]), "+v"(v[2]), "+v"(v[3]), "+v"(v[4]), "+v"(v[5]), "+v"(v[6]), "+v"(v[7])::"memory");
      const u32 a = v[0] & v[1] & v[2] & v[3] & v[4] & v[5] & v[6] & v[7] & 1u;
      if (__builtin_amdgcn_ballot_w64(a == 0u) == 0ull || budget == 0u) break;
      --budget;
      __builtin_amdgcn_s_sleep(2);
    }
  }
  float lo = 0.f, hi = 0.f;
#pragma unroll
  for (int k = 0; k < 8; ++k) {
    lo += bflo(v[k]);
    hi += bfhi(v[k]);
  }
  u32* px = reinterpret_cast<u32*>(smem + L_HB + 160 * i + 4 * l);
  const u32 xo = *px, y = cvt2(lo, hi);
  if (lane < 40) *px = cvt2(bflo(xo) + bflo(y), bfhi(xo) + bfhi(y));
}
template <int N>
__device__ __forceinline__ void col_poll(u32x4 rq, const u32 (&off)[N], float (&y)[N]) {
  u32x2 v[N];
  u32 budget = SPIN_CAP;
#pragma unroll 1
  for (;;) {
    u32 a = 1u;
#pragma unroll
    for (int k = 0; k < N; ++k)
      asm volatile("buffer_load_dwordx2 %0, %1, %2, 0 offen sc1" : "=v"(v[k]) : "v"(off[k]), "s"(rq) : "memory");
    asm volatile("s_waitcnt vmcnt(0)" ::: "memory");
#pragma unroll
    for (int k = 0; k < N; ++k) {
      asm volatile("" : "+v"(v[k]));
      a &= v[k][0] & v[k][1];
    }
    if (__builtin_amdgcn_ballot_w64(a == 0u) == 0ull || budget == 0u) break;
    --budget;
  }
#pragma unroll
  for (int k = 0; k < N; ++k) y[k] = bf16r(tsum8(f32x4{bflo(v[k][0]), bfhi(v[k][0]), bflo(v[k][1]), bfhi(v[k][1])}));
}

#define CKL 22
#define CKV 5
#define CW 6
#define CWAIT asm volatile("s_waitcnt vmcnt(%0)" ::"n"(CW) : "memory")
#define QACK 5
#define QP 11
#define BEAT0 (QP + QACK)
#define PRE 50
#define AL_R 3
#define AL_L 8
#define AL_A 11
#define AL_2 5
#define FR 3
#define FW 3
#define FGN 162
#define FGT 25
#define FGA 6
#define FGM 200
#define FGS 2
#define AL_OFF 8
#define E_QK 300
#define E_QN 2
#define WBIG 10
#define TSTEP 234
#define QI 7
#define NINO 4
#define NINF 0
#define PO0 8
#define POS 10
#define PF0 25
#define PFS 2
#define TFL 220
static_assert(AL_A + 15 >= 24 && QI <= PO0 && PO0 + (NINO - 1) * POS < 42 && PF0 + (NINF - 1) * PFS < 42, "pieces");
#define CAL 10
#define XTAB 12
#define NPRE 2
#define FEARLY 1
#define NPF NPRE
#define TPF 220
#define TPV 220
#define XA 15
#define XB 18
#define TS_O (TSTEP * (20 * (48 + NINO) + 12 * (48 + NINF)) / (32 * (48 + NINO)))
#define TS_F (TSTEP * (20 * (48 + NINO) + 12 * (48 + NINF)) / (32 * (48 + NINF)))
#define NP0 3
#define TQ0 660
#define TSQ 136
static_assert(CKL > BEAT0 && QP > QI && 23 - CKL + CKV >= CW && CKV + 1 + CW <= XB - CW && XB - CW <= XA, "window");
#define PACE_ASM                                                                                                     \
  "1:\n\ts_memrealtime s[98:99]\n\ts_waitcnt lgkmcnt(0)\n\ts_lshl_b32 s98, s98, 3\n\ts_sub_i32 s98, s98, %[tn]\n\t"  \
  "s_cmp_lt_i32 s98, 0\n\ts_cbranch_scc0 2f\n\ts_sleep 1\n\ts_branch 1b\n2:\n\ts_add_i32 %[tn], %[tn], %[ts]\n\t"
__device__ __forceinline__ void pace(u32& tnext, u32 tstep) {
  asm volatile(PACE_ASM : [tn] "+s"(tnext) : [ts] "s"(tstep) : "scc", "s98", "s99");
}
constexpr int kreg(int i) { return i % 3 < 2 ? 64 + 8 * (i / 3) + 4 * (i % 3) : 128 + 4 * (i / 3); }
constexpr int vreg(int i) { return 160 + 24 * (i / 6) + 8 * (i % 3) + 4 * ((i / 3) & 1); }
#define LOADW(reg, voff, sbase, imm)                                                                                \
  asm volatile(PACE_ASM "global_load_dwordx4 v[%[r0]:%[r1]], %[p], %[sb] offset:%[im] nt\n\ts_waitcnt vmcnt(%[w])"  \
               : [tn] "+s"(tnext)                                                                                   \
               : [r0] "n"(reg), [r1] "n"((reg) + 3), [p] "v"(voff), [sb] "s"(sbase), [im] "n"(imm), [ts] "s"(tstep), \
                 [w] "n"(WBIG - 1)                                                                                  \
               : "scc", "s98", "s99")

#define HS_WAIT(COND)                                                                                \
  _Pragma("unroll 1") for (;;) {                                                                     \
    gd = CTL[512 + lane0] - CTL[576 + lane0];                                                        \
    const u64 ch = __builtin_amdgcn_ballot_w64(gd != 0u) & 0x1ffull;                                 \
    if (ch > 0xffull || (COND) || tmo == 0u) break;                                                  \
    if ((tmo & 3u) == 0u && lane0 == 0)                                                              \
      __builtin_amdgcn_raw_buffer_store_b32(hb, rs, WS_HB + h * 128 + c * 4, 0, 16);                 \
    hb += 4u;                                                                                        \
    --tmo;                                                                                           \
    __builtin_amdgcn_s_sleep(3);                                                                     \
    ctl_load<false>(rq, hso, (u32)(smb + L_CTL + 2048));                                             \
    asm volatile("s_waitcnt vmcnt(0)" ::: "memory");                                                 \
  }

struct QKAddr {
  int qad[6], idxq, wa;
  f32x4 D0, D1;
};
struct PVAddr {
  int ra, idx, oa, sa, la, ma;
  u32 dst;
  f32x4 O0, O1, O2;
  u32x4 A0, A1, A2, A3;
};
__device__ __forceinline__ void att_table(unsigned char* smem, int lane, int wave, u32 go) {
  const int n = lane & 15, G = lane >> 4;
  const int seg = n & 1, pr = (n >> 1) & 1, tq = n >> 2, xy = G >> 1;
  const int qa = L_Q + (pr * 48 + seg * 8 + (G & 1) * 4) * 4;
  u32 t[16];
#pragma unroll
  for (int k = 0; k < 4; ++k) t[k] = tq == k ? qa + xy * 64 : L_ZERO;
#pragma unroll
  for (int k = 0; k < 2; ++k) t[4 + k] = tq == 2 * k + xy ? qa + 128 : L_ZERO;
  t[6] = 0x4444 + pr * 0xAAAA;
  t[7] = L_P + wave * P_W + ((n >> 1) & 1) * P_PAR + ((((G & 1) * 2 + (n >> 3)) * 4 + (n & 1) * 2 + (G >> 1)) * 16) + ((n >> 2) & 1) * 8;
  t[8] = L_P + wave * P_W + ((n >> 2) & 1) * P_U + (n >> 3) * P_PAR + G * 64;
  t[9] = (n & 3) * 0x5555;
  t[10] = L_OM + (wave * OM_S + (n >> 3) * OM_P + (n & 7) * 4 + (G & 1) * 32) * 4;
  t[11] = L_OM + (wave * OM_S + DH + G) * 4;
  const int src = wave < 6 ? lane >> 3 : lane & 7;
  t[12] = L_OM + (src * OM_S + (wave < 6 ? (wave >> 1) * 64 + (lane & 1) * 32 + (wave & 1) * 16 + (lane & 6) * 2 : DH)) * 4;
  t[13] = L_OM + (src * OM_S + DH + 4) * 4;
  t[14] = lane >= (wave < 6 ? 16 : (wave == 6 ? 2 : 0)) ? 0x8000u
          : go + (wave < 6 ? (wave >= 3 ? 2048 - 192 : 0) + wave * 64 + (lane & 7) * 8 + (lane >> 3) * 4 : lane * 2048 + 192);
  t[15] = wave < 6 ? q_addr(wave * 32 + (lane & 7) * 4 + (lane >> 4)) : (wave == 6 ? L_RI : L_ACT);
#pragma unroll
  for (int k = 0; k < 2; ++k)
    *reinterpret_cast<u32x4*>(smem + L_TAB + wave * 2048 + k * 1024 + lane * 16) =
        u32x4{t[8 * k] | (t[8 * k + 4] << 16), t[8 * k + 1] | (t[8 * k + 5] << 16), t[8 * k + 2] | (t[8 * k + 6] << 16),
              t[8 * k + 3] | (t[8 * k + 7] << 16)};
}
__device__ __forceinline__ void att_setup(QKAddr& r, PVAddr& v, unsigned char* smem, int lane, int wave, int& qw) {
  const u32x4 a = afrag(smem, L_TAB + wave * 2048 + lane * 16), b = afrag(smem, L_TAB + wave * 2048 + 1024 + lane * 16);
  r.D0 = zero4();
  r.D1 = zero4();
  qw = fresh(b[3] >> 16);
#pragma unroll
  for (int k = 0; k < 4; ++k) r.qad[k] = fresh(a[k] & 0xffffu);
  r.qad[4] = fresh(a[0] >> 16);
  r.qad[5] = fresh(a[1] >> 16);
  r.idxq = fresh(a[2] >> 16);
  r.wa = fresh(a[3] >> 16);
  v.ra = fresh(b[0] & 0xffffu);
  v.idx = fresh(b[1] & 0xffffu);
  v.oa = fresh(b[2] & 0xffffu);
  v.la = fresh(b[3] & 0xffffu);
  v.sa = fresh(b[0] >> 16);
  v.ma = fresh(b[1] >> 16);
  v.dst = fresh((int)b[2] >> 16);
  asm volatile("" : "+v"(r.D0), "+v"(r.D1));
}
#define SM "v_smfmac_f32_16x16x64_bf16 "
#define P_WRITE_READ(QA)                                                                                              \
  asm volatile("ds_write2_b32 %[wa], %[p], %[p] offset1:%[o1]\n\t"                                                    \
               "ds_read_b128 %[a0], %[ra]\n\tds_read_b128 %[a1], %[ra] offset:16\n\t"                                 \
               "ds_read_b128 %[a2], %[ra] offset:32\n\tds_read_b128 %[a3], %[ra] offset:48"                           \
               : [a0] "=&v"(pv.A0), [a1] "=&v"(pv.A1), [a2] "=&v"(pv.A2), [a3] "=&v"(pv.A3), "+v"(sk)                 \
               : [wa] "v"(QA.wa + smb), [p] "v"(pb), [ra] "v"(pv.ra + smb), [o1] "n"(P_U / 4 + 1)        \
               : "memory");
#define QS(D, A, K) SM "v[" #D ":" #D "+3], v[" #A ":" #A "+3], v[" #K ":" #K "+7], %[ix]\n\t"
#define RDS "ds_read_b32 %[s], %[z] offset:%[ri]\n\t"
#define LW(N) "s_waitcnt lgkmcnt(" #N ")\n\t"
#define QK_BODY                                                                                                       \
  "ds_read_b128 v[40:43], %[q0]\n\tds_read_b128 v[44:47], %[q1]\n\tds_read_b128 v[48:51], %[q2]\n\t"                   \
  "ds_read_b128 v[52:55], %[q3]\n\tds_read_b128 v[56:59], %[q4]\n\tds_read_b128 v[60:63], %[q5]\n\t"                   \
  LW(5) QS(32, 40, 64) QS(36, 40, 96) LW(4) QS(32, 44, 72) QS(36, 44, 104) LW(3) QS(32, 48, 80) QS(36, 48, 112)       \
  LW(2) QS(32, 52, 88) QS(36, 52, 120) LW(0) QS(32, 56, 128) QS(36, 56, 144) RDS QS(32, 60, 136) QS(36, 60, 152)
#define QK_IN(QA) [q0] "v"(QA.qad[0]), [q1] "v"(QA.qad[1]), [q2] "v"(QA.qad[2]), [q3] "v"(QA.qad[3]), [q4] "v"(QA.qad[4]), [q5] "v"(QA.qad[5]),
#define UNSAFE(X) ((__builtin_amdgcn_ballot_w64(!(X <= 24.f)) != 0ull) | (__builtin_amdgcn_ballot_w64(X >= -24.f) == 0ull))
#define QK_SOFTMAX(QA)                                                                                                \
  {                                                                                                                   \
    float scl, raw, sk;                                                                                               \
    u32 pb;                                                                                                           \
                                                                      \
    asm volatile(QK_BODY                                                                                              \
                 "v_mov_b64 v[52:53], 0\n\tv_mov_b64 v[54:55], 0\n\tv_mov_b64 v[56:57], 0\n\tv_mov_b64 v[58:59], 0\n\t" \
                 "v_mov_b64 v[60:61], 0\n\tv_mov_b64 v[62:63], 0\n\ts_waitcnt lgkmcnt(0)\n\t"                         \
                 PKA("v[32:33]", "v[34:35]") PKA("v[36:37]", "v[38:39]") "s_nop 0\n\t"                                \
                 "v_add_f32_dpp v32, v33, v32 quad_perm:[1,0,3,2] row_mask:0xf bank_mask:0xf\n\t"                     \
                 "v_add_f32_dpp v36, v36, v37 quad_perm:[1,0,3,2] row_mask:0xf bank_mask:0xf\n\t"                     \
                 "v_cndmask_b32 %[raw], v32, v36, %[od]\n\tv_mul_f32 %[sk], %[s], %[raw]\n\tv_exp_f32 %[p], %[sk]\n\t" \
                 "s_nop 0\n\tv_cvt_pk_bf16_f32 %[pb], %[p], 0"                                                        \
                 : "+{v[32:35]}"(QA.D0), "+{v[36:39]}"(QA.D1), [s] "=&v"(scl), [raw] "=&v"(raw), [sk] "=&v"(sk),      \
                   [p] "=&v"(pend), [pb] "=&v"(pb), "=&{v[52:55]}"(pv.O0), "=&{v[56:59]}"(pv.O1), "=&{v[60:63]}"(pv.O2) \
                 : QK_IN(QA) [ix] "v"(QA.idxq), [z] "v"(smb), [ri] "n"(L_RI),                                        \
                   [od] "s"(0xaaaaaaaaaaaaaaaaull), "{v[64:95]}"(kv0), "{v[96:127]}"(kv1), "{v[128:159]}"(kv2)        \
                 : "memory", DCLOB);                                                                                  \
    pmax = 0.f;                                                                                                       \
    P_WRITE_READ(QA)                                                                                                  \
    if (__builtin_expect(UNSAFE(sk) | (__builtin_amdgcn_ballot_w64(__float_as_uint(scl) == 0u) != 0ull), 0)) {        \
      sk = raw * __uint_as_float(lds_wait_nz((volatile LAS u32*)(smem + L_RI)));                                      \
      if (UNSAFE(sk)) pmax = xrow16_max(max_ror8(max8(sk)));                                                          \
      pend = __builtin_amdgcn_exp2f(sk - pmax);                                                                       \
      pb = cvt2(pend, 0.f);                                                                                           \
      asm volatile("s_waitcnt lgkmcnt(0)" : "+v"(pv.A0), "+v"(pv.A1), "+v"(pv.A2), "+v"(pv.A3));                      \
      P_WRITE_READ(QA)                                                                                                \
      asm volatile("s_waitcnt lgkmcnt(0)" : "+v"(pv.A0), "+v"(pv.A1), "+v"(pv.A2), "+v"(pv.A3));                      \
    }                                                                                                                 \
  }

using u32x16 = __attribute__((ext_vector_type(16))) unsigned;
using u32x32 = __attribute__((ext_vector_type(32))) unsigned;
template <int OFF>
__device__ __forceinline__ u16* karg(u64 kp) {
  u64 p;
  asm volatile("s_load_dwordx2 %0, %1, %2\n\ts_waitcnt lgkmcnt(0)" : "=s"(p) : "s"(kp), "n"(OFF));
  return (u16*)p;
}
template <class T>
__device__ __forceinline__ T* mkptr(u32 lo, u32 hi) {
  return (T*)(((u64)hi << 32) | lo);
}

template <int I>
struct IC {
  static constexpr int value = I;
};
template <int A, int B, class Fn>
__device__ __forceinline__ void sfor(Fn&& f) {
  if constexpr (A < B) {
    f(IC<A>{});
    sfor<A + 1, B>(f);
  }
}

__global__ __launch_bounds__(NT, 2) void mega_kernel(Params) {
  __shared__ __attribute__((aligned(16))) unsigned char smem[L_TOTAL];
  u64 kp, ts0;
  u32x16 ka;
  int b;
  u32 wqo, wql;
#define WQL "global_load_lds_dwordx4 %[wqo], s[10:11] nt\n\ts_addk_i32 m0, 0x800\n\tv_add_u32 %[wqo], s22, %[wqo]\n\t"
  asm volatile(
               "s_load_dwordx16 s[4:19], s[0:1], 0x0\n\t"
               "s_getpc_b64 s[20:21]\n.Lmk_pc:\n\t"
               "s_mov_b32 m0, %[dst]\n\t"
               "v_lshlrev_b32 v1, 7, v0\n\tv_min_u32 v1, .Lmk_code_end-.Lmk_pc-4, v1\n\t"
               "global_load_lds_dword v1, s[20:21]\n\t"
               "v_readfirstlane_b32 s3, v0\n\tv_bfe_u32 v1, v0, 2, 3\n\tv_lshrrev_b32 v2, 3, v0\n\tv_and_or_b32 v1, v2, 8, v1\n\t"
               "s_lshr_b32 s20, s2, 5\n\ts_mulk_i32 s20, 80\n\tv_add_u32 v1, s20, v1\n\tv_mul_u32_u24 v1, %[row], v1\n\t"
               "v_lshlrev_b32 v2, 4, v0\n\tv_and_b32 v2, 48, v2\n\tv_lshrrev_b32 v3, 1, v0\n\tv_and_b32 v3, 32, v3\n\t"
               "v_xor_b32 v2, v2, v3\n\tv_lshlrev_b32 v3, 1, v0\n\tv_and_or_b32 v2, v3, 64, v2\n\t"
               "s_and_b32 s20, s2, 7\n\ts_bfe_u32 s21, s2, 0x20003\n\ts_lshl2_add_u32 s23, s20, s21\n\t"
               "s_lshr_b32 s21, s3, 7\n\ts_mul_i32 s20, s23, 3\n\ts_add_u32 s20, s20, s21\n\ts_lshl_b32 s20, s20, 7\n\t"
               "v_add3_u32 %[wqo], v1, v2, s20\n\t"
               "s_mulk_i32 s21, 10240\n\ts_and_b32 s20, s3, 64\n\ts_lshl4_add_u32 s20, s20, s21\n\ts_add_u32 m0, s20, %[lwq]\n\t"
               "s_mov_b32 s22, %[pst]\n\ts_cmpk_lt_u32 s3, 0x180\n\t"
               "s_waitcnt lgkmcnt(0)\n\ts_memrealtime %[ts]\n\ts_cbranch_scc0 1f\n\t"
               ".rept %[np]\n\t" WQL ".endr\n\t"
               "s_mov_b32 %[wql], m0\n\ts_branch 3f\n"
               "1:\n\tv_and_b32 v1, 63, v0\n\ts_movk_i32 m0, %[lx]\n\tv_lshlrev_b32 v2, 4, v1\n\tv_lshlrev_b32 v1, 2, v1\n\t"
               "s_bitcmp1_b32 s3, 6\n\ts_cbranch_scc1 2f\n\t"
               "global_load_lds_dwordx4 v2, s[4:5]\n\tglobal_load_lds_dword v1, s[4:5] offset:1024\n"
               "2:\n\ts_cselect_b32 s22, 0, 0x200\n\ts_add_u32 m0, s22, %[lc]\n\t"
               "s_add_u32 s20, s12, 0x1fffff\n\ts_addc_u32 s21, s13, 0\n\ts_and_b32 s20, s20, 0xffe00000\n\ts_or_b32 s20, s20, %[gof]\n\t"
               "v_cmp_gt_u32 vcc, 32, v1\n\ts_addk_i32 s23, 32\n\tv_lshl_add_u32 v1, s23, 7, v1\n\t"
               "s_and_b32 s22, s2, 7\n\ts_lshl_b32 s22, s22, 7\n\tv_mov_b32 v2, s22\n\tv_cndmask_b32 v1, v2, v1, vcc\n\t"
               "global_load_lds_dword v1, s[20:21] sc1\n"
               "3:\n\t"
               "v_lshrrev_b32 v1, 2, v0\n\tv_cmp_eq_u32 vcc, 3, v1\n\tv_mov_b32 v3, 0x3f803f80\n\ts_nop 0\n\t"
               "v_cndmask_b32 v2, 0, 1, vcc\n\tv_cmp_eq_u32 vcc, 2, v1\n\tv_lshlrev_b32 v1, 2, v0\n\ts_nop 0\n\t"
               "v_cndmask_b32 v2, v2, v3, vcc\n\tv_cmp_eq_u32 vcc, 7, v0\n\tv_mov_b32 v3, %[scl]\n\t"
               "v_min_u32 v1, 80, v1\n\tv_cndmask_b32 v2, v2, v3, vcc\n\t"
               "ds_write_b32 v1, v2 offset:%[sm]\n\t"
               "s_waitcnt lgkmcnt(0)\n\ts_barrier"
               : "={s[0:1]}"(kp), "={s2}"(b), "={s[4:19]}"(ka), [wqo] "=&v"(wqo), [wql] "=&s"(wql), [ts] "=&s"(ts0)
               : "{v0}"(threadIdx.x), [dst] "n"(L_PF), [scl] "n"(__builtin_bit_cast(u32, SCL)), [sm] "n"(0), [row] "n"(H * DH * 2),
                 [lwq] "n"(L_WQ), [pst] "n"(16 * H * DH * 2), [np] "n"(NP0), [lx] "n"(L_HB), [lc] "n"(L_CTL + 2304),
                 [gof] "n"(WS_GOF)
               : "memory", "vcc", "scc", "v1", "v2", "v3", "s3", "s20", "s21", "s22", "s23");
  static_assert(WS_HB == WS_GOF + 32 * 128, "layout");
  const u32 tstart = (u32)ts0;
  const int tid = threadIdx.x, lane0 = tid & 63;
  const int wave = __builtin_amdgcn_readfirstlane(tid >> 6);
  const int g = b & 7, j = b >> 3, hsub = j & 3, c = j >> 2, h = g * 4 + hsub;
  const bool isO = j < 20;
  const bool leader = b == 0 && wave == 6;
  const u32 fslot = (u32)((j & 1) * 768 + ((j - 20) >> 1) * 128);
  u32 tmo = SPIN_CAP;
  volatile LAS u32* cnt = (volatile LAS u32*)(smem + L_CNT);
  Params P;
  P.x0 = mkptr<const u16>(ka[0], ka[1]);
  P.kc = mkptr<const u16>(ka[2], ka[3]);
  P.vc = mkptr<const u16>(ka[4], ka[5]);
  P.wq = mkptr<const u16>(ka[6], ka[7]);
  P.ws = mkptr<u32>(ka[8], ka[9]);
  P.w1 = mkptr<const u16>(ka[10], ka[11]);
  P.w3 = mkptr<const u16>(ka[12], ka[13]);
  P.wo = mkptr<const u16>(ka[14], ka[15]);
  u32* const wsb = (u32*)(((uintptr_t)P.ws + 0x1fffffu) & ~(uintptr_t)0x1fffffu);
  const __amdgpu_buffer_rsrc_t rs = __builtin_amdgcn_make_buffer_rsrc(wsb, 0, (int)WS_END, 0x00020000);
  const u32x4 rq = {(u32)(uintptr_t)wsb, (u32)((uintptr_t)wsb >> 32) & 0xffffu, WS_END, 0x00020000u};

  u32 anc = 0u, tsh = 0u;
  u32 tref = 0u, ar = 0u;
  u32x32 kv0, kv1, kv2, kv3, kv4, kv5;
  asm volatile(""
               : "={v[64:95]}"(kv0), "={v[96:127]}"(kv1), "={v[128:159]}"(kv2), "={v[160:191]}"(kv3), "={v[192:223]}"(kv4),
                 "={v[224:255]}"(kv5));
  const int smb = (int)(uintptr_t)(LAS unsigned char*)smem;
  const u32 gop = A_GO + go_slot(hsub * 2) + c * 256;
  u32 tnext = tstart * 8u + (u32)wave * (TSTEP / 8) + TQ0;
  const size_t rowbase = (size_t)h * SK + (SK - WIN) + (wave * 8 + c) * 8;
  const u16* vb = P.vc + rowbase * DH;
  const u16* ws0 = isO ? P.wo + (size_t)(768 * g) * D + 32 * j : ((j & 1) ? P.w3 : P.w1) + 192 * g + 32 * ((j - 20) >> 1);
  const u16* w2p;
  const bool sO = isO || ((u32)(uintptr_t)ws0 & 0x380u) == 0x180u;
  u32 tstep = sO ? TS_O : TS_F;
  int pn = isO ? wave * 6 : wave * 5;
  {
    volatile LAS u32* const CTL = (volatile LAS u32*)(smem + L_CTL);
    u32 gd, hb = 0u;
    const u32 lho = WS_HB + (lane0 >> 1) * 128 + (lane0 & 1) * 16;
    const u32 hso = lane0 < 8 ? WS_HB + (u32)h * 128 + lane0 * 4 : WS_GOF + g * 128;
    if (wave >= 6) {
      if (b < 8 && wave == 7 && lane0 < 32)
        __builtin_amdgcn_raw_buffer_store_b32(tstart, rs, WS_GOF + 2048 + b * 128 + lane0 * 4, 0, 16);
      if (leader) {
        ctl_load<true>(rq, lho, (u32)(smb + L_CTL));
        ctl_load<false>(rq, WS_GOF + (lane0 & 7) * 128, (u32)(smb + L_CTL + 2560));
      }
    }
    if (tid == 0) *(volatile LAS u32*)(smem + L_ANC) = 0u;
    *reinterpret_cast<u32x4*>(smem + L_P + wave * P_W + lane0 * 16) = u32x4{0u, 0u, 0u, 0u};
    *reinterpret_cast<u32x4*>(smem + (lane0 < 16 ? L_P + wave * P_W + 1024 + lane0 * 16 : L_OM + (wave * OM_S + OM_P + DH) * 4)) =
        u32x4{0u, 0u, 0u, 0u};
    const u16* kb = P.kc + rowbase * DH;
    const u32 ko = ((lane0 & 15) >> 1) * 384 + 16 * ((lane0 & 1) * 4 + (lane0 >> 4)), vo = (lane0 >> 3) * 384 + 16 * (lane0 & 7);
    const u32 pre = wave < 6 ? PRE : 0;
    tstep -= pre;
    asm volatile("" ::"s"(kb + (h & 7) * 512 * DH), "s"(kb), "s"(vb), "s"(ws0), "v"(ko), "v"(vo), "s"(tnext), "s"(tstep), "s"(pn) : "memory");
    if (wave < 6) {
      u32 tq = tnext - (5 - NP0) * TSQ;
#pragma unroll 1
      for (int p = NP0; p < 5; ++p) {
        pace(tq, TSQ);
        dma_kib(P.wq, wqo, smem, wql - smb);
        wqo += 16 * H * DH * 2;
        wql += 2048;
      }
    } else {
      u32 rtt;
      {
        u32 pa = 0u, pb = 0u;
        u64 t1, t2;
        const u32 pbo = WS_PB + (u32)(wave & 1) * (SIDE + 128u) + (u32)((b >> 3) & 7) * 16384u + (u32)((b & 7) * 4 + (b >> 6)) * 256u;
        asm volatile("s_load_dwordx2 %2, %7, 0x40\n\ts_waitcnt vmcnt(0) lgkmcnt(0)\n\t.p2align 6\n\ts_mov_b64 exec, 1\n\t"
                     "buffer_load_dword %0, off, %4, %5 sc1 nt\n\tbuffer_load_dword %1, off, %4, %6 sc1 nt\n\t"
                     "s_mov_b64 exec, -1\n\t"
                     "s_waitcnt vmcnt(1)\n\ts_memtime %2\n\ts_waitcnt vmcnt(0)\n\ts_memtime %3\n\ts_waitcnt lgkmcnt(0)"
                     : "+v"(pa), "+v"(pb), "=&s"(t1), "=&s"(t2)
                     : "s"(rq), "s"(pbo), "s"(pbo ^ SIDE), "s"(kp)
                     : "memory");
        rtt = __builtin_amdgcn_readfirstlane((u32)t2 - (u32)t1) + 1u;
      }
      if (wave == 6) {
        *(volatile LAS u32*)(smem + L_VOTE) = rtt;
      } else {
        const u32 ca = gy_rep(g) + fresh(lane0) * 16;
        store16(rs, ca + (u32)(j >> 3) * 16384u + (u32)(j & 7) * 1024u, u32x4{0u, 0u, 0u, 0u});
        if (lane0 < 8) store16(rs, ca + A_GH + (u32)j * 128u, u32x4{0u, 0u, 0u, 0u});
        hb = rtt;
      }
    }
#define WPO(n) ((n) >= PO0 && ((n) - PO0) % POS == 0 && ((n) - PO0) / POS < NINO)
#define WPF(n) ((n) >= PF0 && ((n) - PF0) % PFS == 0 && ((n) - PF0) / PFS < NINF)
#define WPIECE(n)                                                                                                   \
  if ((WPO(n) && sO) || (WPF(n) && !sO)) {                                                                          \
    pace(tnext, tstep);                                                                                             \
    if ((n) != PO0 || isO) dma_piece(ws0, isO ? D : F, smem, L_WB, pn++, lane0);                             \
  }
#define PAUSE                                                                                \
  {                                                                                          \
    const u32 x = __builtin_amdgcn_readfirstlane(*(volatile LAS u32*)(smem + L_VOTE + 16));  \
    tnext += x - tsh;                                                                        \
    tsh = x;                                                                                 \
  }
    sfor<0, 24>([&](auto ic) __attribute__((always_inline)) {
      constexpr int i = ic.value;
      LOADW(kreg(i), ko, kb + ((i / 3 + h) & 7) * 512 * DH, 128 * (i % 3));
      if (i == QI) {
        tstep += pre;
        tnext += (QI + 1) * pre;
      }
      WPIECE(i)
      if (i == XTAB) att_table(smem, fresh(lane0), wave, gop);
      if (i == XTAB + 1) w2p = karg<64>(kp) + (size_t)(192 * g) * D + 32 * j;
      if (i == 1 && wave == 6) {
        const u32x4 lv = afrag(smem, L_HB + lane0 * 16);
        const u32 xb = *reinterpret_cast<const u32*>(smem + L_HB + 1024 + lane0 * 4);
        float v[10];
#pragma unroll
        for (int k = 0; k < 4; ++k) {
          v[2 * k] = bflo(lv[k]);
          v[2 * k + 1] = bfhi(lv[k]);
        }
        v[8] = bflo(xb);
        v[9] = bfhi(xb);
        float sm = 0.f;
#pragma unroll
        for (int k = 0; k < 10; ++k) sm += v[k];
        const float mean = wave_sum(sm) * (1.0f / D);
        float q = 0.f;
#pragma unroll
        for (int k = 0; k < 10; ++k) q = fmaf(v[k] - mean, v[k] - mean, q);
        const float var = wave_sum(q) * (1.0f / D);
        const float mb = bf16r(mean);
        const float ri = __builtin_amdgcn_rcpf(bf16r(__builtin_amdgcn_sqrtf(bf16r(bf16r(var) + 1e-5f))));
        u32 hp[5];
#pragma unroll
        for (int k = 0; k < 5; ++k) {
          const u32 cc = cvt2(v[2 * k] - mb, v[2 * k + 1] - mb);
          hp[k] = cvt2(bflo(cc) * ri, bfhi(cc) * ri);
        }
        *reinterpret_cast<u32x4*>(smem + L_LN + lane0 * 16) = u32x4{hp[0], hp[1], hp[2], hp[3]};
        *reinterpret_cast<u32*>(smem + L_LN + 1024 + lane0 * 4) = hp[4];
        asm volatile("s_waitcnt lgkmcnt(0)" ::: "memory");
        if (lane0 == 0) lds_add(smem, L_VOTE + 4, 1u);
      }
      if (wave == 6) {
        if (i == AL_R || i == AL_R + AL_2) {
          const int ln = fresh(lane0);
          ctl_load<false>(rq, WS_GOF + (ln & 7) * 128 + (i == AL_R ? 2048 : 3072 + (ln >> 3) * 4), (u32)(smb + L_CTL + 3072));
        }
        if (i == AL_L || i == AL_L + AL_2) {
          asm volatile("s_waitcnt vmcnt(%0)" ::"n"(AL_L - AL_R) : "memory");
          const u32 d = CTL[768 + lane0] - tstart + AL_OFF;
          const int sd = (int)sum8(d + 128u < 512u ? (float)(int)d : 0.f);
          if (i == AL_L) {
            *(volatile LAS int*)(smem + L_VOTE + 8) = sd >> 4;
          } else {
            const int s0 = __builtin_amdgcn_readlane(sd, 0);
            int p = __builtin_amdgcn_readlane(sd, 8) - s0;
            p = p > FGT * 8 ? p * FGA >> 2 : 0;
            p = p > FGM * 8 ? FGM * 8 : p;
            *(volatile LAS u32x2*)(smem + L_VOTE + 12) = u32x2{tstart + (u32)((s0 + p) >> 3) + (TQ0 + (48 + NINO) * TS_O) / 8 - 2, (u32)p};
          }
        }
      }
      if (i == FR && wave == 0 && b < 8) {
        asm volatile("s_waitcnt vmcnt(%0)" ::"n"(FW) : "memory");
        const u32 land = rtnow();
        if (lane0 < 32)
          __builtin_amdgcn_raw_buffer_store_b32((lane0 & 1) ? land - FGN : tstart, rs, WS_GOF + 3072 + b * 128 + lane0 * 4, 0, 16);
      }
      if (i == AL_A) tstep += __builtin_amdgcn_readfirstlane(*(volatile LAS u32*)(smem + L_VOTE + 8));
      if (i == QI - 1 && wave < 6) {
        asm volatile("s_waitcnt vmcnt(%0)" ::"n"(QI) : "memory");
        if (lane0 == 0) lds_add(smem, L_VOTE + 4, 1u);
      }
      if (i == QI && wave < 6) {
        lds_wait((volatile LAS u32*)(smem + L_VOTE + 4), 7u);
        G80 a;
        g80_w<1024>(a, smem, lane0, L_WQ + (wave >> 1) * 10240 + (wave & 1) * 512);
        g80_a(a, lane0, L_LN + 160 * c, -1);
        float r0, r1;
        g80_run(a, smem, r0, r1);
        auto sw = __builtin_amdgcn_permlane32_swap(__float_as_uint(r0), __float_as_uint(r1), false, false);
        const f32x4 qd = quad4(__uint_as_float(sw[1]));
        const int qp = wave * 128 + (lane0 >> 5) * 64 + (lane0 & 15) * 4;
        if ((lane0 & 19) == 0) *reinterpret_cast<u32x2*>(smem + L_RED + (qp >> 1)) = u32x2{cvt2(qd[0], qd[1]), cvt2(qd[2], qd[3])};
        if ((lane0 & 19) == 16) *reinterpret_cast<f32x4*>(smem + L_CS + qp) = qd;
        if (lane0 == 0) lds_add(smem, L_CNT + 4, 1u);
      }
      if (i > AL_L + AL_2 && (i - AL_L - AL_2) % FGS == 1) PAUSE
      if (wave == 7) {
        if (i == QP) {
          lds_wait(cnt + 1, 6u);
          const int ln = fresh(lane0);
          if (ln < 24) store16(rs, WS_GQ1 + (u32)(h * 8 + c) * 2048 + ln * 16, afrag(smem, L_RED + ln * 16));
        }
        if (i == BEAT0) {
          asm volatile("s_waitcnt vmcnt(%0)" ::"n"(BEAT0 - QP) : "memory");
          const int df = (int)(__builtin_amdgcn_readfirstlane(*(volatile LAS u32*)(smem + L_VOTE)) - hb);
          hb = df > PTHR ? 1u << (g >> 2) : (df < -PTHR ? 2u >> (g >> 2) : 0u);
          hb += (__builtin_amdgcn_readfirstlane(CTL[576 + c]) & ~3u) + 4u;
          if (lane0 == 0) __builtin_amdgcn_raw_buffer_store_b32(hb, rs, WS_HB + h * 128 + c * 4, 0, 16);
          hb += 4u;
        }
        if (i == CKL) ctl_load<false>(rq, hso, (u32)(smb + L_CTL + 2048));
      }
      if (i == CKL && leader) {
        ctl_load<true>(rq, lho, (u32)(smb + L_CTL + 1024));
      }
    });
#define VLOADS(A, B)                                                                           \
  _Pragma("unroll") for (int i = A; i < B; ++i) {                                              \
    if (i == XA - E_QN) tnext -= wave < 4 ? E_QK : 0;                                          \
    if (i == XB - E_QN) tnext -= wave < 4 ? 0 : E_QK;                                          \
    LOADW(vreg(i), vo, vb + ((i / 3 + h) & 7) * 512 * DH, 128 * (i % 3));                      \
    if (24 + i == AL_A + 15) {                                                                 \
                        \
      constexpr int RF = 47 - AL_A - 15, RO = RF + NINO - (AL_A + 15 - PO0 + POS - 1) / POS;    \
      tstep = sO ? TS_O : TS_F;                                                                \
      PAUSE                                                                                    \
      const int d = (int)(__builtin_amdgcn_readfirstlane(*(volatile LAS u32*)(smem + L_VOTE + 12)) * 8u +   \
                          (u32)wave * (TSTEP / 8) - tnext - (sO ? RO * TS_O : RF * TS_F));     \
      if ((u32)(d + 512) < 1024u) tstep += (d * (sO ? 65536 / RO : 65536 / RF) + 32768) >> 16; \
    }                                                                                          \
    WPIECE(24 + i)                                                                             \
  }
    VLOADS(0, CKV)
    if (wave == 7) {
      CWAIT;
      HS_WAIT((u32)ch == 0xffu)
      if (lane0 == 0) cnt[1] = 7u;
    }
    if (leader) {
      CWAIT;
      const u32x4 xa = afrag(smem, L_CTL + lane0 * 16);
      u32x4 lv;
#pragma unroll 1
      for (;;) {
        asm volatile("" ::: "memory");
        lv = afrag(smem, L_CTL + 1024 + lane0 * 16);
        const bool same = lv[0] == xa[0] || lv[1] == xa[1] || lv[2] == xa[2] || lv[3] == xa[3];
        if (__builtin_amdgcn_ballot_w64(same) == 0ull || tmo == 0u) break;
        --tmo;
        __builtin_amdgcn_s_sleep(4);
        ctl_load<true>(rq, lho, (u32)(smb + L_CTL + 1024));
        asm volatile("s_waitcnt vmcnt(0)" ::: "memory");
      }
      u32 nv = 256u;
#pragma unroll
      for (int e = 0; e < 4; ++e)
        nv += (u32)__builtin_popcountll(__builtin_amdgcn_ballot_w64((lv[e] & 2u) != 0u)) -
              (u32)__builtin_popcountll(__builtin_amdgcn_ballot_w64((lv[e] & 1u) != 0u));
      if (lane0 < 8) __builtin_amdgcn_raw_buffer_store_b32(CTL[640 + lane0] + (nv > 256u ? 2u : 1u), rs, WS_GOF + lane0 * 128, 0, 16);
    }
    VLOADS(CKV, CKV + 1)
    if (wave < 3) {
      const int ln = fresh(lane0);
      lds_wait(cnt + 1, 7u);
      ctl_load<true>(rq, WS_GQ1 + (u32)(h * 8 + (ln >> 3)) * 2048 + wave * 128 + (ln & 7) * 16, (u32)(smb + L_LAND + wave * 2048));
    }
    VLOADS(CKV + 1, CKV + 1 + CW)
    if (wave < 3) {
      const int ln = fresh(lane0);
      CWAIT;
      const u32x4 v = afrag(smem, L_LAND + wave * 2048 + ln * 16);
      const float t0 = tsum8(f32x4{bflo(v[0]), bflo(v[1]), bflo(v[2]), bflo(v[3])});
      const float t1 = tsum8(f32x4{bfhi(v[0]), bfhi(v[1]), bfhi(v[2]), bfhi(v[3])});
      *reinterpret_cast<u32*>(smem + q_addr(wave * 64 + (ln & 7) * 8 + ((ln >> 4) & 1) * 4 + (ln >> 5) * 2)) = cvt2(t0, t1);
      asm volatile("s_waitcnt lgkmcnt(0)" ::: "memory");
      if (lane0 == 0) lds_add(smem, L_CNT, 1u);
    }
    VLOADS(CKV + 1 + CW, XB - CW)
    if (wave == 7) ctl_load<false>(rq, hso, (u32)(smb + L_CTL + 2048));
    VLOADS(XB - CW, XA)
    if (wave >= 4) {
      VLOADS(XA, XB)
      if (wave == 7) {
        CWAIT;
        HS_WAIT(false)
        if (lane0 == 8) cnt[2] = (gd >> 1) | 2u;
      }
    }
  }

  asm volatile("s_branch 1f\n\t.p2align 7\n\t.rept %0\n\ts_nop 0\n\t.endr\n1:" ::"n"(CAL));
#pragma unroll 1
  for (int half = 0;; ++half) {
    {
      float pend, pmax;
      PVAddr pv;
      QKAddr qa;
      const int lane = lane_id();
      const u32 off = ar + A_GQ + hsub * 16384 + (wave < 6 ? (lane >> 3) * 896 + wave * 128 + (lane & 7) * 16 : (lane & 7) * 896 + 768);
      u32x4 qv;
      asm volatile("v_mov_b64 v[56:57], -1\n\tv_mov_b64 v[58:59], -1" : "={v[56:59]}"(qv));
      if (half == 1) {
        if (wave == 6) *(volatile LAS u32*)(smem + L_RI) = 0u;
        asm volatile("s_cmp_lt_u32 %[wv], 7\n\ts_cselect_b64 exec, -1, 0\n\t" WAIT_RT
                     "buffer_load_dwordx4 v[56:59], %[off], %[rq], 0 offen sc1\n\ts_mov_b64 exec, -1"
                     : "+{v[56:59]}"(qv)
                     : [off] "v"(off), [rq] "s"(rq), [wv] "s"(wave), [tt] "s"(anc + (wave < 7 ? A_Q : 0))
                     : "memory", "scc", "s98", "s99");
        if (wave < 4) __builtin_amdgcn_s_setprio(2);
      }
      int qw;
      att_setup(qa, pv, smem, lane, wave, qw);
      {
        u32 t, bud;
#define QRETRY(L) "s_sub_u32 %[bud], %[bud], 1\n\ts_cbranch_scc1 " L "\n\ts_sleep 1\n\tbuffer_load_dwordx4 v[56:59], %[off], %[rq], 0 offen sc1\n"
        asm volatile("s_mov_b32 %[bud], 0x10000\n\ts_cmp_eq_u32 %[hf], 0\n\ts_cbranch_scc1 0f\n\t"
                     "s_cmp_eq_u32 %[wv], 6\n\ts_cbranch_scc0 2f\n\ts_branch 5f\n"
                     "0:\n\ts_mov_b32 %[bud], 0x400000\n"
                     "0:\n\tds_read_b32 %[t], %[z] offset:%[cn]\n\ts_sub_u32 %[bud], %[bud], 1\n\ts_cbranch_scc1 8f\n\ts_waitcnt lgkmcnt(0)\n\t"
                     "v_cmp_eq_u32 vcc, 3, %[t]\n\ts_cbranch_vccnz 8f\n\ts_sleep 1\n\ts_branch 0b\n"
                     "4:\n\t" QRETRY("6f")
                     "5:\n\ts_waitcnt vmcnt(0)\n\t" AND3("v56", "v57", "v58") AND3("%[t]", "1", "v59")
                     "v_cmp_eq_u32 vcc, 0, %[t]\n\ts_cbranch_vccnz 4b\n"
                     "6:\n\tv_add_f32 v56, v56, v57\n\t" SD("v59", "quad_perm:[1,0,3,2]") "v_add_f32 v56, v56, v58\n\t"
                     "s_cmp_eq_u32 %[wv], 7\n\t" SD("v59", "quad_perm:[2,3,0,1]") SD("v56", "quad_perm:[1,0,3,2]")
                     "s_nop 0\n\t" SD("v59", "row_half_mirror") SD("v56", "quad_perm:[2,3,0,1]")
                     "v_mul_f32 v59, 0x3acccccd, v59\n\tv_mul_f32 v57, -v59, v59\n\t" SD("v56", "row_half_mirror")
                     "s_waitcnt lgkmcnt(0)\n\ts_barrier\n\ts_cbranch_scc1 8f\n"
                     "7:\n\t"
                     "v_fmac_f32 v57, 0x3acccccd, v56\n\t" RND("v57", "|v57|") "v_add_f32 v57, 0x3727c5ac, v57\n\t" RND("v57", "v57")
                     "v_sqrt_f32 v57, v57\n\ts_nop 0\n\t" RND("v57", "v57") "v_rcp_f32 v57, v57\n\ts_nop 0\n\t"
                     "v_mul_f32 v57, %[scl], v57\n\tv_or_b32 v57, 1, v57\n\tds_write_b32 %[qw], v57\n\t"
                     "s_cbranch_scc1 6b\n\ts_branch 8f\n"
                     "1:\n\t" QRETRY("3f")
                     "2:\n\ts_cmp_eq_u32 %[wv], 7\n\ts_waitcnt vmcnt(0)\n\t"
                     "v_permlane32_swap_b32 v56, v58\n\tv_permlane32_swap_b32 v57, v59\n\t"
                     "v_pk_add_f32 v[60:61], v[56:57], v[58:59]\n\t" AND3("v56", "v57", "v58") AND3("%[t]", "1", "v59")
                     "v_permlane16_swap_b32 v60, v61\n\tv_add_f32 v60, v60, v61\n\t"
                     "v_cmp_eq_u32 vcc, 0, %[t]\n\ts_nop 0\n\t" SD("v60", "row_ror:8")
                     "v_cvt_pk_bf16_f32 v60, v60, v60\n\tds_write_b16 %[qw], v60\n\ts_cbranch_vccnz 1b\n\ts_cbranch_scc1 7b\n"
                     "3:\n\ts_waitcnt lgkmcnt(0)\n\ts_barrier\n"
                     "8:"
                     : [t] "=&v"(t), [bud] "=&s"(bud), "+{v[56:59]}"(qv)
                     : [hf] "s"(half), [wv] "s"(wave), [z] "v"(smb), [cn] "n"(L_CNT), [off] "v"(off), [rq] "s"(rq), [qw] "v"(qw),
                       [scl] "n"(__builtin_bit_cast(u32, SCL))
                     : "memory", "vcc", "scc", "v60", "v61");
      }
      QK_SOFTMAX(qa)
      if (half == 0) {
        const int lv_ = lane_id();
        const u32 vo = (lv_ >> 3) * 384 + 16 * (lv_ & 7);
        tnext += E_QK;
        if (wave < 4) {
          VLOADS(XA, XB)
        }
        VLOADS(XB, 24)
      }
#define DA(CTL) "v_add_f32_dpp %[l], %[l], %[l] " CTL " row_mask:0xf bank_mask:0xf\n\t"
#define PX(o, v, a) SM "%[o" #o "], %[a" #a "], v[160+24*" #a "+8*" #o ":167+24*" #a "+8*" #o "], %[ix]\n\t"
      asm volatile("s_waitcnt vmcnt(6) lgkmcnt(3)\n\t" PX(0, x, 0) DA("quad_perm:[1,0,3,2]") PX(1, y, 0) PX(2, z, 0)
                   "s_waitcnt lgkmcnt(2)\n\t" PX(0, x, 1) DA("quad_perm:[2,3,0,1]") PX(1, y, 1) PX(2, z, 1)
                   "s_waitcnt lgkmcnt(0)\n\t" PX(0, x, 2) DA("row_half_mirror") PX(1, y, 2) PX(2, z, 2)
                   : [o0] "+v"(pv.O0), [o1] "+v"(pv.O1), [o2] "+v"(pv.O2), [a0] "+v"(pv.A0), [a1] "+v"(pv.A1),
                     [a2] "+v"(pv.A2), [a3] "+v"(pv.A3), [l] "+v"(pend)
                   : [ix] "v"(pv.idx), "{v[160:191]}"(kv3), "{v[192:223]}"(kv4), "{v[224:255]}"(kv5)
                   : "memory");
#define CLEAR2                                                                                                         \
  {                                                                                                                    \
    const int p2 = (isO ? j : j - 8) * 2 + (wave & 1);                                                                 \
    const u32 ca = ((isO ? j < 12 : (p2 < 40 || (g & 3) == 0)) ? gy_rep(g) : OOB) + (u32)(4 + (p2 >> 2)) * 16384u +    \
                   (u32)(p2 & 3) * 2048u + lane_id() * 16;                                                          \
    const u32x4 z = __builtin_bit_cast(u32x4, zero4());                                                                \
    store16(rs, ca, z);                                                                                                \
    store16(rs, ca + 1024, z);                                                                                         \
  }
      static_assert(5 - NINF >= NPF && 6 - NINO >= NPRE && NPF >= NPRE, "pieces behind V");
      if (half == 0) {
        {
          const int ln = lane_id();
          if (wave < 6 && ln < 8)
            store16(rs, WS_GQ1 + (u32)(h * 8 + c) * 2048 + 1024 + wave * 128 + ln * 16, afrag(smem, L_CS + wave * 128 + ln * 16));
        }
        anc = __builtin_amdgcn_readfirstlane(*(volatile LAS u32*)(smem + L_VOTE + 12));
#pragma unroll 1
        for (int p = 0; p < (sO ? NPRE : NPF); ++p) {
          pace(tnext, sO ? TPV : TPF);
          dma_piece(ws0, isO ? D : F, smem, L_WB, pn++, lane_id());
        }
        ar = (u32)(g & 3) * AREA + (((u32)(g >> 2) ^ lds_wait_nz(cnt + 2)) & 1u) * SIDE;
      }
      const u32 gob = ar + (u32)half * GO_SZ;
      if (NPF != NPRE && sO) asm volatile("s_waitcnt vmcnt(%0)" ::"n"(NPRE) : "memory");
      asm volatile("s_waitcnt vmcnt(%[np])\n\t" PX(0, x, 3) DA("row_ror:8") PX(1, y, 3) PX(2, z, 3) "s_setprio 0"
                   : [o0] "+v"(pv.O0), [o1] "+v"(pv.O1), [o2] "+v"(pv.O2), [a3] "+v"(pv.A3), [l] "+v"(pend)
                   : [ix] "v"(pv.idx), [np] "n"(NPF), "{v[224:255]}"(kv5)
                   : "memory");
      static_assert(NINO + NPRE == 6 && PF0 != PO0, "pieces");
      if (half == 0 && FEARLY && !sO) {
#pragma unroll 1
        for (int p = NINF + NPF; p < NINF + NPF + FEARLY; ++p) dma_piece(ws0, F, smem, L_WB, wave * 5 + p, lane_id());
      }
#define MX(CTL) "s_nop 1\n\tv_max_f32_dpp v49, v49, v49 " CTL " row_mask:0xf bank_mask:0xf\n\t"
#define MXP(W) "v_mov_b32 v50, v49\n\ts_nop 1\n\tv_permlane" W "_swap_b32 v49, v50\n\tv_max_f32 v49, v49, v50\n\t"
#define RB(D, A, BANK) "v_add_f32_dpp " D ", " A ", " A " row_ror:8 row_mask:0xf bank_mask:" BANK "\n\t"
#define SWP(W) "v_permlane" W "_swap_b32 v40, v42\n\tv_permlane" W "_swap_b32 v41, v43\n\t" PKA("v[40:41]", "v[42:43]")
#define SC2(R) "v_pk_mul_f32 " R ", " R ", v[50:51] op_sel_hi:[1,0]\n\t"
      asm volatile("ds_write2_b32 %[la], %[l], %[mx] offset1:4\n\ts_mov_b64 exec, %[m]\n\ts_cmp_lt_u32 %[wv], 6\n\ts_nop 1\n\t"
                   "ds_write_b128 %[a], %[o0]\n\tds_write_b128 %[a], %[o1] offset:256\n\t"
                   "ds_write_b128 %[a], %[o2] offset:512\n\t"
                   "s_mov_b64 exec, -1\n\ts_waitcnt lgkmcnt(0)\n\ts_barrier\n\t"
                   "ds_read_b32 v48, %[ma]\n\tds_read_b128 v[40:43], %[sa]\n\tds_read_b128 v[44:47], %[sa] offset:%[op]\n\t"
                   "s_waitcnt lgkmcnt(2)\n\tv_cmp_ne_u32 vcc, 0, v48\n\ts_cbranch_vccnz 5f\n"
                   "4:\n\ts_cbranch_scc0 6f\n\ts_waitcnt lgkmcnt(0)\n\t"
                   PKA("v[40:41]", "v[44:45]") PKA("v[42:43]", "v[46:47]") "s_nop 0\n\t"
                   RB("v40", "v40", "0x3") RB("v41", "v41", "0x3") RB("v40", "v42", "0xc") RB("v41", "v43", "0xc")
                   "s_nop 0\n\t" SWP("16") "s_nop 1\n\t" SWP("32")
                   "v_cvt_pk_bf16_f32 v40, v40, v41\n\tv_or_b32 v40, 1, v40\n\t"
                   "buffer_store_dword v40, %[dst], %[rq], %[so] offen sc1\n\ts_branch 9f\n"
                   "5:\n\tv_mov_b32 v49, v48\n\t" MX("quad_perm:[1,0,3,2]") MX("quad_perm:[2,3,0,1]") MX("row_half_mirror") MX("row_ror:8")
                   MXP("16") MXP("32") "v_sub_f32 v50, v48, v49\n\tv_exp_f32 v50, v50\n\tv_mov_b32 v48, v49\n\ts_waitcnt lgkmcnt(0)\n\t"
                   SC2("v[40:41]") SC2("v[42:43]") SC2("v[44:45]") SC2("v[46:47]") "s_branch 4b\n"
                   "6:\n\ts_waitcnt lgkmcnt(0)\n\t" PKA("v[42:43]", "v[40:41]") "v_add_f32 v41, v42, v43\n\t"
                   "v_or_b32 v40, 1, v48\n\tv_or_b32 v42, 1, v48\n\t" SD("v41", "quad_perm:[1,0,3,2]")
                   "v_mov_b32 v43, 1\n\ts_nop 0\n\t" SD("v41", "quad_perm:[2,3,0,1]") "s_nop 1\n\t" SD("v41", "row_half_mirror")
                   "v_or_b32 v41, 1, v41\n\tbuffer_store_dwordx4 v[40:43], %[dst], %[rq], %[so] offen sc1\n\ts_nop 1\n"
                   "9:"
                   :
                   : [m] "s"(0xff00ff0000ff00ffull), [a] "v"(pv.oa + smb), [o0] "v"(pv.O0), [o1] "v"(pv.O1), [o2] "v"(pv.O2),
                     [la] "v"(pv.la + smb), [l] "v"(pend), [mx] "v"(pmax), [wv] "s"(wave), [ma] "v"(pv.ma), [sa] "v"(pv.sa),
                     [op] "n"(OM_P * 4), [dst] "v"(pv.dst), [rq] "s"(rq), [so] "s"(gob)
                   : "memory", "vcc", "scc", DCLOB);
      tref = now();
      if (half == 0 && wave >= 6) CLEAR2
    }
    const u32 go = ar + A_GO + (u32)half * GO_SZ;
    const u32 gyw = A_GY + (u32)half * GY_PAIR;
    const u32 gy = gyw + ar;
    const u32 gh = ar + A_GH + (u32)half * 1536u;
    const int land = L_LAND + wave * 2048;
    const u32 landa = (u32)(uintptr_t)(LAS unsigned char*)smem + (u32)land;
#define CSLOAD                                                                                                        \
  if (wave < 6) {                                                                                                     \
    const int ln = lane_id();                                                                                         \
    ctl_load<true>(rq, WS_GQ1 + 1024 + (u32)(h * 8 + (ln >> 3)) * 2048 + wave * 128 + (ln & 7) * 16, (u32)(smb + L_OM + wave * OM_S * 4)); \
  }
    if (isO) {
      {
        const int lane = lane_id();
        const u32 voff = fresh(go + go_slot(wave) + ((lane * 16) ^ ((lane >> 4) * 64)));
        if (!half) wait_until(tref + 24 * T_O);
        wait_rt(anc + (half ? A_O : X_O));
        dma_poll<false>(rq, voff, landa, ~0ull, ~0ull);
        const int n = lane & 15, G = lane >> 4;
        const int r0_ = land + (n & 7) * 256 + (n & 3) * 64;
        const int ra = fresh(n < 8 ? r0_ + G * 16 : r0_ ^ 192), rb = fresh((r0_ + G * 16) ^ 64), rc = fresh((r0_ + G * 16) ^ 128);
        const int l0 = fresh(L_WB + wave * 6144 + wlane(lane, 0)), l1 = fresh(L_WB + wave * 6144 + wlane(lane, 1));
        asm volatile("s_waitcnt vmcnt(%0)" ::"n"(3) : "memory");
        u32x4 wf[6];
#pragma unroll
        for (int k = 0; k < 3; ++k) {
          wf[2 * k] = wfrag(smem, l0, 2048 * k);
          wf[2 * k + 1] = wfrag(smem, l1, 2048 * k);
        }
        const int rw = fresh(lane < 32 ? L_RED + wave * 160 + lane * 4 : L_ACT);
        const int rr = fresh(L_RED + (lane & 8) * 80 + (lane & 7) * 16);
        const u32 dst = fresh(gyw + gy_rep(lane >> 3) + gy_chunk(4 * j + ((lane >> 1) & 3)) + g * 256 + (lane & 1) * 8);
        f32x4 d0, d1;
        float mc, t1_, rl;
        u32 t, bud, rare;
        asm volatile(POLL_HEAD("s_lshr_b32 s99, s98, 8\n\ts_or_b32 s98, s98, s99\n\t", "")
                     "s_waitcnt lgkmcnt(2)\n\t"
                     MF R0 ", " F0 ", %[w0], 0\n\t"
                     "v_add_f32_dpp v48, v53, v53 quad_perm:[1,0,3,2] row_mask:0xf bank_mask:0xf\n\t"
                     MF R1 ", " F0 ", %[w1], 0\n\t" AND3("v52", "v53", "v54") "v_cmp_ne_u32 s[98:99], 1, v52\n\t"
                     "s_waitcnt lgkmcnt(1)\n\t"
                     MF R0 ", " F1 ", %[w2], " R0 "\n\t" ZD("quad_perm:[2,3,0,1]", "0xf") AND3("%[t]", "v55", "v56")
                     MF R1 ", " F1 ", %[w3], " R1 "\n\t" ZD("row_half_mirror", "0xf") AND3("%[t]", "v57", "v58")
                     "v_rcp_f32 v48, v48\n\t"
                     "s_waitcnt lgkmcnt(0)\n\t"
                     MF R0 ", " F2 ", %[w4], " R0 "\n\t" AND3("%[t]", "v59", "v60") AND3("%[t]", "v61", "v62") AND3("%[t]", "1", "v63")
                     MF R1 ", " F2 ", %[w5], " R1 "\n\t"
                     "v_cmp_eq_u32 vcc, 0, %[t]\n\ts_cbranch_vccnz 1b\n"
                     "3:\n\tv_readlane_b32 %[rl], v48, 8\n\ts_and_b32 %[rare], s98, 0xff00"
                     : "=&{v[40:43]}"(d0), "=&{v[44:47]}"(d1), [t] "=&v"(t), [bud] "=&s"(bud), "=&{v52}"(mc), "=&{v53}"(t1_),
                       [rl] "=&s"(rl), [rare] "=&s"(rare)
                     : [ra] "v"(ra), [rb] "v"(rb), [rc] "v"(rc), [w0] "v"(wf[0]), [w1] "v"(wf[1]), [w2] "v"(wf[2]), [w3] "v"(wf[3]),
                       [w4] "v"(wf[4]), [w5] "v"(wf[5]), [vo] "v"(voff), [rq] "s"(rq), [land] "s"(landa)
                     : "memory", "vcc", "scc", "s98", "s99", "v48", "v54", "v55", "v56", "v57", "v58", "v59", "v60", "v61", "v62",
                       "v63");
        if (__builtin_expect(rare != 0u, 0)) {
          asm volatile("s_nop 7" : "+v"(d0), "+v"(d1));
          const float fc = __builtin_amdgcn_exp2f(mc - max8(mc));
          const u32 sc = __float_as_uint(fc * __builtin_amdgcn_rcpf(sum8(fc * t1_)));
#pragma unroll
          for (int e = 0; e < 4; ++e) {
            const float se = __uint_as_float(G == 0 ? __builtin_amdgcn_readlane(sc, 8 + e) : __builtin_amdgcn_readlane(sc, 12 + e));
            d0[e] *= se;
            d1[e] *= se;
          }
          rl = 1.0f;
        }
        asm volatile("v_pk_add_f32 v[40:41], v[40:41], v[42:43]\n\tv_add_f32 v40, v40, v41\n\t"
                     "v_pk_add_f32 v[44:45], v[44:45], v[46:47]\n\tv_add_f32 v44, v44, v45\n\t"
                     "s_cmp_eq_u32 %[wv], 0\n\ts_nop 0\n\t"
                     "v_permlane16_swap_b32 v40, v44\n\tv_add_f32 v40, v40, v44\n\tv_mul_f32 v40, %[rl], v40\n\t"
                     "ds_write_b32 %[rw], v40\n\ts_waitcnt lgkmcnt(0)\n\ts_barrier\n\ts_cbranch_scc0 9f\n\t"
                     "ds_read_b128 v[40:43], %[rr]\n\tds_read_b128 v[44:47], %[rr] offset:160\n\t"
                     "ds_read_b128 v[48:51], %[rr] offset:320\n\tds_read_b128 v[52:55], %[rr] offset:480\n\t"
                     "s_waitcnt lgkmcnt(2)\n\t" PKA("v[40:41]", "v[44:45]") PKA("v[42:43]", "v[46:47]")
                     "s_waitcnt lgkmcnt(0)\n\t" PKA("v[48:49]", "v[52:53]") PKA("v[50:51]", "v[54:55]")
                     PKA("v[40:41]", "v[48:49]") PKA("v[42:43]", "v[50:51]") "s_nop 0\n\t"
                     SD("v40", "row_ror:8") SD("v41", "row_ror:8") SD("v42", "row_ror:8") SD("v43", "row_ror:8")
                     "v_cvt_pk_bf16_f32 v40, v40, v41\n\tv_cvt_pk_bf16_f32 v41, v42, v43\n\t"
                     "v_or_b32 v40, 1, v40\n\tv_or_b32 v41, 1, v41\n\t"
                     "buffer_store_dwordx2 v[40:41], %[dst], %[rq], 0 offen sc1\n"
                     "9:"
                     : "+{v[40:43]}"(d0), "+{v[44:47]}"(d1)
                     : [wv] "s"(wave), [rl] "s"(rl), [rw] "v"(rw), [rr] "v"(rr), [dst] "v"(dst), [rq] "s"(rq)
                     : "memory", "scc", "v48", "v49", "v50", "v51", "v52", "v53", "v54", "v55");
      }
      tref = now();
      if (half == 0) {
        if (wave == 0) {
          const int lane = lane_id();
          u32 w2o = ((lane >> 2) * D + ((((lane >> 1) ^ (lane >> 5)) & 1) * 16 + (lane & 1) * 8)) * 2;
#define W2L "global_load_lds_dwordx4 %[wo], %[wp] nt\n\ts_addk_i32 m0, 0x400\n\tv_add_u32 %[wo], %[rs], %[wo]\n\t"
          asm volatile("s_mov_b32 s98, m0\n\ts_mov_b32 m0, %[wl]\n\ts_nop 2\n8:\n\t" W2L W2L W2L W2L
                       "s_cmp_lt_u32 m0, %[we]\n\ts_cbranch_scc1 8b\n\ts_mov_b32 m0, s98"
                       : [wo] "+v"(w2o)
                       : [wp] "s"(w2p), [wl] "n"(L_W2), [we] "n"(L_W2 + 12288), [rs] "n"(16 * D * 2)
                       : "memory", "scc", "s98");
        }
        CSLOAD
        if (wave == 0) {
          asm volatile("s_waitcnt vmcnt(0)" ::: "memory");
          cnt[1] = 8u;
        }
        if (wave == 5) {
          wait_until(tref + 24 * T_LAZY);
          x_update<false>(smem, rq, lane_id(), gy + gy_row(c), c);
        }
      } else if (g == 0 && wave == 2) {
        const int lane = lane_id();
        const int col = j * 32 + (lane & 7) * 4 + ((lane >> 4) & 1) * 2 + (lane >> 5);
        u32 x0v, dummy;
        u16* const po = karg<72>(kp) + col;
        asm volatile("global_load_ushort %0, %2, off\n\tglobal_load_ushort %1, %3, off\n\ts_waitcnt vmcnt(0)"
                     : "=&v"(x0v), "=&v"(dummy)
                     : "v"(karg<0>(kp) + col), "v"(po)
                     : "memory");
        wait_until(tref + 24 * T_LAZY);
        const u32 o = gy + gy_chunk(4 * j + ((lane >> 1) & 3)) + (lane >> 3) * 256 + (lane & 1) * 8;
        const u32 off[3] = {o - GY_PAIR, o - GY_PAIR + GY_INST, o};
        float y[3];
        col_poll<3>(rq, off, y);
        float x3 = bf16r(bf16r(bf16r(bflo(x0v) + y[0]) + y[1]) + y[2]);
        const u32 off2 = fresh(o + GY_INST);
        asm volatile("" : "+v"(x3));
        wait_rt(anc + A_F);
        u32 t, bud;
        asm volatile("s_mov_b32 %[bud], 0x10000\n\ts_branch 2f\n"
                     "1:\n\ts_sub_u32 %[bud], %[bud], 1\n\ts_cbranch_scc1 3f\n"
                     "2:\n\tbuffer_load_dwordx2 v[40:41], %[off], %[rq], 0 offen sc1\n\ts_waitcnt vmcnt(0)\n\t"
                     "v_lshlrev_b32 v42, 16, v40\n\tv_and_b32 v44, 0xffff0000, v40\n\t"
                     "v_lshlrev_b32 v43, 16, v41\n\tv_and_b32 v45, 0xffff0000, v41\n\t"
                     "v_permlane32_swap_b32 v42, v44\n\t" AND3("v40", "1", "v41") "v_permlane32_swap_b32 v43, v45\n\t"
                     PKA("v[42:43]", "v[44:45]") "v_cmp_eq_u32 vcc, 0, %[t]\n\ts_cbranch_vccnz 1b\n"
                     "3:\n\tv_permlane16_swap_b32 v42, v43\n\tv_add_f32 v42, v42, v43\n\ts_nop 1\n\t" SD("v42", "row_ror:8")
                     RND("v42", "v42") "v_add_f32 v42, v42, %[x3]\n\tv_cvt_pk_bf16_f32 v42, v42, v42\n\t"
                     "global_store_short %[po], v42, off"
                     : [t] "=&v"(t), [bud] "=&s"(bud)
                     : [off] "v"(off2), [rq] "s"(rq), [x3] "v"(x3), [po] "v"(po)
                     : "memory", "vcc", "scc", "s98", "s99", DCLOB);
      }
      if (wave >= 6) {
        const int lane = lane_id(), wv = wave - 6;
        const int n = lane & 15, G = lane >> 4;
        const int l0 = fresh(L_W2 + wlane(lane, wv));
        const int l48 = lane < 48 ? lane : 0;
        const u32 off = fresh(gh + (u32)l48 * 16);
        const int aw = fresh(L_ACT + wv * 384 + l48 * 8);
        const int ab = fresh(L_ACT + wv * 384 + 16 * G);
        const u32 dst = fresh(((G & (half ? 3 : 0)) ? OOB : gyw) + GY_INST + gy_rep((n & 1) * 4 + G) + gy_chunk(4 * j + wv * 2 + (n >> 3)) +
                              g * 256 + (n & 6) * 2);
        if (!half) wait_until(tref + 24 * T_H);
#define HPOLL                                                      \
  "buffer_load_dwordx4 v[32:35], %[off], %[rq], 0 offen sc1\n\t" \
  "buffer_load_dwordx4 v[36:39], %[off], %[rq], 0 offen offset:768 sc1\n\t"
        u32x4 p0, p1;
        asm volatile(WAIT_RT HPOLL "v_mov_b32 v41, 0\n\ts_mov_b32 s98, 0x400000\n"
                     "4:\n\tds_read_b32 v40, v41 offset:%[cn]\n\ts_sub_u32 s98, s98, 1\n\ts_waitcnt lgkmcnt(0)\n\ts_cbranch_scc1 5f\n\t"
                     "v_cmp_eq_u32 vcc, 8, v40\n\ts_cbranch_vccz 4b\n5:"
                     : "=&{v[32:35]}"(p0), "=&{v[36:39]}"(p1)
                     : [off] "v"(off), [rq] "s"(rq), [tt] "s"(anc + (half ? A_H : X_H)), [cn] "n"(L_CNT + 4)
                     : "memory", "scc", "vcc", "s98", "s99", "v40", "v41");
        u32x4 wf[6];
#pragma unroll
        for (int k = 0; k < 6; ++k) wf[k] = wfrag(smem, l0, 2048 * k);
        u32 t, bud;
        asm volatile("s_mov_b32 %[bud], 0x10000\n\ts_branch 2f\n"
                     "1:\n\ts_sub_u32 %[bud], %[bud], 1\n\ts_cbranch_scc1 3f\n\ts_waitcnt lgkmcnt(0)\n\ts_sleep 1\n\ts_mov_b64 exec, vcc\n\t"
                     HPOLL "s_mov_b64 exec, -1\n"
                     "2:\n\ts_waitcnt vmcnt(0)\n\t"
                     "v_pk_mul_f32 v[40:41], v[32:33], v[36:37]\n\tv_pk_mul_f32 v[42:43], v[34:35], v[38:39]\n\t"
                     "v_cvt_pk_bf16_f32 v40, v40, v41\n\tv_cvt_pk_bf16_f32 v41, v42, v43\n\t"
                     "ds_write_b64 %[aw], v[40:41]\n\t"
                     "ds_read_b128 v[40:43], %[ab]\n\tds_read_b128 v[44:47], %[ab] offset:64\n\t"
                     "ds_read_b128 v[48:51], %[ab] offset:128\n\tds_read_b128 v[52:55], %[ab] offset:192\n\t"
                     "ds_read_b128 v[56:59], %[ab] offset:256\n\tds_read_b128 v[60:63], %[ab] offset:320\n\t"
                     AND3("v32", "v33", "v34") AND3("%[t]", "v35", "v36") AND3("%[t]", "v37", "v38") AND3("%[t]", "1", "v39")
                     "v_cmp_eq_u32 vcc, 0, %[t]\n\ts_cbranch_vccnz 1b\n"
                     "3:\n\ts_waitcnt lgkmcnt(5)\n\t" MF "v[32:35], v[40:43], %[w0], 0\n\t"
                     "s_waitcnt lgkmcnt(4)\n\t" MF "v[32:35], v[44:47], %[w1], v[32:35]\n\t"
                     "s_waitcnt lgkmcnt(3)\n\t" MF "v[32:35], v[48:51], %[w2], v[32:35]\n\t"
                     "s_waitcnt lgkmcnt(2)\n\t" MF "v[32:35], v[52:55], %[w3], v[32:35]\n\t"
                     "s_waitcnt lgkmcnt(1)\n\t" MF "v[32:35], v[56:59], %[w4], v[32:35]\n\t"
                     "s_waitcnt lgkmcnt(0)\n\t" MF "v[32:35], v[60:63], %[w5], v[32:35]\n\ts_nop 7\n\t"
                     "v_mov_b32_dpp v36, v32 quad_perm:[0,0,2,2] row_mask:0xf bank_mask:0xf\n\t"
                     "v_mov_b32_dpp v37, v32 quad_perm:[1,1,3,3] row_mask:0xf bank_mask:0xf\n\t"
                     "v_cvt_pk_bf16_f32 v36, v36, v37\n\tv_or_b32 v36, 1, v36\n\t"
                     "buffer_store_dword v36, %[dst], %[rq], 0 offen sc1"
                     : [t] "=&v"(t), [bud] "=&s"(bud), "+{v[32:35]}"(p0), "+{v[36:39]}"(p1)
                     : [off] "v"(off), [aw] "v"(aw), [ab] "v"(ab), [dst] "v"(dst), [rq] "s"(rq), [w0] "v"(wf[0]), [w1] "v"(wf[1]),
                       [w2] "v"(wf[2]), [w3] "v"(wf[3]), [w4] "v"(wf[4]), [w5] "v"(wf[5])
                     : "memory", "vcc", "scc", FCLOB, DCLOB);
      }
    } else {
      const int lane = lane_id();
      const int n = lane & 15, G = lane >> 4;
      if (half == 1) {
        x_update<false>(smem, rq, lane, gy - GY_PAIR + GY_INST + gy_row(wave), wave);
      } else if (NINF + NPF + FEARLY < 5 && !sO) {
#pragma unroll 1
        for (int p = NINF + NPF + FEARLY; p < 5; ++p) {
          pace(tnext, TFL);
          dma_piece(ws0, F, smem, L_WB, wave * 5 + p, lane_id());
        }
      }
      G80 a;
      g80_a(a, lane, L_HB + 160 * wave, land);
      const u32 voff = fresh(gy + gy_row(wave) + ((lane * 16) ^ ((lane >> 4) * 64)));
      const int rw = fresh(L_RED + n * 144 + wave * 16 + G * 4);
      const int sw_ = fresh(n == 0 ? L_ST + wave * 16 + G * 4 : L_ACT);
      if (!half) wait_until(tref + 24 * T_YF);
      asm volatile("s_waitcnt vmcnt(0)" ::: "memory");
      const u32 ty = anc + (half ? A_Y : X_Y);
      const bool late = (int)(rtnow() - ty) >= 0;
      if (late) dma_poll<false>(rq, voff, landa, USED, USED);
      g80_w<512>(a, smem, lane, L_WB + wave * 5120);
      if (!late) {
        wait_rt(ty);
        dma_poll<false>(rq, voff, landa, USED, USED);
      }
      {
        u32 t, bud;
        asm volatile(G80_LOOP("s_nop 0\n\t")
                     "ds_write2st64_b32 %[rw], v40, v44 offset1:9\n\t" ZD("quad_perm:[2,3,0,1]", "0xf") "s_nop 1\n\t"
                     ZD("row_half_mirror", "0xf") "ds_write_b32 %[sw], v48"
                     : [t] "=&v"(t), [bud] "=&s"(bud)
                     : G80_IN(a), [nm] "s"(0x00ff00ff00ff00ffull), [rw] "v"(rw), [sw] "v"(sw_)
                     : "memory", "vcc", "scc", "s98", "s99", FCLOB, DCLOB);
      }
      const int ln = lane_id();
      const int rr = fresh(L_RED + (ln & 31) * 144 + (ln >> 5) * 64), rs_ = fresh(L_ST + 16 * (ln & 7));
      const u32 dst = fresh(gh + fslot + ln * 4);
      asm volatile("s_cmp_eq_u32 %[wv], 0\n\ts_waitcnt lgkmcnt(0)\n\ts_barrier\n\ts_cbranch_scc0 9f\n\t"
                   "ds_read_b128 v[40:43], %[rs]\n\tds_read_b128 v[44:47], %[rr]\n\tds_read_b128 v[48:51], %[rr] offset:16\n\t"
                   "ds_read_b128 v[52:55], %[rr] offset:32\n\tds_read_b128 v[56:59], %[rr] offset:48\n\t"
                   "s_bitcmp1_b32 %[j], 0\n\ts_mov_b32 s98, 0x10000\n\t"
                   "s_waitcnt lgkmcnt(4)\n\t"
                   "v_add_f32 v40, v40, v41\n\t" SD("v43", "quad_perm:[1,0,3,2]") "v_add_f32 v40, v40, v42\n\t"
                   "s_waitcnt lgkmcnt(2)\n\t" SD("v43", "quad_perm:[2,3,0,1]") SD("v40", "quad_perm:[1,0,3,2]")
                   PKA("v[44:45]", "v[48:49]") SD("v43", "row_half_mirror") SD("v40", "quad_perm:[2,3,0,1]")
                   "v_mul_f32 v43, 0x3acccccd, v43\n\tv_mul_f32 v41, -v43, v43\n\t" SD("v40", "row_half_mirror")
                   "v_fmac_f32 v41, 0x3acccccd, v40\n\t" RND("v41", "|v41|") "v_add_f32 v41, 0x3727c5ac, v41\n\t" RND("v41", "v41")
                   "v_sqrt_f32 v41, v41\n\t" PKA("v[46:47]", "v[50:51]") RND("v41", "v41") "v_rcp_f32 v41, v41\n\t"
                   "s_waitcnt lgkmcnt(0)\n\t"
                   PKA("v[52:53]", "v[56:57]") PKA("v[54:55]", "v[58:59]") PKA("v[44:45]", "v[52:53]") PKA("v[46:47]", "v[54:55]")
                   RND("v43", "v43") "v_add_f32 v44, v44, v45\n\tv_fma_f32 v46, -v43, v47, v46\n\tv_add_f32 v44, v44, v46\n\t"
                   "s_nop 1\n\tv_permlane32_swap_b32 v44, v45\n\tv_add_f32 v44, v44, v45\n\tv_mul_f32 v44, v44, v41\n\t"
                   "s_cbranch_scc1 8f\n\t" RND("v44", "v44")
                   "v_mul_f32 v45, 0xbfb8aa3b, v44\n\tv_exp_f32 v45, v45\n\ts_nop 0\n\tv_add_f32 v45, 1.0, v45\n\t"
                   "v_rcp_f32 v45, v45\n\ts_nop 0\n\tv_mul_f32 v44, v44, v45\n"
                   "8:\n\ts_mov_b32 exec_hi, 0\n\tv_cvt_pk_bf16_f32 v44, s98, v44\n\t"
                   "buffer_store_dword v44, %[dst], %[rq], 0 offen sc1\n\ts_mov_b32 exec_hi, -1\n"
                   "9:"
                   :
                   : [wv] "s"(wave), [j] "s"(j), [rs] "v"(rs_), [rr] "v"(rr), [dst] "v"(dst), [rq] "s"(rq)
                   : "memory", "scc", "s98", FCLOB, DCLOB);
      tref = now();
      if (half == 0 && wave == 1 && j == 20 && ln < 8) {
        const u32 st = rtnow();
        __builtin_amdgcn_raw_buffer_store_b32((((int)(st - anc) < (int)X_L ? anc + X_S : st) << 1) | 1u, rs, gy_rep(ln) + st_line(g), 0, 16);
      }
      if (half == 0) {
        CSLOAD
        x_update<true>(smem, rq, lane_id(), land, wave);
        if (wave == 7) {
          wait_until(tref + 24 * T_AN);
          stamp_poll(smem, rq, ar + st_line(ln & 7));
        }
      }
    }
    if (half == 1) break;
    {
      if (wave >= (isO ? 6 : 7)) {
        bar();
        anc = __builtin_amdgcn_readfirstlane(*(volatile LAS u32*)(smem + L_ANC));
      } else {
        const int lane = lane_id();
        const int n = lane & 15;
        G80 a;
        g80_a(a, lane, L_HB + 160 * c, land);
        const u32 voff = fresh((wave < 6 ? gy : OOB) + GY_INST + gy_row(c) + ((lane * 16) ^ ((lane >> 4) * 64)));
        const u32 gq = ar + A_GQ + hsub * 16384 + c * 896;
        const u32 dst = fresh((wave < 6 ? gq : OOB) + wave * 128 + (lane >> 5) * 64 + n * 4);
        const u32 dsz = fresh((wave == 5 ? gq : OOB) + 768 + (lane >> 4) * 4);
        const int csw = fresh(wave < 6 ? L_CS + (wave * 32 + (lane & 7) * 4 + ((lane >> 4) & 1) * 2 + (lane >> 5)) * 4 : L_PF);
        if (isO && wave == 4) {
          wait_until(tref + 24 * T_H);
          wait_rt(anc + X_H);
          stamp_poll(smem, rq, ar + st_line(lane & 7));
        }
        anc = lds_wait_nz((volatile LAS u32*)(smem + L_ANC));
        wait_rt(anc + (wave < 6 ? A_E : 0));
        dma_poll<false>(rq, voff, landa, USED, USED);
        asm volatile("s_waitcnt vmcnt(2)" ::: "memory");
        *reinterpret_cast<float*>(smem + csw) =
            tsum8(__builtin_bit_cast(f32x4, afrag(smem, L_OM + wave * OM_S * 4 + lane * 16))) * (-1.0f / D);
        g80_w<1024>(a, smem, lane, L_WQ + (wave >> 1) * 10240 + (wave & 1) * 512);
        const float cc = *reinterpret_cast<const float*>(smem + L_CS + wave * 128 + (lane >> 5) * 64 + n * 4);
        bar();
        u32 t, bud;
        asm volatile(G80_LOOP("s_mov_b32 vcc_lo, 0xffff\n\t")
                     "s_mov_b32 vcc_hi, 0xffff\n\t" ZD("quad_perm:[2,3,0,1]", "0xf")
                     "v_permlane32_swap_b32 v40, v44\n\tv_add_f32 v44, v40, v44\n\t"
                     ZD("row_half_mirror", "0xf") "v_permlane16_swap_b32 v40, v41\n\tv_readlane_b32 s98, v48, 48\n\t"
                     "v_add_f32 v44, v44, v41\n\ts_mov_b64 exec, vcc\n\t"
                     "v_fmac_f32 v44, s98, %[cc]\n\tv_or_b32 v44, 1, v44\n\t"
                     "buffer_store_dword v44, %[dst], %[rq], 0 offen sc1\n\t"
                     "s_mov_b32 exec_lo, 0x10001\n\ts_mov_b32 exec_hi, 0x10001\n\tv_or_b32 v48, 1, v48\n\t"
                     "buffer_store_dword v48, %[dsz], %[rq], 0 offen sc1\n\ts_mov_b64 exec, -1"
                     : [t] "=&v"(t), [bud] "=&s"(bud)
                     : G80_IN(a), [nm] "s"(wave < 6 ? 0x00ff00ff00ff00ffull : 0ull), [cc] "v"(cc), [dst] "v"(dst), [dsz] "v"(dsz)
                     : "memory", "vcc", "scc", "s98", "s99", FCLOB, DCLOB);
      }
    }
  }
  asm volatile(".Lmk_code_end:");
}

}

extern "C" void kernel_launch(void* const* d_in, const int* in_sizes, int n_in, void* d_out, int out_size, void* d_ws,
                              size_t ws_size, hipStream_t stream) {
  Params P;
  P.x0 = (const u16*)d_in[0];
  P.kc = (const u16*)d_in[1];
  P.vc = (const u16*)d_in[2];
  P.wq = (const u16*)d_in[3];
  P.wo = (const u16*)d_in[4];
  P.w1 = (const u16*)d_in[6];
  P.w3 = (const u16*)d_in[7];
  P.w2 = (const u16*)d_in[8];
  P.out = (u16*)d_out;
  P.ws = (u32*)d_ws;
  mega_kernel<<<NWG, NT, 0, stream>>>(P);
}
```

```cpp
#include <hip/hip_runtime.h>
#include <hip/hip_bf16.h>
#include <stdint.h>

namespace {

typedef unsigned int u32;
typedef unsigned short u16;
typedef unsigned long long u64;
using u32x2 = __attribute__((ext_vector_type(2))) unsigned;
using u32x4 = __attribute__((ext_vector_type(4))) unsigned;
using f32x2 = __attribute__((ext_vector_type(2))) float;
using f32x4 = __attribute__((ext_vector_type(4))) float;
using s16x4 = __attribute__((ext_vector_type(4))) short;
using bf16x2 = __attribute__((ext_vector_type(2))) __bf16;
using bf16x8 = __attribute__((ext_vector_type(8))) __bf16;
#define LAS __attribute__((address_space(3)))

constexpr int D = 640, H = 32, DH = 192, WIN = 4096, SK = 32768, F = 1536;
constexpr int NWG = 256, NT = 512;
constexpr u32 SPIN_CAP = 1u << 16;
constexpr float SCL = 0.07216878364870322f * 1.4426950408889634f;

#define T_O 12
#define T_YF 88
#define T_H 95
#define T_AN 28
#define PTHR 60
#define T_LAZY 60

#define A_E 100
#define A_Q (A_E + 78)
#define A_O (A_Q + 142)
#define A_Y (A_O + 87)
#define A_H (A_Y + 82)
#define A_F (A_H + 50)

#define X_O 150
#define X_Y (X_O + 93)
#define X_H (X_Y + 85)
#define X_L (X_H - 16)
#define X_E (X_H + 56)
#define X_S (X_E - A_E)

constexpr u32 SIDE = 8192, AREA = 17 * 16384;
constexpr u32 A_GO = 0;
constexpr u32 A_GY = 2 * 16384;
constexpr u32 A_GQ = 6 * 16384;
constexpr u32 A_GH = 16 * 16384;
constexpr u32 GO_SZ = 10 * 16384, GY_PAIR = GO_SZ, GY_INST = 32768;
constexpr u32 WS_PB = 4 * AREA;
constexpr u32 WS_GOF = WS_PB + (128 << 10), WS_HB = WS_GOF + 4096;
constexpr u32 WS_GQ1 = WS_HB + 256 * 128;
constexpr u32 WS_END = WS_GQ1 + 256 * 2048;
static_assert(WS_END <= (2u << 20), "layout");
constexpr u32 OOB = 1u << 30;
__device__ __forceinline__ u32 go_slot(int w) { return (u32)(w >> 2) * 16384u + (u32)(w & 3) * 2048u; }
__device__ __forceinline__ u32 gy_row(int i) { return go_slot(i); }
__device__ __forceinline__ u32 gy_chunk(int u) {
  const int i = (u * 205) >> 11;
  return gy_row(i) + (u32)(u - 10 * i) * 16u;
}
__device__ __forceinline__ u32 st_line(int g) { return A_GH + 3072u + (u32)g * 128u; }
__device__ __forceinline__ u32 gy_rep(int r) { return (u32)(r & 3) * AREA + (u32)(r >> 2) * SIDE; }

constexpr int L_ZERO = 0;
constexpr int L_CNT = 16;
constexpr int L_RI = 28;
constexpr int L_ONE = 32;
constexpr int L_TAG = 48;
constexpr int L_ST = 64;
constexpr int L_VOTE = L_ST;
constexpr int L_ANC = 192;
constexpr int L_Q = 256;
constexpr int L_CS = 640;
constexpr int L_HB = L_CS + 768;
constexpr int L_ACT = L_HB + D * 2;
constexpr int L_RED = L_ACT + 2 * 384;
constexpr int L_P = L_RED + 32 * 144;
constexpr int P_W = 1280, P_U = 592, P_PAR = 288;
constexpr int OM_S = 400, OM_P = 200;
constexpr int L_OM = L_P + 8 * P_W;
constexpr int L_LN = L_OM;
constexpr int L_LAND = (L_OM + 8 * OM_S * 4 + 255) / 256 * 256;
constexpr int L_CTL = L_LAND + 6 * 2048;
constexpr int L_WQ = L_LAND + 8 * 2048;
constexpr int L_WB = L_WQ + 6 * 5120;
constexpr int L_W2 = L_WB + 768 * 64;
constexpr int L_PF = L_W2 + 192 * 64;
constexpr int L_TAB = L_PF + 256;
constexpr int L_TOTAL = L_TAB + 8 * 2048;
static_assert(L_LAND % 256 == 0 && L_P % 16 == 0 && L_WQ % 16 == 0 && L_HB % 16 == 0 && L_RED % 16 == 0, "alignment");

struct Params {
  const u16* x0;
  const u16* kc;
  const u16* vc;
  const u16* wq;
  u32* ws;
  const u16* w1;
  const u16* w3;
  const u16* wo;
  const u16* w2;
  u16* out;
};

__device__ __forceinline__ u32 cvt2(float lo, float hi) {
  return __builtin_bit_cast(u32, __builtin_convertvector(f32x2{lo, hi}, bf16x2));
}
__device__ __forceinline__ float bflo(u32 w) { return __uint_as_float(w << 16); }
__device__ __forceinline__ float bfhi(u32 w) { return __uint_as_float(w & 0xffff0000u); }
__device__ __forceinline__ float bf16r(float x) { return bflo(cvt2(x, x)); }

template <int CTRL>
__device__ __forceinline__ float dpp(float x) {
  return __builtin_bit_cast(float, __builtin_amdgcn_mov_dpp(__builtin_bit_cast(int, x), CTRL, 0xf, 0xf, true));
}
constexpr int XOR1 = 0xB1, XOR2 = 0x4E, XOR7 = 0x141, ROR8 = 0x128;
__device__ __forceinline__ float fmx(float a, float b) {
  float r;
  asm("v_max_f32 %0, %1, %2" : "=v"(r) : "v"(a), "v"(b));
  return r;
}
__device__ __forceinline__ float xrow16_max(float x) {
  auto s = __builtin_amdgcn_permlane16_swap(__float_as_uint(x), __float_as_uint(x), false, false);
  x = fmx(__uint_as_float(s[0]), __uint_as_float(s[1]));
  auto t = __builtin_amdgcn_permlane32_swap(__float_as_uint(x), __float_as_uint(x), false, false);
  return fmx(__uint_as_float(t[0]), __uint_as_float(t[1]));
}
__device__ __forceinline__ float xrow16_sum(float x) {
  auto s = __builtin_amdgcn_permlane16_swap(__float_as_uint(x), __float_as_uint(x), false, false);
  x = __uint_as_float(s[0]) + __uint_as_float(s[1]);
  auto t = __builtin_amdgcn_permlane32_swap(__float_as_uint(x), __float_as_uint(x), false, false);
  return __uint_as_float(t[0]) + __uint_as_float(t[1]);
}
__device__ __forceinline__ float sum8(float v) {
  v += dpp<XOR1>(v);
  v += dpp<XOR2>(v);
  v += dpp<XOR7>(v);
  return v;
}
__device__ __forceinline__ float max8(float v) {
  asm("s_nop 1\n\tv_max_f32_dpp %0, %0, %0 quad_perm:[1,0,3,2] row_mask:0xf bank_mask:0xf\n\t"
      "s_nop 1\n\tv_max_f32_dpp %0, %0, %0 quad_perm:[2,3,0,1] row_mask:0xf bank_mask:0xf\n\t"
      "s_nop 1\n\tv_max_f32_dpp %0, %0, %0 row_half_mirror row_mask:0xf bank_mask:0xf"
      : "+v"(v));
  return v;
}
__device__ __forceinline__ float max_ror8(float v) {
  asm("s_nop 1\n\tv_max_f32_dpp %0, %0, %0 row_ror:8 row_mask:0xf bank_mask:0xf" : "+v"(v));
  return v;
}
__device__ __forceinline__ f32x4 sum4x4(f32x4 v) {
  float a = v[0], b = v[1], c = v[2], d = v[3];
#define S4(CTL)                                               \
  "v_add_f32_dpp %0, %0, %0 " CTL " row_mask:0xf bank_mask:0xf\n\t" \
  "v_add_f32_dpp %1, %1, %1 " CTL " row_mask:0xf bank_mask:0xf\n\t" \
  "v_add_f32_dpp %2, %2, %2 " CTL " row_mask:0xf bank_mask:0xf\n\t" \
  "v_add_f32_dpp %3, %3, %3 " CTL " row_mask:0xf bank_mask:0xf\n\t"
  asm("s_nop 1\n\t" S4("quad_perm:[1,0,3,2]") S4("quad_perm:[2,3,0,1]") : "+v"(a), "+v"(b), "+v"(c), "+v"(d));
#undef S4
  return f32x4{a, b, c, d};
}
__device__ __forceinline__ float wave_sum(float v) {
  v = sum8(v);
  v += dpp<ROR8>(v);
  return xrow16_sum(v);
}
__device__ __forceinline__ float tsum8(f32x4 a) {
  auto s0 = __builtin_amdgcn_permlane32_swap(__float_as_uint(a[0]), __float_as_uint(a[1]), false, false);
  auto s1 = __builtin_amdgcn_permlane32_swap(__float_as_uint(a[2]), __float_as_uint(a[3]), false, false);
  const float r0 = __uint_as_float(s0[0]) + __uint_as_float(s0[1]);
  const float r1 = __uint_as_float(s1[0]) + __uint_as_float(s1[1]);
  auto t = __builtin_amdgcn_permlane16_swap(__float_as_uint(r0), __float_as_uint(r1), false, false);
  float r = __uint_as_float(t[0]) + __uint_as_float(t[1]);
  r += dpp<ROR8>(r);
  return r;
}

__device__ __forceinline__ u32 tagv(float v) { return __float_as_uint(v) | 1u; }
__device__ __forceinline__ void store_quad(__amdgpu_buffer_rsrc_t rs, u32 off, f32x4 v) {
  const u32x4 s = {tagv(v[0]), tagv(v[1]), tagv(v[2]), tagv(v[3])};
  __builtin_amdgcn_raw_buffer_store_b128(s, rs, off, 0, 16);
}
template <int N>
__device__ __forceinline__ void gpoll4(u32x4 rs, const u32 (&off)[N], f32x4 (&v)[N]) {
  bool need = true;
  u32 budget = SPIN_CAP;
#pragma unroll
  for (int k = 0; k < N; ++k) asm volatile("" : "=v"(v[k]));
#pragma unroll 1
  for (;;) {
    if (need) {
#pragma unroll
      for (int k = 0; k < N; ++k)
        asm volatile("buffer_load_dwordx4 %0, %1, %2, 0 offen sc1" : "+v"(v[k]) : "v"(off[k]), "s"(rs) : "memory");
    }
    asm volatile("s_waitcnt vmcnt(0)" ::: "memory");
    u32 a = 1u;
#pragma unroll
    for (int k = 0; k < N; ++k) {
      asm volatile("" : "+v"(v[k]));
      a &= __float_as_uint(v[k][0]) & __float_as_uint(v[k][1]) & __float_as_uint(v[k][2]) & __float_as_uint(v[k][3]);
    }
    need = a == 0u;
    if (__builtin_amdgcn_ballot_w64(need) == 0ull || budget == 0u) break;
    --budget;
    __builtin_amdgcn_s_sleep(1);
  }
}
__device__ __forceinline__ u32 now() { return (u32)__builtin_amdgcn_s_memtime(); }
__device__ __forceinline__ void wait_until(u32 t) {
#pragma unroll 1
  while ((int)(now() - t) < -160) __builtin_amdgcn_s_sleep(1);
#pragma unroll 1
  while ((int)(now() - t) < 0) {
  }
}
__device__ __forceinline__ u32 rtnow() { return (u32)__builtin_amdgcn_s_memrealtime(); }
#define WAIT_RT                                                                                                       \
  "s_branch 1f\n0:\n\ts_cmp_lt_i32 s98, -8\n\ts_cbranch_scc0 1f\n\ts_sleep 1\n"                                        \
  "1:\n\ts_memrealtime s[98:99]\n\ts_waitcnt lgkmcnt(0)\n\ts_sub_i32 s98, s98, %[tt]\n\t"                              \
  "s_cmp_ge_u32 s98, 0xfffffc00\n\ts_cbranch_scc1 0b\n\t"
__device__ __forceinline__ void wait_rt(u32 t) { asm volatile(WAIT_RT ::[tt] "s"(t) : "scc", "s98", "s99", "memory"); }
template <int BITS>
__device__ __forceinline__ u32 youngest(u32 t);
__device__ __forceinline__ void lds_wait(volatile LAS u32* p, u32 want) {
  u32 spins = 0;
#pragma unroll 1
  while (__builtin_amdgcn_readfirstlane(*p) != want) {
    if (++spins > (SPIN_CAP << 6)) break;
    __builtin_amdgcn_s_sleep(1);
  }
  asm volatile("" ::: "memory");
}
__device__ __forceinline__ u32 lds_wait_nz(volatile LAS u32* p) {
  u32 spins = 0, v;
#pragma unroll 1
  while ((v = __builtin_amdgcn_readfirstlane(*p)) == 0u) {
    if (++spins > (SPIN_CAP << 6)) break;
    __builtin_amdgcn_s_sleep(1);
  }
  asm volatile("" ::: "memory");
  return v;
}
template <int BITS>
__device__ __forceinline__ u32 youngest(u32 t) {
  const u32 nw = rtnow();
  const float age = (float)((nw - t) & ((1u << BITS) - 1u));
  return nw - (u32)__builtin_amdgcn_readfirstlane((int)-xrow16_max(max_ror8(max8(-age))));
}
__device__ __forceinline__ void stamp_poll(unsigned char* smem, u32x4 rq, u32 so) {
  u32 sv, budget = SPIN_CAP;
#pragma unroll 1
  for (;;) {
    asm volatile("buffer_load_dword %0, %1, %2, 0 offen sc1\n\ts_waitcnt vmcnt(0)" : "=&v"(sv) : "v"(so), "s"(rq) : "memory");
    if (__builtin_amdgcn_ballot_w64((sv & 1u) == 0u) == 0ull || budget == 0u) break;
    --budget;
    __builtin_amdgcn_s_sleep(1);
  }
  *(volatile LAS u32*)(smem + L_ANC) = youngest<24>(sv >> 1) | 1u;
}
__device__ __forceinline__ void lds_add(unsigned char* smem, int off, u32 v) {
  __hip_atomic_fetch_add((LAS u32*)(smem + off), v, __ATOMIC_RELAXED, __HIP_MEMORY_SCOPE_WORKGROUP);
}

__device__ __forceinline__ int fresh(int t) {
  asm volatile("" : "+v"(t));
  return t;
}
__device__ __forceinline__ int lane_id() {
  int t;
  asm volatile("v_mbcnt_lo_u32_b32 %0, -1, 0\n\tv_mbcnt_hi_u32_b32 %0, -1, %0" : "=v"(t));
  return t;
}
__device__ __forceinline__ f32x4 zero4() {
  f32x2 a, b;
  asm volatile("v_mov_b64 %0, 0\n\tv_mov_b64 %1, 0" : "=v"(a), "=v"(b));
  return f32x4{a[0], a[1], b[0], b[1]};
}
__device__ __forceinline__ void bar() { asm volatile("s_waitcnt lgkmcnt(0)\n\ts_barrier" ::: "memory"); }

__device__ __forceinline__ u32x2 trread(unsigned char* smem, int off) {
  return __builtin_bit_cast(u32x2, __builtin_amdgcn_ds_read_tr16_b64_v4i16((LAS s16x4*)(smem + off)));
}
__device__ __forceinline__ u32x4 wfrag(unsigned char* smem, int lb, int off) {
  const u32x2 a = trread(smem, lb + off), b = trread(smem, lb + off + 256);
  return u32x4{a[0], a[1], b[0], b[1]};
}
template <int GS = 512>
__device__ __forceinline__ int wlane(int lane, int cb) {
  const int i = lane & 15, G = lane >> 4;
  return G * GS + (i >> 2) * 64 + (i & 3) * 8 + ((cb ^ (G & 1)) * 32);
}
__device__ __forceinline__ u32x4 afrag(unsigned char* smem, int a) { return *reinterpret_cast<const u32x4*>(smem + a); }
__device__ __forceinline__ f32x4 mfma32(u32x4 w, u32x4 a, f32x4 c) {
  return __builtin_amdgcn_mfma_f32_16x16x32_bf16(__builtin_bit_cast(bf16x8, w), __builtin_bit_cast(bf16x8, a), c, 0, 0, 0);
}

__device__ __forceinline__ void dma_kib(const u16* src, u32 voff, unsigned char* smem, int ldsoff) {
  const u32 dst = (u32)(uintptr_t)(LAS unsigned char*)smem + (u32)ldsoff;
  u32 keep;
  asm volatile("s_mov_b32 %0, m0\n\ts_mov_b32 m0, %2\n\ts_nop 2\n\tglobal_load_lds_dwordx4 %1, %3 nt\n\ts_mov_b32 m0, %0"
               : "=&s"(keep)
               : "v"(voff), "s"(dst), "s"(src)
               : "memory");
}
__device__ __forceinline__ void dma_piece(const u16* src, int ld, unsigned char* smem, int ldsoff, int p, int lane) {
  const int row = 16 * p + (lane >> 2), part = lane & 3;
  const int cb = (part >> 1) ^ (lane >> 5);
  dma_kib(src, (u32)(row * ld + cb * 16 + (part & 1) * 8) * 2u, smem, ldsoff + p * 1024);
}

template <int CTRL>
__device__ __forceinline__ u32 dppu(u32 x) {
  return (u32)__builtin_amdgcn_mov_dpp((int)x, CTRL, 0xf, 0xf, true);
}
__device__ __forceinline__ float sum4(f32x4 d) { return (d[0] + d[1]) + (d[2] + d[3]); }
__device__ __forceinline__ u32 and4(u32x4 v) { return v[0] & v[1] & v[2] & v[3]; }
__device__ __forceinline__ void store16(__amdgpu_buffer_rsrc_t rs, u32 off, u32x4 v) {
  __builtin_amdgcn_raw_buffer_store_b128(v, rs, off, 0, 16);
}
#define POLL16(dst, voff, imm) \
  asm volatile("buffer_load_dwordx4 %0, %1, %2, 0 offen offset:%3 sc1" : "+v"(dst) : "v"(voff), "s"(rq), "n"(imm) : "memory")

struct LnS {
  float mb, ri;
};
__device__ __forceinline__ LnS ln_stats(float s1, float s2) {
#define S2(CTL)                                                     \
  "v_add_f32_dpp %0, %0, %0 " CTL " row_mask:0xf bank_mask:0xf\n\t" \
  "v_add_f32_dpp %1, %1, %1 " CTL " row_mask:0xf bank_mask:0xf\n\ts_nop 0\n\t"
  asm("s_nop 1\n\t" S2("quad_perm:[1,0,3,2]") S2("quad_perm:[2,3,0,1]") S2("row_half_mirror") : "+v"(s1), "+v"(s2));
#undef S2
  const float dm = s1 * (1.0f / D);
  const u32 mv = cvt2(dm, __builtin_fmaxf(fmaf(-dm, dm, s2 * (1.0f / D)), 0.f));
  LnS r;
  r.mb = bflo(mv);
  r.ri = __builtin_amdgcn_rcpf(bf16r(__builtin_amdgcn_sqrtf(bf16r(bfhi(mv) + 1e-5f))));
  return r;
}

struct G80 {
  u32x4 w[6];
  int ra, rb, rc;
};
template <int GS>
__device__ __forceinline__ void g80_w(G80& a, unsigned char* smem, int lane, int wb) {
  const int l0 = wb + wlane<GS>(lane, 0), l1 = wb + wlane<GS>(lane, 1), bk = 8 * GS - (lane >> 5) * 2 * GS;
  a.w[0] = wfrag(smem, l0, 0);
  a.w[1] = wfrag(smem, l1, 0);
  a.w[2] = wfrag(smem, l0, 4 * GS);
  a.w[3] = wfrag(smem, l1, 4 * GS);
  a.w[4] = wfrag(smem, l0 + bk, 0);
  a.w[5] = wfrag(smem, l1 + bk, 0);
}
__device__ __forceinline__ void g80_a(G80& a, int lane, int xs, int land) {
  const int m = lane & 15, G = lane >> 4;
  const bool vec = m == 8 || (m < 8 && land >= 0);
  const int st = vec ? 64 : 0;
  const int sa = vec ? (m == 8 ? xs : land + 256 * m) + 16 * G : (m == 15 ? L_ONE : L_ZERO);
  const int sw = (vec && m < 8) ? (m & 3) * 64 : 0;
  a.ra = fresh(sa ^ sw);
  a.rb = fresh((sa + st) ^ sw);
  a.rc = fresh(G < 2 ? (sa + 2 * st) ^ sw : L_TAG);
}
template <bool WAIT = true>
__device__ __forceinline__ void dma_poll(u32x4 rq, u32 voff, u32 land, u64 ea, u64 eb) {
  u32 keep;
  asm volatile("s_mov_b32 %0, m0\n\ts_mov_b32 m0, %3\n\ts_mov_b64 exec, %4\n\t"
               "buffer_load_dwordx4 %1, %2, 0 offen sc1 lds\n\t"
               "s_mov_b64 exec, %5\n\t"
               "buffer_load_dwordx4 %1, %2, 0 offen offset:1024 sc1 lds\n\t"
               "s_mov_b64 exec, -1\n\ts_mov_b32 m0, %0"
               : "=&s"(keep)
               : "v"(voff), "s"(rq), "s"(land), "s"(ea), "s"(eb)
               : "memory");
  if (WAIT) asm volatile("s_waitcnt vmcnt(0)" ::: "memory");
}
template <bool X4>
__device__ __forceinline__ void ctl_load(u32x4 rq, u32 voff, u32 lds) {
  u32 keep;
  if (X4)
    asm volatile("s_mov_b32 %0, m0\n\ts_mov_b32 m0, %3\n\ts_nop 2\n\tbuffer_load_dwordx4 %1, %2, 0 offen sc1 lds\n\ts_mov_b32 m0, %0"
                 : "=&s"(keep) : "v"(voff), "s"(rq), "s"(lds) : "memory");
  else
    asm volatile("s_mov_b32 %0, m0\n\ts_mov_b32 m0, %3\n\ts_nop 2\n\tbuffer_load_dword %1, %2, 0 offen sc1 lds\n\ts_mov_b32 m0, %0"
                 : "=&s"(keep) : "v"(voff), "s"(rq), "s"(lds) : "memory");
}
template <int UN>
__device__ __forceinline__ void retry_masks(u64 need, int lane, u64& ea, u64& eb) {
  u32 m = (u32)need | (u32)(need >> 32);
  m |= m >> 16;
  m = (m | (m >> 8)) & 0xffu;
  const int ln = fresh(lane);
  ea = __builtin_amdgcn_ballot_w64(((m >> (ln / UN)) & 1u) != 0u);
  eb = __builtin_amdgcn_ballot_w64(((m >> ((ln + 64) / UN)) & 1u) != 0u);
}
#define MF "v_mfma_f32_16x16x32_bf16 "
#define F0 "v[52:55]"
#define F1 "v[56:59]"
#define F2 "v[60:63]"
#define FCLOB "v52", "v53", "v54", "v55", "v56", "v57", "v58", "v59", "v60", "v61", "v62", "v63"
#define AND3(a, b, c) "v_bitop3_b32 %[t], " a ", " b ", " c " bitop3:0x80\n\t"
#define BREP "s_bitreplicate_b64_b32 "
#define USEX "s_and_b32 exec_lo, exec_lo, 0x30ff03ff\n\ts_and_b32 exec_hi, exec_hi, 0xff30ff03\n\t"
constexpr u64 USED = 0xff30ff0330ff03ffull;
#define POLL_HEAD(FOLD, MASK)                                                                                        \
  "s_mov_b32 %[bud], 0x10000\n\ts_branch 2f\n"                                                                       \
  "1:\n\ts_sub_u32 %[bud], %[bud], 1\n\ts_cbranch_scc1 3f\n\t"                                                       \
  "s_or_b32 s98, vcc_lo, vcc_hi\n\ts_lshr_b32 s99, s98, 16\n\ts_or_b32 s98, s98, s99\n\t" FOLD                       \
  BREP "s[98:99], s98\n\t" BREP "s[98:99], s98\n\t" BREP "s[98:99], s98\n\t"                                         \
  "s_mov_b32 vcc_lo, m0\n\ts_mov_b32 m0, %[land]\n\t" BREP "exec, s98\n\t" MASK                                      \
  "buffer_load_dwordx4 %[vo], %[rq], 0 offen sc1 lds\n\t" BREP "exec, s99\n\t" MASK                                  \
  "buffer_load_dwordx4 %[vo], %[rq], 0 offen offset:1024 sc1 lds\n\t"                                                \
  "s_mov_b64 exec, -1\n\ts_mov_b32 m0, vcc_lo\n"                                                                     \
  "2:\n\ts_waitcnt vmcnt(0)\n\t"                                                                                     \
  "ds_read_b128 " F0 ", %[ra]\n\tds_read_b128 " F1 ", %[rb]\n\tds_read_b128 " F2 ", %[rc]\n\t"
#define R0 "v[40:43]"
#define R1 "v[44:47]"
#define R2 "v[48:51]"
#define DCLOB "v40", "v41", "v42", "v43", "v44", "v45", "v46", "v47", "v48", "v49", "v50", "v51"
#define ZD(CTL, BANK) "v_add_f32_dpp v48, v48, v48 " CTL " row_mask:0xf bank_mask:" BANK "\n\t"
#define G80_LOOP(FILL)                                                                                               \
  POLL_HEAD("", USEX)                                                                                                \
  "s_waitcnt lgkmcnt(2)\n\t"                                                                                         \
  MF R2 ", " F0 ", " F0 ", 0\n\t" AND3("v52", "v53", "v54")                                                          \
  MF R0 ", " F0 ", %[w0], 0\n\t" MF R1 ", " F0 ", %[w1], 0\n\t"                                                      \
  "s_waitcnt lgkmcnt(1)\n\t"                                                                                         \
  MF R2 ", " F1 ", " F1 ", " R2 "\n\t" AND3("%[t]", "v55", "v56")                                                    \
  MF R0 ", " F1 ", %[w2], " R0 "\n\t" AND3("%[t]", "v57", "v58")                                                     \
  MF R1 ", " F1 ", %[w3], " R1 "\n\t"                                                                                \
  "s_waitcnt lgkmcnt(0)\n\t"                                                                                         \
  MF R2 ", " F2 ", " F2 ", " R2 "\n\t" AND3("%[t]", "v59", "v60") AND3("%[t]", "v61", "v62")                         \
  MF R0 ", " F2 ", %[w4], " R0 "\n\t" AND3("%[t]", "1", "v63")                                                       \
  "v_cmp_eq_u32 vcc, 0, %[t]\n\t"                                                                                    \
  MF R1 ", " F2 ", %[w5], " R1 "\n\t"                                                                                \
  "s_and_b64 vcc, vcc, %[nm]\n\ts_cbranch_scc1 1b\n"                                                                 \
  "3:\n\tv_pk_add_f32 v[48:49], v[48:49], v[50:51]\n\tv_add_f32 v48, v48, v49\n\t" FILL                              \
  "v_pk_add_f32 v[40:41], v[40:41], v[42:43]\n\t" ZD("row_ror:8", "0x1") "v_add_f32 v40, v40, v41\n\t"               \
  "v_pk_add_f32 v[44:45], v[44:45], v[46:47]\n\t" ZD("quad_perm:[1,0,3,2]", "0xf") "v_add_f32 v44, v44, v45\n\t"
#define SD(R, CTL) "v_add_f32_dpp " R ", " R ", " R " " CTL " row_mask:0xf bank_mask:0xf\n\t"
#define PKA(A, B) "v_pk_add_f32 " A ", " A ", " B "\n\t"
#define RND(R, X) "v_cvt_pk_bf16_f32 " R ", 0, " X "\n\t"
#define G80_IN(A)                                                                                                    \
  [ra] "v"(A.ra), [rb] "v"(A.rb), [rc] "v"(A.rc), [w0] "v"(A.w[0]), [w1] "v"(A.w[1]), [w2] "v"(A.w[2]),              \
      [w3] "v"(A.w[3]), [w4] "v"(A.w[4]), [w5] "v"(A.w[5]), [vo] "v"(voff), [rq] "s"(rq), [land] "s"(landa)
__device__ __forceinline__ void g80_run(G80& a, unsigned char* smem, float& r0, float& r1) {
  const u32x4 f0 = afrag(smem, a.ra), f1 = afrag(smem, a.rb), f2 = afrag(smem, a.rc);
  f32x4 d0 = {0.f, 0.f, 0.f, 0.f}, d1 = d0;
  d0 = mfma32(f0, a.w[0], d0);
  d1 = mfma32(f0, a.w[1], d1);
  d0 = mfma32(f1, a.w[2], d0);
  d1 = mfma32(f1, a.w[3], d1);
  d0 = mfma32(f2, a.w[4], d0);
  d1 = mfma32(f2, a.w[5], d1);
  r0 = sum4(d0);
  r1 = sum4(d1);
}
__device__ __forceinline__ f32x4 quad4(float r) { return f32x4{r, dpp<0x55>(r), dpp<0xAA>(r), dpp<0xFF>(r)}; }

__device__ __forceinline__ int q_addr(int d) {
  const int w = d >> 1;
  return L_Q + ((w & 1) * 48 + (w >> 1)) * 4 + (d & 1) * 2;
}

template <bool LDS>
__device__ __forceinline__ void x_update(unsigned char* smem, u32x4 rq, int lane, u32 src, int i) {
  const int l = lane < 40 ? lane : 0;
  const u32 off = src + 4 * l;
  u32 v[8];
  if (LDS) {
#pragma unroll
    for (int k = 0; k < 8; ++k) v[k] = *reinterpret_cast<const u32*>(smem + (off ^ ((k & 3) * 64)) + 256 * k);
  } else {
    u32 budget = SPIN_CAP;
#pragma unroll 1
    for (;;) {
#pragma unroll
      for (int k = 0; k < 8; ++k)
        asm volatile("buffer_load_dword %0, %1, %2, 0 offen offset:%3 sc1" : "=v"(v[k]) : "v"(off), "s"(rq), "n"(256 * k) : "memory");
      asm volatile("s_waitcnt vmcnt(0)"
                   : "+v"(v[0]), "+v"(v[1]), "+v"(v[2]), "+v"(v[3]), "+v"(v[4]), "+v"(v[5]), "+v"(v[6]), "+v"(v[7])::"memory");
      const u32 a = v[0] & v[1] & v[2] & v[3] & v[4] & v[5] & v[6] & v[7] & 1u;
      if (__builtin_amdgcn_ballot_w64(a == 0u) == 0ull || budget == 0u) break;
      --budget;
      __builtin_amdgcn_s_sleep(2);
    }
  }
  float lo = 0.f, hi = 0.f;
#pragma unroll
  for (int k = 0; k < 8; ++k) {
    lo += bflo(v[k]);
    hi += bfhi(v[k]);
  }
  u32* px = reinterpret_cast<u32*>(smem + L_HB + 160 * i + 4 * l);
  const u32 xo = *px, y = cvt2(lo, hi);
  if (lane < 40) *px = cvt2(bflo(xo) + bflo(y), bfhi(xo) + bfhi(y));
}
template <int N>
__device__ __forceinline__ void col_poll(u32x4 rq, const u32 (&off)[N], float (&y)[N]) {
  u32x2 v[N];
  u32 budget = SPIN_CAP;
#pragma unroll 1
  for (;;) {
    u32 a = 1u;
#pragma unroll
    for (int k = 0; k < N; ++k)
      asm volatile("buffer_load_dwordx2 %0, %1, %2, 0 offen sc1" : "=v"(v[k]) : "v"(off[k]), "s"(rq) : "memory");
    asm volatile("s_waitcnt vmcnt(0)" ::: "memory");
#pragma unroll
    for (int k = 0; k < N; ++k) {
      asm volatile("" : "+v"(v[k]));
      a &= v[k][0] & v[k][1];
    }
    if (__builtin_amdgcn_ballot_w64(a == 0u) == 0ull || budget == 0u) break;
    --budget;
  }
#pragma unroll
  for (int k = 0; k < N; ++k) y[k] = bf16r(tsum8(f32x4{bflo(v[k][0]), bfhi(v[k][0]), bflo(v[k][1]), bfhi(v[k][1])}));
}

#define CKL 22
#define CKV 5
#define CW 6
#define CWAIT asm volatile("s_waitcnt vmcnt(%0)" ::"n"(CW) : "memory")
#define QACK 5
#define QP 11
#define BEAT0 (QP + QACK)
#define PRE 50
#define AL_R 3
#define AL_L 8
#define AL_A 11
#define AL_2 5
#define FR 3
#define FW 3
#define FGN 162
#define FGT 25
#define FGA 6
#define FGM 200
#define FGS 2
#define AL_OFF 8
#define E_QK 300
#define E_QN 2
#define WBIG 10
#define TSTEP 232
#define QI 7
#define NINO 4
#define NINF 0
#define PO0 8
#define POS 10
#define PF0 25
#define PFS 2
#define TFL 220
static_assert(AL_A + 15 >= 24 && QI <= PO0 && PO0 + (NINO - 1) * POS < 42 && PF0 + (NINF - 1) * PFS < 42, "pieces");
#define CAL 10
#define XTAB 12
#define NPRE 2
#define FEARLY 1
#define NPF NPRE
#define TPF 220
#define TPV 220
#define XA 15
#define XB 18
#define TS_O (TSTEP * (20 * (48 + NINO) + 12 * (48 + NINF)) / (32 * (48 + NINO)))
#define TS_F (TSTEP * (20 * (48 + NINO) + 12 * (48 + NINF)) / (32 * (48 + NINF)))
#define NP0 3
#define TQ0 660
#define TSQ 136
static_assert(CKL > BEAT0 && QP > QI && 23 - CKL + CKV >= CW && CKV + 1 + CW <= XB - CW && XB - CW <= XA, "window");
#define PACE_ASM                                                                                                     \
  "1:\n\ts_memrealtime s[98:99]\n\ts_waitcnt lgkmcnt(0)\n\ts_lshl_b32 s98, s98, 3\n\ts_sub_i32 s98, s98, %[tn]\n\t"  \
  "s_cmp_lt_i32 s98, 0\n\ts_cbranch_scc0 2f\n\ts_sleep 1\n\ts_branch 1b\n2:\n\ts_add_i32 %[tn], %[tn], %[ts]\n\t"
__device__ __forceinline__ void pace(u32& tnext, u32 tstep) {
  asm volatile(PACE_ASM : [tn] "+s"(tnext) : [ts] "s"(tstep) : "scc", "s98", "s99");
}
constexpr int kreg(int i) { return i % 3 < 2 ? 64 + 8 * (i / 3) + 4 * (i % 3) : 128 + 4 * (i / 3); }
constexpr int vreg(int i) { return 160 + 24 * (i / 6) + 8 * (i % 3) + 4 * ((i / 3) & 1); }
#define LOADW(reg, voff, sbase, imm)                                                                                \
  asm volatile(PACE_ASM "global_load_dwordx4 v[%[r0]:%[r1]], %[p], %[sb] offset:%[im] nt\n\ts_waitcnt vmcnt(%[w])"  \
               : [tn] "+s"(tnext)                                                                                   \
               : [r0] "n"(reg), [r1] "n"((reg) + 3), [p] "v"(voff), [sb] "s"(sbase), [im] "n"(imm), [ts] "s"(tstep), \
                 [w] "n"(WBIG - 1)                                                                                  \
               : "scc", "s98", "s99")

#define HS_WAIT(COND)                                                                                \
  _Pragma("unroll 1") for (;;) {                                                                     \
    gd = CTL[512 + lane0] - CTL[576 + lane0];                                                        \
    const u64 ch = __builtin_amdgcn_ballot_w64(gd != 0u) & 0x1ffull;                                 \
    if (ch > 0xffull || (COND) || tmo == 0u) break;                                                  \
    if ((tmo & 3u) == 0u && lane0 == 0)                                                              \
      __builtin_amdgcn_raw_buffer_store_b32(hb, rs, WS_HB + h * 128 + c * 4, 0, 16);                 \
    hb += 4u;                                                                                        \
    --tmo;                                                                                           \
    __builtin_amdgcn_s_sleep(3);                                                                     \
    ctl_load<false>(rq, hso, (u32)(smb + L_CTL + 2048));                                             \
    asm volatile("s_waitcnt vmcnt(0)" ::: "memory");                                                 \
  }

struct QKAddr {
  int qad[6], idxq, wa;
  f32x4 D0, D1;
};
struct PVAddr {
  int ra, idx, oa, sa, la, ma;
  u32 dst;
  f32x4 O0, O1, O2;
  u32x4 A0, A1, A2, A3;
};
__device__ __forceinline__ void att_table(unsigned char* smem, int lane, int wave, u32 go) {
  const int n = lane & 15, G = lane >> 4;
  const int seg = n & 1, pr = (n >> 1) & 1, tq = n >> 2, xy = G >> 1;
  const int qa = L_Q + (pr * 48 + seg * 8 + (G & 1) * 4) * 4;
  u32 t[16];
#pragma unroll
  for (int k = 0; k < 4; ++k) t[k] = tq == k ? qa + xy * 64 : L_ZERO;
#pragma unroll
  for (int k = 0; k < 2; ++k) t[4 + k] = tq == 2 * k + xy ? qa + 128 : L_ZERO;
  t[6] = 0x4444 + pr * 0xAAAA;
  t[7] = L_P + wave * P_W + ((n >> 1) & 1) * P_PAR + ((((G & 1) * 2 + (n >> 3)) * 4 + (n & 1) * 2 + (G >> 1)) * 16) + ((n >> 2) & 1) * 8;
  t[8] = L_P + wave * P_W + ((n >> 2) & 1) * P_U + (n >> 3) * P_PAR + G * 64;
  t[9] = (n & 3) * 0x5555;
  t[10] = L_OM + (wave * OM_S + (n >> 3) * OM_P + (n & 7) * 4 + (G & 1) * 32) * 4;
  t[11] = L_OM + (wave * OM_S + DH + G) * 4;
  const int src = wave < 6 ? lane >> 3 : lane & 7;
  t[12] = L_OM + (src * OM_S + (wave < 6 ? (wave >> 1) * 64 + (lane & 1) * 32 + (wave & 1) * 16 + (lane & 6) * 2 : DH)) * 4;
  t[13] = L_OM + (src * OM_S + DH + 4) * 4;
  t[14] = lane >= (wave < 6 ? 16 : (wave == 6 ? 2 : 0)) ? 0x8000u
          : go + (wave < 6 ? (wave >= 3 ? 2048 - 192 : 0) + wave * 64 + (lane & 7) * 8 + (lane >> 3) * 4 : lane * 2048 + 192);
  t[15] = wave < 6 ? q_addr(wave * 32 + (lane & 7) * 4 + (lane >> 4)) : (wave == 6 ? L_RI : L_ACT);
#pragma unroll
  for (int k = 0; k < 2; ++k)
    *reinterpret_cast<u32x4*>(smem + L_TAB + wave * 2048 + k * 1024 + lane * 16) =
        u32x4{t[8 * k] | (t[8 * k + 4] << 16), t[8 * k + 1] | (t[8 * k + 5] << 16), t[8 * k + 2] | (t[8 * k + 6] << 16),
              t[8 * k + 3] | (t[8 * k + 7] << 16)};
}
__device__ __forceinline__ void att_setup(QKAddr& r, PVAddr& v, unsigned char* smem, int lane, int wave, int& qw) {
  const u32x4 a = afrag(smem, L_TAB + wave * 2048 + lane * 16), b = afrag(smem, L_TAB + wave * 2048 + 1024 + lane * 16);
  r.D0 = zero4();
  r.D1 = zero4();
  qw = fresh(b[3] >> 16);
#pragma unroll
  for (int k = 0; k < 4; ++k) r.qad[k] = fresh(a[k] & 0xffffu);
  r.qad[4] = fresh(a[0] >> 16);
  r.qad[5] = fresh(a[1] >> 16);
  r.idxq = fresh(a[2] >> 16);
  r.wa = fresh(a[3] >> 16);
  v.ra = fresh(b[0] & 0xffffu);
  v.idx = fresh(b[1] & 0xffffu);
  v.oa = fresh(b[2] & 0xffffu);
  v.la = fresh(b[3] & 0xffffu);
  v.sa = fresh(b[0] >> 16);
  v.ma = fresh(b[1] >> 16);
  v.dst = fresh((int)b[2] >> 16);
  asm volatile("" : "+v"(r.D0), "+v"(r.D1));
}
#define SM "v_smfmac_f32_16x16x64_bf16 "
#define P_WRITE_READ(QA)                                                                                              \
  asm volatile("ds_write2_b32 %[wa], %[p], %[p] offset1:%[o1]\n\t"                                                    \
               "ds_read_b128 %[a0], %[ra]\n\tds_read_b128 %[a1], %[ra] offset:16\n\t"                                 \
               "ds_read_b128 %[a2], %[ra] offset:32\n\tds_read_b128 %[a3], %[ra] offset:48"                           \
               : [a0] "=&v"(pv.A0), [a1] "=&v"(pv.A1), [a2] "=&v"(pv.A2), [a3] "=&v"(pv.A3), "+v"(sk)                 \
               : [wa] "v"(QA.wa + smb), [p] "v"(pb), [ra] "v"(pv.ra + smb), [o1] "n"(P_U / 4 + 1)        \
               : "memory");
#define QS(D, A, K) SM "v[" #D ":" #D "+3], v[" #A ":" #A "+3], v[" #K ":" #K "+7], %[ix]\n\t"
#define RDS "ds_read_b32 %[s], %[z] offset:%[ri]\n\t"
#define LW(N) "s_waitcnt lgkmcnt(" #N ")\n\t"
#define QK_BODY                                                                                                       \
  "ds_read_b128 v[40:43], %[q0]\n\tds_read_b128 v[44:47], %[q1]\n\tds_read_b128 v[48:51], %[q2]\n\t"                   \
  "ds_read_b128 v[52:55], %[q3]\n\tds_read_b128 v[56:59], %[q4]\n\tds_read_b128 v[60:63], %[q5]\n\t"                   \
  LW(5) QS(32, 40, 64) QS(36, 40, 96) LW(4) QS(32, 44, 72) QS(36, 44, 104) LW(3) QS(32, 48, 80) QS(36, 48, 112)       \
  LW(2) QS(32, 52, 88) QS(36, 52, 120) LW(0) QS(32, 56, 128) QS(36, 56, 144) RDS QS(32, 60, 136) QS(36, 60, 152)
#define QK_IN(QA) [q0] "v"(QA.qad[0]), [q1] "v"(QA.qad[1]), [q2] "v"(QA.qad[2]), [q3] "v"(QA.qad[3]), [q4] "v"(QA.qad[4]), [q5] "v"(QA.qad[5]),
#define UNSAFE(X) ((__builtin_amdgcn_ballot_w64(!(X <= 24.f)) != 0ull) | (__builtin_amdgcn_ballot_w64(X >= -24.f) == 0ull))
#define QK_SOFTMAX(QA)                                                                                                \
  {                                                                                                                   \
    float scl, raw, sk;                                                                                               \
    u32 pb;                                                                                                           \
                                                                      \
    asm volatile(QK_BODY                                                                                              \
                 "v_mov_b64 v[52:53], 0\n\tv_mov_b64 v[54:55], 0\n\tv_mov_b64 v[56:57], 0\n\tv_mov_b64 v[58:59], 0\n\t" \
                 "v_mov_b64 v[60:61], 0\n\tv_mov_b64 v[62:63], 0\n\ts_waitcnt lgkmcnt(0)\n\t"                         \
                 PKA("v[32:33]", "v[34:35]") PKA("v[36:37]", "v[38:39]") "s_nop 0\n\t"                                \
                 "v_add_f32_dpp v32, v33, v32 quad_perm:[1,0,3,2] row_mask:0xf bank_mask:0xf\n\t"                     \
                 "v_add_f32_dpp v36, v36, v37 quad_perm:[1,0,3,2] row_mask:0xf bank_mask:0xf\n\t"                     \
                 "v_cndmask_b32 %[raw], v32, v36, %[od]\n\tv_mul_f32 %[sk], %[s], %[raw]\n\tv_exp_f32 %[p], %[sk]\n\t" \
                 "s_nop 0\n\tv_cvt_pk_bf16_f32 %[pb], %[p], 0"                                                        \
                 : "+{v[32:35]}"(QA.D0), "+{v[36:39]}"(QA.D1), [s] "=&v"(scl), [raw] "=&v"(raw), [sk] "=&v"(sk),      \
                   [p] "=&v"(pend), [pb] "=&v"(pb), "=&{v[52:55]}"(pv.O0), "=&{v[56:59]}"(pv.O1), "=&{v[60:63]}"(pv.O2) \
                 : QK_IN(QA) [ix] "v"(QA.idxq), [z] "v"(smb), [ri] "n"(L_RI),                                        \
                   [od] "s"(0xaaaaaaaaaaaaaaaaull), "{v[64:95]}"(kv0), "{v[96:127]}"(kv1), "{v[128:159]}"(kv2)        \
                 : "memory", DCLOB);                                                                                  \
    pmax = 0.f;                                                                                                       \
    P_WRITE_READ(QA)                                                                                                  \
    if (__builtin_expect(UNSAFE(sk) | (__builtin_amdgcn_ballot_w64(__float_as_uint(scl) == 0u) != 0ull), 0)) {        \
      sk = raw * __uint_as_float(lds_wait_nz((volatile LAS u32*)(smem + L_RI)));                                      \
      if (UNSAFE(sk)) pmax = xrow16_max(max_ror8(max8(sk)));                                                          \
      pend = __builtin_amdgcn_exp2f(sk - pmax);                                                                       \
      pb = cvt2(pend, 0.f);                                                                                           \
      asm volatile("s_waitcnt lgkmcnt(0)" : "+v"(pv.A0), "+v"(pv.A1), "+v"(pv.A2), "+v"(pv.A3));                      \
      P_WRITE_READ(QA)                                                                                                \
      asm volatile("s_waitcnt lgkmcnt(0)" : "+v"(pv.A0), "+v"(pv.A1), "+v"(pv.A2), "+v"(pv.A3));                      \
    }                                                                                                                 \
  }

using u32x16 = __attribute__((ext_vector_type(16))) unsigned;
using u32x32 = __attribute__((ext_vector_type(32))) unsigned;
template <int OFF>
__device__ __forceinline__ u16* karg(u64 kp) {
  u64 p;
  asm volatile("s_load_dwordx2 %0, %1, %2\n\ts_waitcnt lgkmcnt(0)" : "=s"(p) : "s"(kp), "n"(OFF));
  return (u16*)p;
}
template <class T>
__device__ __forceinline__ T* mkptr(u32 lo, u32 hi) {
  return (T*)(((u64)hi << 32) | lo);
}

template <int I>
struct IC {
  static constexpr int value = I;
};
template <int A, int B, class Fn>
__device__ __forceinline__ void sfor(Fn&& f) {
  if constexpr (A < B) {
    f(IC<A>{});
    sfor<A + 1, B>(f);
  }
}

__global__ __launch_bounds__(NT, 2) void mega_kernel(Params) {
  __shared__ __attribute__((aligned(16))) unsigned char smem[L_TOTAL];
  u64 kp, ts0;
  u32x16 ka;
  int b;
  u32 wqo, wql;
#define WQL "global_load_lds_dwordx4 %[wqo], s[10:11] nt\n\ts_addk_i32 m0, 0x800\n\tv_add_u32 %[wqo], s22, %[wqo]\n\t"
  asm volatile(
               "s_load_dwordx16 s[4:19], s[0:1], 0x0\n\t"
               "s_getpc_b64 s[20:21]\n.Lmk_pc:\n\t"
               "s_mov_b32 m0, %[dst]\n\t"
               "s_bfe_u32 s3, s2, 0x30003\n\ts_lshl_b32 s3, s3, 7\n\tv_lshl_add_u32 v1, v0, 10, s3\n\t"
               "v_min_u32 v1, .Lmk_code_end-.Lmk_pc-4, v1\n\t"
               "global_load_lds_dword v1, s[20:21]\n\t"
               "v_readfirstlane_b32 s3, v0\n\tv_bfe_u32 v1, v0, 2, 3\n\tv_lshrrev_b32 v2, 3, v0\n\tv_and_or_b32 v1, v2, 8, v1\n\t"
               "s_lshr_b32 s20, s2, 5\n\ts_mulk_i32 s20, 80\n\tv_add_u32 v1, s20, v1\n\tv_mul_u32_u24 v1, %[row], v1\n\t"
               "v_lshlrev_b32 v2, 4, v0\n\tv_and_b32 v2, 48, v2\n\tv_lshrrev_b32 v3, 1, v0\n\tv_and_b32 v3, 32, v3\n\t"
               "v_xor_b32 v2, v2, v3\n\tv_lshlrev_b32 v3, 1, v0\n\tv_and_or_b32 v2, v3, 64, v2\n\t"
               "s_and_b32 s20, s2, 7\n\ts_bfe_u32 s21, s2, 0x20003\n\ts_lshl2_add_u32 s23, s20, s21\n\t"
               "s_lshr_b32 s21, s3, 7\n\ts_mul_i32 s20, s23, 3\n\ts_add_u32 s20, s20, s21\n\ts_lshl_b32 s20, s20, 7\n\t"
               "v_add3_u32 %[wqo], v1, v2, s20\n\t"
               "s_mulk_i32 s21, 10240\n\ts_and_b32 s20, s3, 64\n\ts_lshl4_add_u32 s20, s20, s21\n\ts_add_u32 m0, s20, %[lwq]\n\t"
               "s_mov_b32 s22, %[pst]\n\ts_cmpk_lt_u32 s3, 0x180\n\t"
               "s_waitcnt lgkmcnt(0)\n\ts_memrealtime %[ts]\n\ts_cbranch_scc0 1f\n\t"
               ".rept %[np]\n\t" WQL ".endr\n\t"
               "s_mov_b32 %[wql], m0\n\ts_branch 3f\n"
               "1:\n\tv_and_b32 v1, 63, v0\n\ts_movk_i32 m0, %[lx]\n\tv_lshlrev_b32 v2, 4, v1\n\tv_lshlrev_b32 v1, 2, v1\n\t"
               "s_bitcmp1_b32 s3, 6\n\ts_cbranch_scc1 2f\n\t"
               "global_load_lds_dwordx4 v2, s[4:5]\n\tglobal_load_lds_dword v1, s[4:5] offset:1024\n"
               "2:\n\ts_cselect_b32 s22, 0, 0x200\n\ts_add_u32 m0, s22, %[lc]\n\t"
               "s_add_u32 s20, s12, 0x1fffff\n\ts_addc_u32 s21, s13, 0\n\ts_and_b32 s20, s20, 0xffe00000\n\ts_or_b32 s20, s20, %[gof]\n\t"
               "v_cmp_gt_u32 vcc, 32, v1\n\ts_addk_i32 s23, 32\n\tv_lshl_add_u32 v1, s23, 7, v1\n\t"
               "s_and_b32 s22, s2, 7\n\ts_lshl_b32 s22, s22, 7\n\tv_mov_b32 v2, s22\n\tv_cndmask_b32 v1, v2, v1, vcc\n\t"
               "global_load_lds_dword v1, s[20:21] sc1\n"
               "3:\n\t"
               "v_lshrrev_b32 v1, 2, v0\n\tv_cmp_eq_u32 vcc, 3, v1\n\tv_mov_b32 v3, 0x3f803f80\n\ts_nop 0\n\t"
               "v_cndmask_b32 v2, 0, 1, vcc\n\tv_cmp_eq_u32 vcc, 2, v1\n\tv_lshlrev_b32 v1, 2, v0\n\ts_nop 0\n\t"
               "v_cndmask_b32 v2, v2, v3, vcc\n\tv_cmp_eq_u32 vcc, 7, v0\n\tv_mov_b32 v3, %[scl]\n\t"
               "v_min_u32 v1, 80, v1\n\tv_cndmask_b32 v2, v2, v3, vcc\n\t"
               "ds_write_b32 v1, v2 offset:%[sm]\n\t"
               "s_waitcnt lgkmcnt(0)\n\ts_barrier"
               : "={s[0:1]}"(kp), "={s2}"(b), "={s[4:19]}"(ka), [wqo] "=&v"(wqo), [wql] "=&s"(wql), [ts] "=&s"(ts0)
               : "{v0}"(threadIdx.x), [dst] "n"(L_PF), [scl] "n"(__builtin_bit_cast(u32, SCL)), [sm] "n"(0), [row] "n"(H * DH * 2),
                 [lwq] "n"(L_WQ), [pst] "n"(16 * H * DH * 2), [np] "n"(NP0), [lx] "n"(L_HB), [lc] "n"(L_CTL + 2304),
                 [gof] "n"(WS_GOF)
               : "memory", "vcc", "scc", "v1", "v2", "v3", "s3", "s20", "s21", "s22", "s23");
  static_assert(WS_HB == WS_GOF + 32 * 128, "layout");
  const u32 tstart = (u32)ts0;
  const int tid = threadIdx.x, lane0 = tid & 63;
  const int wave = __builtin_amdgcn_readfirstlane(tid >> 6);
  const int g = b & 7, j = b >> 3, hsub = j & 3, c = j >> 2, h = g * 4 + hsub;
  const bool isO = j < 20;
  const bool leader = b == 0 && wave == 6;
  const u32 fslot = (u32)((j & 1) * 768 + ((j - 20) >> 1) * 128);
  u32 tmo = SPIN_CAP;
  volatile LAS u32* cnt = (volatile LAS u32*)(smem + L_CNT);
  Params P;
  P.x0 = mkptr<const u16>(ka[0], ka[1]);
  P.kc = mkptr<const u16>(ka[2], ka[3]);
  P.vc = mkptr<const u16>(ka[4], ka[5]);
  P.wq = mkptr<const u16>(ka[6], ka[7]);
  P.ws = mkptr<u32>(ka[8], ka[9]);
  P.w1 = mkptr<const u16>(ka[10], ka[11]);
  P.w3 = mkptr<const u16>(ka[12], ka[13]);
  P.wo = mkptr<const u16>(ka[14], ka[15]);
  u32* const wsb = (u32*)(((uintptr_t)P.ws + 0x1fffffu) & ~(uintptr_t)0x1fffffu);
  const __amdgpu_buffer_rsrc_t rs = __builtin_amdgcn_make_buffer_rsrc(wsb, 0, (int)WS_END, 0x00020000);
  const u32x4 rq = {(u32)(uintptr_t)wsb, (u32)((uintptr_t)wsb >> 32) & 0xffffu, WS_END, 0x00020000u};

  u32 anc = 0u, tsh = 0u;
  u32 tref = 0u, ar = 0u;
  u32x32 kv0, kv1, kv2, kv3, kv4, kv5;
  asm volatile(""
               : "={v[64:95]}"(kv0), "={v[96:127]}"(kv1), "={v[128:159]}"(kv2), "={v[160:191]}"(kv3), "={v[192:223]}"(kv4),
                 "={v[224:255]}"(kv5));
  const int smb = (int)(uintptr_t)(LAS unsigned char*)smem;
  const u32 gop = A_GO + go_slot(hsub * 2) + c * 256;
  u32 tnext = tstart * 8u + (u32)wave * (TSTEP / 8) + TQ0;
  const size_t rowbase = (size_t)h * SK + (SK - WIN) + (wave * 8 + c) * 8;
  const u16* vb = P.vc + rowbase * DH;
  const u16* ws0 = isO ? P.wo + (size_t)(768 * g) * D + 32 * j : ((j & 1) ? P.w3 : P.w1) + 192 * g + 32 * ((j - 20) >> 1);
  const u16* w2p;
  const bool sO = isO || ((u32)(uintptr_t)ws0 & 0x380u) == 0x180u;
  u32 tstep = sO ? TS_O : TS_F;
  int pn = isO ? wave * 6 : wave * 5;
  {
    volatile LAS u32* const CTL = (volatile LAS u32*)(smem + L_CTL);
    u32 gd, hb = 0u;
    const u32 lho = WS_HB + (lane0 >> 1) * 128 + (lane0 & 1) * 16;
    const u32 hso = lane0 < 8 ? WS_HB + (u32)h * 128 + lane0 * 4 : WS_GOF + g * 128;
    if (wave >= 6) {
      if (b < 8 && wave == 7 && lane0 < 32)
        __builtin_amdgcn_raw_buffer_store_b32(tstart, rs, WS_GOF + 2048 + b * 128 + lane0 * 4, 0, 16);
      if (leader) {
        ctl_load<true>(rq, lho, (u32)(smb + L_CTL));
        ctl_load<false>(rq, WS_GOF + (lane0 & 7) * 128, (u32)(smb + L_CTL + 2560));
      }
    }
    if (tid == 0) *(volatile LAS u32*)(smem + L_ANC) = 0u;
    *reinterpret_cast<u32x4*>(smem + L_P + wave * P_W + lane0 * 16) = u32x4{0u, 0u, 0u, 0u};
    *reinterpret_cast<u32x4*>(smem + (lane0 < 16 ? L_P + wave * P_W + 1024 + lane0 * 16 : L_OM + (wave * OM_S + OM_P + DH) * 4)) =
        u32x4{0u, 0u, 0u, 0u};
    const u16* kb = P.kc + rowbase * DH;
    const u32 ko = ((lane0 & 15) >> 1) * 384 + 16 * ((lane0 & 1) * 4 + (lane0 >> 4)), vo = (lane0 >> 3) * 384 + 16 * (lane0 & 7);
    const u32 pre = wave < 6 ? PRE : 0;
    tstep -= pre;
    asm volatile("" ::"s"(kb + (h & 7) * 512 * DH), "s"(kb), "s"(vb), "s"(ws0), "v"(ko), "v"(vo), "s"(tnext), "s"(tstep), "s"(pn) : "memory");
    if (wave < 6) {
      u32 tq = tnext - (5 - NP0) * TSQ;
#pragma unroll 1
      for (int p = NP0; p < 5; ++p) {
        pace(tq, TSQ);
        dma_kib(P.wq, wqo, smem, wql - smb);
        wqo += 16 * H * DH * 2;
        wql += 2048;
      }
    } else {
      u32 rtt;
      {
        u32 pa = 0u, pb = 0u;
        u64 t1, t2;
        const u32 pbo = WS_PB + (u32)(wave & 1) * (SIDE + 128u) + (u32)((b >> 3) & 7) * 16384u + (u32)((b & 7) * 4 + (b >> 6)) * 256u;
        asm volatile("s_load_dwordx2 %2, %7, 0x40\n\ts_waitcnt vmcnt(0) lgkmcnt(0)\n\t.p2align 6\n\ts_mov_b64 exec, 1\n\t"
                     "buffer_load_dword %0, off, %4, %5 sc1 nt\n\tbuffer_load_dword %1, off, %4, %6 sc1 nt\n\t"
                     "s_mov_b64 exec, -1\n\t"
                     "s_waitcnt vmcnt(1)\n\ts_memtime %2\n\ts_waitcnt vmcnt(0)\n\ts_memtime %3\n\ts_waitcnt lgkmcnt(0)"
                     : "+v"(pa), "+v"(pb), "=&s"(t1), "=&s"(t2)
                     : "s"(rq), "s"(pbo), "s"(pbo ^ SIDE), "s"(kp)
                     : "memory");
        rtt = __builtin_amdgcn_readfirstlane((u32)t2 - (u32)t1) + 1u;
      }
      if (wave == 6) {
        *(volatile LAS u32*)(smem + L_VOTE) = rtt;
      } else {
        const u32 ca = gy_rep(g) + fresh(lane0) * 16;
        store16(rs, ca + (u32)(j >> 3) * 16384u + (u32)(j & 7) * 1024u, u32x4{0u, 0u, 0u, 0u});
        if (lane0 < 8) store16(rs, ca + A_GH + (u32)j * 128u, u32x4{0u, 0u, 0u, 0u});
        hb = rtt;
      }
    }
#define WPO(n) ((n) >= PO0 && ((n) - PO0) % POS == 0 && ((n) - PO0) / POS < NINO)
#define WPF(n) ((n) >= PF0 && ((n) - PF0) % PFS == 0 && ((n) - PF0) / PFS < NINF)
#define WPIECE(n)                                                                                                   \
  if ((WPO(n) && sO) || (WPF(n) && !sO)) {                                                                          \
    pace(tnext, tstep);                                                                                             \
    if ((n) != PO0 || isO) dma_piece(ws0, isO ? D : F, smem, L_WB, pn++, lane0);                             \
  }
#define PAUSE                                                                                \
  {                                                                                          \
    const u32 x = __builtin_amdgcn_readfirstlane(*(volatile LAS u32*)(smem + L_VOTE + 16));  \
    tnext += x - tsh;                                                                        \
    tsh = x;                                                                                 \
  }
    sfor<0, 24>([&](auto ic) __attribute__((always_inline)) {
      constexpr int i = ic.value;
      LOADW(kreg(i), ko, kb + ((i / 3 + h) & 7) * 512 * DH, 128 * (i % 3));
      if (i == QI) {
        tstep += pre;
        tnext += (QI + 1) * pre;
      }
      WPIECE(i)
      if (i == XTAB) att_table(smem, fresh(lane0), wave, gop);
      if (i == XTAB + 1) w2p = karg<64>(kp) + (size_t)(192 * g) * D + 32 * j;
      if (i == 1 && wave == 6) {
        const u32x4 lv = afrag(smem, L_HB + lane0 * 16);
        const u32 xb = *reinterpret_cast<const u32*>(smem + L_HB + 1024 + lane0 * 4);
        float v[10];
#pragma unroll
        for (int k = 0; k < 4; ++k) {
          v[2 * k] = bflo(lv[k]);
          v[2 * k + 1] = bfhi(lv[k]);
        }
        v[8] = bflo(xb);
        v[9] = bfhi(xb);
        float sm = 0.f;
#pragma unroll
        for (int k = 0; k < 10; ++k) sm += v[k];
        const float mean = wave_sum(sm) * (1.0f / D);
        float q = 0.f;
#pragma unroll
        for (int k = 0; k < 10; ++k) q = fmaf(v[k] - mean, v[k] - mean, q);
        const float var = wave_sum(q) * (1.0f / D);
        const float mb = bf16r(mean);
        const float ri = __builtin_amdgcn_rcpf(bf16r(__builtin_amdgcn_sqrtf(bf16r(bf16r(var) + 1e-5f))));
        u32 hp[5];
#pragma unroll
        for (int k = 0; k < 5; ++k) {
          const u32 cc = cvt2(v[2 * k] - mb, v[2 * k + 1] - mb);
          hp[k] = cvt2(bflo(cc) * ri, bfhi(cc) * ri);
        }
        *reinterpret_cast<u32x4*>(smem + L_LN + lane0 * 16) = u32x4{hp[0], hp[1], hp[2], hp[3]};
        *reinterpret_cast<u32*>(smem + L_LN + 1024 + lane0 * 4) = hp[4];
        asm volatile("s_waitcnt lgkmcnt(0)" ::: "memory");
        if (lane0 == 0) lds_add(smem, L_VOTE + 4, 1u);
      }
      if (wave == 6) {
        if (i == AL_R || i == AL_R + AL_2) {
          const int ln = fresh(lane0);
          ctl_load<false>(rq, WS_GOF + (ln & 7) * 128 + (i == AL_R ? 2048 : 3072 + (ln >> 3) * 4), (u32)(smb + L_CTL + 3072));
        }
        if (i == AL_L || i == AL_L + AL_2) {
          asm volatile("s_waitcnt vmcnt(%0)" ::"n"(AL_L - AL_R) : "memory");
          const u32 d = CTL[768 + lane0] - tstart + AL_OFF;
          const int sd = (int)sum8(d + 128u < 512u ? (float)(int)d : 0.f);
          if (i == AL_L) {
            *(volatile LAS int*)(smem + L_VOTE + 8) = sd >> 5;
          } else {
            const int s0 = __builtin_amdgcn_readlane(sd, 0);
            int p = __builtin_amdgcn_readlane(sd, 8) - s0;
            p = p > FGT * 8 ? p * FGA >> 2 : 0;
            p = p > FGM * 8 ? FGM * 8 : p;
            *(volatile LAS u32x2*)(smem + L_VOTE + 12) = u32x2{tstart + (u32)((s0 + p) >> 3) + (TQ0 + (48 + NINO) * TS_O) / 8 - 2, (u32)p};
          }
        }
      }
      if (i == FR && wave == 0 && b < 8) {
        asm volatile("s_waitcnt vmcnt(%0)" ::"n"(FW) : "memory");
        const u32 land = rtnow();
        if (lane0 < 32)
          __builtin_amdgcn_raw_buffer_store_b32((lane0 & 1) ? land - FGN : tstart, rs, WS_GOF + 3072 + b * 128 + lane0 * 4, 0, 16);
      }
      if (i == AL_A) tstep += __builtin_amdgcn_readfirstlane(*(volatile LAS u32*)(smem + L_VOTE + 8));
      if (i == QI - 1 && wave < 6) {
        asm volatile("s_waitcnt vmcnt(%0)" ::"n"(QI) : "memory");
        if (lane0 == 0) lds_add(smem, L_VOTE + 4, 1u);
      }
      if (i == QI && wave < 6) {
        lds_wait((volatile LAS u32*)(smem + L_VOTE + 4), 7u);
        G80 a;
        g80_w<1024>(a, smem, lane0, L_WQ + (wave >> 1) * 10240 + (wave & 1) * 512);
        g80_a(a, lane0, L_LN + 160 * c, -1);
        float r0, r1;
        g80_run(a, smem, r0, r1);
        auto sw = __builtin_amdgcn_permlane32_swap(__float_as_uint(r0), __float_as_uint(r1), false, false);
        const f32x4 qd = quad4(__uint_as_float(sw[1]));
        const int qp = wave * 128 + (lane0 >> 5) * 64 + (lane0 & 15) * 4;
        if ((lane0 & 19) == 0) *reinterpret_cast<u32x2*>(smem + L_RED + (qp >> 1)) = u32x2{cvt2(qd[0], qd[1]), cvt2(qd[2], qd[3])};
        if ((lane0 & 19) == 16) *reinterpret_cast<f32x4*>(smem + L_CS + qp) = qd;
        if (lane0 == 0) lds_add(smem, L_CNT + 4, 1u);
      }
      if (i > AL_L + AL_2 && (i - AL_L - AL_2) % FGS == 1) PAUSE
      if (wave == 7) {
        if (i == QP) {
          lds_wait(cnt + 1, 6u);
          const int ln = fresh(lane0);
          if (ln < 24)
            __builtin_amdgcn_raw_buffer_store_b128(afrag(smem, L_RED + ln * 16), rs, WS_GQ1 + (u32)(h * 8 + c) * 2048 + ln * 16, 0, 18);
        }
        if (i == BEAT0) {
          asm volatile("s_waitcnt vmcnt(%0)" ::"n"(BEAT0 - QP) : "memory");
          const int df = (int)(__builtin_amdgcn_readfirstlane(*(volatile LAS u32*)(smem + L_VOTE)) - hb);
          hb = df > PTHR ? 1u << (g >> 2) : (df < -PTHR ? 2u >> (g >> 2) : 0u);
          hb += (__builtin_amdgcn_readfirstlane(CTL[576 + c]) & ~3u) + 4u;
          if (lane0 == 0) __builtin_amdgcn_raw_buffer_store_b32(hb, rs, WS_HB + h * 128 + c * 4, 0, 16);
          hb += 4u;
        }
        if (i == CKL) ctl_load<false>(rq, hso, (u32)(smb + L_CTL + 2048));
      }
      if (i == CKL && leader) {
        ctl_load<true>(rq, lho, (u32)(smb + L_CTL + 1024));
      }
    });
#define VLOADS(A, B)                                                                           \
  _Pragma("unroll") for (int i = A; i < B; ++i) {                                              \
    if (i == XA - E_QN) tnext -= wave < 4 ? E_QK : 0;                                          \
    if (i == XB - E_QN) tnext -= wave < 4 ? 0 : E_QK;                                          \
    LOADW(vreg(i), vo, vb + ((i / 3 + h) & 7) * 512 * DH, 128 * (i % 3));                      \
    if (24 + i == AL_A + 15) {                                                                 \
                        \
      constexpr int RF = 47 - AL_A - 15, RO = RF + NINO - (AL_A + 15 - PO0 + POS - 1) / POS;    \
      tstep = sO ? TS_O : TS_F;                                                                \
      PAUSE                                                                                    \
      const int d = (int)(__builtin_amdgcn_readfirstlane(*(volatile LAS u32*)(smem + L_VOTE + 12)) * 8u +   \
                          (u32)wave * (TSTEP / 8) - tnext - (sO ? RO * TS_O : RF * TS_F));     \
      if ((u32)(d + 512) < 1024u) tstep += (d * (sO ? 65536 / RO : 65536 / RF) + 32768) >> 16; \
    }                                                                                          \
    WPIECE(24 + i)                                                                             \
  }
    VLOADS(0, CKV)
    if (wave == 7) {
      CWAIT;
      HS_WAIT((u32)ch == 0xffu)
      if (lane0 == 0) cnt[1] = 7u;
    }
    if (leader) {
      CWAIT;
      const u32x4 xa = afrag(smem, L_CTL + lane0 * 16);
      u32x4 lv;
#pragma unroll 1
      for (;;) {
        asm volatile("" ::: "memory");
        lv = afrag(smem, L_CTL + 1024 + lane0 * 16);
        const bool same = lv[0] == xa[0] || lv[1] == xa[1] || lv[2] == xa[2] || lv[3] == xa[3];
        if (__builtin_amdgcn_ballot_w64(same) == 0ull || tmo == 0u) break;
        --tmo;
        __builtin_amdgcn_s_sleep(4);
        ctl_load<true>(rq, lho, (u32)(smb + L_CTL + 1024));
        asm volatile("s_waitcnt vmcnt(0)" ::: "memory");
      }
      u32 nv = 256u;
#pragma unroll
      for (int e = 0; e < 4; ++e)
        nv += (u32)__builtin_popcountll(__builtin_amdgcn_ballot_w64((lv[e] & 2u) != 0u)) -
              (u32)__builtin_popcountll(__builtin_amdgcn_ballot_w64((lv[e] & 1u) != 0u));
      if (lane0 < 8) __builtin_amdgcn_raw_buffer_store_b32(CTL[640 + lane0] + (nv > 256u ? 2u : 1u), rs, WS_GOF + lane0 * 128, 0, 16);
    }
    VLOADS(CKV, CKV + 1)
    if (wave < 3) {
      const int ln = fresh(lane0);
      lds_wait(cnt + 1, 7u);
      ctl_load<true>(rq, WS_GQ1 + (u32)(h * 8 + (ln >> 3)) * 2048 + wave * 128 + (ln & 7) * 16, (u32)(smb + L_LAND + wave * 2048));
    }
    VLOADS(CKV + 1, CKV + 1 + CW)
    if (wave < 3) {
      const int ln = fresh(lane0);
      CWAIT;
      const u32x4 v = afrag(smem, L_LAND + wave * 2048 + ln * 16);
      const float t0 = tsum8(f32x4{bflo(v[0]), bflo(v[1]), bflo(v[2]), bflo(v[3])});
      const float t1 = tsum8(f32x4{bfhi(v[0]), bfhi(v[1]), bfhi(v[2]), bfhi(v[3])});
      *reinterpret_cast<u32*>(smem + q_addr(wave * 64 + (ln & 7) * 8 + ((ln >> 4) & 1) * 4 + (ln >> 5) * 2)) = cvt2(t0, t1);
      asm volatile("s_waitcnt lgkmcnt(0)" ::: "memory");
      if (lane0 == 0) lds_add(smem, L_CNT, 1u);
    }
    VLOADS(CKV + 1 + CW, XB - CW)
    if (wave == 7) ctl_load<false>(rq, hso, (u32)(smb + L_CTL + 2048));
    VLOADS(XB - CW, XA)
    if (wave >= 4) {
      VLOADS(XA, XB)
      if (wave == 7) {
        CWAIT;
        HS_WAIT(false)
        if (lane0 == 8) cnt[2] = (gd >> 1) | 2u;
      }
    }
  }

  asm volatile("s_branch 1f\n\t.p2align 7\n\t.rept %0\n\ts_nop 0\n\t.endr\n1:" ::"n"(CAL));
#pragma unroll 1
  for (int half = 0;; ++half) {
    {
      float pend, pmax;
      PVAddr pv;
      QKAddr qa;
      const int lane = lane_id();
      const u32 off = ar + A_GQ + hsub * 16384 + (wave < 6 ? (lane >> 3) * 896 + wave * 128 + (lane & 7) * 16 : (lane & 7) * 896 + 768);
      u32x4 qv;
      asm volatile("v_mov_b64 v[56:57], -1\n\tv_mov_b64 v[58:59], -1" : "={v[56:59]}"(qv));
      if (half == 1) {
        if (wave == 6) *(volatile LAS u32*)(smem + L_RI) = 0u;
        asm volatile("s_cmp_lt_u32 %[wv], 7\n\ts_cselect_b64 exec, -1, 0\n\t" WAIT_RT
                     "buffer_load_dwordx4 v[56:59], %[off], %[rq], 0 offen sc1\n\ts_mov_b64 exec, -1"
                     : "+{v[56:59]}"(qv)
                     : [off] "v"(off), [rq] "s"(rq), [wv] "s"(wave), [tt] "s"(anc + (wave < 7 ? A_Q : 0))
                     : "memory", "scc", "s98", "s99");
        if (wave < 4) __builtin_amdgcn_s_setprio(2);
      }
      int qw;
      att_setup(qa, pv, smem, lane, wave, qw);
      {
        u32 t, bud;
#define QRETRY(L) "s_sub_u32 %[bud], %[bud], 1\n\ts_cbranch_scc1 " L "\n\ts_sleep 1\n\tbuffer_load_dwordx4 v[56:59], %[off], %[rq], 0 offen sc1\n"
        asm volatile("s_mov_b32 %[bud], 0x10000\n\ts_cmp_eq_u32 %[hf], 0\n\ts_cbranch_scc1 0f\n\t"
                     "s_cmp_eq_u32 %[wv], 6\n\ts_cbranch_scc0 2f\n\ts_branch 5f\n"
                     "0:\n\ts_mov_b32 %[bud], 0x400000\n"
                     "0:\n\tds_read_b32 %[t], %[z] offset:%[cn]\n\ts_sub_u32 %[bud], %[bud], 1\n\ts_cbranch_scc1 8f\n\ts_waitcnt lgkmcnt(0)\n\t"
                     "v_cmp_eq_u32 vcc, 3, %[t]\n\ts_cbranch_vccnz 8f\n\ts_sleep 1\n\ts_branch 0b\n"
                     "4:\n\t" QRETRY("6f")
                     "5:\n\ts_waitcnt vmcnt(0)\n\t" AND3("v56", "v57", "v58") AND3("%[t]", "1", "v59")
                     "v_cmp_eq_u32 vcc, 0, %[t]\n\ts_cbranch_vccnz 4b\n"
                     "6:\n\tv_add_f32 v56, v56, v57\n\t" SD("v59", "quad_perm:[1,0,3,2]") "v_add_f32 v56, v56, v58\n\t"
                     "s_cmp_eq_u32 %[wv], 7\n\t" SD("v59", "quad_perm:[2,3,0,1]") SD("v56", "quad_perm:[1,0,3,2]")
                     "s_nop 0\n\t" SD("v59", "row_half_mirror") SD("v56", "quad_perm:[2,3,0,1]")
                     "v_mul_f32 v59, 0x3acccccd, v59\n\tv_mul_f32 v57, -v59, v59\n\t" SD("v56", "row_half_mirror")
                     "s_waitcnt lgkmcnt(0)\n\ts_barrier\n\ts_cbranch_scc1 8f\n"
                     "7:\n\t"
                     "v_fmac_f32 v57, 0x3acccccd, v56\n\t" RND("v57", "|v57|") "v_add_f32 v57, 0x3727c5ac, v57\n\t" RND("v57", "v57")
                     "v_sqrt_f32 v57, v57\n\ts_nop 0\n\t" RND("v57", "v57") "v_rcp_f32 v57, v57\n\ts_nop 0\n\t"
                     "v_mul_f32 v57, %[scl], v57\n\tv_or_b32 v57, 1, v57\n\tds_write_b32 %[qw], v57\n\t"
                     "s_cbranch_scc1 6b\n\ts_branch 8f\n"
                     "1:\n\t" QRETRY("3f")
                     "2:\n\ts_cmp_eq_u32 %[wv], 7\n\ts_waitcnt vmcnt(0)\n\t"
                     "v_permlane32_swap_b32 v56, v58\n\tv_permlane32_swap_b32 v57, v59\n\t"
                     "v_pk_add_f32 v[60:61], v[56:57], v[58:59]\n\t" AND3("v56", "v57", "v58") AND3("%[t]", "1", "v59")
                     "v_permlane16_swap_b32 v60, v61\n\tv_add_f32 v60, v60, v61\n\t"
                     "v_cmp_eq_u32 vcc, 0, %[t]\n\ts_nop 0\n\t" SD("v60", "row_ror:8")
                     "v_cvt_pk_bf16_f32 v60, v60, v60\n\tds_write_b16 %[qw], v60\n\ts_cbranch_vccnz 1b\n\ts_cbranch_scc1 7b\n"
                     "3:\n\ts_waitcnt lgkmcnt(0)\n\ts_barrier\n"
                     "8:"
                     : [t] "=&v"(t), [bud] "=&s"(bud), "+{v[56:59]}"(qv)
                     : [hf] "s"(half), [wv] "s"(wave), [z] "v"(smb), [cn] "n"(L_CNT), [off] "v"(off), [rq] "s"(rq), [qw] "v"(qw),
                       [scl] "n"(__builtin_bit_cast(u32, SCL))
                     : "memory", "vcc", "scc", "v60", "v61");
      }
      QK_SOFTMAX(qa)
      if (half == 0) {
        const int lv_ = lane_id();
        const u32 vo = (lv_ >> 3) * 384 + 16 * (lv_ & 7);
        tnext += E_QK;
        if (wave < 4) {
          VLOADS(XA, XB)
        }
        VLOADS(XB, 24)
      }
#define DA(CTL) "v_add_f32_dpp %[l], %[l], %[l] " CTL " row_mask:0xf bank_mask:0xf\n\t"
#define PX(o, v, a) SM "%[o" #o "], %[a" #a "], v[160+24*" #a "+8*" #o ":167+24*" #a "+8*" #o "], %[ix]\n\t"
      asm volatile("s_waitcnt vmcnt(6) lgkmcnt(3)\n\t" PX(0, x, 0) DA("quad_perm:[1,0,3,2]") PX(1, y, 0) PX(2, z, 0)
                   "s_waitcnt lgkmcnt(2)\n\t" PX(0, x, 1) DA("quad_perm:[2,3,0,1]") PX(1, y, 1) PX(2, z, 1)
                   "s_waitcnt lgkmcnt(0)\n\t" PX(0, x, 2) DA("row_half_mirror") PX(1, y, 2) PX(2, z, 2)
                   : [o0] "+v"(pv.O0), [o1] "+v"(pv.O1), [o2] "+v"(pv.O2), [a0] "+v"(pv.A0), [a1] "+v"(pv.A1),
                     [a2] "+v"(pv.A2), [a3] "+v"(pv.A3), [l] "+v"(pend)
                   : [ix] "v"(pv.idx), "{v[160:191]}"(kv3), "{v[192:223]}"(kv4), "{v[224:255]}"(kv5)
                   : "memory");
#define CLEAR2                                                                                                         \
  {                                                                                                                    \
    const int p2 = (isO ? j : j - 8) * 2 + (wave & 1);                                                                 \
    const u32 ca = ((isO ? j < 12 : (p2 < 40 || (g & 3) == 0)) ? gy_rep(g) : OOB) + (u32)(4 + (p2 >> 2)) * 16384u +    \
                   (u32)(p2 & 3) * 2048u + lane_id() * 16;                                                          \
    const u32x4 z = __builtin_bit_cast(u32x4, zero4());                                                                \
    store16(rs, ca, z);                                                                                                \
    store16(rs, ca + 1024, z);                                                                                         \
  }
      static_assert(5 - NINF >= NPF && 6 - NINO >= NPRE && NPF >= NPRE, "pieces behind V");
      if (half == 0) {
        {
          const int ln = lane_id();
          if (wave < 6 && ln < 8)
            store16(rs, WS_GQ1 + (u32)(h * 8 + c) * 2048 + 1024 + wave * 128 + ln * 16, afrag(smem, L_CS + wave * 128 + ln * 16));
        }
        anc = __builtin_amdgcn_readfirstlane(*(volatile LAS u32*)(smem + L_VOTE + 12));
#pragma unroll 1
        for (int p = 0; p < (sO ? NPRE : NPF); ++p) {
          pace(tnext, sO ? TPV : TPF);
          dma_piece(ws0, isO ? D : F, smem, L_WB, pn++, lane_id());
        }
        ar = (u32)(g & 3) * AREA + (((u32)(g >> 2) ^ lds_wait_nz(cnt + 2)) & 1u) * SIDE;
      }
      const u32 gob = ar + (u32)half * GO_SZ;
      if (NPF != NPRE && sO) asm volatile("s_waitcnt vmcnt(%0)" ::"n"(NPRE) : "memory");
      asm volatile("s_waitcnt vmcnt(%[np])\n\t" PX(0, x, 3) DA("row_ror:8") PX(1, y, 3) PX(2, z, 3) "s_setprio 0"
                   : [o0] "+v"(pv.O0), [o1] "+v"(pv.O1), [o2] "+v"(pv.O2), [a3] "+v"(pv.A3), [l] "+v"(pend)
                   : [ix] "v"(pv.idx), [np] "n"(NPF), "{v[224:255]}"(kv5)
                   : "memory");
      static_assert(NINO + NPRE == 6 && PF0 != PO0, "pieces");
      if (half == 0 && FEARLY && !sO) {
#pragma unroll 1
        for (int p = NINF + NPF; p < NINF + NPF + FEARLY; ++p) dma_piece(ws0, F, smem, L_WB, wave * 5 + p, lane_id());
      }
#define MX(CTL) "s_nop 1\n\tv_max_f32_dpp v49, v49, v49 " CTL " row_mask:0xf bank_mask:0xf\n\t"
#define MXP(W) "v_mov_b32 v50, v49\n\ts_nop 1\n\tv_permlane" W "_swap_b32 v49, v50\n\tv_max_f32 v49, v49, v50\n\t"
#define RB(D, A, BANK) "v_add_f32_dpp " D ", " A ", " A " row_ror:8 row_mask:0xf bank_mask:" BANK "\n\t"
#define SWP(W) "v_permlane" W "_swap_b32 v40, v42\n\tv_permlane" W "_swap_b32 v41, v43\n\t" PKA("v[40:41]", "v[42:43]")
#define SC2(R) "v_pk_mul_f32 " R ", " R ", v[50:51] op_sel_hi:[1,0]\n\t"
      asm volatile("ds_write2_b32 %[la], %[l], %[mx] offset1:4\n\ts_mov_b64 exec, %[m]\n\ts_cmp_lt_u32 %[wv], 6\n\ts_nop 1\n\t"
                   "ds_write_b128 %[a], %[o0]\n\tds_write_b128 %[a], %[o1] offset:256\n\t"
                   "ds_write_b128 %[a], %[o2] offset:512\n\t"
                   "s_mov_b64 exec, -1\n\ts_waitcnt lgkmcnt(0)\n\ts_barrier\n\t"
                   "ds_read_b32 v48, %[ma]\n\tds_read_b128 v[40:43], %[sa]\n\tds_read_b128 v[44:47], %[sa] offset:%[op]\n\t"
                   "s_waitcnt lgkmcnt(2)\n\tv_cmp_ne_u32 vcc, 0, v48\n\ts_cbranch_vccnz 5f\n"
                   "4:\n\ts_cbranch_scc0 6f\n\ts_waitcnt lgkmcnt(0)\n\t"
                   PKA("v[40:41]", "v[44:45]") PKA("v[42:43]", "v[46:47]") "s_nop 0\n\t"
                   RB("v40", "v40", "0x3") RB("v41", "v41", "0x3") RB("v40", "v42", "0xc") RB("v41", "v43", "0xc")
                   "s_nop 0\n\t" SWP("16") "s_nop 1\n\t" SWP("32")
                   "v_cvt_pk_bf16_f32 v40, v40, v41\n\tv_or_b32 v40, 1, v40\n\t"
                   "buffer_store_dword v40, %[dst], %[rq], %[so] offen sc1\n\ts_branch 9f\n"
                   "5:\n\tv_mov_b32 v49, v48\n\t" MX("quad_perm:[1,0,3,2]") MX("quad_perm:[2,3,0,1]") MX("row_half_mirror") MX("row_ror:8")
                   MXP("16") MXP("32") "v_sub_f32 v50, v48, v49\n\tv_exp_f32 v50, v50\n\tv_mov_b32 v48, v49\n\ts_waitcnt lgkmcnt(0)\n\t"
                   SC2("v[40:41]") SC2("v[42:43]") SC2("v[44:45]") SC2("v[46:47]") "s_branch 4b\n"
                   "6:\n\ts_waitcnt lgkmcnt(0)\n\t" PKA("v[42:43]", "v[40:41]") "v_add_f32 v41, v42, v43\n\t"
                   "v_or_b32 v40, 1, v48\n\tv_or_b32 v42, 1, v48\n\t" SD("v41", "quad_perm:[1,0,3,2]")
                   "v_mov_b32 v43, 1\n\ts_nop 0\n\t" SD("v41", "quad_perm:[2,3,0,1]") "s_nop 1\n\t" SD("v41", "row_half_mirror")
                   "v_or_b32 v41, 1, v41\n\tbuffer_store_dwordx4 v[40:43], %[dst], %[rq], %[so] offen sc1\n\ts_nop 1\n"
                   "9:"
                   :
                   : [m] "s"(0xff00ff0000ff00ffull), [a] "v"(pv.oa + smb), [o0] "v"(pv.O0), [o1] "v"(pv.O1), [o2] "v"(pv.O2),
                     [la] "v"(pv.la + smb), [l] "v"(pend), [mx] "v"(pmax), [wv] "s"(wave), [ma] "v"(pv.ma), [sa] "v"(pv.sa),
                     [op] "n"(OM_P * 4), [dst] "v"(pv.dst), [rq] "s"(rq), [so] "s"(gob)
                   : "memory", "vcc", "scc", DCLOB);
      tref = now();
      if (half == 0 && wave >= 6) CLEAR2
    }
    const u32 go = ar + A_GO + (u32)half * GO_SZ;
    const u32 gyw = A_GY + (u32)half * GY_PAIR;
    const u32 gy = gyw + ar;
    const u32 gh = ar + A_GH + (u32)half * 1536u;
    const int land = L_LAND + wave * 2048;
    const u32 landa = (u32)(uintptr_t)(LAS unsigned char*)smem + (u32)land;
#define CSLOAD                                                                                                        \
  if (wave < 6) {                                                                                                     \
    const int ln = lane_id();                                                                                         \
    ctl_load<true>(rq, WS_GQ1 + 1024 + (u32)(h * 8 + (ln >> 3)) * 2048 + wave * 128 + (ln & 7) * 16, (u32)(smb + L_OM + wave * OM_S * 4)); \
  }
    if (isO) {
      {
        const int lane = lane_id();
        const u32 voff = fresh(go + go_slot(wave) + ((lane * 16) ^ ((lane >> 4) * 64)));
        if (!half) wait_until(tref + 24 * T_O);
        wait_rt(anc + (half ? A_O : X_O));
        dma_poll<false>(rq, voff, landa, ~0ull, ~0ull);
        const int n = lane & 15, G = lane >> 4;
        const int r0_ = land + (n & 7) * 256 + (n & 3) * 64;
        const int ra = fresh(n < 8 ? r0_ + G * 16 : r0_ ^ 192), rb = fresh((r0_ + G * 16) ^ 64), rc = fresh((r0_ + G * 16) ^ 128);
        const int l0 = fresh(L_WB + wave * 6144 + wlane(lane, 0)), l1 = fresh(L_WB + wave * 6144 + wlane(lane, 1));
        asm volatile("s_waitcnt vmcnt(%0)" ::"n"(3) : "memory");
        u32x4 wf[6];
#pragma unroll
        for (int k = 0; k < 3; ++k) {
          wf[2 * k] = wfrag(smem, l0, 2048 * k);
          wf[2 * k + 1] = wfrag(smem, l1, 2048 * k);
        }
        const int rw = fresh(lane < 32 ? L_RED + wave * 160 + lane * 4 : L_ACT);
        const int rr = fresh(L_RED + (lane & 8) * 80 + (lane & 7) * 16);
        const u32 dst = fresh(gyw + gy_rep(lane >> 3) + gy_chunk(4 * j + ((lane >> 1) & 3)) + g * 256 + (lane & 1) * 8);
        f32x4 d0, d1;
        float mc, t1_, rl;
        u32 t, bud, rare;
        asm volatile(POLL_HEAD("s_lshr_b32 s99, s98, 8\n\ts_or_b32 s98, s98, s99\n\t", "")
                     "s_waitcnt lgkmcnt(2)\n\t"
                     MF R0 ", " F0 ", %[w0], 0\n\t"
                     "v_add_f32_dpp v48, v53, v53 quad_perm:[1,0,3,2] row_mask:0xf bank_mask:0xf\n\t"
                     MF R1 ", " F0 ", %[w1], 0\n\t" AND3("v52", "v53", "v54") "v_cmp_ne_u32 s[98:99], 1, v52\n\t"
                     "s_waitcnt lgkmcnt(1)\n\t"
                     MF R0 ", " F1 ", %[w2], " R0 "\n\t" ZD("quad_perm:[2,3,0,1]", "0xf") AND3("%[t]", "v55", "v56")
                     MF R1 ", " F1 ", %[w3], " R1 "\n\t" ZD("row_half_mirror", "0xf") AND3("%[t]", "v57", "v58")
                     "v_rcp_f32 v48, v48\n\t"
                     "s_waitcnt lgkmcnt(0)\n\t"
                     MF R0 ", " F2 ", %[w4], " R0 "\n\t" AND3("%[t]", "v59", "v60") AND3("%[t]", "v61", "v62") AND3("%[t]", "1", "v63")
                     MF R1 ", " F2 ", %[w5], " R1 "\n\t"
                     "v_cmp_eq_u32 vcc, 0, %[t]\n\ts_cbranch_vccnz 1b\n"
                     "3:\n\tv_readlane_b32 %[rl], v48, 8\n\ts_and_b32 %[rare], s98, 0xff00"
                     : "=&{v[40:43]}"(d0), "=&{v[44:47]}"(d1), [t] "=&v"(t), [bud] "=&s"(bud), "=&{v52}"(mc), "=&{v53}"(t1_),
                       [rl] "=&s"(rl), [rare] "=&s"(rare)
                     : [ra] "v"(ra), [rb] "v"(rb), [rc] "v"(rc), [w0] "v"(wf[0]), [w1] "v"(wf[1]), [w2] "v"(wf[2]), [w3] "v"(wf[3]),
                       [w4] "v"(wf[4]), [w5] "v"(wf[5]), [vo] "v"(voff), [rq] "s"(rq), [land] "s"(landa)
                     : "memory", "vcc", "scc", "s98", "s99", "v48", "v54", "v55", "v56", "v57", "v58", "v59", "v60", "v61", "v62",
                       "v63");
        if (__builtin_expect(rare != 0u, 0)) {
          asm volatile("s_nop 7" : "+v"(d0), "+v"(d1));
          const float fc = __builtin_amdgcn_exp2f(mc - max8(mc));
          const u32 sc = __float_as_uint(fc * __builtin_amdgcn_rcpf(sum8(fc * t1_)));
#pragma unroll
          for (int e = 0; e < 4; ++e) {
            const float se = __uint_as_float(G == 0 ? __builtin_amdgcn_readlane(sc, 8 + e) : __builtin_amdgcn_readlane(sc, 12 + e));
            d0[e] *= se;
            d1[e] *= se;
          }
          rl = 1.0f;
        }
        asm volatile("v_pk_add_f32 v[40:41], v[40:41], v[42:43]\n\tv_add_f32 v40, v40, v41\n\t"
                     "v_pk_add_f32 v[44:45], v[44:45], v[46:47]\n\tv_add_f32 v44, v44, v45\n\t"
                     "s_cmp_eq_u32 %[wv], 0\n\ts_nop 0\n\t"
                     "v_permlane16_swap_b32 v40, v44\n\tv_add_f32 v40, v40, v44\n\tv_mul_f32 v40, %[rl], v40\n\t"
                     "ds_write_b32 %[rw], v40\n\ts_waitcnt lgkmcnt(0)\n\ts_barrier\n\ts_cbranch_scc0 9f\n\t"
                     "ds_read_b128 v[40:43], %[rr]\n\tds_read_b128 v[44:47], %[rr] offset:160\n\t"
                     "ds_read_b128 v[48:51], %[rr] offset:320\n\tds_read_b128 v[52:55], %[rr] offset:480\n\t"
                     "s_waitcnt lgkmcnt(2)\n\t" PKA("v[40:41]", "v[44:45]") PKA("v[42:43]", "v[46:47]")
                     "s_waitcnt lgkmcnt(0)\n\t" PKA("v[48:49]", "v[52:53]") PKA("v[50:51]", "v[54:55]")
                     PKA("v[40:41]", "v[48:49]") PKA("v[42:43]", "v[50:51]") "s_nop 0\n\t"
                     SD("v40", "row_ror:8") SD("v41", "row_ror:8") SD("v42", "row_ror:8") SD("v43", "row_ror:8")
                     "v_cvt_pk_bf16_f32 v40, v40, v41\n\tv_cvt_pk_bf16_f32 v41, v42, v43\n\t"
                     "v_or_b32 v40, 1, v40\n\tv_or_b32 v41, 1, v41\n\t"
                     "buffer_store_dwordx2 v[40:41], %[dst], %[rq], 0 offen sc1\n"
                     "9:"
                     : "+{v[40:43]}"(d0), "+{v[44:47]}"(d1)
                     : [wv] "s"(wave), [rl] "s"(rl), [rw] "v"(rw), [rr] "v"(rr), [dst] "v"(dst), [rq] "s"(rq)
                     : "memory", "scc", "v48", "v49", "v50", "v51", "v52", "v53", "v54", "v55");
      }
      tref = now();
      if (half == 0) {
        if (wave == 0) {
          const int lane = lane_id();
          u32 w2o = ((lane >> 2) * D + ((((lane >> 1) ^ (lane >> 5)) & 1) * 16 + (lane & 1) * 8)) * 2;
#define W2L "global_load_lds_dwordx4 %[wo], %[wp] nt\n\ts_addk_i32 m0, 0x400\n\tv_add_u32 %[wo], %[rs], %[wo]\n\t"
          asm volatile("s_mov_b32 s98, m0\n\ts_mov_b32 m0, %[wl]\n\ts_nop 2\n8:\n\t" W2L W2L W2L W2L
                       "s_cmp_lt_u32 m0, %[we]\n\ts_cbranch_scc1 8b\n\ts_mov_b32 m0, s98"
                       : [wo] "+v"(w2o)
                       : [wp] "s"(w2p), [wl] "n"(L_W2), [we] "n"(L_W2 + 12288), [rs] "n"(16 * D * 2)
                       : "memory", "scc", "s98");
        }
        CSLOAD
        if (wave == 0) {
          asm volatile("s_waitcnt vmcnt(0)" ::: "memory");
          cnt[1] = 8u;
        }
        if (wave == 5) {
          wait_until(tref + 24 * T_LAZY);
          x_update<false>(smem, rq, lane_id(), gy + gy_row(c), c);
        }
      } else if (g == 0 && wave == 2) {
        const int lane = lane_id();
        const int col = j * 32 + (lane & 7) * 4 + ((lane >> 4) & 1) * 2 + (lane >> 5);
        u32 x0v, dummy;
        u16* const po = karg<72>(kp) + col;
        asm volatile("global_load_ushort %0, %2, off\n\tglobal_load_ushort %1, %3, off\n\ts_waitcnt vmcnt(0)"
                     : "=&v"(x0v), "=&v"(dummy)
                     : "v"(karg<0>(kp) + col), "v"(po)
                     : "memory");
        wait_until(tref + 24 * T_LAZY);
        const u32 o = gy + gy_chunk(4 * j + ((lane >> 1) & 3)) + (lane >> 3) * 256 + (lane & 1) * 8;
        const u32 off[3] = {o - GY_PAIR, o - GY_PAIR + GY_INST, o};
        float y[3];
        col_poll<3>(rq, off, y);
        float x3 = bf16r(bf16r(bf16r(bflo(x0v) + y[0]) + y[1]) + y[2]);
        const u32 off2 = fresh(o + GY_INST);
        asm volatile("" : "+v"(x3));
        wait_rt(anc + A_F);
        u32 t, bud;
        asm volatile("s_mov_b32 %[bud], 0x10000\n\ts_branch 2f\n"
                     "1:\n\ts_sub_u32 %[bud], %[bud], 1\n\ts_cbranch_scc1 3f\n"
                     "2:\n\tbuffer_load_dwordx2 v[40:41], %[off], %[rq], 0 offen sc1\n\ts_waitcnt vmcnt(0)\n\t"
                     "v_lshlrev_b32 v42, 16, v40\n\tv_and_b32 v44, 0xffff0000, v40\n\t"
                     "v_lshlrev_b32 v43, 16, v41\n\tv_and_b32 v45, 0xffff0000, v41\n\t"
                     "v_permlane32_swap_b32 v42, v44\n\t" AND3("v40", "1", "v41") "v_permlane32_swap_b32 v43, v45\n\t"
                     PKA("v[42:43]", "v[44:45]") "v_cmp_eq_u32 vcc, 0, %[t]\n\ts_cbranch_vccnz 1b\n"
                     "3:\n\tv_permlane16_swap_b32 v42, v43\n\tv_add_f32 v42, v42, v43\n\ts_nop 1\n\t" SD("v42", "row_ror:8")
                     RND("v42", "v42") "v_add_f32 v42, v42, %[x3]\n\tv_cvt_pk_bf16_f32 v42, v42, v42\n\t"
                     "global_store_short %[po], v42, off"
                     : [t] "=&v"(t), [bud] "=&s"(bud)
                     : [off] "v"(off2), [rq] "s"(rq), [x3] "v"(x3), [po] "v"(po)
                     : "memory", "vcc", "scc", "s98", "s99", DCLOB);
      }
      if (wave >= 6) {
        const int lane = lane_id(), wv = wave - 6;
        const int n = lane & 15, G = lane >> 4;
        const int l0 = fresh(L_W2 + wlane(lane, wv));
        const int l48 = lane < 48 ? lane : 0;
        const u32 off = fresh(gh + (u32)l48 * 16);
        const int aw = fresh(L_ACT + wv * 384 + l48 * 8);
        const int ab = fresh(L_ACT + wv * 384 + 16 * G);
        const u32 dst = fresh(((G & (half ? 3 : 0)) ? OOB : gyw) + GY_INST + gy_rep((n & 1) * 4 + G) + gy_chunk(4 * j + wv * 2 + (n >> 3)) +
                              g * 256 + (n & 6) * 2);
        if (!half) wait_until(tref + 24 * T_H);
#define HPOLL                                                      \
  "buffer_load_dwordx4 v[32:35], %[off], %[rq], 0 offen sc1\n\t" \
  "buffer_load_dwordx4 v[36:39], %[off], %[rq], 0 offen offset:768 sc1\n\t"
        u32x4 p0, p1;
        asm volatile(WAIT_RT HPOLL "v_mov_b32 v41, 0\n\ts_mov_b32 s98, 0x400000\n"
                     "4:\n\tds_read_b32 v40, v41 offset:%[cn]\n\ts_sub_u32 s98, s98, 1\n\ts_waitcnt lgkmcnt(0)\n\ts_cbranch_scc1 5f\n\t"
                     "v_cmp_eq_u32 vcc, 8, v40\n\ts_cbranch_vccz 4b\n5:"
                     : "=&{v[32:35]}"(p0), "=&{v[36:39]}"(p1)
                     : [off] "v"(off), [rq] "s"(rq), [tt] "s"(anc + (half ? A_H : X_H)), [cn] "n"(L_CNT + 4)
                     : "memory", "scc", "vcc", "s98", "s99", "v40", "v41");
        u32x4 wf[6];
#pragma unroll
        for (int k = 0; k < 6; ++k) wf[k] = wfrag(smem, l0, 2048 * k);
        u32 t, bud;
        asm volatile("s_mov_b32 %[bud], 0x10000\n\ts_branch 2f\n"
                     "1:\n\ts_sub_u32 %[bud], %[bud], 1\n\ts_cbranch_scc1 3f\n\ts_waitcnt lgkmcnt(0)\n\ts_sleep 1\n\ts_mov_b64 exec, vcc\n\t"
                     HPOLL "s_mov_b64 exec, -1\n"
                     "2:\n\ts_waitcnt vmcnt(0)\n\t"
                     "v_pk_mul_f32 v[40:41], v[32:33], v[36:37]\n\tv_pk_mul_f32 v[42:43], v[34:35], v[38:39]\n\t"
                     "v_cvt_pk_bf16_f32 v40, v40, v41\n\tv_cvt_pk_bf16_f32 v41, v42, v43\n\t"
                     "ds_write_b64 %[aw], v[40:41]\n\t"
                     "ds_read_b128 v[40:43], %[ab]\n\tds_read_b128 v[44:47], %[ab] offset:64\n\t"
                     "ds_read_b128 v[48:51], %[ab] offset:128\n\tds_read_b128 v[52:55], %[ab] offset:192\n\t"
                     "ds_read_b128 v[56:59], %[ab] offset:256\n\tds_read_b128 v[60:63], %[ab] offset:320\n\t"
                     AND3("v32", "v33", "v34") AND3("%[t]", "v35", "v36") AND3("%[t]", "v37", "v38") AND3("%[t]", "1", "v39")
                     "v_cmp_eq_u32 vcc, 0, %[t]\n\ts_cbranch_vccnz 1b\n"
                     "3:\n\ts_waitcnt lgkmcnt(5)\n\t" MF "v[32:35], v[40:43], %[w0], 0\n\t"
                     "s_waitcnt lgkmcnt(4)\n\t" MF "v[32:35], v[44:47], %[w1], v[32:35]\n\t"
                     "s_waitcnt lgkmcnt(3)\n\t" MF "v[32:35], v[48:51], %[w2], v[32:35]\n\t"
                     "s_waitcnt lgkmcnt(2)\n\t" MF "v[32:35], v[52:55], %[w3], v[32:35]\n\t"
                     "s_waitcnt lgkmcnt(1)\n\t" MF "v[32:35], v[56:59], %[w4], v[32:35]\n\t"
                     "s_waitcnt lgkmcnt(0)\n\t" MF "v[32:35], v[60:63], %[w5], v[32:35]\n\ts_nop 7\n\t"
                     "v_mov_b32_dpp v36, v32 quad_perm:[0,0,2,2] row_mask:0xf bank_mask:0xf\n\t"
                     "v_mov_b32_dpp v37, v32 quad_perm:[1,1,3,3] row_mask:0xf bank_mask:0xf\n\t"
                     "v_cvt_pk_bf16_f32 v36, v36, v37\n\tv_or_b32 v36, 1, v36\n\t"
                     "buffer_store_dword v36, %[dst], %[rq], 0 offen sc1"
                     : [t] "=&v"(t), [bud] "=&s"(bud), "+{v[32:35]}"(p0), "+{v[36:39]}"(p1)
                     : [off] "v"(off), [aw] "v"(aw), [ab] "v"(ab), [dst] "v"(dst), [rq] "s"(rq), [w0] "v"(wf[0]), [w1] "v"(wf[1]),
                       [w2] "v"(wf[2]), [w3] "v"(wf[3]), [w4] "v"(wf[4]), [w5] "v"(wf[5])
                     : "memory", "vcc", "scc", FCLOB, DCLOB);
      }
    } else {
      const int lane = lane_id();
      const int n = lane & 15, G = lane >> 4;
      if (half == 1) {
        x_update<false>(smem, rq, lane, gy - GY_PAIR + GY_INST + gy_row(wave), wave);
      } else if (NINF + NPF + FEARLY < 5 && !sO) {
#pragma unroll 1
        for (int p = NINF + NPF + FEARLY; p < 5; ++p) {
          pace(tnext, TFL);
          dma_piece(ws0, F, smem, L_WB, wave * 5 + p, lane_id());
        }
      }
      G80 a;
      g80_a(a, lane, L_HB + 160 * wave, land);
      const u32 voff = fresh(gy + gy_row(wave) + ((lane * 16) ^ ((lane >> 4) * 64)));
      const int rw = fresh(L_RED + n * 144 + wave * 16 + G * 4);
      const int sw_ = fresh(n == 0 ? L_ST + wave * 16 + G * 4 : L_ACT);
      if (!half) wait_until(tref + 24 * T_YF);
      asm volatile("s_waitcnt vmcnt(0)" ::: "memory");
      const u32 ty = anc + (half ? A_Y : X_Y);
      const bool late = (int)(rtnow() - ty) >= 0;
      if (late) dma_poll<false>(rq, voff, landa, USED, USED);
      g80_w<512>(a, smem, lane, L_WB + wave * 5120);
      if (!late) {
        wait_rt(ty);
        dma_poll<false>(rq, voff, landa, USED, USED);
      }
      {
        u32 t, bud;
        asm volatile(G80_LOOP("s_nop 0\n\t")
                     "ds_write2st64_b32 %[rw], v40, v44 offset1:9\n\t" ZD("quad_perm:[2,3,0,1]", "0xf") "s_nop 1\n\t"
                     ZD("row_half_mirror", "0xf") "ds_write_b32 %[sw], v48"
                     : [t] "=&v"(t), [bud] "=&s"(bud)
                     : G80_IN(a), [nm] "s"(0x00ff00ff00ff00ffull), [rw] "v"(rw), [sw] "v"(sw_)
                     : "memory", "vcc", "scc", "s98", "s99", FCLOB, DCLOB);
      }
      const int ln = lane_id();
      const int rr = fresh(L_RED + (ln & 31) * 144 + (ln >> 5) * 64), rs_ = fresh(L_ST + 16 * (ln & 7));
      const u32 dst = fresh(gh + fslot + ln * 4);
      asm volatile("s_cmp_eq_u32 %[wv], 0\n\ts_waitcnt lgkmcnt(0)\n\ts_barrier\n\ts_cbranch_scc0 9f\n\t"
                   "ds_read_b128 v[40:43], %[rs]\n\tds_read_b128 v[44:47], %[rr]\n\tds_read_b128 v[48:51], %[rr] offset:16\n\t"
                   "ds_read_b128 v[52:55], %[rr] offset:32\n\tds_read_b128 v[56:59], %[rr] offset:48\n\t"
                   "s_bitcmp1_b32 %[j], 0\n\ts_mov_b32 s98, 0x10000\n\t"
                   "s_waitcnt lgkmcnt(4)\n\t"
                   "v_add_f32 v40, v40, v41\n\t" SD("v43", "quad_perm:[1,0,3,2]") "v_add_f32 v40, v40, v42\n\t"
                   "s_waitcnt lgkmcnt(2)\n\t" SD("v43", "quad_perm:[2,3,0,1]") SD("v40", "quad_perm:[1,0,3,2]")
                   PKA("v[44:45]", "v[48:49]") SD("v43", "row_half_mirror") SD("v40", "quad_perm:[2,3,0,1]")
                   "v_mul_f32 v43, 0x3acccccd, v43\n\tv_mul_f32 v41, -v43, v43\n\t" SD("v40", "row_half_mirror")
                   "v_fmac_f32 v41, 0x3acccccd, v40\n\t" RND("v41", "|v41|") "v_add_f32 v41, 0x3727c5ac, v41\n\t" RND("v41", "v41")
                   "v_sqrt_f32 v41, v41\n\t" PKA("v[46:47]", "v[50:51]") RND("v41", "v41") "v_rcp_f32 v41, v41\n\t"
                   "s_waitcnt lgkmcnt(0)\n\t"
                   PKA("v[52:53]", "v[56:57]") PKA("v[54:55]", "v[58:59]") PKA("v[44:45]", "v[52:53]") PKA("v[46:47]", "v[54:55]")
                   RND("v43", "v43") "v_add_f32 v44, v44, v45\n\tv_fma_f32 v46, -v43, v47, v46\n\tv_add_f32 v44, v44, v46\n\t"
                   "s_nop 1\n\tv_permlane32_swap_b32 v44, v45\n\tv_add_f32 v44, v44, v45\n\tv_mul_f32 v44, v44, v41\n\t"
                   "s_cbranch_scc1 8f\n\t" RND("v44", "v44")
                   "v_mul_f32 v45, 0xbfb8aa3b, v44\n\tv_exp_f32 v45, v45\n\ts_nop 0\n\tv_add_f32 v45, 1.0, v45\n\t"
                   "v_rcp_f32 v45, v45\n\ts_nop 0\n\tv_mul_f32 v44, v44, v45\n"
                   "8:\n\ts_mov_b32 exec_hi, 0\n\tv_cvt_pk_bf16_f32 v44, s98, v44\n\t"
                   "buffer_store_dword v44, %[dst], %[rq], 0 offen sc1\n\ts_mov_b32 exec_hi, -1\n"
                   "9:"
                   :
                   : [wv] "s"(wave), [j] "s"(j), [rs] "v"(rs_), [rr] "v"(rr), [dst] "v"(dst), [rq] "s"(rq)
                   : "memory", "scc", "s98", FCLOB, DCLOB);
      tref = now();
      if (half == 0 && wave == 1 && j == 20 && ln < 8) {
        const u32 st = rtnow();
        __builtin_amdgcn_raw_buffer_store_b32((((int)(st - anc) < (int)X_L ? anc + X_S : st) << 1) | 1u, rs, gy_rep(ln) + st_line(g), 0, 16);
      }
      if (half == 0) {
        CSLOAD
        x_update<true>(smem, rq, lane_id(), land, wave);
        if (wave == 7) {
          wait_until(tref + 24 * T_AN);
          stamp_poll(smem, rq, ar + st_line(ln & 7));
        }
      }
    }
    if (half == 1) break;
    {
      if (wave >= (isO ? 6 : 7)) {
        bar();
        anc = __builtin_amdgcn_readfirstlane(*(volatile LAS u32*)(smem + L_ANC));
      } else {
        const int lane = lane_id();
        const int n = lane & 15;
        G80 a;
        g80_a(a, lane, L_HB + 160 * c, land);
        const u32 voff = fresh((wave < 6 ? gy : OOB) + GY_INST + gy_row(c) + ((lane * 16) ^ ((lane >> 4) * 64)));
        const u32 gq = ar + A_GQ + hsub * 16384 + c * 896;
        const u32 dst = fresh((wave < 6 ? gq : OOB) + wave * 128 + (lane >> 5) * 64 + n * 4);
        const u32 dsz = fresh((wave == 5 ? gq : OOB) + 768 + (lane >> 4) * 4);
        const int csw = fresh(wave < 6 ? L_CS + (wave * 32 + (lane & 7) * 4 + ((lane >> 4) & 1) * 2 + (lane >> 5)) * 4 : L_PF);
        if (isO && wave == 4) {
          wait_until(tref + 24 * T_H);
          wait_rt(anc + X_H);
          stamp_poll(smem, rq, ar + st_line(lane & 7));
        }
        anc = lds_wait_nz((volatile LAS u32*)(smem + L_ANC));
        wait_rt(anc + (wave < 6 ? A_E : 0));
        dma_poll<false>(rq, voff, landa, USED, USED);
        asm volatile("s_waitcnt vmcnt(2)" ::: "memory");
        *reinterpret_cast<float*>(smem + csw) =
            tsum8(__builtin_bit_cast(f32x4, afrag(smem, L_OM + wave * OM_S * 4 + lane * 16))) * (-1.0f / D);
        g80_w<1024>(a, smem, lane, L_WQ + (wave >> 1) * 10240 + (wave & 1) * 512);
        const float cc = *reinterpret_cast<const float*>(smem + L_CS + wave * 128 + (lane >> 5) * 64 + n * 4);
        bar();
        u32 t, bud;
        asm volatile(G80_LOOP("s_mov_b32 vcc_lo, 0xffff\n\t")
                     "s_mov_b32 vcc_hi, 0xffff\n\t" ZD("quad_perm:[2,3,0,1]", "0xf")
                     "v_permlane32_swap_b32 v40, v44\n\tv_add_f32 v44, v40, v44\n\t"
                     ZD("row_half_mirror", "0xf") "v_permlane16_swap_b32 v40, v41\n\tv_readlane_b32 s98, v48, 48\n\t"
                     "v_add_f32 v44, v44, v41\n\ts_mov_b64 exec, vcc\n\t"
                     "v_fmac_f32 v44, s98, %[cc]\n\tv_or_b32 v44, 1, v44\n\t"
                     "buffer_store_dword v44, %[dst], %[rq], 0 offen sc1\n\t"
                     "s_mov_b32 exec_lo, 0x10001\n\ts_mov_b32 exec_hi, 0x10001\n\tv_or_b32 v48, 1, v48\n\t"
                     "buffer_store_dword v48, %[dsz], %[rq], 0 offen sc1\n\ts_mov_b64 exec, -1"
                     : [t] "=&v"(t), [bud] "=&s"(bud)
                     : G80_IN(a), [nm] "s"(wave < 6 ? 0x00ff00ff00ff00ffull : 0ull), [cc] "v"(cc), [dst] "v"(dst), [dsz] "v"(dsz)
                     : "memory", "vcc", "scc", "s98", "s99", FCLOB, DCLOB);
      }
    }
  }
  asm volatile(".Lmk_code_end:");
}

}

extern "C" void kernel_launch(void* const* d_in, const int* in_sizes, int n_in, void* d_out, int out_size, void* d_ws,
                              size_t ws_size, hipStream_t stream) {
  Params P;
  P.x0 = (const u16*)d_in[0];
  P.kc = (const u16*)d_in[1];
  P.vc = (const u16*)d_in[2];
  P.wq = (const u16*)d_in[3];
  P.wo = (const u16*)d_in[4];
  P.w1 = (const u16*)d_in[6];
  P.w3 = (const u16*)d_in[7];
  P.w2 = (const u16*)d_in[8];
  P.out = (u16*)d_out;
  P.ws = (u32*)d_ws;
  mega_kernel<<<NWG, NT, 0, stream>>>(P);
}
```

```cpp
#define TSTEP 236
#include <hip/hip_runtime.h>
#include <hip/hip_bf16.h>
#include <stdint.h>

namespace {

typedef unsigned int u32;
typedef unsigned short u16;
typedef unsigned long long u64;
using u32x2 = __attribute__((ext_vector_type(2))) unsigned;
using u32x4 = __attribute__((ext_vector_type(4))) unsigned;
using f32x2 = __attribute__((ext_vector_type(2))) float;
using f32x4 = __attribute__((ext_vector_type(4))) float;
using s16x4 = __attribute__((ext_vector_type(4))) short;
using bf16x2 = __attribute__((ext_vector_type(2))) __bf16;
using bf16x8 = __attribute__((ext_vector_type(8))) __bf16;
#define LAS __attribute__((address_space(3)))

constexpr int D = 640, H = 32, DH = 192, WIN = 4096, SK = 32768, F = 1536;
constexpr int NWG = 256, NT = 512;
constexpr u32 SPIN_CAP = 1u << 16;
constexpr float SCL = 0.07216878364870322f * 1.4426950408889634f;

#define T_O 12
#define T_YF 88
#define T_H 80
#define T_AN 28
#define PTHR 60
#define T_LAZY 60

#define I_Q 58
#define I_O 142
#define I_Y 77
#define I_H 75
#define I_F 42
#define J_Y 84
#define J_H 86
#define J_E 57
#define J_L 16
#define AN_M 26
#define A_E 100
#define A_Q (A_E + I_Q)
#define A_O (A_Q + I_O)
#define A_Y (A_O + I_Y)
#define A_H (A_Y + I_H)
#define A_F (A_H + I_F)

#define X_F 90
#define X_O 64
#define X_Y (X_O + J_Y)
#define X_H (X_Y + J_H)
#define X_L (X_H - J_L)
#define X_E (X_H + J_E)
#define X_S (X_E - A_E)
#define CKD 60
#define CKF (128.f / 22.8f)
#define KAP 8
#define TT(T) (anc + ((u32)(T) * tsc >> 10))

constexpr u32 SIDE = 8192, AREA = 17 * 16384;
constexpr u32 A_GO = 0;
constexpr u32 A_GY = 2 * 16384;
constexpr u32 A_GQ = 6 * 16384;
constexpr u32 A_GH = 16 * 16384;
constexpr u32 GO_SZ = 10 * 16384, GY_PAIR = GO_SZ, GY_INST = 32768;
constexpr u32 WS_PB = 4 * AREA;
constexpr u32 WS_GOF = WS_PB + (128 << 10), WS_HB = WS_GOF + 4096;
constexpr u32 WS_RP = WS_HB + 4096;
constexpr u32 WS_GQ1 = WS_HB + 256 * 128;
constexpr u32 WS_END = WS_GQ1 + 256 * 2048;
static_assert(WS_END <= (2u << 20), "layout");
constexpr u32 OOB = 1u << 30;
__device__ __forceinline__ u32 go_slot(int w) { return (u32)(w >> 2) * 16384u + (u32)(w & 3) * 2048u; }
__device__ __forceinline__ u32 gy_row(int i) { return go_slot(i); }
__device__ __forceinline__ u32 gy_chunk(int u) {
  const int i = (u * 205) >> 11;
  return gy_row(i) + (u32)(u - 10 * i) * 16u;
}
__device__ __forceinline__ u32 st_line(int g) { return A_GH + 3072u + (u32)g * 128u; }
__device__ __forceinline__ u32 gy_rep(int r) { return (u32)(r & 3) * AREA + (u32)(r >> 2) * SIDE; }

constexpr int L_ZERO = 0;
constexpr int L_CNT = 16;
constexpr int L_RI = 28;
constexpr int L_ONE = 32;
constexpr int L_TAG = 48;
constexpr int L_ST = 64;
constexpr int L_VOTE = L_ST;
constexpr int L_ANC = 192;
constexpr int L_Q = 256;
constexpr int L_CS = 640;
constexpr int L_HB = L_CS + 768 + 64;
constexpr int L_ACT = L_HB + D * 2;
constexpr int L_RED = L_ACT + 2 * 384;
constexpr int L_P = L_RED + 32 * 144;
constexpr int P_W = 1280, P_U = 592, P_PAR = 288;
constexpr int OM_S = 400, OM_P = 200;
constexpr int L_OM = L_P + 8 * P_W;
constexpr int L_LN = L_OM;
constexpr int L_LAND = (L_OM + 8 * OM_S * 4 + 255) / 256 * 256;
constexpr int L_CTL = L_LAND + 6 * 2048;
constexpr int L_WQ = L_LAND + 8 * 2048;
constexpr int L_WB = L_WQ + 6 * 5120;
constexpr int L_W2 = L_WB + 768 * 64;
constexpr int L_PF = L_W2 + 192 * 64;
constexpr int L_TAB = L_PF + 256;
constexpr int L_TB = L_TAB + 8 * 2048;
constexpr int L_SH = L_TB + 2048;
constexpr int L_TOTAL = L_SH + 16;
static_assert(L_LAND % 256 == 0 && L_P % 16 == 0 && L_WQ % 16 == 0 && L_HB % 16 == 0 && L_RED % 16 == 0, "alignment");

struct Params {
  const u16* x0;
  const u16* kc;
  const u16* vc;
  const u16* wq;
  u32* ws;
  const u16* w1;
  const u16* w3;
  const u16* wo;
  const u16* w2;
  u16* out;
};

__device__ __forceinline__ u32 cvt2(float lo, float hi) {
  return __builtin_bit_cast(u32, __builtin_convertvector(f32x2{lo, hi}, bf16x2));
}
__device__ __forceinline__ float bflo(u32 w) { return __uint_as_float(w << 16); }
__device__ __forceinline__ float bfhi(u32 w) { return __uint_as_float(w & 0xffff0000u); }
__device__ __forceinline__ float bf16r(float x) { return bflo(cvt2(x, x)); }

template <int CTRL>
__device__ __forceinline__ float dpp(float x) {
  return __builtin_bit_cast(float, __builtin_amdgcn_mov_dpp(__builtin_bit_cast(int, x), CTRL, 0xf, 0xf, true));
}
constexpr int XOR1 = 0xB1, XOR2 = 0x4E, XOR7 = 0x141, ROR8 = 0x128;
__device__ __forceinline__ float fmx(float a, float b) {
  float r;
  asm("v_max_f32 %0, %1, %2" : "=v"(r) : "v"(a), "v"(b));
  return r;
}
__device__ __forceinline__ float xrow16_max(float x) {
  auto s = __builtin_amdgcn_permlane16_swap(__float_as_uint(x), __float_as_uint(x), false, false);
  x = fmx(__uint_as_float(s[0]), __uint_as_float(s[1]));
  auto t = __builtin_amdgcn_permlane32_swap(__float_as_uint(x), __float_as_uint(x), false, false);
  return fmx(__uint_as_float(t[0]), __uint_as_float(t[1]));
}
__device__ __forceinline__ float xrow16_sum(float x) {
  auto s = __builtin_amdgcn_permlane16_swap(__float_as_uint(x), __float_as_uint(x), false, false);
  x = __uint_as_float(s[0]) + __uint_as_float(s[1]);
  auto t = __builtin_amdgcn_permlane32_swap(__float_as_uint(x), __float_as_uint(x), false, false);
  return __uint_as_float(t[0]) + __uint_as_float(t[1]);
}
__device__ __forceinline__ float sum8(float v) {
  v += dpp<XOR1>(v);
  v += dpp<XOR2>(v);
  v += dpp<XOR7>(v);
  return v;
}
__device__ __forceinline__ float max8(float v) {
  asm("s_nop 1\n\tv_max_f32_dpp %0, %0, %0 quad_perm:[1,0,3,2] row_mask:0xf bank_mask:0xf\n\t"
      "s_nop 1\n\tv_max_f32_dpp %0, %0, %0 quad_perm:[2,3,0,1] row_mask:0xf bank_mask:0xf\n\t"
      "s_nop 1\n\tv_max_f32_dpp %0, %0, %0 row_half_mirror row_mask:0xf bank_mask:0xf"
      : "+v"(v));
  return v;
}
__device__ __forceinline__ float max_ror8(float v) {
  asm("s_nop 1\n\tv_max_f32_dpp %0, %0, %0 row_ror:8 row_mask:0xf bank_mask:0xf" : "+v"(v));
  return v;
}
__device__ __forceinline__ f32x4 sum4x4(f32x4 v) {
  float a = v[0], b = v[1], c = v[2], d = v[3];
#define S4(CTL)                                               \
  "v_add_f32_dpp %0, %0, %0 " CTL " row_mask:0xf bank_mask:0xf\n\t" \
  "v_add_f32_dpp %1, %1, %1 " CTL " row_mask:0xf bank_mask:0xf\n\t" \
  "v_add_f32_dpp %2, %2, %2 " CTL " row_mask:0xf bank_mask:0xf\n\t" \
  "v_add_f32_dpp %3, %3, %3 " CTL " row_mask:0xf bank_mask:0xf\n\t"
  asm("s_nop 1\n\t" S4("quad_perm:[1,0,3,2]") S4("quad_perm:[2,3,0,1]") : "+v"(a), "+v"(b), "+v"(c), "+v"(d));
#undef S4
  return f32x4{a, b, c, d};
}
__device__ __forceinline__ float wave_sum(float v) {
  v = sum8(v);
  v += dpp<ROR8>(v);
  return xrow16_sum(v);
}
__device__ __forceinline__ float tsum8(f32x4 a) {
  auto s0 = __builtin_amdgcn_permlane32_swap(__float_as_uint(a[0]), __float_as_uint(a[1]), false, false);
  auto s1 = __builtin_amdgcn_permlane32_swap(__float_as_uint(a[2]), __float_as_uint(a[3]), false, false);
  const float r0 = __uint_as_float(s0[0]) + __uint_as_float(s0[1]);
  const float r1 = __uint_as_float(s1[0]) + __uint_as_float(s1[1]);
  auto t = __builtin_amdgcn_permlane16_swap(__float_as_uint(r0), __float_as_uint(r1), false, false);
  float r = __uint_as_float(t[0]) + __uint_as_float(t[1]);
  r += dpp<ROR8>(r);
  return r;
}

__device__ __forceinline__ u32 tagv(float v) { return __float_as_uint(v) | 1u; }
__device__ __forceinline__ void store_quad(__amdgpu_buffer_rsrc_t rs, u32 off, f32x4 v) {
  const u32x4 s = {tagv(v[0]), tagv(v[1]), tagv(v[2]), tagv(v[3])};
  __builtin_amdgcn_raw_buffer_store_b128(s, rs, off, 0, 16);
}
template <int N>
__device__ __forceinline__ void gpoll4(u32x4 rs, const u32 (&off)[N], f32x4 (&v)[N]) {
  bool need = true;
  u32 budget = SPIN_CAP;
#pragma unroll
  for (int k = 0; k < N; ++k) asm volatile("" : "=v"(v[k]));
#pragma unroll 1
  for (;;) {
    if (need) {
#pragma unroll
      for (int k = 0; k < N; ++k)
        asm volatile("buffer_load_dwordx4 %0, %1, %2, 0 offen sc1" : "+v"(v[k]) : "v"(off[k]), "s"(rs) : "memory");
    }
    asm volatile("s_waitcnt vmcnt(0)" ::: "memory");
    u32 a = 1u;
#pragma unroll
    for (int k = 0; k < N; ++k) {
      asm volatile("" : "+v"(v[k]));
      a &= __float_as_uint(v[k][0]) & __float_as_uint(v[k][1]) & __float_as_uint(v[k][2]) & __float_as_uint(v[k][3]);
    }
    need = a == 0u;
    if (__builtin_amdgcn_ballot_w64(need) == 0ull || budget == 0u) break;
    --budget;
    __builtin_amdgcn_s_sleep(1);
  }
}
__device__ __forceinline__ u32 now() { return (u32)__builtin_amdgcn_s_memtime(); }
__device__ __forceinline__ void wait_until(u32 t) {
#pragma unroll 1
  while ((int)(now() - t) < -160) __builtin_amdgcn_s_sleep(1);
#pragma unroll 1
  while ((int)(now() - t) < 0) {
  }
}
__device__ __forceinline__ u32 rtnow() { return (u32)__builtin_amdgcn_s_memrealtime(); }
#define WAIT_RT                                                                                                       \
  "s_branch 1f\n\t.p2align 5\n0:\n\ts_sleep 1\n"                                                                       \
  "1:\n\ts_memrealtime s[98:99]\n\ts_waitcnt lgkmcnt(0)\n\ts_sub_i32 vcc_hi, s98, %[tt]\n\t"                           \
  "s_cmp_ge_u32 vcc_hi, 0xfffffc00\n\ts_cbranch_scc0 5f\n\t"                                                           \
  "s_cmpk_lt_i32 vcc_hi, -20\n\ts_cselect_b32 vcc_lo, s98, %[tt]\n\ts_cselect_b32 vcc_hi, -1, vcc_hi\n\t"              \
  "s_orn2_b32 vcc_hi, vcc_hi, 3\n\ts_add_i32 vcc_lo, vcc_lo, vcc_hi\n"                                                 \
  "2:\n\ts_memrealtime s[98:99]\n\ts_cmp_ge_i32 s98, vcc_lo\n\ts_cbranch_scc0 2b\n\t"                                  \
  "s_sub_i32 vcc_lo, vcc_lo, vcc_hi\n\ts_waitcnt lgkmcnt(0)\n"                                                         \
  "4:\n\ts_add_u32 vcc_hi, vcc_hi, 1\n\ts_cbranch_scc0 4b\n\ts_cmp_eq_u32 vcc_lo, %[tt]\n\ts_cbranch_scc0 0b\n5:\n\t"
__device__ __forceinline__ void wait_rt(u32 t) { asm volatile(WAIT_RT ::[tt] "s"(t) : "scc", "vcc", "s98", "s99", "memory"); }
__device__ __forceinline__ u32 safe_ref(u32 t) { return t - 0x7ff00000u < 0x110000u ? t - 0x200000u : t; }
template <int BITS>
__device__ __forceinline__ u32 youngest(u32 t);
__device__ __forceinline__ void lds_wait(volatile LAS u32* p, u32 want) {
  u32 spins = 0;
#pragma unroll 1
  while (__builtin_amdgcn_readfirstlane(*p) != want) {
    if (++spins > (SPIN_CAP << 6)) break;
    __builtin_amdgcn_s_sleep(1);
  }
  asm volatile("" ::: "memory");
}
__device__ __forceinline__ u32 lds_wait_nz(volatile LAS u32* p) {
  u32 spins = 0, v;
#pragma unroll 1
  while ((v = __builtin_amdgcn_readfirstlane(*p)) == 0u) {
    if (++spins > (SPIN_CAP << 6)) break;
    __builtin_amdgcn_s_sleep(1);
  }
  asm volatile("" ::: "memory");
  return v;
}
template <int BITS>
__device__ __forceinline__ u32 youngest(u32 t) {
  const u32 nw = rtnow() + 256u;
  const float age = (float)((nw - t) & ((1u << BITS) - 1u));
  return nw - (u32)__builtin_amdgcn_readfirstlane((int)-xrow16_max(max_ror8(max8(-age))));
}
__device__ __forceinline__ void stamp_poll(unsigned char* smem, u32x4 rq, u32 so, u32 usual) {
  u32 sv, budget = SPIN_CAP;
#pragma unroll 1
  for (;;) {
    asm volatile("buffer_load_dword %0, %1, %2, 0 offen sc1\n\ts_waitcnt vmcnt(0)" : "=&v"(sv) : "v"(so), "s"(rq) : "memory");
    if (__builtin_amdgcn_ballot_w64((sv & 1u) == 0u) == 0ull || budget == 0u) break;
    --budget;
    __builtin_amdgcn_s_sleep(1);
  }
  if (__builtin_expect(__builtin_amdgcn_ballot_w64(sv != usual * 2u + 1u) != 0ull, 0)) usual = safe_ref(youngest<24>(sv >> 1));
  *(volatile LAS u32*)(smem + L_ANC) = usual | 1u;
}
__device__ __forceinline__ void lds_add(unsigned char* smem, int off, u32 v) {
  __hip_atomic_fetch_add((LAS u32*)(smem + off), v, __ATOMIC_RELAXED, __HIP_MEMORY_SCOPE_WORKGROUP);
}

__device__ __forceinline__ int fresh(int t) {
  asm volatile("" : "+v"(t));
  return t;
}
__device__ __forceinline__ int lane_id() {
  int t;
  asm volatile("v_mbcnt_lo_u32_b32 %0, -1, 0\n\tv_mbcnt_hi_u32_b32 %0, -1, %0" : "=v"(t));
  return t;
}
__device__ __forceinline__ f32x4 zero4() {
  f32x2 a, b;
  asm volatile("v_mov_b64 %0, 0\n\tv_mov_b64 %1, 0" : "=v"(a), "=v"(b));
  return f32x4{a[0], a[1], b[0], b[1]};
}
__device__ __forceinline__ void bar() { asm volatile("s_waitcnt lgkmcnt(0)\n\ts_barrier" ::: "memory"); }

__device__ __forceinline__ u32x2 trread(unsigned char* smem, int off) {
  return __builtin_bit_cast(u32x2, __builtin_amdgcn_ds_read_tr16_b64_v4i16((LAS s16x4*)(smem + off)));
}
__device__ __forceinline__ u32x4 wfrag(unsigned char* smem, int lb, int off) {
  const u32x2 a = trread(smem, lb + off), b = trread(smem, lb + off + 256);
  return u32x4{a[0], a[1], b[0], b[1]};
}
template <int GS = 512>
__device__ __forceinline__ int wlane(int lane, int cb) {
  const int i = lane & 15, G = lane >> 4;
  return G * GS + (i >> 2) * 64 + (i & 3) * 8 + ((cb ^ (G & 1)) * 32);
}
__device__ __forceinline__ u32x4 afrag(unsigned char* smem, int a) { return *reinterpret_cast<const u32x4*>(smem + a); }
__device__ __forceinline__ f32x4 mfma32(u32x4 w, u32x4 a, f32x4 c) {
  return __builtin_amdgcn_mfma_f32_16x16x32_bf16(__builtin_bit_cast(bf16x8, w), __builtin_bit_cast(bf16x8, a), c, 0, 0, 0);
}

__device__ __forceinline__ void dma_kib(const u16* src, u32 voff, unsigned char* smem, int ldsoff) {
  const u32 dst = (u32)(uintptr_t)(LAS unsigned char*)smem + (u32)ldsoff;
  u32 keep;
  asm volatile("s_mov_b32 %0, m0\n\ts_mov_b32 m0, %2\n\ts_nop 2\n\tglobal_load_lds_dwordx4 %1, %3 nt\n\ts_mov_b32 m0, %0"
               : "=&s"(keep)
               : "v"(voff), "s"(dst), "s"(src)
               : "memory");
}
__device__ __forceinline__ void dma_piece(const u16* src, int ld, unsigned char* smem, int ldsoff, int p, int lane) {
  const int row = 16 * p + (lane >> 2), part = lane & 3;
  const int cb = (part >> 1) ^ (lane >> 5);
  dma_kib(src, (u32)(row * ld + cb * 16 + (part & 1) * 8) * 2u, smem, ldsoff + p * 1024);
}

template <int CTRL>
__device__ __forceinline__ u32 dppu(u32 x) {
  return (u32)__builtin_amdgcn_mov_dpp((int)x, CTRL, 0xf, 0xf, true);
}
__device__ __forceinline__ float sum4(f32x4 d) { return (d[0] + d[1]) + (d[2] + d[3]); }
__device__ __forceinline__ u32 and4(u32x4 v) { return v[0] & v[1] & v[2] & v[3]; }
__device__ __forceinline__ void store16(__amdgpu_buffer_rsrc_t rs, u32 off, u32x4 v) {
  __builtin_amdgcn_raw_buffer_store_b128(v, rs, off, 0, 16);
}
#define POLL16(dst, voff, imm) \
  asm volatile("buffer_load_dwordx4 %0, %1, %2, 0 offen offset:%3 sc1" : "+v"(dst) : "v"(voff), "s"(rq), "n"(imm) : "memory")

struct LnS {
  float mb, ri;
};
__device__ __forceinline__ LnS ln_stats(float s1, float s2) {
#define S2(CTL)                                                     \
  "v_add_f32_dpp %0, %0, %0 " CTL " row_mask:0xf bank_mask:0xf\n\t" \
  "v_add_f32_dpp %1, %1, %1 " CTL " row_mask:0xf bank_mask:0xf\n\ts_nop 0\n\t"
  asm("s_nop 1\n\t" S2("quad_perm:[1,0,3,2]") S2("quad_perm:[2,3,0,1]") S2("row_half_mirror") : "+v"(s1), "+v"(s2));
#undef S2
  const float dm = s1 * (1.0f / D);
  const u32 mv = cvt2(dm, __builtin_fmaxf(fmaf(-dm, dm, s2 * (1.0f / D)), 0.f));
  LnS r;
  r.mb = bflo(mv);
  r.ri = __builtin_amdgcn_rcpf(bf16r(__builtin_amdgcn_sqrtf(bf16r(bfhi(mv) + 1e-5f))));
  return r;
}

struct G80 {
  u32x4 w[6];
  int ra, rb, rc;
};
template <int GS>
__device__ __forceinline__ void g80_w(G80& a, unsigned char* smem, int lane, int wb) {
  const int l0 = wb + wlane<GS>(lane, 0), l1 = wb + wlane<GS>(lane, 1), bk = 8 * GS - (lane >> 5) * 2 * GS;
  a.w[0] = wfrag(smem, l0, 0);
  a.w[1] = wfrag(smem, l1, 0);
  a.w[2] = wfrag(smem, l0, 4 * GS);
  a.w[3] = wfrag(smem, l1, 4 * GS);
  a.w[4] = wfrag(smem, l0 + bk, 0);
  a.w[5] = wfrag(smem, l1 + bk, 0);
}
__device__ __forceinline__ void g80_a(G80& a, int lane, int xs, int land) {
  const int m = lane & 15, G = lane >> 4;
  const bool vec = m == 8 || (m < 8 && land >= 0);
  const int st = vec ? 64 : 0;
  const int sa = vec ? (m == 8 ? xs : land + 256 * m) + 16 * G : (m == 15 ? L_ONE : L_ZERO);
  const int sw = (vec && m < 8) ? (m & 3) * 64 : 0;
  a.ra = fresh(sa ^ sw);
  a.rb = fresh((sa + st) ^ sw);
  a.rc = fresh(G < 2 ? (sa + 2 * st) ^ sw : L_TAG);
}
template <bool WAIT = true>
__device__ __forceinline__ void dma_poll(u32x4 rq, u32 voff, u32 land, u64 ea, u64 eb) {
  u32 keep;
  asm volatile("s_mov_b32 %0, m0\n\ts_mov_b32 m0, %3\n\ts_mov_b64 exec, %4\n\t"
               "buffer_load_dwordx4 %1, %2, 0 offen sc1 lds\n\t"
               "s_mov_b64 exec, %5\n\t"
               "buffer_load_dwordx4 %1, %2, 0 offen offset:1024 sc1 lds\n\t"
               "s_mov_b64 exec, -1\n\ts_mov_b32 m0, %0"
               : "=&s"(keep)
               : "v"(voff), "s"(rq), "s"(land), "s"(ea), "s"(eb)
               : "memory");
  if (WAIT) asm volatile("s_waitcnt vmcnt(0)" ::: "memory");
}
__device__ __forceinline__ void wait_poll(u32 t, u32x4 rq, u32 voff, u32 so, u32 land, u64 e, u64 e2) {
  u32 keep;
  asm volatile("s_mov_b32 %[k], m0\n\ts_mov_b32 m0, %[land]\n\ts_mov_b64 exec, %[e]\n\t" WAIT_RT
               "buffer_load_dwordx4 %[vo], %[rq], %[so] offen sc1 lds\n\ts_mov_b64 exec, %[e2]\n\t"
               "buffer_load_dwordx4 %[vo], %[rq], %[so] offen offset:1024 sc1 lds\n\t"
               "s_mov_b64 exec, -1\n\ts_mov_b32 m0, %[k]"
               : [k] "=&s"(keep)
               : [vo] "v"(voff), [rq] "s"(rq), [so] "s"(so), [land] "s"(land), [e] "s"(e), [e2] "s"(e2), [tt] "s"(t)
               : "memory", "scc", "vcc", "s98", "s99");
}
#define HSC ""
template <bool X4>
__device__ __forceinline__ void ctl_load(u32x4 rq, u32 voff, u32 lds) {
  u32 keep;
  if (X4)
    asm volatile("s_mov_b32 %0, m0\n\ts_mov_b32 m0, %3\n\ts_nop 2\n\tbuffer_load_dwordx4 %1, %2, 0 offen sc1 lds\n\ts_mov_b32 m0, %0"
                 : "=&s"(keep) : "v"(voff), "s"(rq), "s"(lds) : "memory");
  else
    asm volatile("s_mov_b32 %0, m0\n\ts_mov_b32 m0, %3\n\ts_nop 2\n\tbuffer_load_dword %1, %2, 0 offen sc1 lds\n\ts_mov_b32 m0, %0"
                 : "=&s"(keep) : "v"(voff), "s"(rq), "s"(lds) : "memory");
}
__device__ __forceinline__ void hint_load(u32x4 rq, u32 voff, u32 lds) {
  u32 keep;
  asm volatile("s_mov_b32 %0, m0\n\ts_mov_b32 m0, %3\n\ts_nop 2\n\tbuffer_load_dword %1, %2, 0 offen " HSC " lds\n\ts_mov_b32 m0, %0"
               : "=&s"(keep) : "v"(voff), "s"(rq), "s"(lds) : "memory");
}
template <int UN>
__device__ __forceinline__ void retry_masks(u64 need, int lane, u64& ea, u64& eb) {
  u32 m = (u32)need | (u32)(need >> 32);
  m |= m >> 16;
  m = (m | (m >> 8)) & 0xffu;
  const int ln = fresh(lane);
  ea = __builtin_amdgcn_ballot_w64(((m >> (ln / UN)) & 1u) != 0u);
  eb = __builtin_amdgcn_ballot_w64(((m >> ((ln + 64) / UN)) & 1u) != 0u);
}
#define RTI(K, A, B)
#define MF "v_mfma_f32_16x16x32_bf16 "
#define F0 "v[52:55]"
#define F1 "v[56:59]"
#define F2 "v[60:63]"
#define FCLOB "v52", "v53", "v54", "v55", "v56", "v57", "v58", "v59", "v60", "v61", "v62", "v63"
#define AND3(a, b, c) "v_bitop3_b32 %[t], " a ", " b ", " c " bitop3:0x80\n\t"
#define BREP "s_bitreplicate_b64_b32 "
#define USEX "s_and_b32 exec_lo, exec_lo, 0x30ff03ff\n\ts_and_b32 exec_hi, exec_hi, 0xff30ff03\n\t"
constexpr u64 USED = 0xff30ff0330ff03ffull;
#define POLL_HEAD(K, FOLD, MASK)                                                                                      \
  "s_mov_b32 %[bud], 0x10000\n\ts_branch 2f\n"                                                                       \
  "1:\n\ts_sub_u32 %[bud], %[bud], 1\n\ts_cbranch_scc1 3f\n\t" RTI(K, "v48", "%[t]")                                 \
  "s_or_b32 s98, vcc_lo, vcc_hi\n\ts_lshr_b32 s99, s98, 16\n\ts_or_b32 s98, s98, s99\n\t" FOLD                       \
  BREP "s[98:99], s98\n\t" BREP "s[98:99], s98\n\t" BREP "s[98:99], s98\n\t"                                         \
  "s_mov_b32 vcc_lo, m0\n\ts_mov_b32 m0, %[land]\n\t" BREP "exec, s98\n\t" MASK                                      \
  "buffer_load_dwordx4 %[vo], %[rq], %[so] offen sc1 lds\n\t" BREP "exec, s99\n\t" MASK                              \
  "buffer_load_dwordx4 %[vo], %[rq], %[so] offen offset:1024 sc1 lds\n\t"                                            \
  "s_mov_b64 exec, -1\n\ts_mov_b32 m0, vcc_lo\n"                                                                     \
  "2:\n\ts_waitcnt vmcnt(0)\n\t"                                                                                     \
  "ds_read_b128 " F0 ", %[ra]\n\tds_read_b128 " F1 ", %[rb]\n\tds_read_b128 " F2 ", %[rc]\n\t"
#define R0 "v[40:43]"
#define R1 "v[44:47]"
#define R2 "v[48:51]"
#define DCLOB "v40", "v41", "v42", "v43", "v44", "v45", "v46", "v47", "v48", "v49", "v50", "v51"
#define ZD(CTL, BANK) "v_add_f32_dpp v48, v48, v48 " CTL " row_mask:0xf bank_mask:" BANK "\n\t"
#define G80_LOOP(K, FILL)                                                                                            \
  POLL_HEAD(K, "", USEX)                                                                                             \
  "s_waitcnt lgkmcnt(2)\n\t"                                                                                         \
  MF R2 ", " F0 ", " F0 ", 0\n\t" AND3("v52", "v53", "v54")                                                          \
  MF R0 ", " F0 ", %[w0], 0\n\t" MF R1 ", " F0 ", %[w1], 0\n\t"                                                      \
  "s_waitcnt lgkmcnt(1)\n\t"                                                                                         \
  MF R2 ", " F1 ", " F1 ", " R2 "\n\t" AND3("%[t]", "v55", "v56")                                                    \
  MF R0 ", " F1 ", %[w2], " R0 "\n\t" AND3("%[t]", "v57", "v58")                                                     \
  MF R1 ", " F1 ", %[w3], " R1 "\n\t"                                                                                \
  "s_waitcnt lgkmcnt(0)\n\t"                                                                                         \
  MF R2 ", " F2 ", " F2 ", " R2 "\n\t" AND3("%[t]", "v59", "v60") AND3("%[t]", "v61", "v62")                         \
  MF R0 ", " F2 ", %[w4], " R0 "\n\t" AND3("%[t]", "1", "v63")                                                       \
  "v_cmp_eq_u32 vcc, 0, %[t]\n\t"                                                                                    \
  MF R1 ", " F2 ", %[w5], " R1 "\n\t"                                                                                \
  "s_and_b64 vcc, vcc, %[nm]\n\ts_cbranch_scc1 1b\n"                                                                 \
  "3:\n\tv_pk_add_f32 v[48:49], v[48:49], v[50:51]\n\tv_add_f32 v48, v48, v49\n\t" FILL                              \
  "v_pk_add_f32 v[40:41], v[40:41], v[42:43]\n\t" ZD("row_ror:8", "0x1") "v_add_f32 v40, v40, v41\n\t"               \
  "v_pk_add_f32 v[44:45], v[44:45], v[46:47]\n\t" ZD("quad_perm:[1,0,3,2]", "0xf") "v_add_f32 v44, v44, v45\n\t"
#define SD(R, CTL) "v_add_f32_dpp " R ", " R ", " R " " CTL " row_mask:0xf bank_mask:0xf\n\t"
#define PKA(A, B) "v_pk_add_f32 " A ", " A ", " B "\n\t"
#define RND(R, X) "v_cvt_pk_bf16_f32 " R ", 0, " X "\n\t"
#define G80_IN(A)                                                                                                    \
  [ra] "v"(A.ra), [rb] "v"(A.rb), [rc] "v"(A.rc), [w0] "v"(A.w[0]), [w1] "v"(A.w[1]), [w2] "v"(A.w[2]),              \
      [w3] "v"(A.w[3]), [w4] "v"(A.w[4]), [w5] "v"(A.w[5]), [vo] "v"(voff), [so] "s"(so), [rq] "s"(rq), [land] "s"(landa)
__device__ __forceinline__ void g80_run(G80& a, unsigned char* smem, float& r0, float& r1) {
  const u32x4 f0 = afrag(smem, a.ra), f1 = afrag(smem, a.rb), f2 = afrag(smem, a.rc);
  f32x4 d0 = {0.f, 0.f, 0.f, 0.f}, d1 = d0;
  d0 = mfma32(f0, a.w[0], d0);
  d1 = mfma32(f0, a.w[1], d1);
  d0 = mfma32(f1, a.w[2], d0);
  d1 = mfma32(f1, a.w[3], d1);
  d0 = mfma32(f2, a.w[4], d0);
  d1 = mfma32(f2, a.w[5], d1);
  r0 = sum4(d0);
  r1 = sum4(d1);
}
__device__ __forceinline__ f32x4 quad4(float r) { return f32x4{r, dpp<0x55>(r), dpp<0xAA>(r), dpp<0xFF>(r)}; }

__device__ __forceinline__ int q_addr(int d) {
  const int w = d >> 1;
  return L_Q + ((w & 1) * 48 + (w >> 1)) * 4 + (d & 1) * 2;
}

template <bool LDS>
__device__ __forceinline__ void x_update(unsigned char* smem, u32x4 rq, int lane, u32 src, int i) {
  const int l = lane < 40 ? lane : 0;
  const u32 off = src + 4 * l;
  u32 v[8];
  if (LDS) {
#pragma unroll
    for (int k = 0; k < 8; ++k) v[k] = *reinterpret_cast<const u32*>(smem + (off ^ ((k & 3) * 64)) + 256 * k);
  } else {
    u32 budget = SPIN_CAP;
#pragma unroll 1
    for (;;) {
#pragma unroll
      for (int k = 0; k < 8; ++k)
        asm volatile("buffer_load_dword %0, %1, %2, 0 offen offset:%3 sc1" : "=v"(v[k]) : "v"(off), "s"(rq), "n"(256 * k) : "memory");
      asm volatile("s_waitcnt vmcnt(0)"
                   : "+v"(v[0]), "+v"(v[1]), "+v"(v[2]), "+v"(v[3]), "+v"(v[4]), "+v"(v[5]), "+v"(v[6]), "+v"(v[7])::"memory");
      const u32 a = v[0] & v[1] & v[2] & v[3] & v[4] & v[5] & v[6] & v[7] & 1u;
      if (__builtin_amdgcn_ballot_w64(a == 0u) == 0ull || budget == 0u) break;
      --budget;
      __builtin_amdgcn_s_sleep(2);
    }
  }
  float lo = 0.f, hi = 0.f;
#pragma unroll
  for (int k = 0; k < 8; ++k) {
    lo += bflo(v[k]);
    hi += bfhi(v[k]);
  }
  u32* px = reinterpret_cast<u32*>(smem + L_HB + 160 * i + 4 * l);
  const u32 xo = *px, y = cvt2(lo, hi);
  if (lane < 40) *px = cvt2(bflo(xo) + bflo(y), bfhi(xo) + bfhi(y));
}

#define CKL 22
#define CKV 5
#define CW 6
#define CWAIT asm volatile("s_waitcnt vmcnt(%0)" ::"n"(CW) : "memory")
#define QACK 5
#define QP 11
#define BEAT0 (QP + QACK)
#define PRE 50
#define AL_R 3
#define AL_L 8
#define AL_A 11
#define AL_2 5
#define FR 3
#define FW 3
#define FGN 162
#define FGT 25
#define FGA 6
#define FGM 200
#define FGS 2
#define RT_T {13, 18, 22}, {18, 22, 26}, {24, 28, 32}, {29, 33, 37}
#define RT_N 115, 105, 97, 90
constexpr int rt_t[4][3] = {RT_T}, rt_n[4] = {RT_N};
constexpr int rt_at(int n, int k) { return rt_t[0][k] == n ? 0 : (rt_t[1][k] == n ? 1 : (rt_t[2][k] == n ? 2 : (rt_t[3][k] == n ? 3 : -1))); }
#define AF 26
#define MID1 34
#define MD 0
#define PTN 99
#define PTV 8
#define PTX 1
#define PTW 4
#define PTC 6
#define PTS 122816
#define RBM 200
#define RBN 6
#define RBC 5
#define RBK 44
#define RBS 16
#define RBB 0
#define RBX 100
#define RCN -1
#define RCK 64
#define RCC 8
#define AL_OFF 8
#define E_QK 300
#define E_QN 2
#define WBIG 10
#define QI 7
#define NINO 4
#define NINF 0
#define PO0 8
#define POS 10
#define PF0 25
#define PFS 2
#define TFL 220
static_assert(AL_A + 15 >= 24 && QI <= PO0 && PO0 + (NINO - 1) * POS < 42 && PF0 + (NINF - 1) * PFS < 42, "pieces");
#define CAL 10
#define XTAB 12
#define XTB 16
#define NPRE 2
#define FEARLY 1
#define NPF NPRE
#define TPF 220
#define TPV 220
#define XA 15
#define XB 18
#define TS_O (TSTEP * (20 * (48 + NINO) + 12 * (48 + NINF)) / (32 * (48 + NINO)))
#define TS_F (TSTEP * (20 * (48 + NINO) + 12 * (48 + NINF)) / (32 * (48 + NINF)))
#define NP0 3
#define TQ0 660
#define TSQ 136
static_assert(CKL > BEAT0 && QP > QI && 23 - CKL + CKV >= CW && CKV + 1 + CW <= XB - CW && XB - CW <= XA, "window");
#define PACE_ASM                                                                                                     \
  "1:\n\ts_memrealtime s[98:99]\n\ts_waitcnt lgkmcnt(0)\n\ts_lshl_b32 s98, s98, 3\n\ts_sub_i32 s98, s98, %[tn]\n\t"  \
  "s_cmp_lt_i32 s98, 0\n\ts_cbranch_scc0 2f\n\ts_sleep 1\n\ts_branch 1b\n2:\n\ts_add_i32 %[tn], %[tn], %[ts]\n\t"
__device__ __forceinline__ void pace(u32& tnext, u32 tstep) {
  asm volatile(PACE_ASM : [tn] "+s"(tnext) : [ts] "s"(tstep) : "scc", "s98", "s99");
}
constexpr int kreg(int i) { return i % 3 < 2 ? 64 + 8 * (i / 3) + 4 * (i % 3) : 128 + 4 * (i / 3); }
constexpr int vreg(int i) { return 160 + 24 * (i / 6) + 8 * (i % 3) + 4 * ((i / 3) & 1); }
#define LOADW(reg, voff, sbase, imm)                                                                                \
  asm volatile(PACE_ASM "global_load_dwordx4 v[%[r0]:%[r1]], %[p], %[sb] offset:%[im] nt\n\ts_waitcnt vmcnt(%[w])"  \
               : [tn] "+s"(tnext)                                                                                   \
               : [r0] "n"(reg), [r1] "n"((reg) + 3), [p] "v"(voff), [sb] "s"(sbase), [im] "n"(imm), [ts] "s"(tstep), \
                 [w] "n"(WBIG - 1)                                                                                  \
               : "scc", "s98", "s99")

#define HS_WAIT(COND)                                                                                \
  _Pragma("unroll 1") for (;;) {                                                                     \
    gd = CTL[512 + lane0] - CTL[576 + lane0];                                                        \
    const u64 ch = __builtin_amdgcn_ballot_w64(gd != 0u) & 0x1ffull;                                 \
    if (ch > 0xffull || (COND) || tmo == 0u) break;                                                  \
    if ((tmo & 3u) == 0u && lane0 == 0)                                                              \
      __builtin_amdgcn_raw_buffer_store_b32(hb, rs, WS_HB + h * 128 + c * 4, 0, 16);                 \
    hb += 128u;                                                                                      \
    --tmo;                                                                                           \
    __builtin_amdgcn_s_sleep(3);                                                                     \
    ctl_load<false>(rq, hso, (u32)(smb + L_CTL + 2048));                                             \
    asm volatile("s_waitcnt vmcnt(0)" ::: "memory");                                                 \
  }

struct QKAddr {
  int qad[6], idxq, wa;
  f32x4 D0, D1;
};
struct PVAddr {
  int ra, idx, oa, sa, la, ma;
  u32 dst;
  f32x4 O0, O1, O2;
  u32x4 A0, A1, A2, A3;
};
__device__ __forceinline__ void att_table(unsigned char* smem, int lane, int wave, u32 go) {
  const int n = lane & 15, G = lane >> 4;
  const int seg = n & 1, pr = (n >> 1) & 1, tq = n >> 2, xy = G >> 1;
  const int qa = L_Q + (pr * 48 + seg * 8 + (G & 1) * 4) * 4;
  u32 t[16];
#pragma unroll
  for (int k = 0; k < 4; ++k) t[k] = tq == k ? qa + xy * 64 : L_ZERO;
#pragma unroll
  for (int k = 0; k < 2; ++k) t[4 + k] = tq == 2 * k + xy ? qa + 128 : L_ZERO;
  t[6] = 0x4444 + pr * 0xAAAA;
  t[7] = L_P + wave * P_W + ((n >> 1) & 1) * P_PAR + ((((G & 1) * 2 + (n >> 3)) * 4 + (n & 1) * 2 + (G >> 1)) * 16) + ((n >> 2) & 1) * 8;
  t[8] = L_P + wave * P_W + ((n >> 2) & 1) * P_U + (n >> 3) * P_PAR + G * 64;
  t[9] = (n & 3) * 0x5555;
  t[10] = L_OM + (wave * OM_S + (n >> 3) * OM_P + (n & 7) * 4 + (G & 1) * 32) * 4;
  t[11] = L_OM + (wave * OM_S + DH + G) * 4;
  const int src = wave < 6 ? lane >> 3 : lane & 7;
  t[12] = L_OM + (src * OM_S + (wave < 6 ? (wave >> 1) * 64 + (lane & 1) * 32 + (wave & 1) * 16 + (lane & 6) * 2 : DH)) * 4;
  t[13] = L_OM + (src * OM_S + DH + 4) * 4;
  t[14] = lane >= (wave < 6 ? 16 : (wave == 6 ? 2 : 0)) ? 0x8000u
          : go + (wave < 6 ? (wave >= 3 ? 2048 - 192 : 0) + wave * 64 + (lane & 7) * 8 + (lane >> 3) * 4 : lane * 2048 + 192);
  t[15] = wave < 6 ? q_addr(wave * 32 + (lane & 7) * 4 + (lane >> 4)) : (wave == 6 ? L_RI : L_ACT);
#pragma unroll
  for (int k = 0; k < 2; ++k)
    *reinterpret_cast<u32x4*>(smem + L_TAB + wave * 2048 + k * 1024 + lane * 16) =
        u32x4{t[8 * k] | (t[8 * k + 4] << 16), t[8 * k + 1] | (t[8 * k + 5] << 16), t[8 * k + 2] | (t[8 * k + 6] << 16),
              t[8 * k + 3] | (t[8 * k + 7] << 16)};
}
__device__ __forceinline__ void chain_table(unsigned char* smem, int lane, int g, int j, int c) {
  const int n = lane & 15, G = lane >> 4;
  const u32 lv = (lane * 16) ^ (G * 64);
  const u32 r0 = (n & 7) * 256 + (n & 3) * 64 + G * 16;
  *reinterpret_cast<u32x4*>(smem + L_TB + lane * 16) =
      u32x4{lv, (n < 8 ? r0 : (r0 & ~63u) ^ 192u) | ((r0 ^ 64u) << 16), (u32)wlane(lane, 0) | ((L_RED + (lane & 8) * 80 + (lane & 7) * 16) << 16),
            gy_rep(lane >> 3) + gy_chunk(4 * j + ((lane >> 1) & 3)) + g * 256 + (lane & 1) * 8};
  const u32 xs = n == 8 ? L_HB + 160 * c + 16 * G : (n == 15 ? L_ONE : L_ZERO), st = n == 8 ? 64 : 0;
  const u32 l0 = wlane<1024>(lane, 0), r1 = L_LAND + 5120 + r0;
  *reinterpret_cast<u32x4*>(smem + L_TB + 1024 + lane * 16) =
      u32x4{lv | (((G & 1) * 8192 + (G >> 1) * 64 + n * 4) << 16), l0 | ((l0 - (lane >> 5) * 2048) << 16),
            n < 8 ? r1 | ((r1 ^ 64u) << 16) : xs | ((xs + st) << 16),
            (G < 2 ? (n < 8 ? r1 ^ 128u : xs + 2 * st) : L_TAG) | (((lane >> 3) * 896 + (lane & 7) * 16) << 16)};
}
__device__ __forceinline__ void att_setup(QKAddr& r, PVAddr& v, unsigned char* smem, int lane, int wave, int& qw) {
  const u32x4 a = afrag(smem, L_TAB + wave * 2048 + lane * 16), b = afrag(smem, L_TAB + wave * 2048 + 1024 + lane * 16);
  r.D0 = zero4();
  r.D1 = zero4();
  qw = fresh(b[3] >> 16);
#pragma unroll
  for (int k = 0; k < 4; ++k) r.qad[k] = fresh(a[k] & 0xffffu);
  r.qad[4] = fresh(a[0] >> 16);
  r.qad[5] = fresh(a[1] >> 16);
  r.idxq = fresh(a[2] >> 16);
  r.wa = fresh(a[3] >> 16);
  v.ra = fresh(b[0] & 0xffffu);
  v.idx = fresh(b[1] & 0xffffu);
  v.oa = fresh(b[2] & 0xffffu);
  v.la = fresh(b[3] & 0xffffu);
  v.sa = fresh(b[0] >> 16);
  v.ma = fresh(b[1] >> 16);
  v.dst = fresh((int)b[2] >> 16);
  asm volatile("" : "+v"(r.D0), "+v"(r.D1));
}
#define SM "v_smfmac_f32_16x16x64_bf16 "
#define P_WRITE_READ(QA)                                                                                              \
  asm volatile("ds_write2_b32 %[wa], %[p], %[p] offset1:%[o1]\n\t"                                                    \
               "ds_read_b128 %[a0], %[ra]\n\tds_read_b128 %[a1], %[ra] offset:16\n\t"                                 \
               "ds_read_b128 %[a2], %[ra] offset:32\n\tds_read_b128 %[a3], %[ra] offset:48"                           \
               : [a0] "=&v"(pv.A0), [a1] "=&v"(pv.A1), [a2] "=&v"(pv.A2), [a3] "=&v"(pv.A3), "+v"(sk)                 \
               : [wa] "v"(QA.wa + smb), [p] "v"(pb), [ra] "v"(pv.ra + smb), [o1] "n"(P_U / 4 + 1)        \
               : "memory");
#define QS(D, A, K) SM "v[" #D ":" #D "+3], v[" #A ":" #A "+3], v[" #K ":" #K "+7], %[ix]\n\t"
#define RDS "ds_read_b32 %[s], %[z] offset:%[ri]\n\t"
#define LW(N) "s_waitcnt lgkmcnt(" #N ")\n\t"
#define QK_BODY                                                                                                       \
  "ds_read_b128 v[40:43], %[q0]\n\tds_read_b128 v[44:47], %[q1]\n\tds_read_b128 v[48:51], %[q2]\n\t"                   \
  "ds_read_b128 v[52:55], %[q3]\n\tds_read_b128 v[56:59], %[q4]\n\tds_read_b128 v[60:63], %[q5]\n\t"                   \
  LW(5) QS(32, 40, 64) QS(36, 40, 96) LW(4) QS(32, 44, 72) QS(36, 44, 104) LW(3) QS(32, 48, 80) QS(36, 48, 112)       \
  LW(2) QS(32, 52, 88) QS(36, 52, 120) LW(0) QS(32, 56, 128) QS(36, 56, 144) RDS QS(32, 60, 136) QS(36, 60, 152)
#define QK_IN(QA) [q0] "v"(QA.qad[0]), [q1] "v"(QA.qad[1]), [q2] "v"(QA.qad[2]), [q3] "v"(QA.qad[3]), [q4] "v"(QA.qad[4]), [q5] "v"(QA.qad[5]),
#define UNSAFE(X) ((__builtin_amdgcn_ballot_w64(!(X <= 24.f)) != 0ull) | (__builtin_amdgcn_ballot_w64(X >= -24.f) == 0ull))
#define QK_SOFTMAX(QA)                                                                                                \
  {                                                                                                                   \
    float scl, raw, sk;                                                                                               \
    u32 pb;                                                                                                           \
                                                                      \
    asm volatile(QK_BODY                                                                                              \
                 "v_mov_b64 v[52:53], 0\n\tv_mov_b64 v[54:55], 0\n\tv_mov_b64 v[56:57], 0\n\tv_mov_b64 v[58:59], 0\n\t" \
                 "v_mov_b64 v[60:61], 0\n\tv_mov_b64 v[62:63], 0\n\ts_waitcnt lgkmcnt(0)\n\t"                         \
                 PKA("v[32:33]", "v[34:35]") PKA("v[36:37]", "v[38:39]") "s_nop 0\n\t"                                \
                 "v_add_f32_dpp v32, v33, v32 quad_perm:[1,0,3,2] row_mask:0xf bank_mask:0xf\n\t"                     \
                 "v_add_f32_dpp v36, v36, v37 quad_perm:[1,0,3,2] row_mask:0xf bank_mask:0xf\n\t"                     \
                 "v_cndmask_b32 %[raw], v32, v36, %[od]\n\tv_mul_f32 %[sk], %[s], %[raw]\n\tv_exp_f32 %[p], %[sk]\n\t" \
                 "s_nop 0\n\tv_cvt_pk_bf16_f32 %[pb], %[p], 0"                                                        \
                 : "+{v[32:35]}"(QA.D0), "+{v[36:39]}"(QA.D1), [s] "=&v"(scl), [raw] "=&v"(raw), [sk] "=&v"(sk),      \
                   [p] "=&v"(pend), [pb] "=&v"(pb), "=&{v[52:55]}"(pv.O0), "=&{v[56:59]}"(pv.O1), "=&{v[60:63]}"(pv.O2) \
                 : QK_IN(QA) [ix] "v"(QA.idxq), [z] "v"(smb), [ri] "n"(L_RI),                                        \
                   [od] "s"(0xaaaaaaaaaaaaaaaaull), "{v[64:95]}"(kv0), "{v[96:127]}"(kv1), "{v[128:159]}"(kv2)        \
                 : "memory", DCLOB);                                                                                  \
    pmax = 0.f;                                                                                                       \
    P_WRITE_READ(QA)                                                                                                  \
    if (__builtin_expect(UNSAFE(sk) | (__builtin_amdgcn_ballot_w64(__float_as_uint(scl) == 0u) != 0ull), 0)) {        \
      sk = raw * __uint_as_float(lds_wait_nz((volatile LAS u32*)(smem + L_RI)));                                      \
      if (UNSAFE(sk)) pmax = xrow16_max(max_ror8(max8(sk)));                                                          \
      pend = __builtin_amdgcn_exp2f(sk - pmax);                                                                       \
      pb = cvt2(pend, 0.f);                                                                                           \
      asm volatile("s_waitcnt lgkmcnt(0)" : "+v"(pv.A0), "+v"(pv.A1), "+v"(pv.A2), "+v"(pv.A3));                      \
      P_WRITE_READ(QA)                                                                                                \
      asm volatile("s_waitcnt lgkmcnt(0)" : "+v"(pv.A0), "+v"(pv.A1), "+v"(pv.A2), "+v"(pv.A3));                      \
    }                                                                                                                 \
  }

using u32x16 = __attribute__((ext_vector_type(16))) unsigned;
using u32x32 = __attribute__((ext_vector_type(32))) unsigned;
template <int OFF>
__device__ __forceinline__ u16* karg(u64 kp) {
  u64 p;
  asm volatile("s_load_dwordx2 %0, %1, %2\n\ts_waitcnt lgkmcnt(0)" : "=s"(p) : "s"(kp), "n"(OFF));
  return (u16*)p;
}
template <class T>
__device__ __forceinline__ T* mkptr(u32 lo, u32 hi) {
  return (T*)(((u64)hi << 32) | lo);
}

template <int I>
struct IC {
  static constexpr int value = I;
};
template <int A, int B, class Fn>
__device__ __forceinline__ void sfor(Fn&& f) {
  if constexpr (A < B) {
    f(IC<A>{});
    sfor<A + 1, B>(f);
  }
}

__global__ __launch_bounds__(NT, 2) void mega_kernel(Params) {
  __shared__ __attribute__((aligned(16))) unsigned char smem[L_TOTAL];
  u64 kp, ts0;
  u32x16 ka;
  int b;
  u32 wqo, wql;
#define WQL "global_load_lds_dwordx4 %[wqo], s[10:11] nt\n\ts_addk_i32 m0, 0x800\n\tv_add_u32 %[wqo], s22, %[wqo]\n\t"
  asm volatile(
               "s_load_dwordx16 s[4:19], s[0:1], 0x0\n\t"
               "s_getpc_b64 s[20:21]\n.Lmk_pc:\n\t"
               "s_mov_b32 m0, %[dst]\n\t"
               "s_bfe_u32 s3, s2, 0x30003\n\ts_lshl_b32 s3, s3, 7\n\tv_lshl_add_u32 v1, v0, 10, s3\n\t"
               "v_min_u32 v1, .Lmk_code_end-.Lmk_pc-4, v1\n\t"
               "global_load_lds_dword v1, s[20:21]\n\t"
               "v_readfirstlane_b32 s3, v0\n\tv_bfe_u32 v1, v0, 2, 3\n\tv_lshrrev_b32 v2, 3, v0\n\tv_and_or_b32 v1, v2, 8, v1\n\t"
               "s_lshr_b32 s20, s2, 5\n\ts_mulk_i32 s20, 80\n\tv_add_u32 v1, s20, v1\n\tv_mul_u32_u24 v1, %[row], v1\n\t"
               "v_lshlrev_b32 v2, 4, v0\n\tv_and_b32 v2, 48, v2\n\tv_lshrrev_b32 v3, 1, v0\n\tv_and_b32 v3, 32, v3\n\t"
               "v_xor_b32 v2, v2, v3\n\tv_lshlrev_b32 v3, 1, v0\n\tv_and_or_b32 v2, v3, 64, v2\n\t"
               "s_and_b32 s20, s2, 7\n\ts_bfe_u32 s21, s2, 0x20003\n\ts_lshl2_add_u32 s23, s20, s21\n\t"
               "s_lshr_b32 s21, s3, 7\n\ts_mul_i32 s20, s23, 3\n\ts_add_u32 s20, s20, s21\n\ts_lshl_b32 s20, s20, 7\n\t"
               "v_add3_u32 %[wqo], v1, v2, s20\n\t"
               "s_mulk_i32 s21, 10240\n\ts_and_b32 s20, s3, 64\n\ts_lshl4_add_u32 s20, s20, s21\n\ts_add_u32 m0, s20, %[lwq]\n\t"
               "s_mov_b32 s22, %[pst]\n\ts_cmpk_lt_u32 s3, 0x180\n\t"
               "s_waitcnt lgkmcnt(0)\n\ts_memrealtime %[ts]\n\ts_cbranch_scc0 1f\n\t"
               ".rept %[np]\n\t" WQL ".endr\n\t"
               "s_mov_b32 %[wql], m0\n\ts_branch 3f\n"
               "1:\n\tv_and_b32 v1, 63, v0\n\ts_movk_i32 m0, %[lx]\n\tv_lshlrev_b32 v2, 4, v1\n\tv_lshlrev_b32 v1, 2, v1\n\t"
               "s_bitcmp1_b32 s3, 6\n\ts_cbranch_scc1 2f\n\t"
               "global_load_lds_dwordx4 v2, s[4:5]\n\tglobal_load_lds_dword v1, s[4:5] offset:1024\n"
               "2:\n\ts_cselect_b32 s22, 0, 0x200\n\ts_add_u32 m0, s22, %[lc]\n\t"
               "s_add_u32 s20, s12, 0x1fffff\n\ts_addc_u32 s21, s13, 0\n\ts_and_b32 s20, s20, 0xffe00000\n\ts_or_b32 s20, s20, %[gof]\n\t"
               "v_cmp_gt_u32 vcc, 32, v1\n\ts_addk_i32 s23, 32\n\tv_lshl_add_u32 v1, s23, 7, v1\n\t"
               "s_and_b32 s22, s2, 7\n\ts_lshl_b32 s22, s22, 7\n\tv_mov_b32 v2, s22\n\tv_cndmask_b32 v1, v2, v1, vcc\n\t"
               "global_load_lds_dword v1, s[20:21] sc1\n"
               "3:\n\t"
               "v_lshrrev_b32 v1, 2, v0\n\tv_cmp_eq_u32 vcc, 3, v1\n\tv_mov_b32 v3, 0x3f803f80\n\ts_nop 0\n\t"
               "v_cndmask_b32 v2, 0, 1, vcc\n\tv_cmp_eq_u32 vcc, 2, v1\n\tv_lshlrev_b32 v1, 2, v0\n\ts_nop 0\n\t"
               "v_cndmask_b32 v2, v2, v3, vcc\n\tv_cmp_eq_u32 vcc, 7, v0\n\tv_mov_b32 v3, %[scl]\n\t"
               "v_min_u32 v1, 100, v1\n\tv_cndmask_b32 v2, v2, v3, vcc\n\t"
               "ds_write_b32 v1, v2 offset:%[sm]\n\t"
               "s_waitcnt lgkmcnt(0)\n\ts_barrier"
               : "={s[0:1]}"(kp), "={s2}"(b), "={s[4:19]}"(ka), [wqo] "=&v"(wqo), [wql] "=&s"(wql), [ts] "=&s"(ts0)
               : "{v0}"(threadIdx.x), [dst] "n"(L_PF), [scl] "n"(__builtin_bit_cast(u32, SCL)), [sm] "n"(0), [row] "n"(H * DH * 2),
                 [lwq] "n"(L_WQ), [pst] "n"(16 * H * DH * 2), [np] "n"(NP0), [lx] "n"(L_HB), [lc] "n"(L_CTL + 2304),
                 [gof] "n"(WS_GOF)
               : "memory", "vcc", "scc", "v1", "v2", "v3", "s3", "s20", "s21", "s22", "s23");
  static_assert(WS_HB == WS_GOF + 32 * 128, "layout");
  const u32 tstart = (u32)ts0;
  const int tid = threadIdx.x, lane0 = tid & 63;
  const int wave = __builtin_amdgcn_readfirstlane(tid >> 6);
  const int g = b & 7, j = b >> 3, hsub = j & 3, c = j >> 2, h = g * 4 + hsub;
  const bool isO = j < 20;
  const int jo = j < 10 ? j + 10 : j - 10;
  const bool leader = b == 0 && wave == 6;
  const u32 fslot = (u32)((j & 1) * 768 + ((j - 20) >> 1) * 128);
  u32 tmo = SPIN_CAP;
  volatile LAS u32* cnt = (volatile LAS u32*)(smem + L_CNT);
  Params P;
  P.x0 = mkptr<const u16>(ka[0], ka[1]);
  P.kc = mkptr<const u16>(ka[2], ka[3]);
  P.vc = mkptr<const u16>(ka[4], ka[5]);
  P.wq = mkptr<const u16>(ka[6], ka[7]);
  P.ws = mkptr<u32>(ka[8], ka[9]);
  P.w1 = mkptr<const u16>(ka[10], ka[11]);
  P.w3 = mkptr<const u16>(ka[12], ka[13]);
  P.wo = mkptr<const u16>(ka[14], ka[15]);
  u32* const wsb = (u32*)(((uintptr_t)P.ws + 0x1fffffu) & ~(uintptr_t)0x1fffffu);
  const __amdgpu_buffer_rsrc_t rs = __builtin_amdgcn_make_buffer_rsrc(wsb, 0, (int)WS_END, 0x00020000);
  const u32x4 rq = {(u32)(uintptr_t)wsb, (u32)((uintptr_t)wsb >> 32) & 0xffffu, WS_END, 0x00020000u};

  u32 anc = 0u, tsh = 0u, tsc = 0u;
  u32 tref = 0u, ar = 0u;
  u32x32 kv0, kv1, kv2, kv3, kv4, kv5;
  asm volatile(""
               : "={v[64:95]}"(kv0), "={v[96:127]}"(kv1), "={v[128:159]}"(kv2), "={v[160:191]}"(kv3), "={v[192:223]}"(kv4),
                 "={v[224:255]}"(kv5));
  const int smb = (int)(uintptr_t)(LAS unsigned char*)smem;
  const u32 gop = A_GO + go_slot(hsub * 2) + c * 256;
  u32 tnext = tstart * 8u + (u32)wave * (TSTEP / 8) + TQ0;
  const size_t rowbase = (size_t)h * SK + (SK - WIN) + (wave * 8 + c) * 8;
  const u16* vb = P.vc + rowbase * DH;
  const u16* ws0 = isO ? P.wo + (size_t)(768 * g) * D + 32 * jo : ((j & 1) ? P.w3 : P.w1) + 192 * g + 32 * ((j - 20) >> 1);
  const u16* w2p;
  const bool sO = isO || ((u32)(uintptr_t)ws0 & 0x380u) == 0x180u;
  u32 tstep = sO ? TS_O : TS_F;
  int pn = isO ? wave * 6 : wave * 5;
  {
    volatile LAS u32* const CTL = (volatile LAS u32*)(smem + L_CTL);
    u32 gd, hb = 0u, ck0 = 0u;
#define FT(k, d) (d)
    const u32 lho = WS_HB + (lane0 >> 1) * 128 + (lane0 & 1) * 16;
    const u32 hso = lane0 < 8 ? WS_HB + (u32)h * 128 + lane0 * 4 : WS_GOF + g * 128;
    if (wave >= 6) {
      if (b < 8 && wave == 7 && lane0 < 32)
        __builtin_amdgcn_raw_buffer_store_b32(tstart, rs, WS_GOF + 2048 + b * 128 + lane0 * 4, 0, 16);
      if (leader) {
        ctl_load<true>(rq, lho, (u32)(smb + L_CTL));
        ctl_load<false>(rq, WS_GOF + (lane0 & 7) * 128, (u32)(smb + L_CTL + 2560));
      }
    }
    if (tid == 0) *(volatile LAS u32*)(smem + L_ANC) = 0u;
    *reinterpret_cast<u32x4*>(smem + L_P + wave * P_W + lane0 * 16) = u32x4{0u, 0u, 0u, 0u};
    *reinterpret_cast<u32x4*>(smem + (lane0 < 16 ? L_P + wave * P_W + 1024 + lane0 * 16 : L_OM + (wave * OM_S + OM_P + DH) * 4)) =
        u32x4{0u, 0u, 0u, 0u};
    const u16* kb = P.kc + rowbase * DH;
    const u32 ko = ((lane0 & 15) >> 1) * 384 + 16 * ((lane0 & 1) * 4 + (lane0 >> 4)), vo = (lane0 >> 3) * 384 + 16 * (lane0 & 7);
    const u32 pre = wave < 6 ? PRE : 0;
    tstep -= pre;
    asm volatile("" ::"s"(kb + (h & 7) * 512 * DH), "s"(kb), "s"(vb), "s"(ws0), "v"(ko), "v"(vo), "s"(tnext), "s"(tstep), "s"(pn) : "memory");
    if (wave < 6) {
      u32 tq = tnext - (5 - NP0) * TSQ;
#pragma unroll 1
      for (int p = NP0; p < 5; ++p) {
        pace(tq, TSQ);
        dma_kib(P.wq, wqo, smem, wql - smb);
        wqo += 16 * H * DH * 2;
        wql += 2048;
      }
    } else {
      u32 rtt;
      {
        u32 pa = 0u, pb = 0u;
        u64 t1, t2;
        const u32 pbo = WS_PB + (u32)(wave & 1) * (SIDE + 128u) + (u32)((b >> 3) & 7) * 16384u + (u32)((b & 7) * 4 + (b >> 6)) * 256u;
        asm volatile("s_load_dwordx2 %2, %7, 0x40\n\ts_waitcnt vmcnt(0) lgkmcnt(0)\n\t.p2align 6\n\ts_mov_b64 exec, 1\n\t"
                     "buffer_load_dword %0, off, %4, %5 sc1 nt\n\tbuffer_load_dword %1, off, %4, %6 sc1 nt\n\t"
                     "s_mov_b64 exec, -1\n\t"
                     "s_waitcnt vmcnt(1)\n\ts_memtime %2\n\ts_waitcnt vmcnt(0)\n\ts_memtime %3\n\ts_waitcnt lgkmcnt(0)"
                     : "+v"(pa), "+v"(pb), "=&s"(t1), "=&s"(t2)
                     : "s"(rq), "s"(pbo), "s"(pbo ^ SIDE), "s"(kp)
                     : "memory");
        rtt = __builtin_amdgcn_readfirstlane((u32)t2 - (u32)t1) + 1u;
      }
      if (wave == 6) {
        *(volatile LAS u32*)(smem + L_VOTE) = rtt;
      } else {
        const u32 ca = gy_rep(g) + fresh(lane0) * 16;
        store16(rs, ca + (u32)(j >> 3) * 16384u + (u32)(j & 7) * 1024u, u32x4{0u, 0u, 0u, 0u});
        if (lane0 < 8) store16(rs, ca + A_GH + (u32)j * 128u, u32x4{0u, 0u, 0u, 0u});
        hb = rtt;
        ck0 = 24u * rtnow() - now();
      }
    }
#define WPO(n) ((n) >= PO0 && ((n) - PO0) % POS == 0 && ((n) - PO0) / POS < NINO)
#define WPF(n) ((n) >= PF0 && ((n) - PF0) % PFS == 0 && ((n) - PF0) / PFS < NINF)
#define WPIECE(n)                                                                                                   \
  if ((WPO(n) && sO) || (WPF(n) && !sO)) {                                                                          \
    pace(tnext, tstep);                                                                                             \
    if ((n) != PO0 || isO) dma_piece(ws0, isO ? D : F, smem, L_WB, pn++, lane0);                             \
  }
#define REPORT(n)                                                                                                   \
  if (rt_at(n, 0) >= 0 && b < 8 && wave < 6) {                                                                      \
    constexpr int r = rt_at(n, 0);                                                                                  \
    u32 q;                                                                                                          \
    asm volatile("s_getreg_b32 %0, hwreg(HW_REG_IB_STS, 0, 4)" : "=s"(q));                                          \
    const u32 bl = rtnow() * 8u - tnext + (q + 1u) * tstep + tsh - RBM * 8u;                                        \
    const int ln = lane_id();                                                                                       \
    if (ln == 0) lds_add(smem, L_VOTE + 20 + 4 * r, bl);                                                            \
    if (wave == 5) {                                                                                                \
      const u32x2 o = *(volatile LAS u32x2*)(smem + L_VOTE + 12);                                                   \
      const int m = RBM + ((*(volatile LAS int*)(smem + L_VOTE + 20 + 4 * r)) * 1365 >> 16);                    \
      __builtin_amdgcn_raw_buffer_store_b32(((r ? o[0] : tstart) << 10) + (u32)(m < 1 ? 1 : (m < 1023 ? m : 1023)), rs, \
                                            (ln & 31) * 4, WS_RP + r * 1024 + b * 128, 16);                         \
    }                                                                                                               \
  }
#define RPLOAD(r) hint_load(rq, WS_RP + r * 1024 + (lane_id() & 7) * 128, (u32)(smb + L_CTL + 3072))
#define RPLOG(r)
#define RPVAL(r, BETA, N)                                                                                           \
  const u32 w = *(volatile LAS u32*)(smem + L_CTL + 3072 + lane_id() * 4);                                          \
  const u32x2 o = *(volatile LAS u32x2*)(smem + L_VOTE + 12);                                                       \
  const u32 dg = (w - (((r ? o[0] : tstart) - 512u) << 10)) >> 10;                                                  \
  const bool ok = dg < 1024u;                                                                                       \
  const float v = ok ? (float)(w & 1023u) : 0.f;                                                                    \
  const int m = (int)(sum8(v) * __builtin_amdgcn_rcpf(sum8(ok ? 0.125f : 1e-6f))), mx = (int)max8(v);               \
  RPLOG(r)                                                                                                          \
  const int e = __builtin_amdgcn_readfirstlane(m + ((8 * mx - m) * (BETA) >> 4) - (int)o[1]) - 8 * (rt_n[r] + (N));
#define MDV "n"(MD)
#define MDW
#define RLEFT(n) (47 - (n) + NINO - ((n) - PO0 + POS - 1) / POS)
#define LOOK(n)                                                                                                     \
  if (rt_at(n, 1) >= 0 && wave == 6) RPLOAD(rt_at(n, 1));                                                           \
  if (n > 0 && rt_at(n, 2) >= 0 && wave == 6) {                                                                              \
    constexpr int r = rt_at(n, 2) < 0 ? 0 : rt_at(n, 2);                                                            \
    asm volatile("s_waitcnt vmcnt(%0)" ::"n"(rt_t[r][2] - rt_t[r][1]) : "memory");                                  \
    RPVAL(r, RBB, (r == 3 ? FT(5, RCN) : FT(0, RBN)))                                                        \
    MDW                                                                                                             \
    int p = r == 3 ? (e * FT(6, RCK) >> 4) + 8 * FT(7, RCC) : (e * FT(2, RBK) >> 4) + 8 * FT(1, RBC);               \
    p = e <= 0 ? 0 : (p < 8 * RBX ? p : 8 * RBX);                                                                   \
    const u32 gn = o[0] + ((p + (p * FT(3, RBS) * RLEFT(n) >> 9)) >> 3);                                            \
    *(volatile LAS u32x2*)(smem + L_VOTE + 12) = u32x2{gn, o[1] + p};                                               \
    if (r == 3 && b < 8) __builtin_amdgcn_raw_buffer_store_b32(gn, rs, (lane_id() & 31) * 4, WS_RP + 4096 + b * 128, 16); \
  }
#define AP_AT(n) (((n) > AL_L + AL_2 && (n) < rt_t[0][2] && ((n) - AL_L - AL_2) % FGS == 1) || (n) == AF || (n) == MID1 ||     \
                  rt_at((n) - 1, 2) >= 0 || rt_at((n) - 2, 2) >= 0 || (n) == 24 + XB)
#define APPLY(n)                                                                                                    \
  if (AP_AT(n) && (n < 24 + XA - E_QN || n >= 24 + XA || wave >= 4)) {                                                                                          \
    u32 t0, t1;                                                                                                     \
    asm volatile("ds_read_b32 %[t0], %[z] offset:%[o]\n\tds_read_b32 %[t1], %[z] offset:%[o]+4\n\ts_mul_i32 s98, %[wv], %[sg]\n\ts_waitcnt lgkmcnt(0)\n\t"  \
                 "v_readfirstlane_b32 s99, %[t0]\n\tv_readfirstlane_b32 vcc_lo, %[t1]\n\ts_lshl3_add_u32 s99, s99, s98\n\t" \
                 "s_sub_u32 s98, vcc_lo, %[sh]\n\t.if %[c]\n\ts_cmp_eq_u32 s98, 0\n\ts_cbranch_scc1 1f\n\t.endif\n\t"        \
                 "s_add_u32 %[tn], %[tn], s98\n\ts_mov_b32 %[sh], vcc_lo\n\ts_sub_u32 s99, s99, %[tn]\n\t"               \
                 "s_mul_i32 s99, s99, %[cr]\n\ts_lshr_b32 s99, s99, 16\n\t.if %[mi]\n\ts_sub_u32 s99, s99, %[md]\n\t.endif\n\ts_sub_u32 s98, s99, 0xa0\n\t"                    \
                 "s_cmpk_lt_u32 s98, 0x140\n\ts_cselect_b32 %[ts], s99, %[ts]\n1:"                                       \
                 : [t0] "=&v"(t0), [t1] "=&v"(t1), [tn] "+s"(tnext), [sh] "+s"(tsh), [ts] "+s"(tstep)                        \
                 : [z] "v"(smb), [o] "n"(L_VOTE + 12), [wv] "s"(wave), [sg] "n"(TSTEP / 8), [c] "n"(n != AF && n != MID1), \
                   [mi] "n"(n >= AF && n < MID1), [md] MDV,                                                           \
                   [cr] "s"(sO ? 65536 / (n < 47 ? RLEFT(n) : 1) + 2 : 65536 / (n < 47 ? 47 - (n) : 1) + 2)          \
                 : "memory", "scc", "vcc", "s98", "s99");                                                           \
  }
#define ALLD ctl_load<false>
    sfor<0, 24>([&](auto ic) __attribute__((always_inline)) {
      constexpr int i = ic.value;
      LOADW(kreg(i), ko, kb + ((i / 3 + h) & 7) * 512 * DH, 128 * (i % 3));
      if (i == QI) {
        tstep += pre;
        tnext += (QI + 1) * pre;
      }
      REPORT(i)
      APPLY(i)
      WPIECE(i)
      if (i == PTN && wave == PTW && c == PTC) {
        const u16* p0 = hsub < 2 ? (hsub ? P.w3 : P.w1) : (hsub == 2 ? karg<64>(kp) + (size_t)(192 * g) * D : P.wo + (size_t)(768 * g) * D);
        asm volatile(".if %[nv]\n\ts_mov_b32 s99, (%[nv] - 1) * 196608\n2:\n\ts_load_dword s98, %[v], s99\n\ts_sub_u32 s99, s99, 196608\n\ts_cbranch_scc0 2b\n\t.endif\n\t"
                     ".if %[x]\n\ts_mov_b32 s99, %[e]\n"
                     "1:\n\ts_load_dword s98, %[p], s99\n\ts_sub_u32 s99, s99, %[st]\n\ts_cbranch_scc0 1b\n\t.endif\n\ts_waitcnt lgkmcnt(0)"
                     :
                     : [v] "s"(vb), [p] "s"(p0), [e] "s"(hsub < 2 ? D * F * 2 - 64 : (hsub == 2 ? 192 * D * 2 - 64 : 768 * D * 2 - 64)), [st] "n"(PTS),
                       [nv] "n"(PTV), [x] "n"(PTX)
                     : "memory", "scc", "s98", "s99");
      }
      if (i == XTAB) att_table(smem, fresh(lane0), wave, gop);
      if (i == XTB && wave == 5) chain_table(smem, fresh(lane0), g, jo, c);
      if (i == XTAB + 1) w2p = karg<64>(kp) + (size_t)(192 * g) * D + 32 * jo;
      if (i == 1 && wave == 6) {
        const u32x4 lv = afrag(smem, L_HB + lane0 * 16);
        const u32 xb = *reinterpret_cast<const u32*>(smem + L_HB + 1024 + lane0 * 4);
        float v[10];
#pragma unroll
        for (int k = 0; k < 4; ++k) {
          v[2 * k] = bflo(lv[k]);
          v[2 * k + 1] = bfhi(lv[k]);
        }
        v[8] = bflo(xb);
        v[9] = bfhi(xb);
        float sm = 0.f;
#pragma unroll
        for (int k = 0; k < 10; ++k) sm += v[k];
        const float mean = wave_sum(sm) * (1.0f / D);
        float q = 0.f;
#pragma unroll
        for (int k = 0; k < 10; ++k) q = fmaf(v[k] - mean, v[k] - mean, q);
        const float var = wave_sum(q) * (1.0f / D);
        const float mb = bf16r(mean);
        const float ri = __builtin_amdgcn_rcpf(bf16r(__builtin_amdgcn_sqrtf(bf16r(bf16r(var) + 1e-5f))));
        u32 hp[5];
#pragma unroll
        for (int k = 0; k < 5; ++k) {
          const u32 cc = cvt2(v[2 * k] - mb, v[2 * k + 1] - mb);
          hp[k] = cvt2(bflo(cc) * ri, bfhi(cc) * ri);
        }
        *reinterpret_cast<u32x4*>(smem + L_LN + lane0 * 16) = u32x4{hp[0], hp[1], hp[2], hp[3]};
        *reinterpret_cast<u32*>(smem + L_LN + 1024 + lane0 * 4) = hp[4];
        asm volatile("s_waitcnt lgkmcnt(0)" ::: "memory");
        if (lane0 == 0) lds_add(smem, L_VOTE + 4, 1u);
      }
      if (wave == 6) {
        if (i == AL_R || i == AL_R + AL_2) {
          const int ln = fresh(lane0);
          ALLD(rq, WS_GOF + (ln & 7) * 128 + (i == AL_R ? 2048 : 3072 + (ln >> 3) * 4), (u32)(smb + L_CTL + 3072));
        }
        if (i == AL_L || i == AL_L + AL_2) {
          asm volatile("s_waitcnt vmcnt(%0)" ::"n"(AL_L - AL_R) : "memory");
          const u32 d = CTL[768 + lane0] - tstart + AL_OFF;
          const int sd = (int)sum8(d + 128u < 512u ? (float)(int)d : 0.f);
          if (i == AL_L) {
            *(volatile LAS int*)(smem + L_VOTE + 8) = sd >> 5;
          } else {
            const int s0 = __builtin_amdgcn_readlane(sd, 0);
            int p = __builtin_amdgcn_readlane(sd, 8) - s0;
            p = p > FGT * 8 ? p * FGA >> 2 : 0;
            p = p > FGM * 8 ? FGM * 8 : p;
            *(volatile LAS u32x2*)(smem + L_VOTE + 12) = u32x2{safe_ref(tstart + (u32)((s0 + p) >> 3) + (TQ0 + (48 + NINO) * TS_O) / 8 - 2), (u32)p};
          }
        }
      }
      LOOK(i)
      if (i == FR && wave == 0 && b < 8) {
        asm volatile("s_waitcnt vmcnt(%0)" ::"n"(FW) : "memory");
        const u32 land = rtnow();
        if (lane0 < 32)
          __builtin_amdgcn_raw_buffer_store_b32((lane0 & 1) ? land - FGN : tstart, rs, WS_GOF + 3072 + b * 128 + lane0 * 4, 0, 16);
      }
      if (i == AL_A) tstep += __builtin_amdgcn_readfirstlane(*(volatile LAS u32*)(smem + L_VOTE + 8));
      if (i == QI - 1 && wave < 6) {
        asm volatile("s_waitcnt vmcnt(%0)" ::"n"(QI) : "memory");
        if (lane0 == 0) lds_add(smem, L_VOTE + 4, 1u);
      }
      if (i == QI && wave < 6) {
        lds_wait((volatile LAS u32*)(smem + L_VOTE + 4), 7u);
        G80 a;
        g80_w<1024>(a, smem, lane0, L_WQ + (wave >> 1) * 10240 + (wave & 1) * 512);
        g80_a(a, lane0, L_LN + 160 * c, -1);
        float r0, r1;
        g80_run(a, smem, r0, r1);
        auto sw = __builtin_amdgcn_permlane32_swap(__float_as_uint(r0), __float_as_uint(r1), false, false);
        const f32x4 qd = quad4(__uint_as_float(sw[1]));
        const int qp = wave * 128 + (lane0 >> 5) * 64 + (lane0 & 15) * 4;
        if ((lane0 & 19) == 0) *reinterpret_cast<u32x2*>(smem + L_RED + (qp >> 1)) = u32x2{cvt2(qd[0], qd[1]), cvt2(qd[2], qd[3])};
        if ((lane0 & 19) == 16) *reinterpret_cast<f32x4*>(smem + L_CS + qp) = qd;
        if (lane0 == 0) lds_add(smem, L_CNT + 4, 1u);
      }
      if (wave == 7) {
        if (i == QP) {
          lds_wait(cnt + 1, 6u);
          const int ln = fresh(lane0);
          if (ln < 24)
            __builtin_amdgcn_raw_buffer_store_b128(afrag(smem, L_RED + ln * 16), rs, WS_GQ1 + (u32)(h * 8 + c) * 2048 + ln * 16, 0, 18);
        }
        if (i == BEAT0) {
          asm volatile("s_waitcnt vmcnt(%0)" ::"n"(BEAT0 - QP) : "memory");
          const int df = (int)(__builtin_amdgcn_readfirstlane(*(volatile LAS u32*)(smem + L_VOTE)) - hb);
          hb = df > PTHR ? 1u << (g >> 2) : (df < -PTHR ? 2u >> (g >> 2) : 0u);
          const u32 rn = rtnow(), lag = 24u * rn - now() - ck0;
          const int sl = (int)((float)(int)lag * CKF * __builtin_amdgcn_rcpf((float)(rn - tstart - CKD)) + 0.5f);
          hb += (u32)__builtin_amdgcn_readfirstlane(sl < 0 ? 0 : (sl > 31 ? 31 : sl)) * 4u;
          hb += (__builtin_amdgcn_readfirstlane(CTL[576 + c]) & ~127u) + 128u;
          if (lane0 == 0) __builtin_amdgcn_raw_buffer_store_b32(hb, rs, WS_HB + h * 128 + c * 4, 0, 16);
          hb += 128u;
        }
        if (i == CKL) ctl_load<false>(rq, hso, (u32)(smb + L_CTL + 2048));
      }
      if (i == CKL && leader) {
        ctl_load<true>(rq, lho, (u32)(smb + L_CTL + 1024));
      }
    });
#define VLOADS(A, B)                                                                           \
  sfor<A, B>([&](auto ic) __attribute__((always_inline)) {                                     \
    constexpr int i = ic.value;                                                                \
    if (i == XA - E_QN) tnext -= wave < 4 ? E_QK : 0;                                          \
    if (i == XB - E_QN) tnext -= wave < 4 ? 0 : E_QK;                                          \
    LOADW(vreg(i), vo, vb + ((i / 3 + h) & 7) * 512 * DH, 128 * (i % 3));                      \
    if constexpr (i < XA) {                                                                    \
      REPORT(24 + i)                                                                           \
    }                                                                                          \
    APPLY(24 + i)                                                                              \
    WPIECE(24 + i)                                                                             \
    if constexpr (i < XA) {                                                                    \
      LOOK(24 + i)                                                                             \
    }                                                                                          \
  });
    VLOADS(0, CKV)
    if (wave == 7) {
      CWAIT;
      HS_WAIT((u32)ch == 0xffu)
      if (lane0 == 0) cnt[1] = 7u;
    }
    if (leader) {
      CWAIT;
      const u32x4 xa = afrag(smem, L_CTL + lane0 * 16);
      u32x4 lv;
#pragma unroll 1
      for (;;) {
        asm volatile("" ::: "memory");
        lv = afrag(smem, L_CTL + 1024 + lane0 * 16);
        const bool same = lv[0] == xa[0] || lv[1] == xa[1] || lv[2] == xa[2] || lv[3] == xa[3];
        if (__builtin_amdgcn_ballot_w64(same) == 0ull || tmo == 0u) break;
        --tmo;
        __builtin_amdgcn_s_sleep(4);
        ctl_load<true>(rq, lho, (u32)(smb + L_CTL + 1024));
        asm volatile("s_waitcnt vmcnt(0)" ::: "memory");
      }
      u32 nv = 256u;
#pragma unroll
      for (int e = 0; e < 4; ++e)
        nv += (u32)__builtin_popcountll(__builtin_amdgcn_ballot_w64((lv[e] & 2u) != 0u)) -
              (u32)__builtin_popcountll(__builtin_amdgcn_ballot_w64((lv[e] & 1u) != 0u));
      const u32 s01 = (lv[0] & 124u) > (lv[1] & 124u) ? lv[0] & 124u : lv[1] & 124u, s23 = (lv[2] & 124u) > (lv[3] & 124u) ? lv[2] & 124u : lv[3] & 124u;
      const u32 slow = (u32)xrow16_max(max_ror8(max8((float)(s01 > s23 ? s01 : s23))));
      if (lane0 < 8) __builtin_amdgcn_raw_buffer_store_b32(CTL[640 + lane0] + (nv > 256u ? 2u : 1u) + slow, rs, WS_GOF + lane0 * 128, 0, 16);
    }
    VLOADS(CKV, CKV + 1)
    if (wave < 3) {
      const int ln = fresh(lane0);
      lds_wait(cnt + 1, 7u);
      ctl_load<true>(rq, WS_GQ1 + (u32)(h * 8 + (ln >> 3)) * 2048 + wave * 128 + (ln & 7) * 16, (u32)(smb + L_LAND + wave * 2048));
    }
    VLOADS(CKV + 1, CKV + 1 + CW)
    if (wave < 3) {
      const int ln = fresh(lane0);
      CWAIT;
      const u32x4 v = afrag(smem, L_LAND + wave * 2048 + ln * 16);
      const float t0 = tsum8(f32x4{bflo(v[0]), bflo(v[1]), bflo(v[2]), bflo(v[3])});
      const float t1 = tsum8(f32x4{bfhi(v[0]), bfhi(v[1]), bfhi(v[2]), bfhi(v[3])});
      *reinterpret_cast<u32*>(smem + q_addr(wave * 64 + (ln & 7) * 8 + ((ln >> 4) & 1) * 4 + (ln >> 5) * 2)) = cvt2(t0, t1);
      asm volatile("s_waitcnt lgkmcnt(0)" ::: "memory");
      if (lane0 == 0) lds_add(smem, L_CNT, 1u);
    }
    VLOADS(CKV + 1 + CW, XB - CW)
    if (wave == 7) ctl_load<false>(rq, hso, (u32)(smb + L_CTL + 2048));
    VLOADS(XB - CW, XA)
    if (wave >= 4) {
      VLOADS(XA, XB)
      if (wave == 7) {
        CWAIT;
        HS_WAIT(false)
        if (lane0 == 8) cnt[2] = gd | 128u;
      }
    }
  }

  u32 off;
  u32x4 qv;
  asm volatile("" : "=v"(off), "={v[56:59]}"(qv));
  asm volatile("s_branch 1f\n\t.p2align 7\n\t.rept %0\n\ts_nop 0\n\t.endr\n1:" ::"n"(CAL));
#pragma unroll 1
  for (int half = 0;; ++half) {
    u32x4 tb;
    int lnb;
    {
      float pend, pmax;
      PVAddr pv;
      QKAddr qa;
      const int lane = lane_id();
      int qw;
      att_setup(qa, pv, smem, lane, wave, qw);
      {
        u32 t, bud;
#define QRETRY(L) "s_sub_u32 %[bud], %[bud], 1\n\ts_cbranch_scc1 " L "\n\t" RTI(0, "v60", "%[t]") "s_sleep 1\n\tbuffer_load_dwordx4 v[56:59], %[off], %[rq], 0 offen sc1\n"
        asm volatile("s_mov_b32 %[bud], 0x10000\n\ts_cmp_eq_u32 %[hf], 0\n\ts_cbranch_scc1 0f\n\t"
                     "s_cmp_eq_u32 %[wv], 6\n\ts_cbranch_scc0 2f\n\ts_branch 5f\n"
                     "0:\n\ts_mov_b32 %[bud], 0x400000\n"
                     "0:\n\tds_read_b32 %[t], %[z] offset:%[cn]\n\ts_sub_u32 %[bud], %[bud], 1\n\ts_cbranch_scc1 8f\n\ts_waitcnt lgkmcnt(0)\n\t"
                     "v_cmp_eq_u32 vcc, 3, %[t]\n\ts_cbranch_vccnz 8f\n\ts_sleep 1\n\ts_branch 0b\n"
                     "4:\n\t" QRETRY("6f")
                     "5:\n\ts_waitcnt vmcnt(0) lgkmcnt(0)\n\t" AND3("v56", "v57", "v58") AND3("%[t]", "1", "v59")
                     "v_cmp_eq_u32 vcc, 0, %[t]\n\ts_cbranch_vccnz 4b\n"
                     "6:\n\tv_add_f32 v56, v56, v57\n\t" SD("v59", "quad_perm:[1,0,3,2]") "v_add_f32 v56, v56, v58\n\t"
                     "s_cmp_eq_u32 %[wv], 7\n\t" SD("v59", "quad_perm:[2,3,0,1]") SD("v56", "quad_perm:[1,0,3,2]")
                     "s_nop 0\n\t" SD("v59", "row_half_mirror") SD("v56", "quad_perm:[2,3,0,1]")
                     "v_mul_f32 v59, 0x3acccccd, v59\n\tv_mul_f32 v57, -v59, v59\n\t" SD("v56", "row_half_mirror")
                     "s_waitcnt lgkmcnt(0)\n\ts_barrier\n\ts_cbranch_scc1 8f\n"
                     "7:\n\t"
                     "v_fmac_f32 v57, 0x3acccccd, v56\n\t" RND("v57", "|v57|") "v_add_f32 v57, 0x3727c5ac, v57\n\t" RND("v57", "v57")
                     "v_sqrt_f32 v57, v57\n\ts_nop 0\n\t" RND("v57", "v57") "v_rcp_f32 v57, v57\n\ts_nop 0\n\t"
                     "v_mul_f32 v57, %[scl], v57\n\tv_or_b32 v57, 1, v57\n\tds_write_b32 %[qw], v57\n\t"
                     "s_cbranch_scc1 6b\n\ts_branch 8f\n"
                     "1:\n\t" QRETRY("3f")
                     "2:\n\ts_cmp_eq_u32 %[wv], 7\n\ts_waitcnt vmcnt(0) lgkmcnt(0)\n\t"
                     "v_permlane32_swap_b32 v56, v58\n\tv_permlane32_swap_b32 v57, v59\n\t"
                     "v_pk_add_f32 v[60:61], v[56:57], v[58:59]\n\t" AND3("v56", "v57", "v58") AND3("%[t]", "1", "v59")
                     "v_permlane16_swap_b32 v60, v61\n\tv_add_f32 v60, v60, v61\n\t"
                     "v_cmp_eq_u32 vcc, 0, %[t]\n\ts_nop 0\n\t" SD("v60", "row_ror:8")
                     "v_cvt_pk_bf16_f32 v60, v60, v60\n\tds_write_b16 %[qw], v60\n\ts_cbranch_vccnz 1b\n\ts_cbranch_scc1 7b\n"
                     "3:\n\ts_waitcnt lgkmcnt(0)\n\ts_barrier\n"
                     "8:"
                     : [t] "=&v"(t), [bud] "=&s"(bud), "+{v[56:59]}"(qv)
                     : [hf] "s"(half), [wv] "s"(wave), [z] "v"(smb), [cn] "n"(L_CNT), [off] "v"(off), [rq] "s"(rq), [qw] "v"(qw),
                       [scl] "n"(__builtin_bit_cast(u32, SCL))
                     : "memory", "vcc", "scc", "v60", "v61");
      }
      QK_SOFTMAX(qa)
      if (half == 0) {
        const int lv_ = lane_id();
        const u32 vo = (lv_ >> 3) * 384 + 16 * (lv_ & 7);
        tnext += E_QK;
        if (wave < 4) {
          VLOADS(XA, XB)
        }
        if (wave == 6) RPLOAD(4);
        VLOADS(XB, 24)
      }
#define DA(CTL) "v_add_f32_dpp %[l], %[l], %[l] " CTL " row_mask:0xf bank_mask:0xf\n\t"
#define PX(o, v, a) SM "%[o" #o "], %[a" #a "], v[160+24*" #a "+8*" #o ":167+24*" #a "+8*" #o "], %[ix]\n\t"
      asm volatile("s_waitcnt vmcnt(6) lgkmcnt(3)\n\t" PX(0, x, 0) DA("quad_perm:[1,0,3,2]") PX(1, y, 0) PX(2, z, 0)
                   "s_waitcnt lgkmcnt(2)\n\t" PX(0, x, 1) DA("quad_perm:[2,3,0,1]") PX(1, y, 1) PX(2, z, 1)
                   "s_waitcnt lgkmcnt(0)\n\t" PX(0, x, 2) DA("row_half_mirror") PX(1, y, 2) PX(2, z, 2)
                   : [o0] "+v"(pv.O0), [o1] "+v"(pv.O1), [o2] "+v"(pv.O2), [a0] "+v"(pv.A0), [a1] "+v"(pv.A1),
                     [a2] "+v"(pv.A2), [a3] "+v"(pv.A3), [l] "+v"(pend)
                   : [ix] "v"(pv.idx), "{v[160:191]}"(kv3), "{v[192:223]}"(kv4), "{v[224:255]}"(kv5)
                   : "memory");
#define CLEAR2                                                                                                         \
  {                                                                                                                    \
    const int p2 = (isO ? j : j - 8) * 2 + (wave & 1);                                                                 \
    const u32 ca = ((isO ? j < 12 : (p2 < 40 || (g & 3) == 0)) ? gy_rep(g) : OOB) + (u32)(4 + (p2 >> 2)) * 16384u +    \
                   (u32)(p2 & 3) * 2048u + lane_id() * 16;                                                          \
    const u32x4 z = __builtin_bit_cast(u32x4, zero4());                                                                \
    store16(rs, ca, z);                                                                                                \
    store16(rs, ca + 1024, z);                                                                                         \
  }
      static_assert(5 - NINF >= NPF && 6 - NINO >= NPRE && NPF >= NPRE, "pieces behind V");
      if (half == 0) {
        {
          const int ln = lane_id();
          if (wave < 6 && ln < 8)
            store16(rs, WS_GQ1 + (u32)(h * 8 + c) * 2048 + 1024 + wave * 128 + ln * 16, afrag(smem, L_CS + wave * 128 + ln * 16));
        }
        if (wave == 6) {
          const u32 dg = *(volatile LAS u32*)(smem + L_CTL + 3072 + lane_id() * 4) - *(volatile LAS u32*)(smem + L_VOTE + 12) + 512u;
          const int gx = __builtin_amdgcn_readfirstlane((int)max8(dg < 1024u ? (float)dg : 0.f));
          *(volatile LAS u32*)(smem + L_SH) = gx ? gx - 512 : 0;
        }
        anc = __builtin_amdgcn_readfirstlane(*(volatile LAS u32*)(smem + L_VOTE + 12));
#pragma unroll 1
        for (int p = 0; p < (sO ? NPRE : NPF); ++p) {
          pace(tnext, sO ? TPV : TPF);
          dma_piece(ws0, isO ? D : F, smem, L_WB, pn++, lane_id());
        }
        const u32 gw = lds_wait_nz(cnt + 2);
        ar = (u32)(g & 3) * AREA + (((u32)(g >> 2) ^ (gw >> 1)) & 1u) * SIDE;
        tsc = 1024u + ((gw >> 2) & 31u) * KAP;
        anc += X_F;
      }
      const u32 gob = ar + (u32)half * GO_SZ;
      if (NPF != NPRE && sO) asm volatile("s_waitcnt vmcnt(%0)" ::"n"(NPRE) : "memory");
      asm volatile("s_waitcnt vmcnt(%[np])\n\t" PX(0, x, 3) DA("row_ror:8") PX(1, y, 3) PX(2, z, 3) "s_setprio 0"
                   : [o0] "+v"(pv.O0), [o1] "+v"(pv.O1), [o2] "+v"(pv.O2), [a3] "+v"(pv.A3), [l] "+v"(pend)
                   : [ix] "v"(pv.idx), [np] "n"(NPF), "{v[224:255]}"(kv5)
                   : "memory");
      static_assert(NINO + NPRE == 6 && PF0 != PO0, "pieces");
      if (half == 0 && FEARLY && !sO) {
#pragma unroll 1
        for (int p = NINF + NPF; p < NINF + NPF + FEARLY; ++p) dma_piece(ws0, F, smem, L_WB, wave * 5 + p, lane_id());
      }
      static_assert(L_LAND < 32768, "lanes without a destination stay out of range");
#define MX(CTL) "s_nop 1\n\tv_max_f32_dpp v49, v49, v49 " CTL " row_mask:0xf bank_mask:0xf\n\t"
#define MXP(W) "v_mov_b32 v50, v49\n\ts_nop 1\n\tv_permlane" W "_swap_b32 v49, v50\n\tv_max_f32 v49, v49, v50\n\t"
#define RB(D, A, BANK) "v_add_f32_dpp " D ", " A ", " A " row_ror:8 row_mask:0xf bank_mask:" BANK "\n\t"
#define SWP(W) "v_permlane" W "_swap_b32 v40, v42\n\tv_permlane" W "_swap_b32 v41, v43\n\t" PKA("v[40:41]", "v[42:43]")
#define OWNA "v_xor_b32 v44, %[ox], %[dst]\n\tv_add_u32 v44, %[oa], v44\n\t"
#define SC2(R) "v_pk_mul_f32 " R ", " R ", v[50:51] op_sel_hi:[1,0]\n\t"
      asm volatile("ds_write2_b32 %[la], %[l], %[mx] offset1:4\n\tv_mbcnt_lo_u32_b32 %[ln], -1, 0\n\tv_mbcnt_hi_u32_b32 %[ln], -1, %[ln]\n\t"
                   "s_mov_b64 exec, %[m]\n\ts_cmp_lt_u32 %[wv], 6\n\t"
                   "ds_write_b128 %[a], %[o0]\n\tds_write_b128 %[a], %[o1] offset:256\n\t"
                   "ds_write_b128 %[a], %[o2] offset:512\n\t"
                   "s_mov_b64 exec, -1\n\tv_lshl_add_u32 v51, %[ln], 4, %[ta]\n\ts_waitcnt lgkmcnt(0)\n\ts_barrier\n\t"
                   "ds_read_b32 v48, %[ma]\n\tds_read_b128 v[40:43], %[sa]\n\tds_read_b128 v[44:47], %[sa] offset:%[op]\n\t"
                   "ds_read_b128 %[tb], v51\n\t"
                   "s_waitcnt lgkmcnt(3)\n\tv_cmp_ne_u32 vcc, 0, v48\n\ts_cbranch_vccnz 5f\n"
                   "4:\n\ts_cbranch_scc0 6f\n\ts_waitcnt lgkmcnt(1)\n\t"
                   PKA("v[40:41]", "v[44:45]") PKA("v[42:43]", "v[46:47]") "s_nop 0\n\t"
                   RB("v40", "v40", "0x3") RB("v41", "v41", "0x3") RB("v40", "v42", "0xc") RB("v41", "v43", "0xc")
                   "s_nop 0\n\t" SWP("16") "s_nop 1\n\t" SWP("32")
                   "v_cvt_pk_bf16_f32 v40, v40, v41\n\tv_or_b32 v40, 1, v40\n\t"
                   "buffer_store_dword v40, %[dst], %[rq], %[so] offen sc1\n\t" OWNA "ds_write_b32 v44, v40\n\ts_branch 9f\n"
                   "5:\n\tv_mov_b32 v49, v48\n\t" MX("quad_perm:[1,0,3,2]") MX("quad_perm:[2,3,0,1]") MX("row_half_mirror") MX("row_ror:8")
                   MXP("16") MXP("32") "v_sub_f32 v50, v48, v49\n\tv_exp_f32 v50, v50\n\tv_mov_b32 v48, v49\n\ts_waitcnt lgkmcnt(0)\n\t"
                   SC2("v[40:41]") SC2("v[42:43]") SC2("v[44:45]") SC2("v[46:47]") "s_branch 4b\n"
                   "6:\n\ts_waitcnt lgkmcnt(0)\n\t" PKA("v[42:43]", "v[40:41]") "v_add_f32 v41, v42, v43\n\t"
                   "v_or_b32 v40, 1, v48\n\tv_or_b32 v42, 1, v48\n\t" SD("v41", "quad_perm:[1,0,3,2]")
                   "v_mov_b32 v43, 1\n\ts_nop 0\n\t" SD("v41", "quad_perm:[2,3,0,1]") "s_nop 1\n\t" SD("v41", "row_half_mirror")
                   "v_or_b32 v41, 1, v41\n\tbuffer_store_dwordx4 v[40:43], %[dst], %[rq], %[so] offen sc1\n\t" OWNA "ds_write_b128 v44, v[40:43]\n"
                   "9:\n\ts_waitcnt lgkmcnt(1)"
                   : [tb] "=&v"(tb), [ln] "=&v"(lnb)
                   : [m] "s"(0xff00ff0000ff00ffull), [a] "v"(pv.oa + smb), [o0] "v"(pv.O0), [o1] "v"(pv.O1), [o2] "v"(pv.O2),
                     [la] "v"(pv.la + smb), [l] "v"(pend), [mx] "v"(pmax), [wv] "s"(wave), [ma] "v"(pv.ma), [sa] "v"(pv.sa),
                     [op] "n"(OM_P * 4), [dst] "v"(pv.dst), [rq] "s"(rq), [so] "s"(gob), [ta] "s"(smb + L_TB),
                     [ox] "s"((c & 3) * 64), [oa] "s"(smb + L_LAND - (hsub >> 1) * 8192)
                   : "memory", "vcc", "scc", DCLOB);
      if (half == 0) {
        tref = now();
        anc += __builtin_amdgcn_readfirstlane(*(volatile LAS u32*)(smem + L_SH));
      }
      if (half == 0 && wave >= 6) CLEAR2
    }
    const u32 go = ar + A_GO + (u32)half * GO_SZ;
    const u32 gyw = A_GY + (u32)half * GY_PAIR;
    const u32 gy = gyw + ar;
    const u32 gh = ar + A_GH + (u32)half * 1536u;
    const int land = L_LAND + wave * 2048;
    const u32 landa = (u32)(uintptr_t)(LAS unsigned char*)smem + (u32)land;
    if (isO) {
      {
        const u32 so = go + go_slot(wave);
        if (!half) wait_until(tref + 24 * T_O);
        const u64 om = (wave >> 1) == hsub ? ~(0xffffull << ((c & 3) * 16)) : ~0ull;
        wait_poll(TT(half ? A_O : X_O), rq, tb[0], so, landa, c < 4 ? om : ~0ull, c < 4 ? ~0ull : om);
        asm volatile("" : "+v"(tb), "+v"(lnb));
        const u32 voff = tb[0];
        const int G = lnb >> 4;
        const int l0 = fresh(L_WB + wave * 6144 + (tb[2] & 0xffffu)), l1 = fresh(l0 ^ 32);
        const int ra = fresh(land + (tb[1] & 0xffffu)), rb = fresh(land + (tb[1] >> 16)), rc = fresh(rb ^ 192);
        asm volatile("s_waitcnt vmcnt(%0)" ::"n"(3) : "memory");
        u32x4 wf[6];
#pragma unroll
        for (int k = 0; k < 3; ++k) {
          wf[2 * k] = wfrag(smem, l0, 2048 * k);
          wf[2 * k + 1] = wfrag(smem, l1, 2048 * k);
        }
        const int rw = fresh(L_RED + wave * 160 + lnb * 4);
        const int rr = fresh(tb[2] >> 16);
        const u32 dst = tb[3];
        f32x4 d0, d1;
        float mc, t1_, rl;
        u32 t, bud, rare;
        asm volatile("s_waitcnt lgkmcnt(0)\n\ts_barrier\n\t" POLL_HEAD(1, "s_lshr_b32 s99, s98, 8\n\ts_or_b32 s98, s98, s99\n\t", "")
                     "s_waitcnt lgkmcnt(2)\n\t"
                     MF R0 ", " F0 ", %[w0], 0\n\t"
                     "v_add_f32_dpp v48, v53, v53 quad_perm:[1,0,3,2] row_mask:0xf bank_mask:0xf\n\t"
                     MF R1 ", " F0 ", %[w1], 0\n\t" AND3("v52", "v53", "v54") "v_cmp_ne_u32 s[98:99], 1, v52\n\t"
                     "s_waitcnt lgkmcnt(1)\n\t"
                     MF R0 ", " F1 ", %[w2], " R0 "\n\t" ZD("quad_perm:[2,3,0,1]", "0xf") AND3("%[t]", "v55", "v56")
                     MF R1 ", " F1 ", %[w3], " R1 "\n\t" ZD("row_half_mirror", "0xf") AND3("%[t]", "v57", "v58")
                     "v_rcp_f32 v48, v48\n\t"
                     "s_waitcnt lgkmcnt(0)\n\t"
                     MF R0 ", " F2 ", %[w4], " R0 "\n\t" AND3("%[t]", "v59", "v60") AND3("%[t]", "v61", "v62") AND3("%[t]", "1", "v63")
                     MF R1 ", " F2 ", %[w5], " R1 "\n\t"
                     "v_cmp_eq_u32 vcc, 0, %[t]\n\ts_cbranch_vccnz 1b\n"
                     "3:\n\tv_readlane_b32 %[rl], v48, 8\n\ts_and_b32 %[rare], s98, 0xff00"
                     : "=&{v[40:43]}"(d0), "=&{v[44:47]}"(d1), [t] "=&v"(t), [bud] "=&s"(bud), "=&{v52}"(mc), "=&{v53}"(t1_),
                       [rl] "=&s"(rl), [rare] "=&s"(rare)
                     : [ra] "v"(ra), [rb] "v"(rb), [rc] "v"(rc), [w0] "v"(wf[0]), [w1] "v"(wf[1]), [w2] "v"(wf[2]), [w3] "v"(wf[3]),
                       [w4] "v"(wf[4]), [w5] "v"(wf[5]), [vo] "v"(voff), [so] "s"(so), [rq] "s"(rq), [land] "s"(landa)
                     : "memory", "vcc", "scc", "s98", "s99", "v48", "v54", "v55", "v56", "v57", "v58", "v59", "v60", "v61", "v62",
                       "v63");
        if (__builtin_expect(rare != 0u, 0)) {
          asm volatile("s_nop 7" : "+v"(d0), "+v"(d1));
          const float fc = __builtin_amdgcn_exp2f(mc - max8(mc));
          const u32 sc = __float_as_uint(fc * __builtin_amdgcn_rcpf(sum8(fc * t1_)));
#pragma unroll
          for (int e = 0; e < 4; ++e) {
            const float se = __uint_as_float(G == 0 ? __builtin_amdgcn_readlane(sc, 8 + e) : __builtin_amdgcn_readlane(sc, 12 + e));
            d0[e] *= se;
            d1[e] *= se;
          }
          rl = 1.0f;
        }
        asm volatile("v_pk_add_f32 v[40:41], v[40:41], v[42:43]\n\tv_add_f32 v40, v40, v41\n\t"
                     "v_pk_add_f32 v[44:45], v[44:45], v[46:47]\n\tv_add_f32 v44, v44, v45\n\t"
                     "s_cmp_eq_u32 %[wv], 0\n\ts_mov_b32 exec_hi, 0\n\t"
                     "v_permlane16_swap_b32 v40, v44\n\tv_add_f32 v40, v40, v44\n\tv_mul_f32 v40, %[rl], v40\n\t"
                     "ds_write_b32 %[rw], v40\n\ts_mov_b32 exec_hi, -1\n\ts_waitcnt lgkmcnt(0)\n\ts_barrier\n\ts_cbranch_scc0 9f\n\t"
                     "ds_read_b128 v[40:43], %[rr]\n\tds_read_b128 v[44:47], %[rr] offset:160\n\t"
                     "ds_read_b128 v[48:51], %[rr] offset:320\n\tds_read_b128 v[52:55], %[rr] offset:480\n\t"
                     "s_waitcnt lgkmcnt(2)\n\t" PKA("v[40:41]", "v[44:45]") PKA("v[42:43]", "v[46:47]")
                     "s_waitcnt lgkmcnt(0)\n\t" PKA("v[48:49]", "v[52:53]") PKA("v[50:51]", "v[54:55]")
                     PKA("v[40:41]", "v[48:49]") PKA("v[42:43]", "v[50:51]") "s_nop 0\n\t"
                     SD("v40", "row_ror:8") SD("v41", "row_ror:8") SD("v42", "row_ror:8") SD("v43", "row_ror:8")
                     "v_cvt_pk_bf16_f32 v40, v40, v41\n\tv_cvt_pk_bf16_f32 v41, v42, v43\n\t"
                     "v_or_b32 v40, 1, v40\n\tv_or_b32 v41, 1, v41\n\t"
                     "buffer_store_dwordx2 v[40:41], %[dst], %[rq], %[so] offen sc1\n"
                     "9:"
                     : "+{v[40:43]}"(d0), "+{v[44:47]}"(d1)
                     : [wv] "s"(wave), [rl] "s"(rl), [rw] "v"(rw), [rr] "v"(rr), [dst] "v"(dst), [rq] "s"(rq), [so] "s"(gyw)
                     : "memory", "scc", "v48", "v49", "v50", "v51", "v52", "v53", "v54", "v55");
      }
      tref = now();
      if (half == 0) {
        if (wave == 0) {
          const int lane = lane_id();
          u32 w2o = ((lane >> 2) * D + ((((lane >> 1) ^ (lane >> 5)) & 1) * 16 + (lane & 1) * 8)) * 2;
#define W2L "global_load_lds_dwordx4 %[wo], %[wp] nt\n\ts_addk_i32 m0, 0x400\n\tv_add_u32 %[wo], %[rs], %[wo]\n\t"
          asm volatile("s_mov_b32 s98, m0\n\ts_mov_b32 m0, %[wl]\n\ts_nop 2\n8:\n\t" W2L W2L W2L W2L
                       "s_cmp_lt_u32 m0, %[we]\n\ts_cbranch_scc1 8b\n\ts_mov_b32 m0, s98"
                       : [wo] "+v"(w2o)
                       : [wp] "s"(w2p), [wl] "n"(L_W2), [we] "n"(L_W2 + 12288), [rs] "n"(16 * D * 2)
                       : "memory", "scc", "s98");
          asm volatile("s_waitcnt vmcnt(0)" ::: "memory");
          cnt[1] = 8u;
        }
        if (wave == 5) {
          wait_until(tref + 24 * T_LAZY);
          x_update<false>(smem, rq, lane_id(), gy + gy_row(c), c);
        }
      }
      if (wave >= 6) {
        const int lane = lane_id(), wv = wave - 6;
        const int n = lane & 15, G = lane >> 4;
        const int l0 = fresh(L_W2 + wlane(lane, wv));
        const int l48 = lane < 48 ? lane : 0;
        const u32 off = fresh(gh + (u32)l48 * 16);
        const int aw = fresh(L_ACT + wv * 384 + l48 * 8);
        const int ab = fresh(L_ACT + wv * 384 + 16 * G);
        const u32 dst = fresh(((G & (half ? 3 : 0)) ? OOB : gyw) + GY_INST + gy_rep((n & 1) * 4 + G) + gy_chunk(4 * jo + wv * 2 + (n >> 3)) +
                              g * 256 + (n & 6) * 2);
        if (!half) wait_until(tref + 24 * T_H);
#define HPOLL                                                      \
  "buffer_load_dwordx4 v[32:35], %[off], %[rq], 0 offen sc1\n\t" \
  "buffer_load_dwordx4 v[36:39], %[off], %[rq], 0 offen offset:768 sc1\n\t"
        u32x4 p0, p1;
        asm volatile(WAIT_RT HPOLL "v_mov_b32 v41, 0\n\ts_mov_b32 s98, 0x400000\n"
                     "4:\n\tds_read_b32 v40, v41 offset:%[cn]\n\ts_sub_u32 s98, s98, 1\n\ts_waitcnt lgkmcnt(0)\n\ts_cbranch_scc1 5f\n\t"
                     "v_cmp_eq_u32 vcc, 8, v40\n\ts_cbranch_vccz 4b\n5:"
                     : "=&{v[32:35]}"(p0), "=&{v[36:39]}"(p1)
                     : [off] "v"(off), [rq] "s"(rq), [tt] "s"(TT(half ? A_H : X_H)), [cn] "n"(L_CNT + 4)
                     : "memory", "scc", "vcc", "s98", "s99", "v40", "v41");
        u32x4 wf[6];
#pragma unroll
        for (int k = 0; k < 6; ++k) wf[k] = wfrag(smem, l0, 2048 * k);
        u32 t, bud;
        asm volatile("s_mov_b32 %[bud], 0x10000\n\ts_branch 2f\n"
                     "1:\n\ts_sub_u32 %[bud], %[bud], 1\n\ts_cbranch_scc1 3f\n\ts_waitcnt lgkmcnt(0)\n\t" RTI(3, "v40", "v41") "s_sleep 1\n\ts_mov_b64 exec, vcc\n\t"
                     HPOLL "s_mov_b64 exec, -1\n"
                     "2:\n\ts_waitcnt vmcnt(0)\n\t"
                     "v_pk_mul_f32 v[40:41], v[32:33], v[36:37]\n\tv_pk_mul_f32 v[42:43], v[34:35], v[38:39]\n\t"
                     "v_cvt_pk_bf16_f32 v40, v40, v41\n\tv_cvt_pk_bf16_f32 v41, v42, v43\n\t"
                     "ds_write_b64 %[aw], v[40:41]\n\t"
                     "ds_read_b128 v[40:43], %[ab]\n\tds_read_b128 v[44:47], %[ab] offset:64\n\t"
                     "ds_read_b128 v[48:51], %[ab] offset:128\n\tds_read_b128 v[52:55], %[ab] offset:192\n\t"
                     "ds_read_b128 v[56:59], %[ab] offset:256\n\tds_read_b128 v[60:63], %[ab] offset:320\n\t"
                     AND3("v32", "v33", "v34") AND3("%[t]", "v35", "v36") AND3("%[t]", "v37", "v38") AND3("%[t]", "1", "v39")
                     "v_cmp_eq_u32 vcc, 0, %[t]\n\ts_cbranch_vccnz 1b\n"
                     "3:\n\ts_waitcnt lgkmcnt(5)\n\t" MF "v[32:35], v[40:43], %[w0], 0\n\t"
                     "s_waitcnt lgkmcnt(4)\n\t" MF "v[32:35], v[44:47], %[w1], v[32:35]\n\t"
                     "s_waitcnt lgkmcnt(3)\n\t" MF "v[32:35], v[48:51], %[w2], v[32:35]\n\t"
                     "s_waitcnt lgkmcnt(2)\n\t" MF "v[32:35], v[52:55], %[w3], v[32:35]\n\t"
                     "s_waitcnt lgkmcnt(1)\n\t" MF "v[32:35], v[56:59], %[w4], v[32:35]\n\t"
                     "s_waitcnt lgkmcnt(0)\n\t" MF "v[32:35], v[60:63], %[w5], v[32:35]\n\ts_nop 7\n\t"
                     "v_mov_b32_dpp v36, v32 quad_perm:[0,0,2,2] row_mask:0xf bank_mask:0xf\n\t"
                     "v_mov_b32_dpp v37, v32 quad_perm:[1,1,3,3] row_mask:0xf bank_mask:0xf\n\t"
                     "v_cvt_pk_bf16_f32 v36, v36, v37\n\tv_or_b32 v36, 1, v36\n\t"
                     "buffer_store_dword v36, %[dst], %[rq], 0 offen sc1"
                     : [t] "=&v"(t), [bud] "=&s"(bud), "+{v[32:35]}"(p0), "+{v[36:39]}"(p1)
                     : [off] "v"(off), [aw] "v"(aw), [ab] "v"(ab), [dst] "v"(dst), [rq] "s"(rq), [w0] "v"(wf[0]), [w1] "v"(wf[1]),
                       [w2] "v"(wf[2]), [w3] "v"(wf[3]), [w4] "v"(wf[4]), [w5] "v"(wf[5])
                     : "memory", "vcc", "scc", FCLOB, DCLOB);
      }
    } else {
      const int lane = lane_id();
      const int n = lane & 15, G = lane >> 4;
      if (half == 1) {
        if (g == 0 && wave == 2) {
          u32 keep;
          asm volatile("s_mov_b32 %0, m0\n\ts_mov_b32 m0, %2\n\ts_nop 2\n\tglobal_load_lds_dword %1, %3\n\ts_mov_b32 m0, %0"
                       : "=&s"(keep)
                       : "v"(smb), "n"(L_PF), "s"(karg<72>(kp))
                       : "memory");
        }
        x_update<false>(smem, rq, lane, gy - GY_PAIR + GY_INST + gy_row(wave), wave);
      } else if (NINF + NPF + FEARLY < 5 && !sO) {
#pragma unroll 1
        for (int p = NINF + NPF + FEARLY; p < 5; ++p) {
          pace(tnext, TFL);
          dma_piece(ws0, F, smem, L_WB, wave * 5 + p, lane_id());
        }
      }
      G80 a;
      g80_a(a, lane, L_HB + 160 * wave, land);
      const u32 voff = tb[0], so = gy + gy_row(wave);
      const int rw = fresh(L_RED + n * 144 + wave * 16 + G * 4);
      const int sw_ = fresh(n == 0 ? L_ST + wave * 16 + G * 4 : L_ACT);
      const int rr = fresh(L_RED + (lane & 31) * 144 + (lane >> 5) * 64), rs_ = fresh(L_ST + 16 * (lane & 7));
      const u32 dst = fresh(gh + fslot + lane * 4);
      if (!half) wait_until(tref + 24 * T_YF);
      asm volatile("s_waitcnt vmcnt(0)" ::: "memory");
      const u32 ty = TT(half ? A_Y : X_Y);
      const bool late = (int)(rtnow() - ty) >= 0;
      if (late) wait_poll(ty, rq, voff, so, landa, USED, USED);
      g80_w<512>(a, smem, lane, L_WB + wave * 5120);
      asm volatile("" : "+v"(a.w[0]), "+v"(a.w[1]), "+v"(a.w[2]), "+v"(a.w[3]), "+v"(a.w[4]), "+v"(a.w[5]));
      if (!late) wait_poll(ty, rq, voff, so, landa, USED, USED);
      {
        u32 t, bud;
        asm volatile(G80_LOOP(2, "s_nop 0\n\t")
                     "ds_write2st64_b32 %[rw], v40, v44 offset1:9\n\t" ZD("quad_perm:[2,3,0,1]", "0xf") "s_nop 1\n\t"
                     ZD("row_half_mirror", "0xf") "ds_write_b32 %[sw], v48"
                     : [t] "=&v"(t), [bud] "=&s"(bud)
                     : G80_IN(a), [nm] "s"(0x00ff00ff00ff00ffull), [rw] "v"(rw), [sw] "v"(sw_)
                     : "memory", "vcc", "scc", "s98", "s99", FCLOB, DCLOB);
      }
      asm volatile("s_cmp_eq_u32 %[wv], 0\n\ts_waitcnt lgkmcnt(0)\n\ts_barrier\n\ts_cbranch_scc0 9f\n\t"
                   "ds_read_b128 v[40:43], %[rs]\n\tds_read_b128 v[44:47], %[rr]\n\tds_read_b128 v[48:51], %[rr] offset:16\n\t"
                   "ds_read_b128 v[52:55], %[rr] offset:32\n\tds_read_b128 v[56:59], %[rr] offset:48\n\t"
                   "s_bitcmp1_b32 %[j], 0\n\ts_mov_b32 s98, 0x10000\n\t"
                   "s_waitcnt lgkmcnt(4)\n\t"
                   "v_add_f32 v40, v40, v41\n\t" SD("v43", "quad_perm:[1,0,3,2]") "v_add_f32 v40, v40, v42\n\t"
                   "s_waitcnt lgkmcnt(2)\n\t" SD("v43", "quad_perm:[2,3,0,1]") SD("v40", "quad_perm:[1,0,3,2]")
                   PKA("v[44:45]", "v[48:49]") SD("v43", "row_half_mirror") SD("v40", "quad_perm:[2,3,0,1]")
                   "v_mul_f32 v43, 0x3acccccd, v43\n\tv_mul_f32 v41, -v43, v43\n\t" SD("v40", "row_half_mirror")
                   "v_fmac_f32 v41, 0x3acccccd, v40\n\t" RND("v41", "|v41|") "v_add_f32 v41, 0x3727c5ac, v41\n\t" RND("v41", "v41")
                   "v_sqrt_f32 v41, v41\n\t" PKA("v[46:47]", "v[50:51]") RND("v41", "v41") "v_rcp_f32 v41, v41\n\t"
                   "s_waitcnt lgkmcnt(0)\n\t"
                   PKA("v[52:53]", "v[56:57]") PKA("v[54:55]", "v[58:59]") PKA("v[44:45]", "v[52:53]") PKA("v[46:47]", "v[54:55]")
                   RND("v43", "v43") "v_add_f32 v44, v44, v45\n\tv_fma_f32 v46, -v43, v47, v46\n\tv_add_f32 v44, v44, v46\n\t"
                   "s_nop 1\n\tv_permlane32_swap_b32 v44, v45\n\tv_add_f32 v44, v44, v45\n\tv_mul_f32 v44, v44, v41\n\t"
                   "s_cbranch_scc1 8f\n\t" RND("v44", "v44")
                   "v_mul_f32 v45, 0xbfb8aa3b, v44\n\tv_exp_f32 v45, v45\n\ts_nop 0\n\tv_add_f32 v45, 1.0, v45\n\t"
                   "v_rcp_f32 v45, v45\n\ts_nop 0\n\tv_mul_f32 v44, v44, v45\n"
                   "8:\n\ts_mov_b32 exec_hi, 0\n\tv_cvt_pk_bf16_f32 v44, s98, v44\n\t"
                   "buffer_store_dword v44, %[dst], %[rq], 0 offen sc1\n\ts_mov_b32 exec_hi, -1\n"
                   "9:"
                   :
                   : [wv] "s"(wave), [j] "s"(j), [rs] "v"(rs_), [rr] "v"(rr), [dst] "v"(dst), [rq] "s"(rq)
                   : "memory", "scc", "s98", FCLOB, DCLOB);
      tref = now();
      const int ln = lane_id();
      if (half == 0 && wave == 1 && j == 20 && ln < 8) {
        const u32 st = rtnow();
        __builtin_amdgcn_raw_buffer_store_b32((((int)(st - TT(X_L)) < 0 ? TT(X_S) : st + AN_M) << 1) | 1u, rs, gy_rep(ln) + st_line(g), 0, 16);
      }
      if (half == 0 && wave == 7) {
        wait_until(tref + 24 * T_AN);
        stamp_poll(smem, rq, ar + st_line(ln & 7), TT(X_S));
      }
      if (half == 0 || g == 0) x_update<true>(smem, rq, ln, land, wave);
      if (half == 1 && g == 0) {
        if ((wave >> 1) != 1 || j >= 30) {
          bar();
        } else {
          const int ob = (j - 20) * 2 + (wave & 1);
          const int col = ob * 32 + (ln & 7) * 4 + ((ln >> 4) & 1) * 2 + (ln >> 5);
          u16* po = karg<72>(kp) + col;
          u32 off2 = gy + GY_INST + gy_chunk(4 * ob + ((ln >> 1) & 3)) + (ln >> 3) * 256 + (ln & 1) * 8;
          asm volatile("" : "+v"(po), "+v"(off2));
          bar();
          float x3 = __uint_as_float((u32) * (volatile LAS u16*)(smem + L_HB + col * 2) << 16);
          asm volatile("" : "+v"(x3));
          wait_rt(TT(A_F));
          u32 t, bud;
          asm volatile("s_mov_b32 %[bud], 0x10000\n\ts_branch 2f\n"
                       "1:\n\ts_sub_u32 %[bud], %[bud], 1\n\ts_cbranch_scc1 3f\n\t" RTI(5, "v46", "v47") "\n"
                       "2:\n\tbuffer_load_dwordx2 v[40:41], %[off], %[rq], 0 offen sc1\n\ts_waitcnt vmcnt(0)\n\t"
                       "v_lshlrev_b32 v42, 16, v40\n\tv_and_b32 v44, 0xffff0000, v40\n\t"
                       "v_lshlrev_b32 v43, 16, v41\n\tv_and_b32 v45, 0xffff0000, v41\n\t"
                       "v_permlane32_swap_b32 v42, v44\n\t" AND3("v40", "1", "v41") "v_permlane32_swap_b32 v43, v45\n\t"
                       PKA("v[42:43]", "v[44:45]") "v_cmp_eq_u32 vcc, 0, %[t]\n\ts_cbranch_vccnz 1b\n"
                       "3:\n\tv_permlane16_swap_b32 v42, v43\n\tv_add_f32 v42, v42, v43\n\ts_nop 1\n\t" SD("v42", "row_ror:8")
                       RND("v42", "v42") "v_add_f32 v42, v42, %[x3]\n\tv_cvt_pk_bf16_f32 v42, v42, v42\n\t"
                       "global_store_short %[po], v42, off"
                       : [t] "=&v"(t), [bud] "=&s"(bud)
                       : [off] "v"(off2), [rq] "s"(rq), [x3] "v"(x3), [po] "v"(po)
                       : "memory", "vcc", "scc", "s98", "s99", DCLOB);
        }
      }
    }
    asm volatile("" : "=v"(off), "={v[56:59]}"(qv));
    if (half == 1) break;
    {
      const u32 gq = ar + A_GQ + hsub * 16384;
      u32x4 w[6];
      u32 voff, dst;
      f32x4 cs;
      if (wave >= 6) {
        *(volatile LAS u32*)(smem + L_RI) = 0u;
        asm volatile("" : "=v"(w[0]), "=v"(w[1]), "=v"(w[2]), "=v"(w[3]), "=v"(w[4]), "=v"(w[5]), "=v"(voff), "=v"(dst), "={v[36:39]}"(cs));
      } else {
        const u32x2 te = *reinterpret_cast<const u32x2*>(smem + L_TB + 1024 + lane_id() * 16);
        dst = fresh(wave * 128 + (te[0] >> 16));
        const u32 cso = WS_GQ1 + 1024 + (u32)h * 16384;
        asm volatile("s_mov_b32 s98, %[s0]\n\ts_add_u32 s99, %[s0], 4096\n\t"
                     "buffer_load_dword v36, %[o], %[rq], s98 offen sc1\n\tbuffer_load_dword v37, %[o], %[rq], s98 offen offset:2048 sc1\n\t"
                     "buffer_load_dword v38, %[o], %[rq], s99 offen sc1\n\tbuffer_load_dword v39, %[o], %[rq], s99 offen offset:2048 sc1"
                     : "=&{v[36:39]}"(cs)
                     : [o] "v"(dst), [rq] "s"(rq), [s0] "s"(cso)
                     : "memory", "scc", "s98", "s99");
        const int wb = (wave >> 1) * 10240 + (wave & 1) * 512;
        const int l0 = fresh(wb + (te[1] & 0xffffu)), l2 = fresh(wb + (te[1] >> 16)), l1 = fresh(l0 ^ 32), l3 = fresh(l2 ^ 32);
        w[0] = wfrag(smem, l0, L_WQ);
        w[1] = wfrag(smem, l1, L_WQ);
        w[2] = wfrag(smem, l0, L_WQ + 4096);
        w[3] = wfrag(smem, l1, L_WQ + 4096);
        w[4] = wfrag(smem, l2, L_WQ + 8192);
        w[5] = wfrag(smem, l3, L_WQ + 8192);
        voff = fresh(te[0] & 0xffffu);
        asm volatile("" : "+v"(w[0]), "+v"(w[1]), "+v"(w[2]), "+v"(w[3]), "+v"(w[4]), "+v"(w[5]));
        if (isO && wave == 4) {
          wait_until(tref + 24 * T_H);
          wait_rt(TT(X_H));
          stamp_poll(smem, rq, ar + st_line(lane_id() & 7), TT(X_S));
        }
      }
      {
        u32 t, ra, rb, rc, bud, keep;
#define EPOLL "buffer_load_dwordx4 %[vo], %[rq], %[so] offen sc1 lds\n\ts_mov_b64 exec, -1\n\t"
#define ISP "s_lshr_b32 vcc_lo, %[wv], 1\n\ts_cmp_eq_u32 vcc_lo, 1\n\t"
#define USEM "s_mov_b32 exec_lo, 0x30ff03ff\n\ts_mov_b32 exec_hi, 0xff30ff03\n\t"
#define ON "v[36:39]"
#define RDAN "ds_read_b32 %[t], %[rc] offset:%[an]-%[tg]\n\t"
        asm volatile("s_mov_b32 %[k], m0\n\ts_mov_b32 m0, %[land]\n\ts_mov_b64 s[98:99], 0\n\ts_cmp_eq_u32 %[wv], %[dw]\n\ts_cbranch_scc1 13f\n"
                     "10:\n\t" ISP "s_cbranch_scc0 11f\n\tv_mov_b32 v40, 0\n\ts_mov_b32 s98, 0x400000\n"
                     "9:\n\tds_read_b32 %[t], v40 offset:%[an]\n\ts_sub_u32 s98, s98, 1\n\ts_waitcnt lgkmcnt(0)\n\ts_cbranch_scc1 8f\n\t"
                     "v_cmp_eq_u32 vcc, 0, %[t]\n\ts_cbranch_vccnz 9b\n"
                     "8:\n\tv_readfirstlane_b32 %[tt], %[t]\n\t" USEM "s_add_u32 %[tt], %[tt], %[de]\n\t" WAIT_RT EPOLL
                     "s_waitcnt vmcnt(1)\n\ts_branch 12f\n"
                     "11:\n\ts_waitcnt vmcnt(0)\n"
                     "12:\n\tv_pk_add_f32 v[32:33], v[36:37], v[38:39]\n\tv_add_f32 v32, v32, v33\n\tv_mul_f32 v32, 0xbacccccd, v32\n\t"
                     "v_mov_b32 v33, v32\n\tv_mov_b32 v36, 0x3f803f80\n\tv_mov_b32 v37, 0x3f803f80\n\t"
                     "v_permlane16_swap_b32 v32, v33\n\tv_add_f32 v32, v32, v33\n\tv_mov_b32 v33, v32\n\t"
                     "v_mov_b64 v[38:39], v[36:37]\n\ts_mov_b32 s98, 0xff00ff\n\tv_permlane32_swap_b32 v32, v33\n\t"
                     "s_mov_b32 s99, s98\n\ts_mov_b32 %[tt], 0x10000\n"
                     "13:\n\tv_mbcnt_lo_u32_b32 %[t], -1, 0\n\tv_mbcnt_hi_u32_b32 %[t], -1, %[t]\n\tv_lshl_add_u32 v40, %[t], 4, %[ta]\n\t"
                     "ds_read_b64 v[40:41], v40 offset:1032\n\ts_cmp_lt_u32 %[wv], 6\n\ts_waitcnt lgkmcnt(0)\n\t"
                     "v_and_b32 %[ra], 0xffff, v40\n\tv_lshrrev_b32 %[rb], 16, v40\n\tv_and_b32 %[rc], 0xffff, v41\n\tv_lshrrev_b32 %[off], 16, v41\n\t"
                     "s_cbranch_scc1 14f\n\tv_and_b32 %[off], 7, %[t]\n\tv_mul_u32_u24 %[off], 0x380, %[off]\n\tv_lshrrev_b32 %[dst], 2, %[t]\n"
                     "14:\n\ts_cmp_eq_u32 s98, 0\n\ts_cbranch_scc1 17f\n\ts_branch 16f\n"
                     "15:\n\ts_sub_u32 %[tt], %[tt], 1\n\ts_cbranch_scc1 21f\n\t" RTI(4, "v48", "%[t]") "s_barrier\n\t" ISP
                     "s_cbranch_scc0 16f\n\ts_sleep 1\n\t" USEM EPOLL "\n"
                     "16:\n\ts_cmp_lt_u32 %[wv], 6\n\ts_waitcnt vmcnt(0)\n\ts_barrier\n"
                     "17:\n\tds_read_b128 " F0 ", %[ra]\n\tds_read_b128 " F1 ", %[rb]\n\tds_read_b128 " F2 ", %[rc]\n\ts_cbranch_scc0 19f\n\t"
                     "s_waitcnt lgkmcnt(2)\n\t"
                     MF R2 ", " F0 ", " ON ", 0\n\t" AND3("v52", "v53", "v54") MF R0 ", " F0 ", %[w0], 0\n\t" MF R1 ", " F0 ", %[w1], 0\n\t"
                     "s_waitcnt lgkmcnt(1)\n\t"
                     MF R2 ", " F1 ", " ON ", " R2 "\n\t" AND3("%[t]", "v55", "v56") MF R0 ", " F1 ", %[w2], " R0 "\n\t" AND3("%[t]", "v57", "v58")
                     MF R1 ", " F1 ", %[w3], " R1 "\n\t"
                     "s_waitcnt lgkmcnt(0)\n\t"
                     MF R2 ", " F2 ", " ON ", " R2 "\n\t" AND3("%[t]", "v59", "v60") MF R0 ", " F2 ", %[w4], " R0 "\n\t" AND3("%[t]", "v61", "v62")
                     MF R1 ", " F2 ", %[w5], " R1 "\n\t" AND3("%[t]", "1", "v63")
                     "v_cmp_eq_u32 vcc, 0, %[t]\n\ts_and_b64 vcc, vcc, s[98:99]\n\ts_cbranch_scc1 15b\n"
                     "18:\n\t" PKA("v[48:49]", "v[50:51]") "s_cmp_lg_u32 s98, 0\n\t" PKA("v[40:41]", "v[42:43]")
                     "v_pk_fma_f32 v[40:41], v[48:49], v[32:33], v[40:41] op_sel_hi:[1,0,1]\n\t" PKA("v[44:45]", "v[46:47]")
                     "v_add_f32 v40, v40, v41\n\tv_pk_fma_f32 v[44:45], v[48:49], v[32:33], v[44:45] op_sel:[0,1,0]\n\t"
                     "v_add_f32 v44, v44, v45\n\ts_cselect_b64 vcc, %[sm], 0\n\t" RDAN
                     "v_permlane32_swap_b32 v40, v44\n\tv_add_f32 v44, v40, v44\n\ts_nop 0\n\t"
                     "v_permlane16_swap_b32 v40, v41\n\ts_mov_b64 exec, vcc\n\tv_add_f32 v44, v44, v41\n\tv_or_b32 v44, 1, v44\n\t"
                     "buffer_store_dword v44, %[dst], %[rq], %[sq] offen sc1\n\tds_write_b32 %[dst], v44 offset:%[ow]\n\t"
                     "s_mov_b64 exec, -1\n\ts_cbranch_scc1 22f\n"
                     "19:\n\ts_waitcnt lgkmcnt(2)\n\t" MF R2 ", " F0 ", " F0 ", 0\n\t" AND3("v52", "v53", "v54")
                     "s_waitcnt lgkmcnt(1)\n\t" MF R2 ", " F1 ", " F1 ", " R2 "\n\t" AND3("%[t]", "v55", "v56") AND3("%[t]", "v57", "v58")
                     "s_waitcnt lgkmcnt(0)\n\t" MF R2 ", " F2 ", " F2 ", " R2 "\n\t" AND3("%[t]", "v59", "v60") AND3("%[t]", "v61", "v62")
                     AND3("%[t]", "1", "v63") "v_cmp_eq_u32 vcc, 0, %[t]\n\ts_and_b64 vcc, vcc, s[98:99]\n\ts_cbranch_scc1 15b\n\ts_nop 1\n"
                     "20:\n\t" PKA("v[48:49]", "v[50:51]") "v_add_f32 v48, v48, v49\n\ts_cmp_lg_u32 s98, 0\n\ts_cselect_b64 vcc, %[sm], 0\n\t"
                     ZD("row_ror:8", "0x1") "s_nop 0\n\t" RDAN ZD("quad_perm:[1,0,3,2]", "0xf") "s_nop 1\n\t" ZD("quad_perm:[2,3,0,1]", "0xf")
                     "s_nop 1\n\t" ZD("row_half_mirror", "0xf") "s_mov_b64 exec, vcc\n\tv_or_b32 v48, 1, v48\n\t"
                     "buffer_store_dword v48, %[dst], %[rq], %[sq] offen offset:768 sc1\n\tds_write_b32 %[dst], v48 offset:%[ow]+768\n\t"
                     "s_mov_b64 exec, -1\n\ts_cbranch_scc0 10b\n\ts_branch 22f\n"
                     "21:\n\ts_cmp_lt_u32 %[wv], 6\n\ts_nop 7\n\ts_cbranch_scc1 18b\n\ts_branch 20b\n"
                     "22:\n\ts_mov_b32 m0, %[k]\n\ts_waitcnt lgkmcnt(0)\n\tv_readlane_b32 %[anc], %[t], 63"
                     : [t] "=&v"(t), [ra] "=&v"(ra), [rb] "=&v"(rb), [rc] "=&v"(rc), [off] "=&v"(off), [dst] "+v"(dst), [tt] "=&s"(bud),
                       [k] "=&s"(keep), [anc] "=&s"(anc), "+{v[36:39]}"(cs)
                     : [w0] "v"(w[0]), [w1] "v"(w[1]), [w2] "v"(w[2]), [w3] "v"(w[3]), [w4] "v"(w[4]), [w5] "v"(w[5]), [vo] "v"(voff),
                       [so] "s"(gy + GY_INST + gy_row(c) + (wave & 1) * 1024), [rq] "s"(rq), [land] "s"(smb + L_LAND + 5120 + (wave & 1) * 1024),
                       [wv] "s"(wave), [dw] "s"(isO ? 1 : 6), [de] "s"(A_E * tsc >> 10), [sq] "s"(gq + c * 896), [ta] "s"(smb + L_TB),
                       [sm] "s"(wave < 6 ? 0x0000ffff0000ffffull : (wave == 6 ? 0x0001000100010001ull : 0ull)), [ow] "n"(L_CS), [an] "n"(L_ANC),
                       [tg] "n"(L_TAG)
                     : "memory", "vcc", "scc", "s98", "s99", "v32", "v33", FCLOB, DCLOB);
      }
      const u64 qm = wave < 6 ? 0xffull << (c * 8) : (wave == 6 ? 0x0101010101010101ull << c : 0ull);
      asm volatile("v_mov_b64 v[56:57], -1\n\tv_mov_b64 v[58:59], -1\n\tv_add_u32 v60, %[sl], %[off]\n\tv_add_u32 %[off], %[sq], %[off]\n\t"
                   "s_cmp_lt_u32 %[wv], 7\n\ts_cselect_b64 exec, -1, 0\n\ts_andn2_b64 exec, exec, %[qm]\n\t" WAIT_RT
                   "buffer_load_dwordx4 v[56:59], %[off], %[rq], 0 offen sc1\n\ts_mov_b64 exec, %[qm]\n\t"
                   "ds_read_b128 v[56:59], v60\n\ts_mov_b64 exec, -1"
                   : "={v[56:59]}"(qv), [off] "+v"(off)
                   : [rq] "s"(rq), [wv] "s"(wave), [tt] "s"(TT(wave < 7 ? A_Q : 0)), [sq] "s"(gq + wave * 128),
                     [sl] "s"(smb + L_CS + wave * 128 - c * 896), [qm] "s"(qm)
                   : "memory", "scc", "vcc", "s98", "s99", "v60");
      if (wave < 4) __builtin_amdgcn_s_setprio(2);
    }
  }
  asm volatile(".Lmk_code_end:");
}

}

extern "C" void kernel_launch(void* const* d_in, const int* in_sizes, int n_in, void* d_out, int out_size, void* d_ws,
                              size_t ws_size, hipStream_t stream) {
  Params P;
  P.x0 = (const u16*)d_in[0];
  P.kc = (const u16*)d_in[1];
  P.vc = (const u16*)d_in[2];
  P.wq = (const u16*)d_in[3];
  P.wo = (const u16*)d_in[4];
  P.w1 = (const u16*)d_in[6];
  P.w3 = (const u16*)d_in[7];
  P.w2 = (const u16*)d_in[8];
  P.out = (u16*)d_out;
  P.ws = (u32*)d_ws;
  mega_kernel<<<NWG, NT, 0, stream>>>(P);
}
```
